```python
import torch

def _softmax(x, axis=-1):
    return torch.softmax(x, dim=axis)
def _gelu(x):
    return torch.nn.functional.gelu(x, approximate="tanh")
def _silu(x):
    return torch.nn.functional.silu(x)
def _sigmoid(x):
    return torch.sigmoid(x)
def _layernorm(x, axis=-1, eps=1e-5):
    m = x.mean(dim=axis, keepdim=True)
    v = x.var(dim=axis, keepdim=True, unbiased=False)
    return (x - m) / torch.sqrt(v + eps)
def _scatter_add(tgt, idx, src, axis=0):
    return tgt.clone().index_add_(axis, idx, src)
def _segment_sum(src, seg, n):
    out = torch.zeros((n,) + src.shape[1:], dtype=src.dtype)
    return out.index_add_(0, seg, src)
def _pad(x, pw):
    flat = tuple(v for pair in reversed(pw) for v in pair)
    return torch.nn.functional.pad(x, flat)

kernel_name = 'concat_heads__compressed_kv_cla__mha_block_cached__decode'

def setup_inputs():

    _w = (lambda a: (lambda t: t.to(torch.bfloat16) if t.is_floating_point() else t)(torch.as_tensor(a)))
    def _A_setup():
        B, H, Sq, Sk, D, M = 4, 32, 1, 4096, 128, 512
        Dc = 4 * D
        return {
            "x": _w((lambda *s: torch.randn(*s, dtype=torch.bfloat16) * 3.2)(B, Sq, M)),
            "c_kv": _w((lambda *s: torch.randn(*s, dtype=torch.bfloat16) * 0.1)(B, Sk, Dc)),
            "w_q": _w((lambda *s: torch.randn(*s, dtype=torch.bfloat16) * 6.4)(M, H, D) * (M ** -0.5)),
            "w_uk": _w((lambda *s: torch.randn(*s, dtype=torch.bfloat16) * 6.4)(Dc, H, D) * (Dc ** -0.5)),
            "w_uv": _w((lambda *s: torch.randn(*s, dtype=torch.bfloat16) * 0.1)(Dc, H, D) * (Dc ** -0.5)),
            "w_o": _w((lambda *s: torch.randn(*s, dtype=torch.bfloat16) * 0.1)(H, D, M) * ((H * D) ** -0.5)),
        }
    _A_in = _A_setup()

    def _B_setup():
        B, Sq, D = 4, 1, 512
        H, Sk, Dh = 32, 4096, 128
        return {
            "x": _w((lambda *s: torch.randn(*s, dtype=torch.bfloat16) * 1.6)(B, Sq, D)),
            "k_cache": _w((lambda *s: torch.randn(*s, dtype=torch.bfloat16) * 1.6)(B, H, Sk, Dh)),
            "v_cache": _w((lambda *s: torch.randn(*s, dtype=torch.bfloat16) * 0.05)(B, H, Sk, Dh)),
            "wq": _w((lambda *s: torch.randn(*s, dtype=torch.bfloat16) * 1.6)(D, H * Dh) * (D ** -0.5)),
            "wo": _w((lambda *s: torch.randn(*s, dtype=torch.bfloat16) * 0.05)(H * Dh, D) * ((H * Dh) ** -0.5)),
        }
    _B_in = _B_setup()

    return {
        "A_x": _A_in["x"],
        "A_c_kv": _A_in["c_kv"],
        "A_w_q": _A_in["w_q"],
        "A_w_uk": _A_in["w_uk"],
        "A_w_uv": _A_in["w_uv"],
        "A_w_o": _A_in["w_o"],
        "B_x": _B_in["x"],
        "B_k_cache": _B_in["k_cache"],
        "B_v_cache": _B_in["v_cache"],
        "B_wq": _B_in["wq"],
        "B_wo": _B_in["wo"],
    }

def reference(A_x, A_c_kv, A_w_q, A_w_uk, A_w_uv, A_w_o, B_x, B_k_cache, B_v_cache, B_wq, B_wo):
    def _A_ref(x, c_kv, w_q, w_uk, w_uv, w_o):
        B, Sq, M = x.shape[0], x.shape[1], x.shape[2]
        Sk = c_kv.shape[1]
        H, D = w_q.shape[1], w_q.shape[2]
        q = torch.einsum("bqm,mhd->bhqd", x, w_q)
        k = torch.einsum("bkc,chd->bhkd", c_kv, w_uk)
        v = torch.einsum("bkc,chd->bhkd", c_kv, w_uv)
        scores = torch.einsum("bhqd,bhkd->bhqk", q, k) * (D ** -0.5)
        off = Sk - Sq
        qi = torch.arange(Sq)[:, None]
        kj = torch.arange(Sk)[None, :]
        cmask = kj <= (qi + off)
        scores = torch.where(cmask[None, None], scores, -1e9)
        p = _softmax(scores, axis=-1)
        ctx = torch.einsum("bhqk,bhkd->bhqd", p, v)
        return torch.einsum("bhqd,hdm->bqm", ctx, w_o)

    def _B_ref(x, k_cache, v_cache, wq, wo):
        B, Sq, D = x.shape
        H, Dh = 32, 128
        q = (lambda x, *d: x.permute(*d))((x @ wq).reshape(B, Sq, H, Dh), 0, 2, 1, 3)
        scores = torch.einsum("bhqd,bhkd->bhqk", q, k_cache) * (Dh ** -0.5)
        p = _softmax(scores, axis=-1)
        o = torch.einsum("bhqk,bhkd->bhqd", p, v_cache)
        return (lambda x, *d: x.permute(*d))(o, 0, 2, 1, 3).reshape(B, Sq, H * Dh) @ wo

    a = _A_ref(A_x, A_c_kv, A_w_q, A_w_uk, A_w_uv, A_w_o)
    b = _B_ref(B_x, B_k_cache, B_v_cache, B_wq, B_wo)
    return torch.concatenate([a, b], axis=-1)
```

```cpp
#include <hip/hip_runtime.h>
#include <hip/hip_bf16.h>
#include <stdint.h>

typedef __attribute__((ext_vector_type(4))) unsigned u32x4;
typedef __attribute__((ext_vector_type(2))) unsigned u32x2;
typedef __attribute__((ext_vector_type(4))) float f32x4;
typedef __attribute__((ext_vector_type(2))) float f32x2;
typedef __attribute__((ext_vector_type(16))) float f32x16;
typedef __attribute__((ext_vector_type(8))) __bf16 bf16x8;
typedef __attribute__((ext_vector_type(2))) __bf16 bf16x2;
typedef __attribute__((ext_vector_type(4))) short s16x4;
typedef unsigned long long u64;
typedef __amdgpu_buffer_rsrc_t rsrc_t;
#define LAS __attribute__((address_space(3)))
#define GAS __attribute__((address_space(1)))
#define RLX_AGENT __ATOMIC_RELAXED, __HIP_MEMORY_SCOPE_AGENT

#define HELPER 0
#define STAMPS 0

constexpr int AUX_NT = 2, AUX_SC1 = 16;
constexpr float SCALE = 0.08838834764831845f;
constexpr float LOG2E = 1.4426950408889634f;
constexpr unsigned SPIN_CAP = 1u << 17;

constexpr int WS_EPOCH = 0;
constexpr int WS_F1   = 4096;
constexpr int WS_F2   = WS_F1 + 4096;
constexpr int WS_HINT = WS_F2 + 4096;
constexpr int WS_QP   = WS_HINT + 16384;
constexpr int WS_QABS = WS_QP + 2 * 32 * 8 * 4096;
constexpr int WS_PML  = WS_QABS + 4 * 32 * 512 * 2;
constexpr int WS_PO   = WS_PML + 32 * 32 * 8;
constexpr int REC_B   = 1152;
constexpr int WS_REC  = WS_PO + 32 * 32 * 2048;
constexpr int WS_Y    = WS_REC + 32 * 16 * REC_B;
constexpr int WS_END  = WS_Y + 2 * 4 * 8 * 32 * 512;
constexpr int WS_STAMP = 32 << 20;

constexpr int LDS_BYTES = 163840;
constexpr int LDS_SCR = 131072;

__device__ __forceinline__ unsigned cvtpk(float lo, float hi) {
  f32x2 v = {lo, hi};
  return __builtin_bit_cast(unsigned, __builtin_convertvector(v, bf16x2));
}
__device__ __forceinline__ float bf_lo(unsigned u) { return __uint_as_float(u << 16); }
__device__ __forceinline__ float bf_hi(unsigned u) { return __uint_as_float(u & 0xffff0000u); }
__device__ __forceinline__ float rbf(float x) { return __uint_as_float(cvtpk(x, x) << 16); }
__device__ __forceinline__ float dot2(unsigned k, unsigned q, float acc) {
  return __builtin_amdgcn_fdot2_f32_bf16(__builtin_bit_cast(bf16x2, k), __builtin_bit_cast(bf16x2, q), acc, false);
}
template <int CTRL> __device__ __forceinline__ float dpp(float x) {
  return __builtin_bit_cast(float, __builtin_amdgcn_mov_dpp(__builtin_bit_cast(int, x), CTRL, 0xf, 0xf, true));
}
template <int CTRL> __device__ __forceinline__ unsigned dppu(unsigned x) {
  return (unsigned)__builtin_amdgcn_mov_dpp((int)x, CTRL, 0xf, 0xf, true);
}
constexpr int XOR1 = 0xB1, XOR2 = 0x4E, XOR7 = 0x141, XOR8 = 0x128, ROR4 = 0x124;
__device__ __forceinline__ float xrow_max(float x) {
  auto s = __builtin_amdgcn_permlane16_swap(__float_as_uint(x), __float_as_uint(x), false, false);
  x = fmaxf(__uint_as_float(s[0]), __uint_as_float(s[1]));
  auto t = __builtin_amdgcn_permlane32_swap(__float_as_uint(x), __float_as_uint(x), false, false);
  return fmaxf(__uint_as_float(t[0]), __uint_as_float(t[1]));
}
__device__ __forceinline__ float xrow_sum(float x) {
  auto s = __builtin_amdgcn_permlane16_swap(__float_as_uint(x), __float_as_uint(x), false, false);
  x = __uint_as_float(s[0]) + __uint_as_float(s[1]);
  auto t = __builtin_amdgcn_permlane32_swap(__float_as_uint(x), __float_as_uint(x), false, false);
  return __uint_as_float(t[0]) + __uint_as_float(t[1]);
}
__device__ __forceinline__ float row_max(float x) {
  x = fmaxf(x, dpp<XOR8>(x)); x = fmaxf(x, dpp<ROR4>(x)); x = fmaxf(x, dpp<XOR2>(x)); x = fmaxf(x, dpp<XOR1>(x));
  return x;
}
__device__ __forceinline__ float row_sum(float x) {
  x += dpp<XOR8>(x); x += dpp<ROR4>(x); x += dpp<XOR2>(x); x += dpp<XOR1>(x);
  return x;
}
__device__ __forceinline__ float fexp2(float x) { return __builtin_amdgcn_exp2f(x); }
__device__ __forceinline__ f32x4 mfma16(u32x4 a, u32x4 b, f32x4 c) {
  return __builtin_amdgcn_mfma_f32_16x16x32_bf16(__builtin_bit_cast(bf16x8, a), __builtin_bit_cast(bf16x8, b), c, 0, 0, 0);
}
__device__ __forceinline__ f32x16 mfma32(u32x4 a, u32x4 b, f32x16 c) {
  return __builtin_amdgcn_mfma_f32_32x32x16_bf16(__builtin_bit_cast(bf16x8, a), __builtin_bit_cast(bf16x8, b), c, 0, 0, 0);
}
__device__ __forceinline__ rsrc_t mkrs(const void* p, unsigned bytes) {
  return __builtin_amdgcn_make_buffer_rsrc((void*)p, 0, bytes, 0x00020000);
}
__device__ __forceinline__ void bar() { asm volatile("s_waitcnt lgkmcnt(0)\n\ts_barrier" ::: "memory"); }
__device__ __forceinline__ void drain() { asm volatile("s_waitcnt vmcnt(0)" ::: "memory"); }
#define SB() __builtin_amdgcn_sched_barrier(0)
__device__ __forceinline__ unsigned swz(unsigned row) { return ((row & 3) << 2) | ((row >> 2) & 3); }
__device__ __forceinline__ u32x2 trread(LAS char* p) {
  return __builtin_bit_cast(u32x2, __builtin_amdgcn_ds_read_tr16_b64_v4i16((LAS s16x4*)p));
}

struct Tag { unsigned tl, th; u64 e; };
__device__ __forceinline__ Tag mktag(u64 e0) {
  Tag t;
  asm volatile("" : "+s"(e0));
  t.e = e0 * 6364136223846793005ull + 1442695040888963407ull;
  t.th = (unsigned)(t.e >> 32);
  t.tl = (unsigned)t.e & 0xff000000u;
  return t;
}
__device__ __forceinline__ unsigned gpack(float v, const Tag& t) { return ((__float_as_uint(v) + 0x80u) >> 8) | t.tl; }
__device__ __forceinline__ u32x4 gpair(float a, float b, const Tag& t) { return u32x4{gpack(a, t), t.th, gpack(b, t), t.th}; }
__device__ __forceinline__ bool gok(u32x4 g, const Tag& t) {
  return ((((g[0] ^ t.tl) | (g[2] ^ t.tl)) >> 24) | (g[1] ^ t.th) | (g[3] ^ t.th)) == 0u;
}
__device__ __forceinline__ float gval(unsigned lo) { return __uint_as_float(lo << 8); }

#define STAMP(i) do {} while (0)

__device__ __forceinline__ void diag_gemv(const u32x4 (&wr)[16], const u32x4 (&xp)[2][2], const unsigned (&mask)[4], f32x4 (&acc)[2]) {
#pragma unroll
  for (int i = 0; i < 16; i++) {
#pragma unroll
    for (int s = 0; s < 2; s++) {
      const unsigned src = xp[s][i >> 3][(i >> 1) & 3];
      const unsigned dup = __builtin_amdgcn_perm(src, src, (i & 1) ? 0x03020302u : 0x01000100u);
      const u32x4 a = {dup & mask[0], dup & mask[1], dup & mask[2], dup & mask[3]};
      acc[s] = mfma16(a, wr[i], acc[s]);
    }
  }
}

__device__ __forceinline__ void qprod(const char* W, const char* x, int head, int p, int lane, const unsigned (&mask)[4],
                                      rsrc_t rws, int qpoff, u64 e0) {
  const int g = lane >> 4, c = lane & 15;
  const rsrc_t rw = mkrs(W, 4u << 20);
  const int voff = (64 * p + 16 * g) * 8192 + head * 256 + c * 16;
  u32x4 xp[2][2];
#pragma unroll
  for (int s = 0; s < 2; s++) {
    const u32x4* xs = (const u32x4*)(x + (2 * s + (c >> 3)) * 1024 + (64 * p + 16 * g) * 2);
    xp[s][0] = xs[0]; xp[s][1] = xs[1];
  }
  u32x4 wr[16];
#pragma unroll
  for (int i = 0; i < 16; i++) wr[i] = __builtin_amdgcn_raw_buffer_load_b128(rw, voff, i * 8192, AUX_NT);
  SB();
  const Tag tg = mktag(e0);
  f32x4 acc[2] = {{0.f, 0.f, 0.f, 0.f}, {0.f, 0.f, 0.f, 0.f}};
  diag_gemv(wr, xp, mask, acc);
#pragma unroll
  for (int s = 0; s < 2; s++) {
    __builtin_amdgcn_raw_buffer_store_b128(gpair(acc[s][0], acc[s][1], tg), rws, qpoff + (2 * s) * 1024 + lane * 16, 0, AUX_SC1);
    __builtin_amdgcn_raw_buffer_store_b128(gpair(acc[s][2], acc[s][3], tg), rws, qpoff + (2 * s + 1) * 1024 + lane * 16, 0, AUX_SC1);
  }
  SB();
}

__device__ __forceinline__ unsigned qgather(rsrc_t rws, int qpbase, int b, int lane, const Tag& tg) {
  const int u = lane >> 5, rest = lane & 31;
  const int off = qpbase + ((b >> 1) * 2 + u) * 1024 + (32 * (b & 1) + rest) * 16;
  u32x4 v[8];
  for (unsigned spins = 0;; spins++) {
    asm volatile("" ::: "memory");
#pragma unroll
    for (int p = 0; p < 8; p++) v[p] = __builtin_amdgcn_raw_buffer_load_b128(rws, off, p * 4096, AUX_SC1);
    bool ok = true;
#pragma unroll
    for (int p = 0; p < 8; p++) ok &= gok(v[p], tg);
    if (__all(ok) || spins > SPIN_CAP) break;
    __builtin_amdgcn_s_sleep(1);
  }
  float a = 0.f, bb = 0.f;
#pragma unroll
  for (int p = 0; p < 8; p++) { a += gval(v[p][0]); bb += gval(v[p][2]); }
  return cvtpk(a, bb);
}
__device__ __forceinline__ int qgather_dw(int lane) { return 4 * (lane & 15) + 2 * ((lane >> 4) & 1) + (lane >> 5); }

__device__ __forceinline__ void poll32(char* ws, int off, int lane, const Tag& tg) {
  GAS u64* f = (GAS u64*)(ws + off + (lane & 31) * 128);
  for (unsigned spins = 0;; spins++) {
    const u64 v = __hip_atomic_load(f, RLX_AGENT);
    if (__all(v == tg.e) || spins > SPIN_CAP) break;
    __builtin_amdgcn_s_sleep(4);
  }
  __builtin_amdgcn_fence(__ATOMIC_ACQUIRE, "wavefront");
}

constexpr int NSC = HELPER ? 5 : 6;
__device__ __forceinline__ void b_issue(u32x4 (&ring)[16], const char* kb, int n, int lane) {
  const rsrc_t rk = mkrs(kb, n * 1024);
#pragma unroll
  for (int i = 0; i < 16; i++) ring[i] = __builtin_amdgcn_raw_buffer_load_b128(rk, lane * 16, i * 1024, AUX_NT);
  SB();
}
template <int I> __device__ __forceinline__ void v_slot(u32x4 (&ring)[16], unsigned pk, const unsigned (&mask)[4], f32x4& acc, rsrc_t rn, int voff, int so) {
  const unsigned d = dppu<0x150 + I>(pk);
  const u32x4 a = {d & mask[0], d & mask[1], d & mask[2], d & mask[3]};
  acc = mfma16(a, ring[I], acc);
  asm volatile("" : "+v"(acc));
  ring[I] = __builtin_amdgcn_raw_buffer_load_b128(rn, voff, so + I * 1024, AUX_NT);
  SB();
}
__device__ __forceinline__ void b_stream(u32x4 (&ring)[16], const char* kb, const char* vb, int n, u32x4 q, int lane, const unsigned (&mask)[4],
                                         char* ws, rsrc_t rws, int hintoff, const Tag& tg, float& m_out, float& l_out, f32x4& o_out) {
  const int c = lane & 15, voff = lane * 16;
  const int nit = (n + 15) >> 4;
  const int hoff = lane == 0 ? hintoff : 0x7ffffff0;
  float sc[NSC];
#pragma unroll
  for (int k = 0; k < NSC; k++) sc[k] = -INFINITY;
#pragma unroll 1
  for (int it = 0; it < nit; it++) {
    const bool last = it == nit - 1;
    const rsrc_t rn = mkrs(last ? vb : kb, n * 1024);
    const int so = (last ? it : it + 1) * 16384;
    float x[16];
#pragma unroll
    for (int i = 0; i < 16; i++) {
      float a = dot2(ring[i][0], q[0], 0.f);
      a = dot2(ring[i][1], q[1], a);
      a = dot2(ring[i][2], q[2], a);
      x[i] = dot2(ring[i][3], q[3], a);
      asm volatile("" : "+v"(x[i]));
      ring[i] = __builtin_amdgcn_raw_buffer_load_b128(rn, voff, so + i * 1024, AUX_NT);
      SB();
    }
#define TR_STEP(HALF, CTRL, BIT) _Pragma("unroll") for (int h = 0; h < (HALF); ++h) { \
      const float t1 = x[h] + dpp<CTRL>(x[h]), t2 = x[h + (HALF)] + dpp<CTRL>(x[h + (HALF)]); x[h] = (c & (BIT)) ? t2 : t1; }
    TR_STEP(8, XOR8, 8) TR_STEP(4, XOR7, 4) TR_STEP(2, XOR2, 2) TR_STEP(1, XOR1, 1)
#undef TR_STEP
    float s = rbf(rbf(x[0]) * SCALE);
    s = (it * 16 + c < n) ? s : -INFINITY;
#pragma unroll
    for (int k = NSC - 1; k > 0; k--) sc[k] = sc[k - 1];
    sc[0] = s;
  }
  STAMP(3);
  float m = sc[0];
#pragma unroll
  for (int k = 1; k < NSC; k++) m = fmaxf(m, sc[k]);
  m = xrow_max(row_max(m));
  unsigned pk[NSC];
  float l = 0.f;
#pragma unroll
  for (int k = 0; k < NSC; k++) {
    const float p = fexp2((sc[k] - m) * LOG2E);
    pk[k] = cvtpk(p, p);
    l += bf_lo(pk[k]);
  }
  l = xrow_sum(row_sum(l));
  f32x4 acc = {0.f, 0.f, 0.f, 0.f};
#pragma unroll 1
  for (int it = nit - 1; it >= 0; it--) {
    const rsrc_t rn = mkrs(vb, it > 0 ? n * 1024 : 0);
    const int so = (it > 0 ? it - 1 : 0) * 16384;
    const unsigned p0 = pk[0];
    v_slot<0>(ring, p0, mask, acc, rn, voff, so);  v_slot<1>(ring, p0, mask, acc, rn, voff, so);
    v_slot<2>(ring, p0, mask, acc, rn, voff, so);  v_slot<3>(ring, p0, mask, acc, rn, voff, so);
    v_slot<4>(ring, p0, mask, acc, rn, voff, so);  v_slot<5>(ring, p0, mask, acc, rn, voff, so);
    v_slot<6>(ring, p0, mask, acc, rn, voff, so);  v_slot<7>(ring, p0, mask, acc, rn, voff, so);
    v_slot<8>(ring, p0, mask, acc, rn, voff, so);  v_slot<9>(ring, p0, mask, acc, rn, voff, so);
    v_slot<10>(ring, p0, mask, acc, rn, voff, so); v_slot<11>(ring, p0, mask, acc, rn, voff, so);
    __builtin_amdgcn_raw_buffer_store_b64(u32x2{(unsigned)tg.e, tg.th}, rws, it == 0 ? hoff : 0x7ffffff0, 0, AUX_SC1);
    SB();
    v_slot<12>(ring, p0, mask, acc, rn, voff, so); v_slot<13>(ring, p0, mask, acc, rn, voff, so);
    v_slot<14>(ring, p0, mask, acc, rn, voff, so); v_slot<15>(ring, p0, mask, acc, rn, voff, so);
#pragma unroll
    for (int k = 0; k < NSC - 1; k++) pk[k] = pk[k + 1];
  }
  m_out = m; l_out = l; o_out = acc;
}

template <int NREC>
__device__ __forceinline__ void b_merge(LAS char* scr, int w, int lane, int tid, float m, float l, f32x4 o, rsrc_t rws, int recoff, const Tag& tg) {
  LAS float* mrg = (LAS float*)scr;
  const int c = lane & 15, grp = lane >> 4;
  if (grp < 2) *(LAS f32x4*)(mrg + w * 132 + 4 + 8 * c + 4 * grp) = o;
  if (lane == 0) { mrg[w * 132] = m; mrg[w * 132 + 1] = l; }
  bar();
  constexpr int WPR = 8 / NREC;
  if (tid < NREC * 64) {
    const int r = tid >> 6;
    float mm[WPR], M = -INFINITY;
#pragma unroll
    for (int k = 0; k < WPR; k++) { mm[k] = mrg[(r * WPR + k) * 132]; M = fmaxf(M, mm[k]); }
    float a = 0.f, b = 0.f, L = 0.f;
#pragma unroll
    for (int k = 0; k < WPR; k++) {
      const float f = fexp2((mm[k] - M) * LOG2E);
      const f32x2 v = *(LAS f32x2*)(mrg + (r * WPR + k) * 132 + 4 + 2 * lane);
      a = fmaf(f, v[0], a); b = fmaf(f, v[1], b);
      L = fmaf(f, mrg[(r * WPR + k) * 132 + 1], L);
    }
    __builtin_amdgcn_raw_buffer_store_b128(gpair(a, b, tg), rws, recoff + r * REC_B + 16 + lane * 16, 0, AUX_SC1);
    if (lane == 0) __builtin_amdgcn_raw_buffer_store_b128(gpair(M, L, tg), rws, recoff + r * REC_B, 0, AUX_SC1);
  }
}

template <int NR>
__device__ __forceinline__ void dma_rows(rsrc_t rs, int srcoff, LAS char* img, int r0, int lane) {
#pragma unroll
  for (int u = 0; u < NR; u++) {
    const int r = r0 + u;
    unsigned keep;
    asm volatile("s_mov_b32 %0, m0\n\ts_mov_b32 m0, %1\n\ts_nop 0\n\tbuffer_load_dwordx4 %2, %3, %4 offen nt lds\n\ts_mov_b32 m0, %0"
                 : "=&s"(keep) : "s"((unsigned)(uintptr_t)img + r * 1024), "v"((lane ^ swz(r)) * 16), "s"(rs), "s"(srcoff + r * 1024) : "memory");
  }
  SB();
}

__device__ __forceinline__ void wo_frags(LAS char* img, int w, int lane, u32x4 (&wf)[4][4]) {
  const int g = lane >> 4, q = (lane & 15) >> 2, p = lane & 3;
#pragma unroll
  for (int ks = 0; ks < 4; ks++)
#pragma unroll
    for (int tl = 0; tl < 4; tl++) {
      const int ch = 8 * w + 2 * tl + (p >> 1);
      const int r0 = 32 * ks + 8 * g + q, r1 = r0 + 4;
      const u32x2 a = trread(img + r0 * 1024 + ((ch ^ swz(r0)) * 16) + 8 * (p & 1));
      const u32x2 b = trread(img + r1 * 1024 + ((ch ^ swz(r1)) * 16) + 8 * (p & 1));
      wf[tl][ks] = u32x4{a[0], a[1], b[0], b[1]};
    }
}
__device__ __forceinline__ void wo_apply(const u32x4 (&wf)[4][4], LAS char* cb, int w, int lane, int head, rsrc_t rws, int yoff, const Tag& tg) {
  const int n = lane & 15, grp = lane >> 4;
  u32x4 bf[4];
#pragma unroll
  for (int ks = 0; ks < 4; ks++) bf[ks] = *(LAS u32x4*)(cb + (n & 3) * 256 + ks * 64 + grp * 16);
#pragma unroll
  for (int tl = 0; tl < 4; tl++) {
    f32x4 acc = {0.f, 0.f, 0.f, 0.f};
#pragma unroll
    for (int ks = 0; ks < 4; ks++) acc = mfma16(wf[tl][ks], bf[ks], acc);
    if (n < 4) {
      const int off = yoff + ((n * 8 + w) * 32 + head) * 512 + (16 * tl + 4 * grp) * 8;
      __builtin_amdgcn_raw_buffer_store_b128(gpair(acc[0], acc[1], tg), rws, off, 0, AUX_SC1);
      __builtin_amdgcn_raw_buffer_store_b128(gpair(acc[2], acc[3], tg), rws, off + 16, 0, AUX_SC1);
    }
  }
}
__device__ __forceinline__ void y_reduce(LAS char* scr, int w, int lane, int tid, int head, rsrc_t rws, int yoff, const Tag& tg, unsigned* out, int part) {
  const int off = yoff + head * 32 * 512 + (tid >> 4) * 512 + (tid & 15) * 16;
  u32x4 v0, v1;
  for (unsigned spins = 0;; spins++) {
    asm volatile("" ::: "memory");
    v0 = __builtin_amdgcn_raw_buffer_load_b128(rws, off, 0, AUX_SC1);
    v1 = __builtin_amdgcn_raw_buffer_load_b128(rws, off + 256, 0, AUX_SC1);
    if (__all(gok(v0, tg) & gok(v1, tg)) || spins > SPIN_CAP) break;
    __builtin_amdgcn_s_sleep(1);
  }
  f32x4 s = {xrow_sum(gval(v0[0])), xrow_sum(gval(v0[2])), xrow_sum(gval(v1[0])), xrow_sum(gval(v1[2]))};
  LAS float* red = (LAS float*)scr;
  if (lane < 16) *(LAS f32x4*)(red + w * 64 + lane * 4) = s;
  bar();
  if (tid < 32) {
    const int k = tid & 15, hi = tid >> 4;
    float a = 0.f, b = 0.f;
#pragma unroll
    for (int j = 0; j < 8; j++) { const f32x2 v = *(LAS f32x2*)(red + j * 64 + k * 4 + 2 * hi); a += v[0]; b += v[1]; }
    out[(head >> 3) * 512 + part * 256 + (head & 7) * 32 + hi * 16 + k] = cvtpk(a, b);
  }
  bar();
}

__global__ __launch_bounds__(512, 2) void k_all(const char* __restrict__ xA, const char* __restrict__ ckv, const char* __restrict__ wqA,
                                                const char* __restrict__ wuk, const char* __restrict__ wuv, const char* __restrict__ woA,
                                                const char* __restrict__ xB, const char* __restrict__ kc, const char* __restrict__ vc,
                                                const char* __restrict__ wqB, const char* __restrict__ woB, char* ws, unsigned* out) {
  __shared__ __attribute__((aligned(16))) char smem[LDS_BYTES];
  LAS char* lds = (LAS char*)smem;
  LAS char* scr = lds + LDS_SCR;
  const int tid = threadIdx.x, lane = tid & 63, w = __builtin_amdgcn_readfirstlane(tid >> 6);
  const int blk = blockIdx.x, head = 4 * (blk & 7) + (blk >> 6), sub = (blk >> 3) & 7;
  STAMP(0);
  asm volatile("" :: "s"(xA), "s"(ckv), "s"(wqA), "s"(wuk), "s"(wuv), "s"(woA), "s"(xB), "s"(kc), "s"(vc), "s"(wqB), "s"(woB), "s"(ws), "s"(out));
  const u64 e0 = *(const __attribute__((address_space(4))) u64*)(ws + WS_EPOCH);
  SB();
  const rsrc_t rws = mkrs(ws, WS_END);
  unsigned mask[4];
#pragma unroll
  for (int v = 0; v < 4; v++) mask[v] = (((lane & 7) >> 1) == v) ? ((lane & 1) ? 0xffff0000u : 0x0000ffffu) : 0u;
  const int qpA = WS_QP + head * 32768, qpB = WS_QP + (32 + head) * 32768;

  if (w < 2) {
    __builtin_amdgcn_s_setprio(3);
    qprod(w == 0 ? wqB : wqA, w == 0 ? xB : xA, head, sub, lane, mask, rws, (w == 0 ? qpB : qpA) + sub * 4096, e0);
    __builtin_amdgcn_s_setprio(0);
  }
  STAMP(1);

  if (sub < 6) {
    const int wi = sub * 8 + w, b = wi / 12, r = wi % 12;
    const int g0 = HELPER ? r * 80 : r * 1024 / 12, n = HELPER ? 80 : (r + 1) * 1024 / 12 - g0;
    const size_t base = ((size_t)(b * 32 + head) * 1024 + g0) * 1024;
    u32x4 ring[16];
    if (w == 0) {
      const Tag tg = mktag(e0);
      const int b0 = (sub * 8) / 12, b1 = (sub * 8 + 4) / 12;
      const unsigned q0 = qgather(rws, qpB, b0, lane, tg);
      ((LAS unsigned*)scr)[1280 + qgather_dw(lane)] = q0;
      const unsigned q1 = b1 == b0 ? q0 : qgather(rws, qpB, b1, lane, tg);
      ((LAS unsigned*)scr)[1280 + 64 + qgather_dw(lane)] = q1;
    } else {
      b_issue(ring, kc + base, n, lane);
    }
    bar();
    if (w == 0) b_issue(ring, kc + base, n, lane);
    const Tag tg = mktag(e0);
    STAMP(2);
    const u32x4 q = *(LAS u32x4*)(scr + 5120 + (w >> 2) * 256 + (lane & 15) * 16);
    float m, l; f32x4 o;
    b_stream(ring, kc + base, vc + base, n, q, lane, mask, ws, rws, WS_HINT + (head * 64 + wi) * 8, tg, m, l, o);
    STAMP(4);
    b_merge<2>(scr, w, lane, tid, m, l, o, rws, WS_REC + (head * 16 + 2 * sub) * REC_B, tg);
    STAMP(5);
  } else if (sub == 6) {
    const int b = head >> 3, sp = head & 7;
    const rsrc_t rc = mkrs(ckv + ((size_t)b * 4096 + sp * 512) * 1024, 512 * 1024);
    dma_rows<8>(rc, 0, lds, 8 * w, lane);
    dma_rows<8>(rc, 65536, lds + 65536, 8 * w, lane);
    const Tag tg = mktag(e0);
    if (w == 2) poll32(ws, WS_F1, lane, tg);
    bar();
    STAMP(2);
    {
      u32x4 t[4];
#pragma unroll
      for (int i = 0; i < 4; i++) t[i] = __builtin_amdgcn_raw_buffer_load_b128(rws, WS_QABS + b * 32768 + (8 * i + w) * 1024 + lane * 16, 0, AUX_SC1);
#pragma unroll
      for (int i = 0; i < 4; i++) *(LAS u32x4*)(scr + (8 * i + w) * 1024 + ((lane ^ swz(8 * i + w)) * 16)) = t[i];
    }
    bar();
    const int kb = w & 3, hb = w >> 2, l16 = lane & 15, grp = lane >> 4;
    u32x4 qf[16];
    {
      const int row = 16 * hb + l16;
#pragma unroll
      for (int ks = 0; ks < 16; ks++) qf[ks] = *(LAS u32x4*)(scr + row * 1024 + (((4 * ks + grp) ^ swz(row)) * 16));
    }
    bar();
    STAMP(3);
    LAS float* mx = (LAS float*)scr;
    LAS char* pl = scr + 1024;
    LAS float* lr = (LAS float*)(scr + 8192);
    f32x16 o0, o1;
#pragma unroll
    for (int j = 0; j < 16; j++) { o0[j] = 0.f; o1[j] = 0.f; }
    float m1 = -INFINITY, m2 = -INFINITY, l1 = 0.f;
    const int h1 = 16 * hb + l16, h2 = lane & 31, hh = lane >> 5;
    const int arow = 16 * kb + l16;
    const int tq = (lane & 15) >> 2, tp = lane & 3, tblk = (lane >> 4) & 1;
#pragma unroll 1
    for (int t = 0; t < 8; t++) {
      if (t < 7) asm volatile("s_waitcnt vmcnt(8)" ::: "memory"); else asm volatile("s_waitcnt vmcnt(0)" ::: "memory");
      bar();
      LAS char* img = lds + (t & 1) * 65536;
      f32x4 s = {0.f, 0.f, 0.f, 0.f};
#pragma unroll
      for (int ks = 0; ks < 16; ks++) {
        const u32x4 a = *(LAS u32x4*)(img + arow * 1024 + (((4 * ks + grp) ^ swz(arow)) * 16));
        s = mfma16(a, qf[ks], s);
      }
      float x[4];
#pragma unroll
      for (int j = 0; j < 4; j++) x[j] = rbf(rbf(s[j]) * SCALE);
      const float lm = xrow_max(fmaxf(fmaxf(x[0], x[1]), fmaxf(x[2], x[3])));
      if (grp == 0) mx[h1 * 4 + kb] = lm;
      bar();
      const f32x4 ma = *(LAS f32x4*)(mx + h1 * 4), mb = *(LAS f32x4*)(mx + h2 * 4);
      const float n1 = fmaxf(m1, fmaxf(fmaxf(ma[0], ma[1]), fmaxf(ma[2], ma[3])));
      const float n2 = fmaxf(m2, fmaxf(fmaxf(mb[0], mb[1]), fmaxf(mb[2], mb[3])));
      const float al1 = fexp2((m1 - n1) * LOG2E), al2 = fexp2((m2 - n2) * LOG2E);
      m1 = n1; m2 = n2;
      const u32x2 pp = {cvtpk(fexp2((x[0] - n1) * LOG2E), fexp2((x[1] - n1) * LOG2E)), cvtpk(fexp2((x[2] - n1) * LOG2E), fexp2((x[3] - n1) * LOG2E))};
      l1 = fmaf(l1, al1, (bf_lo(pp[0]) + bf_hi(pp[0])) + (bf_lo(pp[1]) + bf_hi(pp[1])));
      *(LAS u32x2*)(pl + h1 * 144 + (16 * kb + 4 * grp) * 2) = pp;
#pragma unroll
      for (int j = 0; j < 16; j++) { o0[j] *= al2; o1[j] *= al2; }
      bar();
#pragma unroll
      for (int ks = 0; ks < 4; ks++) {
        const u32x4 pb = *(LAS u32x4*)(pl + h2 * 144 + (16 * ks + 8 * hh) * 2);
        const int r0 = 16 * ks + 8 * hh + tq, r1 = r0 + 4;
        const int ch0 = 8 * w + 2 * tblk + (tp >> 1), ch1 = ch0 + 4;
        const u32x2 a0 = trread(img + r0 * 1024 + ((ch0 ^ swz(r0)) * 16) + 8 * (tp & 1));
        const u32x2 a1 = trread(img + r1 * 1024 + ((ch0 ^ swz(r1)) * 16) + 8 * (tp & 1));
        const u32x2 b0 = trread(img + r0 * 1024 + ((ch1 ^ swz(r0)) * 16) + 8 * (tp & 1));
        const u32x2 b1 = trread(img + r1 * 1024 + ((ch1 ^ swz(r1)) * 16) + 8 * (tp & 1));
        o0 = mfma32(u32x4{a0[0], a0[1], a1[0], a1[1]}, pb, o0);
        o1 = mfma32(u32x4{b0[0], b0[1], b1[0], b1[1]}, pb, o1);
      }
      bar();
      if (t < 6) dma_rows<8>(rc, (t + 2) * 65536, img, 8 * w, lane);
    }
    STAMP(4);
    {
      const int off = WS_PO + (head * 32 + h2) * 2048 + (64 * w + 4 * hh) * 4;
#pragma unroll
      for (int j4 = 0; j4 < 4; j4++) {
        const f32x4 v0 = {o0[4 * j4], o0[4 * j4 + 1], o0[4 * j4 + 2], o0[4 * j4 + 3]};
        const f32x4 v1 = {o1[4 * j4], o1[4 * j4 + 1], o1[4 * j4 + 2], o1[4 * j4 + 3]};
        __builtin_amdgcn_raw_buffer_store_b128(__builtin_bit_cast(u32x4, v0), rws, off + j4 * 32, 0, AUX_SC1);
        __builtin_amdgcn_raw_buffer_store_b128(__builtin_bit_cast(u32x4, v1), rws, off + 128 + j4 * 32, 0, AUX_SC1);
      }
      const float ls = xrow_sum(l1);
      if (grp == 0) lr[kb * 32 + h1] = ls;
      bar();
      if (tid < 32) {
        const f32x2 v = {m2, (lr[tid] + lr[32 + tid]) + (lr[64 + tid] + lr[96 + tid])};
        __builtin_amdgcn_raw_buffer_store_b64(__builtin_bit_cast(u32x2, v), rws, WS_PML + (head * 32 + tid) * 8, 0, AUX_SC1);
      }
      drain();
      bar();
      if (tid == 0) __hip_atomic_store((GAS u64*)(ws + WS_F2 + head * 128), tg.e, RLX_AGENT);
    }
    STAMP(5);
  } else {
    const int l16 = lane & 15, grp = lane >> 4;
    u32x4 wr[16];
    {
      const rsrc_t rk = mkrs(wuk, 4u << 20);
      const int voff = (64 * w + l16) * 8192 + head * 256 + grp * 16;
#pragma unroll
      for (int i = 0; i < 16; i++) wr[i] = __builtin_amdgcn_raw_buffer_load_b128(rk, voff + (i & 3) * 64, (i >> 2) * 16 * 8192, AUX_NT);
      SB();
    }
    const Tag tg = mktag(e0);
    if (w < 4) {
      const unsigned q = qgather(rws, qpA, w, lane, tg);
      ((LAS unsigned*)scr)[w * 64 + qgather_dw(lane)] = q;
    }
    bar();
    STAMP(2);
    {
      u32x4 bf[4];
#pragma unroll
      for (int ks = 0; ks < 4; ks++) bf[ks] = *(LAS u32x4*)(scr + (l16 & 3) * 256 + ks * 64 + grp * 16);
#pragma unroll
      for (int rt = 0; rt < 4; rt++) {
        f32x4 acc = {0.f, 0.f, 0.f, 0.f};
#pragma unroll
        for (int ks = 0; ks < 4; ks++) acc = mfma16(wr[4 * rt + ks], bf[ks], acc);
        if (l16 < 4) {
          const u32x2 v = {cvtpk(acc[0], acc[1]), cvtpk(acc[2], acc[3])};
          __builtin_amdgcn_raw_buffer_store_b64(v, rws, WS_QABS + (l16 * 32 + head) * 1024 + (64 * w + 16 * rt + 4 * grp) * 2, 0, AUX_SC1);
        }
      }
    }
    drain();
    bar();
    if (tid == 0) __hip_atomic_store((GAS u64*)(ws + WS_F1 + head * 128), tg.e, RLX_AGENT);
    STAMP(3);
    u32x4 wf[4][4];
    {
      const rsrc_t ro = mkrs(woA + (size_t)head * 131072, 131072);
      dma_rows<16>(ro, 0, lds, 16 * w, lane);
      const rsrc_t rv = mkrs(wuv, 4u << 20);
      const int voff = (64 * w + 16 * grp) * 8192 + head * 256 + l16 * 16;
#pragma unroll
      for (int i = 0; i < 16; i++) wr[i] = __builtin_amdgcn_raw_buffer_load_b128(rv, voff, i * 8192, AUX_NT);
      SB();
      asm volatile("s_waitcnt vmcnt(16)" ::: "memory");
      bar();
      wo_frags(lds, w, lane, wf);
      bar();
      const rsrc_t rb = mkrs(woB + (size_t)head * 131072, 131072);
      dma_rows<16>(rb, 0, lds, 16 * w, lane);
    }
    STAMP(4);
    if (w == 0) poll32(ws, WS_F2, lane, tg);
    bar();
    STAMP(5);
    {
      const int b = tid >> 7, c4 = tid & 127;
      u32x4 po[8]; u32x2 ml[8];
#pragma unroll
      for (int s = 0; s < 8; s++) {
        ml[s] = __builtin_amdgcn_raw_buffer_load_b64(rws, WS_PML + ((8 * b + s) * 32 + head) * 8, 0, AUX_SC1);
        po[s] = __builtin_amdgcn_raw_buffer_load_b128(rws, WS_PO + ((8 * b + s) * 32 + head) * 2048 + c4 * 16, 0, AUX_SC1);
      }
      float M = -INFINITY;
#pragma unroll
      for (int s = 0; s < 8; s++) M = fmaxf(M, __uint_as_float(ml[s][0]));
      float L = 0.f;
      f32x4 a = {0.f, 0.f, 0.f, 0.f};
#pragma unroll
      for (int s = 0; s < 8; s++) {
        const float f = fexp2((__uint_as_float(ml[s][0]) - M) * LOG2E);
        L = fmaf(f, __uint_as_float(ml[s][1]), L);
        a += f * __builtin_bit_cast(f32x4, po[s]);
      }
      const float inv = 1.f / L;
      const u32x2 v = {cvtpk(a[0] * inv, a[1] * inv), cvtpk(a[2] * inv, a[3] * inv)};
      *(LAS u32x2*)(scr + b * 1024 + c4 * 8) = v;
    }
    bar();
    {
      u32x4 xp[2][2];
#pragma unroll
      for (int s = 0; s < 2; s++) {
        LAS u32x4* xs = (LAS u32x4*)(scr + (2 * s + (l16 >> 3)) * 1024 + (64 * w + 16 * grp) * 2);
        xp[s][0] = xs[0]; xp[s][1] = xs[1];
      }
      f32x4 acc[2] = {{0.f, 0.f, 0.f, 0.f}, {0.f, 0.f, 0.f, 0.f}};
      diag_gemv(wr, xp, mask, acc);
      LAS float* red = (LAS float*)(scr + 8192);
#pragma unroll
      for (int s = 0; s < 2; s++) *(LAS f32x4*)(red + w * 512 + (2 * s + (grp >> 1)) * 128 + 8 * l16 + 4 * (grp & 1)) = acc[s];
      bar();
      if (tid < 256) {
        float a = 0.f, b = 0.f;
#pragma unroll
        for (int k = 0; k < 8; k++) { const f32x2 v = *(LAS f32x2*)(red + k * 512 + 2 * tid); a += v[0]; b += v[1]; }
        ((LAS unsigned*)(scr + 4096))[tid] = cvtpk(a, b);
      }
      bar();
    }
    wo_apply(wf, scr + 4096, w, lane, head, rws, WS_Y, tg);
    STAMP(6);
    asm volatile("s_waitcnt vmcnt(8)" ::: "memory");
    bar();
    wo_frags(lds, w, lane, wf);
    y_reduce(scr, w, lane, tid, head, rws, WS_Y, tg, out, 0);
    STAMP(7);
    if (w == 0) {
      GAS u64* f = (GAS u64*)(ws + WS_HINT + (head * 64 + (lane < (HELPER ? 56 : 48) ? lane : 0)) * 8);
      for (unsigned spins = 0;; spins++) {
        const u64 v = __hip_atomic_load(f, RLX_AGENT);
        if (__all(v == tg.e) || spins > SPIN_CAP) break;
        __builtin_amdgcn_s_sleep(4);
      }
    }
    bar();
    STAMP(8);
    {
      const int b = w & 3, hf = w >> 2;
      const bool act = HELPER || grp < 3;
      const int rec = grp < 3 ? 3 * b + grp : (HELPER ? 12 + b : 3 * b);
      const int off = WS_REC + (head * 16 + rec) * REC_B;
      u32x4 g0, g1, g2;
      for (unsigned spins = 0;; spins++) {
        asm volatile("" ::: "memory");
        g0 = __builtin_amdgcn_raw_buffer_load_b128(rws, off, 0, AUX_SC1);
        g1 = __builtin_amdgcn_raw_buffer_load_b128(rws, off + 16 + (32 * hf + l16) * 16, 0, AUX_SC1);
        g2 = __builtin_amdgcn_raw_buffer_load_b128(rws, off + 16 + (32 * hf + 16 + l16) * 16, 0, AUX_SC1);
        if (__all(gok(g0, tg) & gok(g1, tg) & gok(g2, tg)) || spins > SPIN_CAP) break;
      }
      const float mr = gval(g0[0]);
      const float M = xrow_max(mr);
      const float f = act ? fexp2((mr - M) * LOG2E) : 0.f;
      const float inv = 1.f / xrow_sum(f * gval(g0[2]));
      const float a0 = xrow_sum(f * gval(g1[0])) * inv, a1 = xrow_sum(f * gval(g1[2])) * inv;
      const float a2 = xrow_sum(f * gval(g2[0])) * inv, a3 = xrow_sum(f * gval(g2[2])) * inv;
      if (grp == 0) {
        ((LAS unsigned*)(scr + 4096))[b * 64 + 32 * hf + l16] = cvtpk(a0, a1);
        ((LAS unsigned*)(scr + 4096))[b * 64 + 32 * hf + 16 + l16] = cvtpk(a2, a3);
      }
    }
    bar();
    STAMP(9);
    wo_apply(wf, scr + 4096, w, lane, head, rws, WS_Y + 524288, tg);
    y_reduce(scr, w, lane, tid, head, rws, WS_Y + 524288, tg, out, 1);
    if (head == 0 && tid == 0) __hip_atomic_store((GAS u64*)(ws + WS_EPOCH), tg.e, RLX_AGENT);
    STAMP(10);
  }
}

extern "C" void kernel_launch(void* const* d_in, const int* in_sizes, int n_in, void* d_out, int out_size, void* d_ws, size_t ws_size,
                              hipStream_t stream) {
  k_all<<<256, 512, 0, stream>>>((const char*)d_in[0], (const char*)d_in[1], (const char*)d_in[2], (const char*)d_in[3], (const char*)d_in[4],
                                 (const char*)d_in[5], (const char*)d_in[6], (const char*)d_in[7], (const char*)d_in[8], (const char*)d_in[9],
                                 (const char*)d_in[10], (char*)d_ws, (unsigned*)d_out);
}
```

```cpp
#define WTH 8
#include <hip/hip_runtime.h>
#include <hip/hip_bf16.h>
#include <stdint.h>

typedef __attribute__((ext_vector_type(4))) unsigned u32x4;
typedef __attribute__((ext_vector_type(2))) unsigned u32x2;
typedef __attribute__((ext_vector_type(4))) float f32x4;
typedef __attribute__((ext_vector_type(2))) float f32x2;
typedef __attribute__((ext_vector_type(16))) float f32x16;
typedef __attribute__((ext_vector_type(8))) __bf16 bf16x8;
typedef __attribute__((ext_vector_type(2))) __bf16 bf16x2;
typedef __attribute__((ext_vector_type(4))) short s16x4;
typedef unsigned long long u64;
typedef __amdgpu_buffer_rsrc_t rsrc_t;
#define LAS __attribute__((address_space(3)))
#define GAS __attribute__((address_space(1)))
#define RLX_AGENT __ATOMIC_RELAXED, __HIP_MEMORY_SCOPE_AGENT

#define HELPER 0
#define STAMPS 0

#define AUXK 2
#define AUXV 2
#define DELAY0 3
constexpr int AUX_NT = 2, AUX_SC1 = 16;
constexpr float SCALE = 0.08838834764831845f;
constexpr float LOG2E = 1.4426950408889634f;
constexpr unsigned SPIN_CAP = 1u << 17;

constexpr int WS_EPOCH = 0;
constexpr int WS_F1   = 4096;
constexpr int WS_F2   = WS_F1 + 4096;
constexpr int WS_HINT = WS_F2 + 4096;
constexpr int WS_QP   = WS_HINT + 16384;
constexpr int WS_QABS = WS_QP + 2 * 32 * 8 * 4096;
constexpr int WS_PML  = WS_QABS + 4 * 32 * 512 * 2;
constexpr int WS_PO   = WS_PML + 32 * 32 * 8;
constexpr int REC_B   = 1152;
constexpr int WS_REC  = WS_PO + 32 * 32 * 2048;
constexpr int WS_Y    = WS_REC + 32 * 16 * REC_B;
constexpr int WS_END  = WS_Y + 2 * 4 * 8 * 32 * 512;
constexpr int WS_STAMP = 32 << 20;

constexpr int LDS_BYTES = 163840;
constexpr int LDS_SCR = 131072;

__device__ __forceinline__ unsigned cvtpk(float lo, float hi) {
  f32x2 v = {lo, hi};
  return __builtin_bit_cast(unsigned, __builtin_convertvector(v, bf16x2));
}
__device__ __forceinline__ float bf_lo(unsigned u) { return __uint_as_float(u << 16); }
__device__ __forceinline__ float bf_hi(unsigned u) { return __uint_as_float(u & 0xffff0000u); }
__device__ __forceinline__ float rbf(float x) { return __uint_as_float(cvtpk(x, x) << 16); }
__device__ __forceinline__ float dot2(unsigned k, unsigned q, float acc) {
  return __builtin_amdgcn_fdot2_f32_bf16(__builtin_bit_cast(bf16x2, k), __builtin_bit_cast(bf16x2, q), acc, false);
}
template <int CTRL> __device__ __forceinline__ float dpp(float x) {
  return __builtin_bit_cast(float, __builtin_amdgcn_mov_dpp(__builtin_bit_cast(int, x), CTRL, 0xf, 0xf, true));
}
template <int CTRL> __device__ __forceinline__ unsigned dppu(unsigned x) {
  return (unsigned)__builtin_amdgcn_mov_dpp((int)x, CTRL, 0xf, 0xf, true);
}
constexpr int XOR1 = 0xB1, XOR2 = 0x4E, XOR7 = 0x141, XOR8 = 0x128, ROR4 = 0x124;
__device__ __forceinline__ float xrow_max(float x) {
  auto s = __builtin_amdgcn_permlane16_swap(__float_as_uint(x), __float_as_uint(x), false, false);
  x = fmaxf(__uint_as_float(s[0]), __uint_as_float(s[1]));
  auto t = __builtin_amdgcn_permlane32_swap(__float_as_uint(x), __float_as_uint(x), false, false);
  return fmaxf(__uint_as_float(t[0]), __uint_as_float(t[1]));
}
__device__ __forceinline__ float xrow_sum(float x) {
  auto s = __builtin_amdgcn_permlane16_swap(__float_as_uint(x), __float_as_uint(x), false, false);
  x = __uint_as_float(s[0]) + __uint_as_float(s[1]);
  auto t = __builtin_amdgcn_permlane32_swap(__float_as_uint(x), __float_as_uint(x), false, false);
  return __uint_as_float(t[0]) + __uint_as_float(t[1]);
}
__device__ __forceinline__ float row_max(float x) {
  x = fmaxf(x, dpp<XOR8>(x)); x = fmaxf(x, dpp<ROR4>(x)); x = fmaxf(x, dpp<XOR2>(x)); x = fmaxf(x, dpp<XOR1>(x));
  return x;
}
__device__ __forceinline__ float row_sum(float x) {
  x += dpp<XOR8>(x); x += dpp<ROR4>(x); x += dpp<XOR2>(x); x += dpp<XOR1>(x);
  return x;
}
__device__ __forceinline__ float fexp2(float x) { return __builtin_amdgcn_exp2f(x); }
__device__ __forceinline__ f32x4 mfma16(u32x4 a, u32x4 b, f32x4 c) {
  return __builtin_amdgcn_mfma_f32_16x16x32_bf16(__builtin_bit_cast(bf16x8, a), __builtin_bit_cast(bf16x8, b), c, 0, 0, 0);
}
__device__ __forceinline__ f32x16 mfma32(u32x4 a, u32x4 b, f32x16 c) {
  return __builtin_amdgcn_mfma_f32_32x32x16_bf16(__builtin_bit_cast(bf16x8, a), __builtin_bit_cast(bf16x8, b), c, 0, 0, 0);
}
__device__ __forceinline__ rsrc_t mkrs(const void* p, unsigned bytes) {
  return __builtin_amdgcn_make_buffer_rsrc((void*)p, 0, bytes, 0x00020000);
}
__device__ __forceinline__ void bar() { asm volatile("s_waitcnt lgkmcnt(0)\n\ts_barrier" ::: "memory"); }
__device__ __forceinline__ void drain() { asm volatile("s_waitcnt vmcnt(0)" ::: "memory"); }
#define SB() __builtin_amdgcn_sched_barrier(0)
template <int N> __device__ __forceinline__ void vmwait() { asm volatile("s_waitcnt vmcnt(%0)" :: "n"(N) : "memory"); }
__device__ __forceinline__ unsigned swz(unsigned row) { return ((row & 3) << 2) | ((row >> 2) & 3); }
__device__ __forceinline__ u32x2 trread(LAS char* p) {
  return __builtin_bit_cast(u32x2, __builtin_amdgcn_ds_read_tr16_b64_v4i16((LAS s16x4*)p));
}

struct Tag { unsigned tl, th; u64 e; };
__device__ __forceinline__ Tag mktag(u64 e0) {
  Tag t;
  asm volatile("" : "+s"(e0));
  t.e = e0 * 6364136223846793005ull + 1442695040888963407ull;
  t.th = (unsigned)(t.e >> 32);
  t.tl = (unsigned)t.e & 0xff000000u;
  return t;
}
__device__ __forceinline__ unsigned gpack(float v, const Tag& t) { return ((__float_as_uint(v) + 0x80u) >> 8) | t.tl; }
__device__ __forceinline__ u32x4 gpair(float a, float b, const Tag& t) { return u32x4{gpack(a, t), t.th, gpack(b, t), t.th}; }
__device__ __forceinline__ bool gok(u32x4 g, const Tag& t) {
  return ((((g[0] ^ t.tl) | (g[2] ^ t.tl)) >> 24) | (g[1] ^ t.th) | (g[3] ^ t.th)) == 0u;
}
__device__ __forceinline__ float gval(unsigned lo) { return __uint_as_float(lo << 8); }

#define STAMP(i) do {} while (0)

__device__ __forceinline__ void diag_gemv(const u32x4 (&wr)[16], const u32x4 (&xp)[2][2], const unsigned (&mask)[4], f32x4 (&acc)[2]) {
#pragma unroll
  for (int i = 0; i < 16; i++) {
#pragma unroll
    for (int s = 0; s < 2; s++) {
      const unsigned src = xp[s][i >> 3][(i >> 1) & 3];
      const unsigned dup = __builtin_amdgcn_perm(src, src, (i & 1) ? 0x03020302u : 0x01000100u);
      const u32x4 a = {dup & mask[0], dup & mask[1], dup & mask[2], dup & mask[3]};
      acc[s] = mfma16(a, wr[i], acc[s]);
    }
  }
}

__device__ __forceinline__ void qprod(const char* W, const char* x, int head, int p, int lane, const unsigned (&mask)[4],
                                      rsrc_t rws, int qpoff, u64 e0) {
  const int g = lane >> 4, c = lane & 15;
  const rsrc_t rw = mkrs(W, 4u << 20);
  const int voff = (64 * p + 16 * g) * 8192 + head * 256 + c * 16;
  u32x4 xp[2][2];
#pragma unroll
  for (int s = 0; s < 2; s++) {
    const u32x4* xs = (const u32x4*)(x + (2 * s + (c >> 3)) * 1024 + (64 * p + 16 * g) * 2);
    xp[s][0] = xs[0]; xp[s][1] = xs[1];
  }
  u32x4 wr[16];
#pragma unroll
  for (int i = 0; i < 16; i++) wr[i] = __builtin_amdgcn_raw_buffer_load_b128(rw, voff, i * 8192, AUX_NT);
  SB();
  const Tag tg = mktag(e0);
  f32x4 acc[2] = {{0.f, 0.f, 0.f, 0.f}, {0.f, 0.f, 0.f, 0.f}};
  diag_gemv(wr, xp, mask, acc);
#pragma unroll
  for (int s = 0; s < 2; s++) {
    __builtin_amdgcn_raw_buffer_store_b128(gpair(acc[s][0], acc[s][1], tg), rws, qpoff + (2 * s) * 1024 + lane * 16, 0, AUX_SC1);
    __builtin_amdgcn_raw_buffer_store_b128(gpair(acc[s][2], acc[s][3], tg), rws, qpoff + (2 * s + 1) * 1024 + lane * 16, 0, AUX_SC1);
  }
  SB();
}

__device__ __forceinline__ unsigned qgather(rsrc_t rws, int qpbase, int b, int lane, const Tag& tg) {
  const int u = lane >> 5, rest = lane & 31;
  const int off = qpbase + ((b >> 1) * 2 + u) * 1024 + (32 * (b & 1) + rest) * 16;
  u32x4 v[8];
  for (unsigned spins = 0;; spins++) {
    asm volatile("" ::: "memory");
#pragma unroll
    for (int p = 0; p < 8; p++) v[p] = __builtin_amdgcn_raw_buffer_load_b128(rws, off, p * 4096, AUX_SC1);
    bool ok = true;
#pragma unroll
    for (int p = 0; p < 8; p++) ok &= gok(v[p], tg);
    if (__all(ok) || spins > SPIN_CAP) break;
    __builtin_amdgcn_s_sleep(1);
  }
  float a = 0.f, bb = 0.f;
#pragma unroll
  for (int p = 0; p < 8; p++) { a += gval(v[p][0]); bb += gval(v[p][2]); }
  return cvtpk(a, bb);
}
__device__ __forceinline__ int qgather_dw(int lane) { return 4 * (lane & 15) + 2 * ((lane >> 4) & 1) + (lane >> 5); }

__device__ __forceinline__ void poll32(char* ws, int off, int lane, const Tag& tg) {
  GAS u64* f = (GAS u64*)(ws + off + (lane & 31) * 128);
  for (unsigned spins = 0;; spins++) {
    const u64 v = __hip_atomic_load(f, RLX_AGENT);
    if (__all(v == tg.e) || spins > SPIN_CAP) break;
    __builtin_amdgcn_s_sleep(4);
  }
  __builtin_amdgcn_fence(__ATOMIC_ACQUIRE, "wavefront");
}

constexpr int NSC = HELPER ? 5 : 6;
__device__ __forceinline__ void b_issue(u32x4 (&ring)[16], const char* kb, int n, int lane) {
  const rsrc_t rk = mkrs(kb, n * 1024);
#pragma unroll
  for (int i = 0; i < 16; i++) {
    if (i >= WTH) vmwait<WTH - 1>();
    ring[i] = __builtin_amdgcn_raw_buffer_load_b128(rk, lane * 16, i * 1024, AUXK);
    SB();
  }
}
template <int I> __device__ __forceinline__ void v_slot(u32x4 (&ring)[16], unsigned pk, const unsigned (&mask)[4], f32x4& acc, rsrc_t rn, int voff, int so) {
  const unsigned d = dppu<0x150 + I>(pk);
  const u32x4 a = {d & mask[0], d & mask[1], d & mask[2], d & mask[3]};
  acc = mfma16(a, ring[I], acc);
  asm volatile("s_waitcnt vmcnt(%1)" : "+v"(acc) : "n"(WTH - 1));
  ring[I] = __builtin_amdgcn_raw_buffer_load_b128(rn, voff, so + I * 1024, AUXV);
  SB();
}
__device__ __forceinline__ void b_stream(u32x4 (&ring)[16], const char* kb, const char* vb, int n, u32x4 q, int lane, const unsigned (&mask)[4],
                                         char* ws, rsrc_t rws, int hintoff, const Tag& tg, float& m_out, float& l_out, f32x4& o_out) {
  const int c = lane & 15, voff = lane * 16;
  const int nit = (n + 15) >> 4;
  const int hoff = lane == 0 ? hintoff : 0x7ffffff0;
  float sc[NSC];
#pragma unroll
  for (int k = 0; k < NSC; k++) sc[k] = -INFINITY;
#pragma unroll 1
  for (int it = 0; it < nit; it++) {
    const bool last = it == nit - 1;
    const rsrc_t rn = mkrs(last ? vb : kb, n * 1024);
    const int so = (last ? it : it + 1) * 16384;
    float x[16];
#pragma unroll
    for (int i = 0; i < 16; i++) {
      float a = dot2(ring[i][0], q[0], 0.f);
      a = dot2(ring[i][1], q[1], a);
      a = dot2(ring[i][2], q[2], a);
      x[i] = dot2(ring[i][3], q[3], a);
      asm volatile("s_waitcnt vmcnt(%1)" : "+v"(x[i]) : "n"(WTH - 1));
      ring[i] = __builtin_amdgcn_raw_buffer_load_b128(rn, voff, so + i * 1024, AUXK);
      SB();
    }
#define TR_STEP(HALF, CTRL, BIT) _Pragma("unroll") for (int h = 0; h < (HALF); ++h) { \
      const float t1 = x[h] + dpp<CTRL>(x[h]), t2 = x[h + (HALF)] + dpp<CTRL>(x[h + (HALF)]); x[h] = (c & (BIT)) ? t2 : t1; }
    TR_STEP(8, XOR8, 8) TR_STEP(4, XOR7, 4) TR_STEP(2, XOR2, 2) TR_STEP(1, XOR1, 1)
#undef TR_STEP
    float s = rbf(rbf(x[0]) * SCALE);
    s = (it * 16 + c < n) ? s : -INFINITY;
#pragma unroll
    for (int k = NSC - 1; k > 0; k--) sc[k] = sc[k - 1];
    sc[0] = s;
  }
  STAMP(3);
  float m = sc[0];
#pragma unroll
  for (int k = 1; k < NSC; k++) m = fmaxf(m, sc[k]);
  m = xrow_max(row_max(m));
  unsigned pk[NSC];
  float l = 0.f;
#pragma unroll
  for (int k = 0; k < NSC; k++) {
    const float p = fexp2((sc[k] - m) * LOG2E);
    pk[k] = cvtpk(p, p);
    l += bf_lo(pk[k]);
  }
  l = xrow_sum(row_sum(l));
  f32x4 acc = {0.f, 0.f, 0.f, 0.f};
#pragma unroll 1
  for (int it = nit - 1; it >= 0; it--) {
    const rsrc_t rn = mkrs(vb, it > 0 ? n * 1024 : 0);
    const int so = (it > 0 ? it - 1 : 0) * 16384;
    const unsigned p0 = pk[0];
    v_slot<0>(ring, p0, mask, acc, rn, voff, so);  v_slot<1>(ring, p0, mask, acc, rn, voff, so);
    v_slot<2>(ring, p0, mask, acc, rn, voff, so);  v_slot<3>(ring, p0, mask, acc, rn, voff, so);
    v_slot<4>(ring, p0, mask, acc, rn, voff, so);  v_slot<5>(ring, p0, mask, acc, rn, voff, so);
    v_slot<6>(ring, p0, mask, acc, rn, voff, so);  v_slot<7>(ring, p0, mask, acc, rn, voff, so);
    v_slot<8>(ring, p0, mask, acc, rn, voff, so);  v_slot<9>(ring, p0, mask, acc, rn, voff, so);
    v_slot<10>(ring, p0, mask, acc, rn, voff, so); v_slot<11>(ring, p0, mask, acc, rn, voff, so);
    __builtin_amdgcn_raw_buffer_store_b64(u32x2{(unsigned)tg.e, tg.th}, rws, it == 0 ? hoff : 0x7ffffff0, 0, AUX_SC1);
    SB();
    v_slot<12>(ring, p0, mask, acc, rn, voff, so); v_slot<13>(ring, p0, mask, acc, rn, voff, so);
    v_slot<14>(ring, p0, mask, acc, rn, voff, so); v_slot<15>(ring, p0, mask, acc, rn, voff, so);
#pragma unroll
    for (int k = 0; k < NSC - 1; k++) pk[k] = pk[k + 1];
  }
  m_out = m; l_out = l; o_out = acc;
}

template <int NREC>
__device__ __forceinline__ void b_merge(LAS char* scr, int w, int lane, int tid, float m, float l, f32x4 o, rsrc_t rws, int recoff, const Tag& tg) {
  LAS float* mrg = (LAS float*)scr;
  const int c = lane & 15, grp = lane >> 4;
  if (grp < 2) *(LAS f32x4*)(mrg + w * 132 + 4 + 8 * c + 4 * grp) = o;
  if (lane == 0) { mrg[w * 132] = m; mrg[w * 132 + 1] = l; }
  bar();
  constexpr int WPR = 8 / NREC;
  if (tid < NREC * 64) {
    const int r = tid >> 6;
    float mm[WPR], M = -INFINITY;
#pragma unroll
    for (int k = 0; k < WPR; k++) { mm[k] = mrg[(r * WPR + k) * 132]; M = fmaxf(M, mm[k]); }
    float a = 0.f, b = 0.f, L = 0.f;
#pragma unroll
    for (int k = 0; k < WPR; k++) {
      const float f = fexp2((mm[k] - M) * LOG2E);
      const f32x2 v = *(LAS f32x2*)(mrg + (r * WPR + k) * 132 + 4 + 2 * lane);
      a = fmaf(f, v[0], a); b = fmaf(f, v[1], b);
      L = fmaf(f, mrg[(r * WPR + k) * 132 + 1], L);
    }
    __builtin_amdgcn_raw_buffer_store_b128(gpair(a, b, tg), rws, recoff + r * REC_B + 16 + lane * 16, 0, AUX_SC1);
    if (lane == 0) __builtin_amdgcn_raw_buffer_store_b128(gpair(M, L, tg), rws, recoff + r * REC_B, 0, AUX_SC1);
  }
}

template <int NR>
__device__ __forceinline__ void dma_rows(rsrc_t rs, int srcoff, LAS char* img, int r0, int lane) {
#pragma unroll
  for (int u = 0; u < NR; u++) {
    const int r = r0 + u;
    unsigned keep;
    asm volatile("s_mov_b32 %0, m0\n\ts_mov_b32 m0, %1\n\ts_nop 0\n\tbuffer_load_dwordx4 %2, %3, %4 offen nt lds\n\ts_mov_b32 m0, %0"
                 : "=&s"(keep) : "s"((unsigned)(uintptr_t)img + r * 1024), "v"((lane ^ swz(r)) * 16), "s"(rs), "s"(srcoff + r * 1024) : "memory");
  }
  SB();
}

__device__ __forceinline__ void wo_frags(LAS char* img, int w, int lane, u32x4 (&wf)[4][4]) {
  const int g = lane >> 4, q = (lane & 15) >> 2, p = lane & 3;
#pragma unroll
  for (int ks = 0; ks < 4; ks++)
#pragma unroll
    for (int tl = 0; tl < 4; tl++) {
      const int ch = 8 * w + 2 * tl + (p >> 1);
      const int r0 = 32 * ks + 8 * g + q, r1 = r0 + 4;
      const u32x2 a = trread(img + r0 * 1024 + ((ch ^ swz(r0)) * 16) + 8 * (p & 1));
      const u32x2 b = trread(img + r1 * 1024 + ((ch ^ swz(r1)) * 16) + 8 * (p & 1));
      wf[tl][ks] = u32x4{a[0], a[1], b[0], b[1]};
    }
}
__device__ __forceinline__ void wo_apply(const u32x4 (&wf)[4][4], LAS char* cb, int w, int lane, int head, rsrc_t rws, int yoff, const Tag& tg) {
  const int n = lane & 15, grp = lane >> 4;
  u32x4 bf[4];
#pragma unroll
  for (int ks = 0; ks < 4; ks++) bf[ks] = *(LAS u32x4*)(cb + (n & 3) * 256 + ks * 64 + grp * 16);
#pragma unroll
  for (int tl = 0; tl < 4; tl++) {
    f32x4 acc = {0.f, 0.f, 0.f, 0.f};
#pragma unroll
    for (int ks = 0; ks < 4; ks++) acc = mfma16(wf[tl][ks], bf[ks], acc);
    if (n < 4) {
      const int off = yoff + ((n * 8 + w) * 32 + head) * 512 + (16 * tl + 4 * grp) * 8;
      __builtin_amdgcn_raw_buffer_store_b128(gpair(acc[0], acc[1], tg), rws, off, 0, AUX_SC1);
      __builtin_amdgcn_raw_buffer_store_b128(gpair(acc[2], acc[3], tg), rws, off + 16, 0, AUX_SC1);
    }
  }
}
__device__ __forceinline__ void y_reduce(LAS char* scr, int w, int lane, int tid, int head, rsrc_t rws, int yoff, const Tag& tg, unsigned* out, int part) {
  const int off = yoff + head * 32 * 512 + (tid >> 4) * 512 + (tid & 15) * 16;
  u32x4 v0, v1;
  for (unsigned spins = 0;; spins++) {
    asm volatile("" ::: "memory");
    v0 = __builtin_amdgcn_raw_buffer_load_b128(rws, off, 0, AUX_SC1);
    v1 = __builtin_amdgcn_raw_buffer_load_b128(rws, off + 256, 0, AUX_SC1);
    if (__all(gok(v0, tg) & gok(v1, tg)) || spins > SPIN_CAP) break;
    __builtin_amdgcn_s_sleep(1);
  }
  f32x4 s = {xrow_sum(gval(v0[0])), xrow_sum(gval(v0[2])), xrow_sum(gval(v1[0])), xrow_sum(gval(v1[2]))};
  LAS float* red = (LAS float*)scr;
  if (lane < 16) *(LAS f32x4*)(red + w * 64 + lane * 4) = s;
  bar();
  if (tid < 32) {
    const int k = tid & 15, hi = tid >> 4;
    float a = 0.f, b = 0.f;
#pragma unroll
    for (int j = 0; j < 8; j++) { const f32x2 v = *(LAS f32x2*)(red + j * 64 + k * 4 + 2 * hi); a += v[0]; b += v[1]; }
    out[(head >> 3) * 512 + part * 256 + (head & 7) * 32 + hi * 16 + k] = cvtpk(a, b);
  }
  bar();
}

__global__ __launch_bounds__(512, 2) void k_all(const char* __restrict__ xA, const char* __restrict__ ckv, const char* __restrict__ wqA,
                                                const char* __restrict__ wuk, const char* __restrict__ wuv, const char* __restrict__ woA,
                                                const char* __restrict__ xB, const char* __restrict__ kc, const char* __restrict__ vc,
                                                const char* __restrict__ wqB, const char* __restrict__ woB, char* ws, unsigned* out) {
  __shared__ __attribute__((aligned(16))) char smem[LDS_BYTES];
  LAS char* lds = (LAS char*)smem;
  LAS char* scr = lds + LDS_SCR;
  const int tid = threadIdx.x, lane = tid & 63, w = __builtin_amdgcn_readfirstlane(tid >> 6);
  const int blk = blockIdx.x, head = 4 * (blk & 7) + (blk >> 6), sub = (blk >> 3) & 7;
  const u64 tstart = __builtin_amdgcn_s_memrealtime();
  if (tid == 448) *(volatile LAS u32x2*)(scr + 6144) = u32x2{0u, 0u};
  asm volatile("" :: "s"(xA), "s"(ckv), "s"(wqA), "s"(wuk), "s"(wuv), "s"(woA), "s"(xB), "s"(kc), "s"(vc), "s"(wqB), "s"(woB), "s"(ws), "s"(out));
  bar();
  const u64 e0 = *(const __attribute__((address_space(4))) u64*)(ws + WS_EPOCH);
  SB();
  const rsrc_t rws = mkrs(ws, WS_END);
  unsigned mask[4];
#pragma unroll
  for (int v = 0; v < 4; v++) mask[v] = (((lane & 7) >> 1) == v) ? ((lane & 1) ? 0xffff0000u : 0x0000ffffu) : 0u;
  const int qpA = WS_QP + head * 32768, qpB = WS_QP + (32 + head) * 32768;

  if (w < 2) {
    __builtin_amdgcn_s_setprio(3);
    qprod(w == 0 ? wqB : wqA, w == 0 ? xB : xA, head, sub, lane, mask, rws, (w == 0 ? qpB : qpA) + sub * 4096, e0);
    __builtin_amdgcn_s_setprio(0);
  } else {
    __builtin_amdgcn_s_sleep(DELAY0);
  }
  STAMP(1);

  if (sub < 6) {
    const int wi = sub * 8 + w, b = wi / 12, r = wi % 12;
    const int g0 = HELPER ? r * 80 : r * 1024 / 12, n = HELPER ? 80 : (r + 1) * 1024 / 12 - g0;
    const size_t base = ((size_t)(b * 32 + head) * 1024 + g0) * 1024;
    u32x4 ring[16];
    const int b0 = (sub * 8) / 12, b1 = (sub * 8 + 4) / 12;
    const bool gat = w == 0 || (w == 1 && b1 != b0);
    if (gat) {
      const Tag tg = mktag(e0);
      const unsigned q0 = qgather(rws, qpB, w == 0 ? b0 : b1, lane, tg);
      ((LAS unsigned*)scr)[1280 + 64 * w + qgather_dw(lane)] = q0;
      if (b1 == b0) ((LAS unsigned*)scr)[1280 + 64 + qgather_dw(lane)] = q0;
      asm volatile("s_waitcnt lgkmcnt(0)" ::: "memory");
      if (lane < 2 && (lane == w || b1 == b0)) ((volatile LAS unsigned*)(scr + 6144))[lane] = 1u;
      STAMP(11);
    }
    b_issue(ring, kc + base, n, lane);
    STAMP(12);
    for (unsigned spins = 0; spins < SPIN_CAP * 64; spins++) {
      if (__builtin_amdgcn_readfirstlane(((volatile LAS unsigned*)(scr + 6144))[w >> 2])) break;
      __builtin_amdgcn_s_sleep(1);
    }
    asm volatile("" ::: "memory");
    const Tag tg = mktag(e0);
    STAMP(2);
    const u32x4 q = *(LAS u32x4*)(scr + 5120 + (w >> 2) * 256 + (lane & 15) * 16);
    float m, l; f32x4 o;
    b_stream(ring, kc + base, vc + base, n, q, lane, mask, ws, rws, WS_HINT + (head * 64 + wi) * 8, tg, m, l, o);
    STAMP(4);
    b_merge<2>(scr, w, lane, tid, m, l, o, rws, WS_REC + (head * 16 + 2 * sub) * REC_B, tg);
    if (blk < 8) while (__builtin_amdgcn_s_memrealtime() - tstart < 10000) __builtin_amdgcn_s_sleep(1);
  } else if (sub == 6) {
    const int b = head >> 3, sp = head & 7;
    const rsrc_t rc = mkrs(ckv + ((size_t)b * 4096 + sp * 512) * 1024, 512 * 1024);
    dma_rows<8>(rc, 0, lds, 8 * w, lane);
    dma_rows<8>(rc, 65536, lds + 65536, 8 * w, lane);
    const Tag tg = mktag(e0);
    if (w == 2) poll32(ws, WS_F1, lane, tg);
    bar();
    STAMP(2);
    {
      u32x4 t[4];
#pragma unroll
      for (int i = 0; i < 4; i++) t[i] = __builtin_amdgcn_raw_buffer_load_b128(rws, WS_QABS + b * 32768 + (8 * i + w) * 1024 + lane * 16, 0, AUX_SC1);
#pragma unroll
      for (int i = 0; i < 4; i++) *(LAS u32x4*)(scr + (8 * i + w) * 1024 + ((lane ^ swz(8 * i + w)) * 16)) = t[i];
    }
    bar();
    const int kb = w & 3, hb = w >> 2, l16 = lane & 15, grp = lane >> 4;
    u32x4 qf[16];
    {
      const int row = 16 * hb + l16;
#pragma unroll
      for (int ks = 0; ks < 16; ks++) qf[ks] = *(LAS u32x4*)(scr + row * 1024 + (((4 * ks + grp) ^ swz(row)) * 16));
    }
    bar();
    STAMP(3);
    LAS float* mx = (LAS float*)scr;
    LAS char* pl = scr + 1024;
    LAS float* lr = (LAS float*)(scr + 8192);
    f32x16 o0, o1;
#pragma unroll
    for (int j = 0; j < 16; j++) { o0[j] = 0.f; o1[j] = 0.f; }
    float m1 = -INFINITY, m2 = -INFINITY, l1 = 0.f;
    const int h1 = 16 * hb + l16, h2 = lane & 31, hh = lane >> 5;
    const int arow = 16 * kb + l16;
    const int tq = (lane & 15) >> 2, tp = lane & 3, tblk = (lane >> 4) & 1;
#pragma unroll 1
    for (int t = 0; t < 8; t++) {
      if (t < 7) asm volatile("s_waitcnt vmcnt(8)" ::: "memory"); else asm volatile("s_waitcnt vmcnt(0)" ::: "memory");
      bar();
      LAS char* img = lds + (t & 1) * 65536;
      f32x4 s = {0.f, 0.f, 0.f, 0.f};
#pragma unroll
      for (int ks = 0; ks < 16; ks++) {
        const u32x4 a = *(LAS u32x4*)(img + arow * 1024 + (((4 * ks + grp) ^ swz(arow)) * 16));
        s = mfma16(a, qf[ks], s);
      }
      float x[4];
#pragma unroll
      for (int j = 0; j < 4; j++) x[j] = rbf(rbf(s[j]) * SCALE);
      const float lm = xrow_max(fmaxf(fmaxf(x[0], x[1]), fmaxf(x[2], x[3])));
      if (grp == 0) mx[h1 * 4 + kb] = lm;
      bar();
      const f32x4 ma = *(LAS f32x4*)(mx + h1 * 4), mb = *(LAS f32x4*)(mx + h2 * 4);
      const float n1 = fmaxf(m1, fmaxf(fmaxf(ma[0], ma[1]), fmaxf(ma[2], ma[3])));
      const float n2 = fmaxf(m2, fmaxf(fmaxf(mb[0], mb[1]), fmaxf(mb[2], mb[3])));
      const float al1 = fexp2((m1 - n1) * LOG2E), al2 = fexp2((m2 - n2) * LOG2E);
      m1 = n1; m2 = n2;
      const u32x2 pp = {cvtpk(fexp2((x[0] - n1) * LOG2E), fexp2((x[1] - n1) * LOG2E)), cvtpk(fexp2((x[2] - n1) * LOG2E), fexp2((x[3] - n1) * LOG2E))};
      l1 = fmaf(l1, al1, (bf_lo(pp[0]) + bf_hi(pp[0])) + (bf_lo(pp[1]) + bf_hi(pp[1])));
      *(LAS u32x2*)(pl + h1 * 144 + (16 * kb + 4 * grp) * 2) = pp;
#pragma unroll
      for (int j = 0; j < 16; j++) { o0[j] *= al2; o1[j] *= al2; }
      bar();
#pragma unroll
      for (int ks = 0; ks < 4; ks++) {
        const u32x4 pb = *(LAS u32x4*)(pl + h2 * 144 + (16 * ks + 8 * hh) * 2);
        const int r0 = 16 * ks + 8 * hh + tq, r1 = r0 + 4;
        const int ch0 = 8 * w + 2 * tblk + (tp >> 1), ch1 = ch0 + 4;
        const u32x2 a0 = trread(img + r0 * 1024 + ((ch0 ^ swz(r0)) * 16) + 8 * (tp & 1));
        const u32x2 a1 = trread(img + r1 * 1024 + ((ch0 ^ swz(r1)) * 16) + 8 * (tp & 1));
        const u32x2 b0 = trread(img + r0 * 1024 + ((ch1 ^ swz(r0)) * 16) + 8 * (tp & 1));
        const u32x2 b1 = trread(img + r1 * 1024 + ((ch1 ^ swz(r1)) * 16) + 8 * (tp & 1));
        o0 = mfma32(u32x4{a0[0], a0[1], a1[0], a1[1]}, pb, o0);
        o1 = mfma32(u32x4{b0[0], b0[1], b1[0], b1[1]}, pb, o1);
      }
      bar();
      if (t < 6) dma_rows<8>(rc, (t + 2) * 65536, img, 8 * w, lane);
    }
    STAMP(4);
    {
      const int off = WS_PO + (head * 32 + h2) * 2048 + (64 * w + 4 * hh) * 4;
#pragma unroll
      for (int j4 = 0; j4 < 4; j4++) {
        const f32x4 v0 = {o0[4 * j4], o0[4 * j4 + 1], o0[4 * j4 + 2], o0[4 * j4 + 3]};
        const f32x4 v1 = {o1[4 * j4], o1[4 * j4 + 1], o1[4 * j4 + 2], o1[4 * j4 + 3]};
        __builtin_amdgcn_raw_buffer_store_b128(__builtin_bit_cast(u32x4, v0), rws, off + j4 * 32, 0, AUX_SC1);
        __builtin_amdgcn_raw_buffer_store_b128(__builtin_bit_cast(u32x4, v1), rws, off + 128 + j4 * 32, 0, AUX_SC1);
      }
      const float ls = xrow_sum(l1);
      if (grp == 0) lr[kb * 32 + h1] = ls;
      bar();
      if (tid < 32) {
        const f32x2 v = {m2, (lr[tid] + lr[32 + tid]) + (lr[64 + tid] + lr[96 + tid])};
        __builtin_amdgcn_raw_buffer_store_b64(__builtin_bit_cast(u32x2, v), rws, WS_PML + (head * 32 + tid) * 8, 0, AUX_SC1);
      }
      drain();
      bar();
      if (tid == 0) __hip_atomic_store((GAS u64*)(ws + WS_F2 + head * 128), tg.e, RLX_AGENT);
    }
    STAMP(5);
  } else {
    const int l16 = lane & 15, grp = lane >> 4;
    u32x4 wr[16];
    {
      const rsrc_t rk = mkrs(wuk, 4u << 20);
      const int voff = (64 * w + (l16 & 7)) * 8192 + head * 256 + (l16 >> 3) * 64 + grp * 16;
#pragma unroll
      for (int i = 0; i < 16; i++) {
        if (i >= 4) vmwait<3>();
        wr[i] = __builtin_amdgcn_raw_buffer_load_b128(rk, voff + (i & 1) * 128, (i >> 1) * 8 * 8192, AUX_NT);
        SB();
      }
    }
    const Tag tg = mktag(e0);
    if (w < 4) {
      const unsigned q = qgather(rws, qpA, w, lane, tg);
      ((LAS unsigned*)scr)[w * 64 + qgather_dw(lane)] = q;
    }
    bar();
    STAMP(2);
    {
      u32x4 bf[2];
#pragma unroll
      for (int u = 0; u < 2; u++) bf[u] = *(LAS u32x4*)(scr + (l16 & 3) * 256 + u * 128 + ((l16 >> 2) & 1) * 64 + grp * 16);
#pragma unroll
      for (int rp = 0; rp < 4; rp++) {
        u32x2 v[2];
#pragma unroll
        for (int h = 0; h < 2; h++) {
          f32x4 acc = {0.f, 0.f, 0.f, 0.f};
          acc = mfma16(wr[4 * rp + 2 * h], bf[0], acc);
          acc = mfma16(wr[4 * rp + 2 * h + 1], bf[1], acc);
#pragma unroll
          for (int j = 0; j < 4; j++) {
            auto t = __builtin_amdgcn_permlane32_swap(__float_as_uint(acc[j]), __float_as_uint(acc[j]), false, false);
            acc[j] += dpp<0x104>(__uint_as_float(t[1]));
          }
          v[h] = u32x2{cvtpk(acc[0], acc[1]), cvtpk(acc[2], acc[3])};
        }
        if (l16 < 4 && grp < 2) {
          __builtin_amdgcn_raw_buffer_store_b64(v[0], rws, WS_QABS + (l16 * 32 + head) * 1024 + (64 * w + 16 * rp + 4 * grp) * 2, 0, AUX_SC1);
          __builtin_amdgcn_raw_buffer_store_b64(v[1], rws, WS_QABS + (l16 * 32 + head) * 1024 + (64 * w + 16 * rp + 8 + 4 * grp) * 2, 0, AUX_SC1);
        }
      }
    }
    drain();
    bar();
    if (tid == 0) __hip_atomic_store((GAS u64*)(ws + WS_F1 + head * 128), tg.e, RLX_AGENT);
    STAMP(3);
    u32x4 wf[4][4];
    {
      const rsrc_t ro = mkrs(woA + (size_t)head * 131072, 131072);
      dma_rows<16>(ro, 0, lds, 16 * w, lane);
      const rsrc_t rv = mkrs(wuv, 4u << 20);
      const int voff = (64 * w + 16 * grp) * 8192 + head * 256 + l16 * 16;
#pragma unroll
      for (int i = 0; i < 16; i++) wr[i] = __builtin_amdgcn_raw_buffer_load_b128(rv, voff, i * 8192, AUX_NT);
      SB();
      asm volatile("s_waitcnt vmcnt(16)" ::: "memory");
      bar();
      wo_frags(lds, w, lane, wf);
      bar();
      const rsrc_t rb = mkrs(woB + (size_t)head * 131072, 131072);
      dma_rows<16>(rb, 0, lds, 16 * w, lane);
    }
    STAMP(4);
    if (w == 0) poll32(ws, WS_F2, lane, tg);
    bar();
    STAMP(5);
    {
      const int b = tid >> 7, c4 = tid & 127;
      u32x4 po[8]; u32x2 ml[8];
#pragma unroll
      for (int s = 0; s < 8; s++) {
        ml[s] = __builtin_amdgcn_raw_buffer_load_b64(rws, WS_PML + ((8 * b + s) * 32 + head) * 8, 0, AUX_SC1);
        po[s] = __builtin_amdgcn_raw_buffer_load_b128(rws, WS_PO + ((8 * b + s) * 32 + head) * 2048 + c4 * 16, 0, AUX_SC1);
      }
      float M = -INFINITY;
#pragma unroll
      for (int s = 0; s < 8; s++) M = fmaxf(M, __uint_as_float(ml[s][0]));
      float L = 0.f;
      f32x4 a = {0.f, 0.f, 0.f, 0.f};
#pragma unroll
      for (int s = 0; s < 8; s++) {
        const float f = fexp2((__uint_as_float(ml[s][0]) - M) * LOG2E);
        L = fmaf(f, __uint_as_float(ml[s][1]), L);
        a += f * __builtin_bit_cast(f32x4, po[s]);
      }
      const float inv = 1.f / L;
      const u32x2 v = {cvtpk(a[0] * inv, a[1] * inv), cvtpk(a[2] * inv, a[3] * inv)};
      *(LAS u32x2*)(scr + b * 1024 + c4 * 8) = v;
    }
    bar();
    {
      u32x4 xp[2][2];
#pragma unroll
      for (int s = 0; s < 2; s++) {
        LAS u32x4* xs = (LAS u32x4*)(scr + (2 * s + (l16 >> 3)) * 1024 + (64 * w + 16 * grp) * 2);
        xp[s][0] = xs[0]; xp[s][1] = xs[1];
      }
      f32x4 acc[2] = {{0.f, 0.f, 0.f, 0.f}, {0.f, 0.f, 0.f, 0.f}};
      diag_gemv(wr, xp, mask, acc);
      LAS float* red = (LAS float*)(scr + 8192);
#pragma unroll
      for (int s = 0; s < 2; s++) *(LAS f32x4*)(red + w * 512 + (2 * s + (grp >> 1)) * 128 + 8 * l16 + 4 * (grp & 1)) = acc[s];
      bar();
      if (tid < 256) {
        float a = 0.f, b = 0.f;
#pragma unroll
        for (int k = 0; k < 8; k++) { const f32x2 v = *(LAS f32x2*)(red + k * 512 + 2 * tid); a += v[0]; b += v[1]; }
        ((LAS unsigned*)(scr + 4096))[tid] = cvtpk(a, b);
      }
      bar();
    }
    wo_apply(wf, scr + 4096, w, lane, head, rws, WS_Y, tg);
    STAMP(6);
    asm volatile("s_waitcnt vmcnt(8)" ::: "memory");
    bar();
    wo_frags(lds, w, lane, wf);
    y_reduce(scr, w, lane, tid, head, rws, WS_Y, tg, out, 0);
    STAMP(7);
    if (w == 0) {
      GAS u64* f = (GAS u64*)(ws + WS_HINT + (head * 64 + (lane < (HELPER ? 56 : 48) ? lane : 0)) * 8);
      for (unsigned spins = 0;; spins++) {
        const u64 v = __hip_atomic_load(f, RLX_AGENT);
        if (__all(v == tg.e) || spins > SPIN_CAP) break;
        __builtin_amdgcn_s_sleep(4);
      }
    }
    bar();
    STAMP(8);
    {
      const int b = w & 3, hf = w >> 2;
      const bool act = HELPER || grp < 3;
      const int rec = grp < 3 ? 3 * b + grp : (HELPER ? 12 + b : 3 * b);
      const int off = WS_REC + (head * 16 + rec) * REC_B;
      u32x4 g0, g1, g2;
      for (unsigned spins = 0;; spins++) {
        asm volatile("" ::: "memory");
        g0 = __builtin_amdgcn_raw_buffer_load_b128(rws, off, 0, AUX_SC1);
        g1 = __builtin_amdgcn_raw_buffer_load_b128(rws, off + 16 + (32 * hf + l16) * 16, 0, AUX_SC1);
        g2 = __builtin_amdgcn_raw_buffer_load_b128(rws, off + 16 + (32 * hf + 16 + l16) * 16, 0, AUX_SC1);
        if (__all(gok(g0, tg) & gok(g1, tg) & gok(g2, tg)) || spins > SPIN_CAP) break;
      }
      const float mr = gval(g0[0]);
      const float M = xrow_max(mr);
      const float f = act ? fexp2((mr - M) * LOG2E) : 0.f;
      const float inv = 1.f / xrow_sum(f * gval(g0[2]));
      const float a0 = xrow_sum(f * gval(g1[0])) * inv, a1 = xrow_sum(f * gval(g1[2])) * inv;
      const float a2 = xrow_sum(f * gval(g2[0])) * inv, a3 = xrow_sum(f * gval(g2[2])) * inv;
      if (grp == 0) {
        ((LAS unsigned*)(scr + 4096))[b * 64 + 32 * hf + l16] = cvtpk(a0, a1);
        ((LAS unsigned*)(scr + 4096))[b * 64 + 32 * hf + 16 + l16] = cvtpk(a2, a3);
      }
    }
    bar();
    STAMP(9);
    wo_apply(wf, scr + 4096, w, lane, head, rws, WS_Y + 524288, tg);
    y_reduce(scr, w, lane, tid, head, rws, WS_Y + 524288, tg, out, 1);
    if (head == 0 && tid == 0) __hip_atomic_store((GAS u64*)(ws + WS_EPOCH), tg.e, RLX_AGENT);
    STAMP(10);
  }
}

extern "C" void kernel_launch(void* const* d_in, const int* in_sizes, int n_in, void* d_out, int out_size, void* d_ws, size_t ws_size,
                              hipStream_t stream) {
  k_all<<<256, 512, 0, stream>>>((const char*)d_in[0], (const char*)d_in[1], (const char*)d_in[2], (const char*)d_in[3], (const char*)d_in[4],
                                 (const char*)d_in[5], (const char*)d_in[6], (const char*)d_in[7], (const char*)d_in[8], (const char*)d_in[9],
                                 (const char*)d_in[10], (char*)d_ws, (unsigned*)d_out);
}
```

```cpp
#include <hip/hip_runtime.h>
#include <hip/hip_bf16.h>
#include <stdint.h>

typedef __attribute__((ext_vector_type(4))) unsigned u32x4;
typedef __attribute__((ext_vector_type(2))) unsigned u32x2;
typedef __attribute__((ext_vector_type(4))) float f32x4;
typedef __attribute__((ext_vector_type(2))) float f32x2;
typedef __attribute__((ext_vector_type(16))) float f32x16;
typedef __attribute__((ext_vector_type(8))) __bf16 bf16x8;
typedef __attribute__((ext_vector_type(2))) __bf16 bf16x2;
typedef __attribute__((ext_vector_type(4))) short s16x4;
typedef unsigned long long u64;
typedef __amdgpu_buffer_rsrc_t rsrc_t;
#define LAS __attribute__((address_space(3)))
#define GAS __attribute__((address_space(1)))
#define RLX_AGENT __ATOMIC_RELAXED, __HIP_MEMORY_SCOPE_AGENT

#define STAMPS 0

#define WTH 8
#define WFILL 6
#define AUXK 2
#define AUXV 2
#define DELAY0 3
constexpr int AUX_NT = 2, AUX_SC1 = 16;
constexpr float SCALE = 0.08838834764831845f;
constexpr float LOG2E = 1.4426950408889634f;
constexpr unsigned SPIN_CAP = 1u << 17;

constexpr int WS_EPOCH = 0;
constexpr int WS_F1   = 4096;
constexpr int WS_F2   = WS_F1 + 4096;
constexpr int WS_HINT = WS_F2 + 4096;
constexpr int WS_QP   = WS_HINT + 16384;
constexpr int WS_QABS = WS_QP + 2 * 32 * 8 * 4096;
constexpr int WS_PML  = WS_QABS + 4 * 32 * 512 * 2;
constexpr int WS_PO   = WS_PML + 32 * 32 * 8;
constexpr int WS_REC  = WS_PO + 32 * 32 * 2048;
constexpr int WS_HDR  = WS_REC + 32 * 8 * 4 * 2048;
constexpr int WS_CNT  = WS_HDR + 32 * 4 * 8 * 32;
constexpr int WS_Y    = WS_CNT + 128 * 128;
constexpr int WS_END  = WS_Y + 2 * 4 * 8 * 32 * 512;
constexpr int WS_STAMP = 32 << 20;

constexpr int LDS_BYTES = 163840;
constexpr int LDS_SCR = 131072;

__device__ __forceinline__ unsigned cvtpk(float lo, float hi) {
  f32x2 v = {lo, hi};
  return __builtin_bit_cast(unsigned, __builtin_convertvector(v, bf16x2));
}
__device__ __forceinline__ float bf_lo(unsigned u) { return __uint_as_float(u << 16); }
__device__ __forceinline__ float bf_hi(unsigned u) { return __uint_as_float(u & 0xffff0000u); }
__device__ __forceinline__ float rbf(float x) { return __uint_as_float(cvtpk(x, x) << 16); }
__device__ __forceinline__ float dot2(unsigned k, unsigned q, float acc) {
  return __builtin_amdgcn_fdot2_f32_bf16(__builtin_bit_cast(bf16x2, k), __builtin_bit_cast(bf16x2, q), acc, false);
}
template <int CTRL> __device__ __forceinline__ float dpp(float x) {
  return __builtin_bit_cast(float, __builtin_amdgcn_mov_dpp(__builtin_bit_cast(int, x), CTRL, 0xf, 0xf, true));
}
template <int CTRL> __device__ __forceinline__ unsigned dppu(unsigned x) {
  return (unsigned)__builtin_amdgcn_mov_dpp((int)x, CTRL, 0xf, 0xf, true);
}
constexpr int XOR1 = 0xB1, XOR2 = 0x4E, XOR7 = 0x141, XOR8 = 0x128, ROR4 = 0x124;
__device__ __forceinline__ float xrow_max(float x) {
  auto s = __builtin_amdgcn_permlane16_swap(__float_as_uint(x), __float_as_uint(x), false, false);
  x = fmaxf(__uint_as_float(s[0]), __uint_as_float(s[1]));
  auto t = __builtin_amdgcn_permlane32_swap(__float_as_uint(x), __float_as_uint(x), false, false);
  return fmaxf(__uint_as_float(t[0]), __uint_as_float(t[1]));
}
__device__ __forceinline__ float xrow_sum(float x) {
  auto s = __builtin_amdgcn_permlane16_swap(__float_as_uint(x), __float_as_uint(x), false, false);
  x = __uint_as_float(s[0]) + __uint_as_float(s[1]);
  auto t = __builtin_amdgcn_permlane32_swap(__float_as_uint(x), __float_as_uint(x), false, false);
  return __uint_as_float(t[0]) + __uint_as_float(t[1]);
}
__device__ __forceinline__ float row_max(float x) {
  x = fmaxf(x, dpp<XOR8>(x)); x = fmaxf(x, dpp<ROR4>(x)); x = fmaxf(x, dpp<XOR2>(x)); x = fmaxf(x, dpp<XOR1>(x));
  return x;
}
__device__ __forceinline__ float row_sum(float x) {
  x += dpp<XOR8>(x); x += dpp<ROR4>(x); x += dpp<XOR2>(x); x += dpp<XOR1>(x);
  return x;
}
__device__ __forceinline__ float fexp2(float x) { return __builtin_amdgcn_exp2f(x); }
__device__ __forceinline__ f32x4 mfma16(u32x4 a, u32x4 b, f32x4 c) {
  return __builtin_amdgcn_mfma_f32_16x16x32_bf16(__builtin_bit_cast(bf16x8, a), __builtin_bit_cast(bf16x8, b), c, 0, 0, 0);
}
__device__ __forceinline__ f32x16 mfma32(u32x4 a, u32x4 b, f32x16 c) {
  return __builtin_amdgcn_mfma_f32_32x32x16_bf16(__builtin_bit_cast(bf16x8, a), __builtin_bit_cast(bf16x8, b), c, 0, 0, 0);
}
__device__ __forceinline__ rsrc_t mkrs(const void* p, unsigned bytes) {
  return __builtin_amdgcn_make_buffer_rsrc((void*)p, 0, bytes, 0x00020000);
}
__device__ __forceinline__ void bar() { asm volatile("s_waitcnt lgkmcnt(0)\n\ts_barrier" ::: "memory"); }
__device__ __forceinline__ void drain() { asm volatile("s_waitcnt vmcnt(0)" ::: "memory"); }
#define SB() __builtin_amdgcn_sched_barrier(0)
template <int N> __device__ __forceinline__ void vmwait() { asm volatile("s_waitcnt vmcnt(%0)" :: "n"(N) : "memory"); }
__device__ __forceinline__ unsigned swz(unsigned row) { return ((row & 3) << 2) | ((row >> 2) & 3); }
__device__ __forceinline__ u32x2 trread(LAS char* p) {
  return __builtin_bit_cast(u32x2, __builtin_amdgcn_ds_read_tr16_b64_v4i16((LAS s16x4*)p));
}

struct Tag { unsigned tl, th; u64 e; };
__device__ __forceinline__ Tag mktag(u64 e0) {
  Tag t;
  asm volatile("" : "+s"(e0));
  t.e = e0 * 6364136223846793005ull + 1442695040888963407ull;
  t.th = (unsigned)(t.e >> 32);
  t.tl = (unsigned)t.e & 0xff000000u;
  return t;
}
__device__ __forceinline__ unsigned gpack(float v, const Tag& t) { return ((__float_as_uint(v) + 0x80u) >> 8) | t.tl; }
__device__ __forceinline__ u32x4 gpair(float a, float b, const Tag& t) { return u32x4{gpack(a, t), t.th, gpack(b, t), t.th}; }
__device__ __forceinline__ bool gok(u32x4 g, const Tag& t) {
  return ((((g[0] ^ t.tl) | (g[2] ^ t.tl)) >> 24) | (g[1] ^ t.th) | (g[3] ^ t.th)) == 0u;
}
__device__ __forceinline__ float gval(unsigned lo) { return __uint_as_float(lo << 8); }

#define STAMP(i) do {} while (0)

__device__ __forceinline__ void diag_gemv(const u32x4 (&wr)[16], const u32x4 (&xp)[2][2], const unsigned (&mask)[4], f32x4 (&acc)[2]) {
#pragma unroll
  for (int i = 0; i < 16; i++) {
#pragma unroll
    for (int s = 0; s < 2; s++) {
      const unsigned src = xp[s][i >> 3][(i >> 1) & 3];
      const unsigned dup = __builtin_amdgcn_perm(src, src, (i & 1) ? 0x03020302u : 0x01000100u);
      const u32x4 a = {dup & mask[0], dup & mask[1], dup & mask[2], dup & mask[3]};
      acc[s] = mfma16(a, wr[i], acc[s]);
    }
  }
}

__device__ __forceinline__ void qprod(const char* W, const char* x, int head, int p, int lane, const unsigned (&mask)[4],
                                      rsrc_t rws, int qpoff, u64 e0) {
  const int g = lane >> 4, c = lane & 15;
  const rsrc_t rw = mkrs(W, 4u << 20);
  const int voff = (64 * p + 16 * g) * 8192 + head * 256 + c * 16;
  u32x4 xp[2][2];
#pragma unroll
  for (int s = 0; s < 2; s++) {
    const u32x4* xs = (const u32x4*)(x + (2 * s + (c >> 3)) * 1024 + (64 * p + 16 * g) * 2);
    xp[s][0] = xs[0]; xp[s][1] = xs[1];
  }
  u32x4 wr[16];
#pragma unroll
  for (int i = 0; i < 16; i++) wr[i] = __builtin_amdgcn_raw_buffer_load_b128(rw, voff, i * 8192, AUX_NT);
  SB();
  const Tag tg = mktag(e0);
  f32x4 acc[2] = {{0.f, 0.f, 0.f, 0.f}, {0.f, 0.f, 0.f, 0.f}};
  diag_gemv(wr, xp, mask, acc);
  drain();
#pragma unroll
  for (int s = 0; s < 2; s++) {
    __builtin_amdgcn_raw_buffer_store_b128(gpair(acc[s][0], acc[s][1], tg), rws, qpoff + (2 * s) * 1024 + lane * 16, 0, AUX_SC1);
    __builtin_amdgcn_raw_buffer_store_b128(gpair(acc[s][2], acc[s][3], tg), rws, qpoff + (2 * s + 1) * 1024 + lane * 16, 0, AUX_SC1);
  }
  SB();
}

__device__ __forceinline__ void qgather(rsrc_t rws, int qpbase, int s, int lane, const Tag& tg, LAS char* qdst) {
  const int off = qpbase + s * 2048 + lane * 16;
  u32x4 v[16];
  for (unsigned spins = 0;; spins++) {
    asm volatile("" ::: "memory");
#pragma unroll
    for (int i = 0; i < 16; i++) v[i] = __builtin_amdgcn_raw_buffer_load_b128(rws, off, (i >> 1) * 4096 + (i & 1) * 1024, AUX_SC1);
    bool ok = true;
#pragma unroll
    for (int i = 0; i < 16; i++) ok &= gok(v[i], tg);
    if (__all(ok) || spins > SPIN_CAP) break;
    __builtin_amdgcn_s_sleep(1);
  }
  f32x4 a = {0.f, 0.f, 0.f, 0.f};
#pragma unroll
  for (int p = 0; p < 8; p++) a += f32x4{gval(v[2 * p][0]), gval(v[2 * p][2]), gval(v[2 * p + 1][0]), gval(v[2 * p + 1][2])};
  *(LAS u32x2*)(qdst + (2 * s + (lane >> 5)) * 256 + (lane & 15) * 16 + ((lane >> 4) & 1) * 8) = u32x2{cvtpk(a[0], a[1]), cvtpk(a[2], a[3])};
}

__device__ __forceinline__ void poll32(char* ws, int off, int lane, const Tag& tg) {
  GAS u64* f = (GAS u64*)(ws + off + (lane & 31) * 128);
  for (unsigned spins = 0;; spins++) {
    const u64 v = __hip_atomic_load(f, RLX_AGENT);
    if (__all(v == tg.e) || spins > SPIN_CAP) break;
    __builtin_amdgcn_s_sleep(4);
  }
  __builtin_amdgcn_fence(__ATOMIC_ACQUIRE, "wavefront");
}

constexpr int NSTAT = 64, NUNIT = (1024 - 12 * NSTAT) / 4;
struct Acc { double o[4]; double l; int R; };
template <int W> __device__ __forceinline__ void b_issue(u32x4 (&ra)[16], u32x4 (&rb)[16], rsrc_t rk, int so, int lane) {
#pragma unroll
  for (int i = 0; i < 32; i++) {
    if (i >= W) vmwait<W - 1>();
    const u32x4 v = __builtin_amdgcn_raw_buffer_load_b128(rk, lane * 16, so + i * 1024, AUXK);
    if (i < 16) ra[i] = v; else rb[i - 16] = v;
    SB();
  }
}
__device__ __forceinline__ float dot8(u32x4 k, u32x4 q) { return dot2(k[3], q[3], dot2(k[2], q[2], dot2(k[1], q[1], dot2(k[0], q[0], 0.f)))); }
#define TR_STEP(HALF, CTRL, BIT) _Pragma("unroll") for (int h = 0; h < (HALF); ++h) { \
    const float t1 = x[h] + dpp<CTRL>(x[h]), t2 = x[h + (HALF)] + dpp<CTRL>(x[h + (HALF)]); x[h] = (c & (BIT)) ? t2 : t1; }
__device__ __forceinline__ float k_step(u32x4 (&ring)[16], u32x4 q, int c, rsrc_t rn, int voff, int so) {
  float x[16];
#pragma unroll
  for (int i = 0; i < 16; i++) {
    x[i] = dot8(ring[i], q);
    asm volatile("s_waitcnt vmcnt(%1)" : "+v"(x[i]) : "n"(WTH - 1));
    ring[i] = __builtin_amdgcn_raw_buffer_load_b128(rn, voff, so + i * 1024, AUXK);
    SB();
  }
  TR_STEP(8, XOR8, 8) TR_STEP(4, XOR7, 4) TR_STEP(2, XOR2, 2) TR_STEP(1, XOR1, 1)
  return rbf(rbf(x[0]) * SCALE);
}
template <int BC> __device__ __forceinline__ void pv(u32x4 v, unsigned pk, const unsigned (&mask)[4], f32x4& acc) {
  const unsigned d = dppu<0x150 + BC>(pk);
  acc = mfma16(u32x4{d & mask[0], d & mask[1], d & mask[2], d & mask[3]}, v, acc);
}
template <int I, int MODE> __device__ __forceinline__ void v_step(u32x4 (&ring)[16], u32x4 (&other)[16], unsigned pk, const unsigned (&mask)[4], f32x4& acc,
                                                                  rsrc_t rk, rsrc_t rv, int voff, int so) {
  pv<I>(ring[I], pk, mask, acc);
  if (MODE == 1) {
    asm volatile("s_waitcnt vmcnt(%1)" : "+v"(acc) : "n"(WTH - 1));
    ring[I] = __builtin_amdgcn_raw_buffer_load_b128(rv, voff, so + I * 1024, AUXV);
  } else {
    asm volatile("" : "+v"(acc));
    if (MODE == 2 && I < 8) other[I] = __builtin_amdgcn_raw_buffer_load_b128(I < 4 ? rk : rv, voff, so + (I & 3) * 1024, AUXV);
  }
  SB();
  if constexpr (I < 15) v_step<I + 1, MODE>(ring, other, pk, mask, acc, rk, rv, voff, so);
}
__device__ __forceinline__ unsigned ticket(char* ws, int off, unsigned k, int lane) {
  unsigned t = 0, z = 0;
  asm volatile("" : "+v"(z));
  if (lane == 0) t = __hip_atomic_fetch_add((GAS unsigned*)(ws + off + z), k, RLX_AGENT);
  SB();
  return t;
}
__device__ __forceinline__ int unit_off(unsigned t) { return (12 * NSTAT + 4 * (int)min(t, (unsigned)NUNIT)) * 1024; }
__device__ __forceinline__ float ceil_exp(float mx) { return __builtin_ceilf(mx * LOG2E); }

__device__ __forceinline__ unsigned b_static(u32x4 (&ra)[16], u32x4 (&rb)[16], rsrc_t rk, rsrc_t rv, int so, u32x4 q, int lane, const unsigned (&mask)[4],
                                             char* ws, int cntoff, Acc& A) {
  const int c = lane & 15, voff = lane * 16;
  float sc[4] = {0.f, 0.f, 0.f, 0.f};
#pragma unroll 1
  for (int it = 0; it < 2; it++) {
    const rsrc_t rn = it ? rv : rk;
    sc[2] = sc[0]; sc[3] = sc[1];
    sc[1] = k_step(ra, q, c, rn, voff, so + (it ? 3 : 2) * 16384);
    sc[0] = k_step(rb, q, c, rn, voff, so + (it ? 2 : 3) * 16384);
  }
  STAMP(3);
  const float M = ceil_exp(xrow_max(row_max(fmaxf(fmaxf(sc[0], sc[1]), fmaxf(sc[2], sc[3])))));
  unsigned pk[4];
  float l = 0.f;
#pragma unroll
  for (int k = 0; k < 4; k++) {
    const float p = fexp2(fmaf(sc[k], LOG2E, -M));
    pk[k] = cvtpk(p, p);
    l += bf_lo(pk[k]);
  }
  f32x4 acc = {0.f, 0.f, 0.f, 0.f};
  v_step<0, 1>(ra, rb, pk[0], mask, acc, rk, rv, voff, so + 16384);
  const unsigned tv = ticket(ws, cntoff, 2, lane);
  v_step<0, 1>(rb, ra, pk[1], mask, acc, rk, rv, voff, so);
  v_step<0, 0>(ra, rb, pk[2], mask, acc, rk, rv, voff, 0);
  const unsigned t0 = __builtin_amdgcn_readfirstlane(tv);
  v_step<0, 2>(rb, ra, pk[3], mask, acc, rk, rv, voff, unit_off(t0));
#pragma unroll
  for (int j = 0; j < 4; j++) A.o[j] = acc[j];
  A.l = l; A.R = (int)M;
  return t0;
}
__device__ __forceinline__ void b_units(u32x4 (&ra)[16], rsrc_t rk, rsrc_t rv, unsigned t0, u32x4 q, int lane, const unsigned (&mask)[4],
                                        char* ws, int cntoff, rsrc_t rws, int hintoff, const Tag& tg, Acc& A) {
  const int c = lane & 15, voff = lane * 16;
  const int hoff = lane == 0 ? hintoff : 0x7ffffff0;
  unsigned cur = t0, nxt = t0 + 1;
  if (cur >= NUNIT) __builtin_amdgcn_raw_buffer_store_b64(u32x2{(unsigned)tg.e, tg.th}, rws, hoff, 0, AUX_SC1);
#pragma unroll 1
  while (cur < NUNIT) {
    const unsigned tv = ticket(ws, cntoff, 1, lane);
    __builtin_amdgcn_raw_buffer_store_b64(u32x2{(unsigned)tg.e, tg.th}, rws, nxt >= NUNIT ? hoff : 0x7ffffff0, 0, AUX_SC1);
    SB();
    const int so = unit_off(nxt);
    float x[4];
#pragma unroll
    for (int i = 0; i < 4; i++) {
      x[i] = dot8(ra[i], q);
      asm volatile("" : "+v"(x[i]));
      ra[i] = __builtin_amdgcn_raw_buffer_load_b128(rk, voff, so + i * 1024, AUXK);
      SB();
    }
    TR_STEP(2, XOR8, 8) TR_STEP(1, XOR7, 4)
    x[0] += dpp<XOR2>(x[0]); x[0] += dpp<XOR1>(x[0]);
    const float s = rbf(rbf(x[0]) * SCALE);
    float mx = fmaxf(s, dpp<XOR8>(s));
    mx = fmaxf(mx, dpp<ROR4>(mx));
    const float M = ceil_exp(xrow_max(mx));
    const float p = fexp2(fmaf(s, LOG2E, -M));
    const unsigned pk = cvtpk(p, p);
    f32x4 acc = {0.f, 0.f, 0.f, 0.f};
    pv<0>(ra[4], pk, mask, acc);  asm volatile("" : "+v"(acc)); ra[4] = __builtin_amdgcn_raw_buffer_load_b128(rv, voff, so, AUXV);        SB();
    pv<4>(ra[5], pk, mask, acc);  asm volatile("" : "+v"(acc)); ra[5] = __builtin_amdgcn_raw_buffer_load_b128(rv, voff, so + 1024, AUXV); SB();
    pv<8>(ra[6], pk, mask, acc);  asm volatile("" : "+v"(acc)); ra[6] = __builtin_amdgcn_raw_buffer_load_b128(rv, voff, so + 2048, AUXV); SB();
    pv<12>(ra[7], pk, mask, acc); asm volatile("" : "+v"(acc)); ra[7] = __builtin_amdgcn_raw_buffer_load_b128(rv, voff, so + 3072, AUXV); SB();
    const int Mi = (int)M, Rn = max(A.R, Mi), d0 = A.R - Rn, d1 = Mi - Rn;
#pragma unroll
    for (int j = 0; j < 4; j++) A.o[j] = __builtin_ldexp(A.o[j], d0) + __builtin_ldexp((double)acc[j], d1);
    A.l = __builtin_ldexp(A.l, d0) + __builtin_ldexp((double)bf_lo(pk), d1 - 2);
    A.R = Rn;
    cur = nxt; nxt = __builtin_amdgcn_readfirstlane(tv);
  }
}
#undef TR_STEP
template <int CTRL> __device__ __forceinline__ double dppd(double x) {
  const u32x2 u = __builtin_bit_cast(u32x2, x);
  return __builtin_bit_cast(double, u32x2{dppu<CTRL>(u[0]), dppu<CTRL>(u[1])});
}
__device__ __forceinline__ u32x4 hilo(double v, const Tag& t) {
  const float hi = (float)v, lo = (float)(v - (double)hi);
  return u32x4{__float_as_uint(hi), t.th, __float_as_uint(lo), (unsigned)t.e};
}
__device__ __forceinline__ bool hok(u32x4 g, const Tag& t) { return ((g[1] ^ t.th) | (g[3] ^ (unsigned)t.e)) == 0u; }
__device__ __forceinline__ double hval(u32x4 g) { return (double)__uint_as_float(g[0]) + (double)__uint_as_float(g[2]); }

__device__ __forceinline__ void b_merge(LAS char* scr, int w, int lane, int tid, int rot, const Acc& A, rsrc_t rws, int head, int slot, const Tag& tg) {
  const int c = lane & 15, grp = lane >> 4;
  LAS char* mrg = scr + 8192;
  double l = A.l;
  l += dppd<XOR8>(l); l += dppd<ROR4>(l); l += dppd<XOR2>(l); l += dppd<XOR1>(l);
  if (grp < 2) {
    LAS double* d = (LAS double*)(mrg + w * 1088 + (8 * c + 4 * grp) * 8);
    d[0] = A.o[0]; d[1] = A.o[1]; d[2] = A.o[2]; d[3] = A.o[3];
  }
  if (c == 0) { *(LAS double*)(mrg + w * 1088 + 1024 + grp * 8) = l; *(LAS int*)(mrg + w * 1088 + 1056) = A.R; }
  bar();
  const int b = tid >> 7, d = tid & 127;
  LAS char* m0 = mrg + ((b - rot) & 3) * 1088;
  LAS char* m1 = m0 + 4 * 1088;
  const int R0 = *(LAS int*)(m0 + 1056), R1 = *(LAS int*)(m1 + 1056), R = max(R0, R1);
  const double v = __builtin_ldexp(*(LAS double*)(m0 + d * 8), R0 - R) + __builtin_ldexp(*(LAS double*)(m1 + d * 8), R1 - R);
  __builtin_amdgcn_raw_buffer_store_b128(hilo(v, tg), rws, WS_REC + ((head * 8 + slot) * 4 + b) * 2048 + d * 16, 0, AUX_SC1);
  if (d < 2) {
    LAS double* l0 = (LAS double*)(m0 + 1024);
    LAS double* l1 = (LAS double*)(m1 + 1024);
    const double L = __builtin_ldexp((l0[0] + l0[1]) + (l0[2] + l0[3]), R0 - R) + __builtin_ldexp((l1[0] + l1[1]) + (l1[2] + l1[3]), R1 - R);
    __builtin_amdgcn_raw_buffer_store_b128(hilo(d ? L : (double)R, tg), rws, WS_HDR + ((head * 4 + b) * 8 + slot) * 32 + d * 16, 0, AUX_SC1);
  }
}

template <int NR>
__device__ __forceinline__ void dma_rows(rsrc_t rs, int srcoff, LAS char* img, int r0, int lane) {
#pragma unroll
  for (int u = 0; u < NR; u++) {
    const int r = r0 + u;
    unsigned keep;
    asm volatile("s_mov_b32 %0, m0\n\ts_mov_b32 m0, %1\n\ts_nop 0\n\tbuffer_load_dwordx4 %2, %3, %4 offen nt lds\n\ts_mov_b32 m0, %0"
                 : "=&s"(keep) : "s"((unsigned)(uintptr_t)img + r * 1024), "v"((lane ^ swz(r)) * 16), "s"(rs), "s"(srcoff + r * 1024) : "memory");
  }
  SB();
}

__device__ __forceinline__ void wo_frags(LAS char* img, int w, int lane, u32x4 (&wf)[4][4]) {
  const int g = lane >> 4, q = (lane & 15) >> 2, p = lane & 3;
#pragma unroll
  for (int ks = 0; ks < 4; ks++)
#pragma unroll
    for (int tl = 0; tl < 4; tl++) {
      const int ch = 8 * w + 2 * tl + (p >> 1);
      const int r0 = 32 * ks + 8 * g + q, r1 = r0 + 4;
      const u32x2 a = trread(img + r0 * 1024 + ((ch ^ swz(r0)) * 16) + 8 * (p & 1));
      const u32x2 b = trread(img + r1 * 1024 + ((ch ^ swz(r1)) * 16) + 8 * (p & 1));
      wf[tl][ks] = u32x4{a[0], a[1], b[0], b[1]};
    }
}
__device__ __forceinline__ void wo_apply(const u32x4 (&wf)[4][4], LAS char* cb, int w, int lane, int head, rsrc_t rws, int yoff, const Tag& tg) {
  const int n = lane & 15, grp = lane >> 4;
  u32x4 bf[4];
#pragma unroll
  for (int ks = 0; ks < 4; ks++) bf[ks] = *(LAS u32x4*)(cb + (n & 3) * 256 + ks * 64 + grp * 16);
#pragma unroll
  for (int tl = 0; tl < 4; tl++) {
    f32x4 acc = {0.f, 0.f, 0.f, 0.f};
#pragma unroll
    for (int ks = 0; ks < 4; ks++) acc = mfma16(wf[tl][ks], bf[ks], acc);
    if (n < 4) {
      const int off = yoff + ((n * 8 + w) * 32 + head) * 512 + (16 * tl + 4 * grp) * 8;
      __builtin_amdgcn_raw_buffer_store_b128(gpair(acc[0], acc[1], tg), rws, off, 0, AUX_SC1);
      __builtin_amdgcn_raw_buffer_store_b128(gpair(acc[2], acc[3], tg), rws, off + 16, 0, AUX_SC1);
    }
  }
}
__device__ __forceinline__ void y_reduce(LAS char* scr, int w, int lane, int tid, int head, rsrc_t rws, int yoff, const Tag& tg, unsigned* out, int part) {
  const int off = yoff + head * 32 * 512 + (tid >> 4) * 512 + (tid & 15) * 16;
  u32x4 v0, v1;
  for (unsigned spins = 0;; spins++) {
    asm volatile("" ::: "memory");
    v0 = __builtin_amdgcn_raw_buffer_load_b128(rws, off, 0, AUX_SC1);
    v1 = __builtin_amdgcn_raw_buffer_load_b128(rws, off + 256, 0, AUX_SC1);
    if (__all(gok(v0, tg) & gok(v1, tg)) || spins > SPIN_CAP) break;
    __builtin_amdgcn_s_sleep(1);
  }
  f32x4 s = {xrow_sum(gval(v0[0])), xrow_sum(gval(v0[2])), xrow_sum(gval(v1[0])), xrow_sum(gval(v1[2]))};
  LAS float* red = (LAS float*)scr;
  if (lane < 16) *(LAS f32x4*)(red + w * 64 + lane * 4) = s;
  bar();
  if (tid < 32) {
    const int k = tid & 15, hi = tid >> 4;
    float a = 0.f, b = 0.f;
#pragma unroll
    for (int j = 0; j < 8; j++) { const f32x2 v = *(LAS f32x2*)(red + j * 64 + k * 4 + 2 * hi); a += v[0]; b += v[1]; }
    out[(head >> 3) * 512 + part * 256 + (head & 7) * 32 + hi * 16 + k] = cvtpk(a, b);
  }
  bar();
}

__global__ __launch_bounds__(512, 2) void k_all(const char* __restrict__ xA, const char* __restrict__ ckv, const char* __restrict__ wqA,
                                                const char* __restrict__ wuk, const char* __restrict__ wuv, const char* __restrict__ woA,
                                                const char* __restrict__ xB, const char* __restrict__ kc, const char* __restrict__ vc,
                                                const char* __restrict__ wqB, const char* __restrict__ woB, char* ws, unsigned* out) {
  __shared__ __attribute__((aligned(16))) char smem[LDS_BYTES];
  LAS char* lds = (LAS char*)smem;
  LAS char* scr = lds + LDS_SCR;
  const int tid = threadIdx.x, lane = tid & 63, w = __builtin_amdgcn_readfirstlane(tid >> 6);
  const int blk = blockIdx.x, head = blk >> 3, prod = (blk + head) & 7;
  const int sub = prod == 0 ? 6 : prod == 4 ? 7 : prod - 1 - (prod > 4);
  STAMP(0);
  if (tid == 448) *(volatile LAS u32x2*)(scr + 6144) = u32x2{0u, 0u};
  asm volatile("" :: "s"(xA), "s"(ckv), "s"(wqA), "s"(wuk), "s"(wuv), "s"(woA), "s"(xB), "s"(kc), "s"(vc), "s"(wqB), "s"(woB), "s"(ws), "s"(out));
  bar();
  const u64 e0 = *(const __attribute__((address_space(4))) u64*)(ws + WS_EPOCH);
  SB();
  const rsrc_t rws = mkrs(ws, WS_END);
  unsigned mask[4];
#pragma unroll
  for (int v = 0; v < 4; v++) mask[v] = (((lane & 7) >> 1) == v) ? ((lane & 1) ? 0xffff0000u : 0x0000ffffu) : 0u;
  const int qpA = WS_QP + head * 32768, qpB = WS_QP + (32 + head) * 32768;

  if (w < 2) {
    __builtin_amdgcn_s_setprio(3);
    __builtin_amdgcn_raw_buffer_store_b32(0u, rws, (sub == 7 && tid < 4) ? WS_CNT + (head * 4 + tid) * 128 : 0x7ffffff0, 0, AUX_SC1);
    SB();
    qprod(w == 0 ? wqB : wqA, w == 0 ? xB : xA, head, prod, lane, mask, rws, (w == 0 ? qpB : qpA) + prod * 4096, e0);
    __builtin_amdgcn_s_setprio(0);
  } else {
    __builtin_amdgcn_s_sleep(DELAY0);
  }
  STAMP(1);

  if (sub == 6) {
    const int b = head >> 3, sp = head & 7;
    const rsrc_t rc = mkrs(ckv + ((size_t)b * 4096 + sp * 512) * 1024, 512 * 1024);
    dma_rows<8>(rc, 0, lds, 8 * w, lane);
    dma_rows<8>(rc, 65536, lds + 65536, 8 * w, lane);
    const Tag tg = mktag(e0);
    if (w == 2) poll32(ws, WS_F1, lane, tg);
    bar();
    STAMP(2);
    {
      u32x4 t[4];
#pragma unroll
      for (int i = 0; i < 4; i++) t[i] = __builtin_amdgcn_raw_buffer_load_b128(rws, WS_QABS + b * 32768 + (8 * i + w) * 1024 + lane * 16, 0, AUX_SC1);
#pragma unroll
      for (int i = 0; i < 4; i++) *(LAS u32x4*)(scr + (8 * i + w) * 1024 + ((lane ^ swz(8 * i + w)) * 16)) = t[i];
    }
    bar();
    const int kb = w & 3, hb = w >> 2, l16 = lane & 15, grp = lane >> 4;
    u32x4 qf[16];
    {
      const int row = 16 * hb + l16;
#pragma unroll
      for (int ks = 0; ks < 16; ks++) qf[ks] = *(LAS u32x4*)(scr + row * 1024 + (((4 * ks + grp) ^ swz(row)) * 16));
    }
    bar();
    STAMP(3);
    LAS float* mx = (LAS float*)scr;
    LAS char* pl = scr + 1024;
    LAS float* lr = (LAS float*)(scr + 8192);
    f32x16 o0, o1;
#pragma unroll
    for (int j = 0; j < 16; j++) { o0[j] = 0.f; o1[j] = 0.f; }
    float m1 = -INFINITY, m2 = -INFINITY, l1 = 0.f;
    const int h1 = 16 * hb + l16, h2 = lane & 31, hh = lane >> 5;
    const int arow = 16 * kb + l16;
    const int tq = (lane & 15) >> 2, tp = lane & 3, tblk = (lane >> 4) & 1;
#pragma unroll 1
    for (int t = 0; t < 8; t++) {
      if (t < 7) asm volatile("s_waitcnt vmcnt(8)" ::: "memory"); else asm volatile("s_waitcnt vmcnt(0)" ::: "memory");
      bar();
      LAS char* img = lds + (t & 1) * 65536;
      f32x4 s = {0.f, 0.f, 0.f, 0.f};
#pragma unroll
      for (int ks = 0; ks < 16; ks++) {
        const u32x4 a = *(LAS u32x4*)(img + arow * 1024 + (((4 * ks + grp) ^ swz(arow)) * 16));
        s = mfma16(a, qf[ks], s);
      }
      float x[4];
#pragma unroll
      for (int j = 0; j < 4; j++) x[j] = rbf(rbf(s[j]) * SCALE);
      const float lm = xrow_max(fmaxf(fmaxf(x[0], x[1]), fmaxf(x[2], x[3])));
      if (grp == 0) mx[h1 * 4 + kb] = lm;
      bar();
      const f32x4 ma = *(LAS f32x4*)(mx + h1 * 4), mb = *(LAS f32x4*)(mx + h2 * 4);
      const float n1 = fmaxf(m1, fmaxf(fmaxf(ma[0], ma[1]), fmaxf(ma[2], ma[3])));
      const float n2 = fmaxf(m2, fmaxf(fmaxf(mb[0], mb[1]), fmaxf(mb[2], mb[3])));
      const float al1 = fexp2((m1 - n1) * LOG2E), al2 = fexp2((m2 - n2) * LOG2E);
      m1 = n1; m2 = n2;
      const u32x2 pp = {cvtpk(fexp2((x[0] - n1) * LOG2E), fexp2((x[1] - n1) * LOG2E)), cvtpk(fexp2((x[2] - n1) * LOG2E), fexp2((x[3] - n1) * LOG2E))};
      l1 = fmaf(l1, al1, (bf_lo(pp[0]) + bf_hi(pp[0])) + (bf_lo(pp[1]) + bf_hi(pp[1])));
      *(LAS u32x2*)(pl + h1 * 144 + (16 * kb + 4 * grp) * 2) = pp;
#pragma unroll
      for (int j = 0; j < 16; j++) { o0[j] *= al2; o1[j] *= al2; }
      bar();
#pragma unroll
      for (int ks = 0; ks < 4; ks++) {
        const u32x4 pb = *(LAS u32x4*)(pl + h2 * 144 + (16 * ks + 8 * hh) * 2);
        const int r0 = 16 * ks + 8 * hh + tq, r1 = r0 + 4;
        const int ch0 = 8 * w + 2 * tblk + (tp >> 1), ch1 = ch0 + 4;
        const u32x2 a0 = trread(img + r0 * 1024 + ((ch0 ^ swz(r0)) * 16) + 8 * (tp & 1));
        const u32x2 a1 = trread(img + r1 * 1024 + ((ch0 ^ swz(r1)) * 16) + 8 * (tp & 1));
        const u32x2 b0 = trread(img + r0 * 1024 + ((ch1 ^ swz(r0)) * 16) + 8 * (tp & 1));
        const u32x2 b1 = trread(img + r1 * 1024 + ((ch1 ^ swz(r1)) * 16) + 8 * (tp & 1));
        o0 = mfma32(u32x4{a0[0], a0[1], a1[0], a1[1]}, pb, o0);
        o1 = mfma32(u32x4{b0[0], b0[1], b1[0], b1[1]}, pb, o1);
      }
      bar();
      if (t < 6) dma_rows<8>(rc, (t + 2) * 65536, img, 8 * w, lane);
    }
    STAMP(4);
    {
      LAS char* tp = lds + h2 * 2064 + (64 * w + 4 * hh) * 4;
#pragma unroll
      for (int j4 = 0; j4 < 4; j4++) {
        *(LAS f32x4*)(tp + j4 * 32) = f32x4{o0[4 * j4], o0[4 * j4 + 1], o0[4 * j4 + 2], o0[4 * j4 + 3]};
        *(LAS f32x4*)(tp + 128 + j4 * 32) = f32x4{o1[4 * j4], o1[4 * j4 + 1], o1[4 * j4 + 2], o1[4 * j4 + 3]};
      }
      const float ls = xrow_sum(l1);
      if (grp == 0) lr[kb * 32 + h1] = ls;
      bar();
#pragma unroll
      for (int i = 0; i < 8; i++) {
        const u32x4 v = *(LAS u32x4*)(lds + (4 * w + (i >> 1)) * 2064 + (i & 1) * 1024 + lane * 16);
        __builtin_amdgcn_raw_buffer_store_b128(v, rws, WS_PO + (head * 32 + 4 * w) * 2048 + lane * 16, i * 1024, AUX_SC1);
      }
      if (tid < 32) {
        const f32x2 v = {m2, (lr[tid] + lr[32 + tid]) + (lr[64 + tid] + lr[96 + tid])};
        __builtin_amdgcn_raw_buffer_store_b64(__builtin_bit_cast(u32x2, v), rws, WS_PML + (head * 32 + tid) * 8, 0, AUX_SC1);
      }
      if (tid == 448) *(volatile LAS u32x2*)(scr + 6144) = u32x2{0u, 0u};
      drain();
      bar();
      if (tid == 0) __hip_atomic_store((GAS u64*)(ws + WS_F2 + head * 128), tg.e, RLX_AGENT);
    }
    STAMP(5);
  }
  if (sub < 7) {
    const int b = (w + sub) & 3;
    const size_t base = (size_t)(b * 32 + head) << 20;
    const rsrc_t rk = mkrs(kc + base, 1u << 20), rv = mkrs(vc + base, 1u << 20);
    const int so = (2 * sub + (w >> 2)) * NSTAT * 1024, cntoff = WS_CNT + (head * 4 + b) * 128;
    u32x4 ra[16], q;
    Acc A;
    unsigned t0;
    auto gather = [&]() {
      if (w < 2) {
        const Tag tg = mktag(e0);
        qgather(rws, qpB, w, lane, tg, scr + 5120);
        asm volatile("s_waitcnt lgkmcnt(0)" ::: "memory");
        if (lane == 0) ((volatile LAS unsigned*)(scr + 6144))[w] = 1u;
        STAMP(11);
      }
    };
    auto getq = [&]() {
      STAMP(12);
      for (unsigned spins = 0; spins < SPIN_CAP * 64; spins++) {
        if (__builtin_amdgcn_readfirstlane(((volatile LAS unsigned*)(scr + 6144))[b >> 1])) break;
        __builtin_amdgcn_s_sleep(1);
      }
      asm volatile("" ::: "memory");
      STAMP(6);
      q = *(LAS u32x4*)(scr + 5120 + b * 256 + (lane & 15) * 16);
    };
    if (sub < 6) {
      u32x4 rb[16];
      gather();
      b_issue<WFILL>(ra, rb, rk, so, lane);
      getq();
      t0 = b_static(ra, rb, rk, rv, so, q, lane, mask, ws, cntoff, A);
    } else {
      gather();
      getq();
      t0 = __builtin_amdgcn_readfirstlane(ticket(ws, cntoff, 2, lane));
#pragma unroll
      for (int i = 0; i < 8; i++) ra[i] = __builtin_amdgcn_raw_buffer_load_b128(i < 4 ? rk : rv, lane * 16, unit_off(t0) + (i & 3) * 1024, AUXV);
      SB();
      A.o[0] = A.o[1] = A.o[2] = A.o[3] = A.l = 0.0; A.R = -(1 << 20);
    }
    const Tag tg = mktag(e0);
    STAMP(7);
    b_units(ra, rk, rv, t0, q, lane, mask, ws, cntoff, rws, WS_HINT + (head * 64 + sub * 8 + w) * 8, tg, A);
    STAMP(8);
    b_merge(scr, w, lane, tid, sub, A, rws, head, sub, tg);
    STAMP(9);
  } else {
    const int l16 = lane & 15, grp = lane >> 4;
    u32x4 wr[16];
    const Tag tg = mktag(e0);
    if (w < 2) {
      qgather(rws, qpA, w, lane, tg, scr);
      STAMP(11);
    }
    {
      const rsrc_t rk = mkrs(wuk, 4u << 20);
      const int voff = (64 * w + (l16 & 7)) * 8192 + head * 256 + (l16 >> 3) * 64 + grp * 16;
#pragma unroll
      for (int i = 0; i < 16; i++) {
        if (i >= 5 && w >= 2) vmwait<4>();
        wr[i] = __builtin_amdgcn_raw_buffer_load_b128(rk, voff + (i & 1) * 128, (i >> 1) * 8 * 8192, AUX_NT);
        SB();
      }
    }
    bar();
    STAMP(2);
    {
      u32x4 bf[2];
#pragma unroll
      for (int u = 0; u < 2; u++) bf[u] = *(LAS u32x4*)(scr + (l16 & 3) * 256 + u * 128 + ((l16 >> 2) & 1) * 64 + grp * 16);
#pragma unroll
      for (int rp = 0; rp < 4; rp++) {
        u32x2 v[2];
#pragma unroll
        for (int h = 0; h < 2; h++) {
          f32x4 acc = {0.f, 0.f, 0.f, 0.f};
          acc = mfma16(wr[4 * rp + 2 * h], bf[0], acc);
          acc = mfma16(wr[4 * rp + 2 * h + 1], bf[1], acc);
#pragma unroll
          for (int j = 0; j < 4; j++) {
            auto t = __builtin_amdgcn_permlane32_swap(__float_as_uint(acc[j]), __float_as_uint(acc[j]), false, false);
            acc[j] += dpp<0x104>(__uint_as_float(t[1]));
          }
          v[h] = u32x2{cvtpk(acc[0], acc[1]), cvtpk(acc[2], acc[3])};
        }
        if (l16 < 4 && grp < 2) {
          __builtin_amdgcn_raw_buffer_store_b64(v[0], rws, WS_QABS + (l16 * 32 + head) * 1024 + (64 * w + 16 * rp + 4 * grp) * 2, 0, AUX_SC1);
          __builtin_amdgcn_raw_buffer_store_b64(v[1], rws, WS_QABS + (l16 * 32 + head) * 1024 + (64 * w + 16 * rp + 8 + 4 * grp) * 2, 0, AUX_SC1);
        }
      }
    }
    drain();
    bar();
    if (tid == 0) __hip_atomic_store((GAS u64*)(ws + WS_F1 + head * 128), tg.e, RLX_AGENT);
    STAMP(3);
    u32x4 wf[4][4];
    {
      const rsrc_t ro = mkrs(woA + (size_t)head * 131072, 131072);
      dma_rows<16>(ro, 0, lds, 16 * w, lane);
      const rsrc_t rv = mkrs(wuv, 4u << 20);
      const int voff = (64 * w + 16 * grp) * 8192 + head * 256 + l16 * 16;
#pragma unroll
      for (int i = 0; i < 16; i++) wr[i] = __builtin_amdgcn_raw_buffer_load_b128(rv, voff, i * 8192, AUX_NT);
      SB();
      asm volatile("s_waitcnt vmcnt(16)" ::: "memory");
      bar();
      wo_frags(lds, w, lane, wf);
      bar();
      const rsrc_t rb = mkrs(woB + (size_t)head * 131072, 131072);
      dma_rows<16>(rb, 0, lds, 16 * w, lane);
    }
    STAMP(4);
    if (w == 0) poll32(ws, WS_F2, lane, tg);
    bar();
    STAMP(5);
    {
      const int b = tid >> 7, c4 = tid & 127;
      u32x4 po[8]; u32x2 ml[8];
#pragma unroll
      for (int s = 0; s < 8; s++) {
        ml[s] = __builtin_amdgcn_raw_buffer_load_b64(rws, WS_PML + ((8 * b + s) * 32 + head) * 8, 0, AUX_SC1);
        po[s] = __builtin_amdgcn_raw_buffer_load_b128(rws, WS_PO + ((8 * b + s) * 32 + head) * 2048 + c4 * 16, 0, AUX_SC1);
      }
      float M = -INFINITY;
#pragma unroll
      for (int s = 0; s < 8; s++) M = fmaxf(M, __uint_as_float(ml[s][0]));
      float L = 0.f;
      f32x4 a = {0.f, 0.f, 0.f, 0.f};
#pragma unroll
      for (int s = 0; s < 8; s++) {
        const float f = fexp2((__uint_as_float(ml[s][0]) - M) * LOG2E);
        L = fmaf(f, __uint_as_float(ml[s][1]), L);
        a += f * __builtin_bit_cast(f32x4, po[s]);
      }
      const float inv = 1.f / L;
      const u32x2 v = {cvtpk(a[0] * inv, a[1] * inv), cvtpk(a[2] * inv, a[3] * inv)};
      *(LAS u32x2*)(scr + b * 1024 + c4 * 8) = v;
    }
    bar();
    {
      u32x4 xp[2][2];
#pragma unroll
      for (int s = 0; s < 2; s++) {
        LAS u32x4* xs = (LAS u32x4*)(scr + (2 * s + (l16 >> 3)) * 1024 + (64 * w + 16 * grp) * 2);
        xp[s][0] = xs[0]; xp[s][1] = xs[1];
      }
      f32x4 acc[2] = {{0.f, 0.f, 0.f, 0.f}, {0.f, 0.f, 0.f, 0.f}};
      diag_gemv(wr, xp, mask, acc);
      LAS float* red = (LAS float*)(scr + 8192);
#pragma unroll
      for (int s = 0; s < 2; s++) *(LAS f32x4*)(red + w * 512 + (2 * s + (grp >> 1)) * 128 + 8 * l16 + 4 * (grp & 1)) = acc[s];
      bar();
      if (tid < 256) {
        float a = 0.f, b = 0.f;
#pragma unroll
        for (int k = 0; k < 8; k++) { const f32x2 v = *(LAS f32x2*)(red + k * 512 + 2 * tid); a += v[0]; b += v[1]; }
        ((LAS unsigned*)(scr + 4096))[tid] = cvtpk(a, b);
      }
      bar();
    }
    wo_apply(wf, scr + 4096, w, lane, head, rws, WS_Y, tg);
    STAMP(6);
    asm volatile("s_waitcnt vmcnt(8)" ::: "memory");
    bar();
    wo_frags(lds, w, lane, wf);
    y_reduce(scr, w, lane, tid, head, rws, WS_Y, tg, out, 0);
    STAMP(7);
    if (w == 0) {
      GAS u64* f = (GAS u64*)(ws + WS_HINT + (head * 64 + (lane < 56 ? lane : 0)) * 8);
      for (unsigned spins = 0;; spins++) {
        const u64 v = __hip_atomic_load(f, RLX_AGENT);
        if (__all(v == tg.e) || spins > SPIN_CAP) break;
        __builtin_amdgcn_s_sleep(4);
      }
    }
    bar();
    STAMP(8);
    {
      const int b = w >> 1, d = tid & 127;
      const int hoff = WS_HDR + (head * 4 + b) * 256 + (l16 < 14 ? l16 : 0) * 16;
      const int off = WS_REC + (head * 32 + b) * 2048 + d * 16;
      u32x4 hd, g[7];
      for (unsigned spins = 0;; spins++) {
        asm volatile("" ::: "memory");
        hd = __builtin_amdgcn_raw_buffer_load_b128(rws, hoff, 0, AUX_SC1);
#pragma unroll
        for (int s = 0; s < 7; s++) g[s] = __builtin_amdgcn_raw_buffer_load_b128(rws, off, s * 8192, AUX_SC1);
        bool ok = hok(hd, tg);
#pragma unroll
        for (int s = 0; s < 7; s++) ok &= hok(g[s], tg);
        if (__all(ok) || spins > SPIN_CAP) break;
      }
      int R[7], Rm = -(1 << 21);
#pragma unroll
      for (int s = 0; s < 7; s++) { R[s] = (int)__uint_as_float(__builtin_amdgcn_readlane(hd[0], 2 * s)); Rm = max(Rm, R[s]); }
      double S = 0.0, L = 0.0;
#pragma unroll
      for (int s = 0; s < 7; s++) {
        const double ls = (double)__uint_as_float(__builtin_amdgcn_readlane(hd[0], 2 * s + 1)) + (double)__uint_as_float(__builtin_amdgcn_readlane(hd[2], 2 * s + 1));
        L += __builtin_ldexp(ls, R[s] - Rm);
        S += __builtin_ldexp(hval(g[s]), R[s] - Rm);
      }
      const float cx = (float)(S / L);
      ((LAS unsigned short*)(scr + 4096))[tid] = (unsigned short)cvtpk(cx, cx);
    }
    bar();
    STAMP(9);
    wo_apply(wf, scr + 4096, w, lane, head, rws, WS_Y + 524288, tg);
    y_reduce(scr, w, lane, tid, head, rws, WS_Y + 524288, tg, out, 1);
    if (head == 0 && tid == 0) __hip_atomic_store((GAS u64*)(ws + WS_EPOCH), tg.e, RLX_AGENT);
    STAMP(10);
  }
}

extern "C" void kernel_launch(void* const* d_in, const int* in_sizes, int n_in, void* d_out, int out_size, void* d_ws, size_t ws_size,
                              hipStream_t stream) {
  k_all<<<256, 512, 0, stream>>>((const char*)d_in[0], (const char*)d_in[1], (const char*)d_in[2], (const char*)d_in[3], (const char*)d_in[4],
                                 (const char*)d_in[5], (const char*)d_in[6], (const char*)d_in[7], (const char*)d_in[8], (const char*)d_in[9],
                                 (const char*)d_in[10], (char*)d_ws, (unsigned*)d_out);
}
```

```cpp
#include <hip/hip_runtime.h>
#include <hip/hip_bf16.h>
#include <stdint.h>

typedef __attribute__((ext_vector_type(4))) unsigned u32x4;
typedef __attribute__((ext_vector_type(2))) unsigned u32x2;
typedef __attribute__((ext_vector_type(4))) float f32x4;
typedef __attribute__((ext_vector_type(2))) float f32x2;
typedef __attribute__((ext_vector_type(2))) double f64x2;
typedef __attribute__((ext_vector_type(16))) float f32x16;
typedef __attribute__((ext_vector_type(8))) __bf16 bf16x8;
typedef __attribute__((ext_vector_type(2))) __bf16 bf16x2;
typedef __attribute__((ext_vector_type(4))) short s16x4;
typedef unsigned long long u64;
typedef __amdgpu_buffer_rsrc_t rsrc_t;
#define LAS __attribute__((address_space(3)))
#define GAS __attribute__((address_space(1)))
#define RLX_AGENT __ATOMIC_RELAXED, __HIP_MEMORY_SCOPE_AGENT

#define STAMPS 0
#define WTH 6
#define WFILL 6
#define HELP 1
#define DCH 3
#define CPF 36000
#define CPFW 0
#define DELAY0 0
#define WF0 WFILL
#define NF0 8
#define PSL 16
#define ABL 0
constexpr int AUX_NT = 2, AUX_SC1 = 16, OOB = 0x7ffffff0;
constexpr float SCALE = 0.08838834764831845f;
constexpr float LOG2E = 1.4426950408889634f;
constexpr unsigned SPIN_CAP = 1u << 17;
constexpr int NSLOT = 6 + HELP, NUNIT = 256;

constexpr int WS_EPOCH = 0;
constexpr int WS_F1   = 4096;
constexpr int WS_F2   = WS_F1 + 4096;
constexpr int CSTR    = 4352;
constexpr int WS_CNT  = WS_F2 + 4096;
constexpr int WS_QB   = WS_CNT + 32 * CSTR;
constexpr int WS_QABS = WS_QB + 32 * 4096;
constexpr int WS_PML  = WS_QABS + 4 * 32 * 1024;
constexpr int WS_PO   = WS_PML + 32 * 32 * 16;
constexpr int WS_RS   = WS_PO + 32 * 32 * 1024;
constexpr int WS_HS   = WS_RS + 32 * 8 * 1024;
constexpr int WS_RD   = WS_HS + 32 * 256;
constexpr int WS_HD   = WS_RD + 32 * 8 * 2048;
constexpr int WS_Y    = WS_HD + 32 * 256;
constexpr int WS_END  = WS_Y + 2 * 4 * 8 * 32 * 512;
constexpr int WS_STAMP = 32 << 20;

constexpr int LDS_BYTES = 163840;
constexpr int LDS_SCR = 131072;

__device__ __forceinline__ unsigned cvtpk(float lo, float hi) {
  f32x2 v = {lo, hi};
  return __builtin_bit_cast(unsigned, __builtin_convertvector(v, bf16x2));
}
__device__ __forceinline__ float bf_lo(unsigned u) { return __uint_as_float(u << 16); }
__device__ __forceinline__ float bf_hi(unsigned u) { return __uint_as_float(u & 0xffff0000u); }
__device__ __forceinline__ float rbf(float x) { return __uint_as_float(cvtpk(x, x) << 16); }
__device__ __forceinline__ float dot2(unsigned k, unsigned q, float acc) {
  return __builtin_amdgcn_fdot2_f32_bf16(__builtin_bit_cast(bf16x2, k), __builtin_bit_cast(bf16x2, q), acc, false);
}
template <int CTRL> __device__ __forceinline__ float dpp(float x) {
  return __builtin_bit_cast(float, __builtin_amdgcn_mov_dpp(__builtin_bit_cast(int, x), CTRL, 0xf, 0xf, true));
}
template <int CTRL> __device__ __forceinline__ unsigned dppu(unsigned x) {
  return (unsigned)__builtin_amdgcn_mov_dpp((int)x, CTRL, 0xf, 0xf, true);
}
constexpr int XOR1 = 0xB1, XOR2 = 0x4E, XOR7 = 0x141, XOR8 = 0x128, ROR4 = 0x124;
__device__ __forceinline__ float xrow_max(float x) {
  auto s = __builtin_amdgcn_permlane16_swap(__float_as_uint(x), __float_as_uint(x), false, false);
  x = fmaxf(__uint_as_float(s[0]), __uint_as_float(s[1]));
  auto t = __builtin_amdgcn_permlane32_swap(__float_as_uint(x), __float_as_uint(x), false, false);
  return fmaxf(__uint_as_float(t[0]), __uint_as_float(t[1]));
}
__device__ __forceinline__ float xrow_sum(float x) {
  auto s = __builtin_amdgcn_permlane16_swap(__float_as_uint(x), __float_as_uint(x), false, false);
  x = __uint_as_float(s[0]) + __uint_as_float(s[1]);
  auto t = __builtin_amdgcn_permlane32_swap(__float_as_uint(x), __float_as_uint(x), false, false);
  return __uint_as_float(t[0]) + __uint_as_float(t[1]);
}
__device__ __forceinline__ float row_max(float x) {
  x = fmaxf(x, dpp<XOR8>(x)); x = fmaxf(x, dpp<ROR4>(x)); x = fmaxf(x, dpp<XOR2>(x)); x = fmaxf(x, dpp<XOR1>(x));
  return x;
}
__device__ __forceinline__ float row_sum(float x) {
  x += dpp<XOR8>(x); x += dpp<ROR4>(x); x += dpp<XOR2>(x); x += dpp<XOR1>(x);
  return x;
}
__device__ __forceinline__ float fexp2(float x) { return __builtin_amdgcn_exp2f(x); }
__device__ __forceinline__ f32x4 mfma16(u32x4 a, u32x4 b, f32x4 c) {
  return __builtin_amdgcn_mfma_f32_16x16x32_bf16(__builtin_bit_cast(bf16x8, a), __builtin_bit_cast(bf16x8, b), c, 0, 0, 0);
}
__device__ __forceinline__ f32x16 mfma32(u32x4 a, u32x4 b, f32x16 c) {
  return __builtin_amdgcn_mfma_f32_32x32x16_bf16(__builtin_bit_cast(bf16x8, a), __builtin_bit_cast(bf16x8, b), c, 0, 0, 0);
}
__device__ __forceinline__ rsrc_t mkrs(const void* p, unsigned bytes) {
  return __builtin_amdgcn_make_buffer_rsrc((void*)p, 0, bytes, 0x00020000);
}
__device__ __forceinline__ void bar() { asm volatile("s_waitcnt lgkmcnt(0)\n\ts_barrier" ::: "memory"); }
__device__ __forceinline__ void drain() { asm volatile("s_waitcnt vmcnt(0)" ::: "memory"); }
#define SB() __builtin_amdgcn_sched_barrier(0)
template <int N> __device__ __forceinline__ void vmwait() { asm volatile("s_waitcnt vmcnt(%0)" :: "n"(N) : "memory"); }
__device__ __forceinline__ unsigned swz(unsigned row) { return ((row & 3) << 2) | ((row >> 2) & 2); }
__device__ __forceinline__ u32x2 trread(LAS char* p) {
  return __builtin_bit_cast(u32x2, __builtin_amdgcn_ds_read_tr16_b64_v4i16((LAS s16x4*)p));
}

struct Tag { unsigned tl, th; u64 e; };
__device__ __forceinline__ Tag mktag(u64 e0) {
  Tag t;
  asm volatile("" : "+s"(e0));
  t.e = e0 * 6364136223846793005ull + 1442695040888963407ull;
  t.th = (unsigned)(t.e >> 32);
  t.tl = (unsigned)t.e & 0xff000000u;
  return t;
}
__device__ __forceinline__ unsigned gpack(float v, const Tag& t) { return ((__float_as_uint(v) + 0x80u) >> 8) | t.tl; }
__device__ __forceinline__ u32x4 gpair(float a, float b, const Tag& t) { return u32x4{gpack(a, t), t.th, gpack(b, t), t.th}; }
__device__ __forceinline__ bool gok(u32x4 g, const Tag& t) {
  return ((((g[0] ^ t.tl) | (g[2] ^ t.tl)) >> 24) | (g[1] ^ t.th) | (g[3] ^ t.th)) == 0u;
}
__device__ __forceinline__ float gval(unsigned lo) { return __uint_as_float(lo << 8); }
__device__ __forceinline__ bool qok(u32x4 g, const Tag& t) {
  return ((((g[0] ^ (unsigned)t.e) | (g[2] ^ (unsigned)t.e)) >> 16) | (g[1] ^ t.th) | (g[3] ^ t.th)) == 0u;
}
__device__ __forceinline__ u32x4 hilo(double v, const Tag& t) {
  const float hi = (float)v, lo = (float)(v - (double)hi);
  return u32x4{__float_as_uint(hi), t.th, __float_as_uint(lo), (unsigned)t.e};
}
__device__ __forceinline__ bool hok(u32x4 g, const Tag& t) { return ((g[1] ^ t.th) | (g[3] ^ (unsigned)t.e)) == 0u; }
__device__ __forceinline__ double hval(u32x4 g) { return (double)__uint_as_float(g[0]) + (double)__uint_as_float(g[2]); }
__device__ __forceinline__ int edif(float a, float b) {
  int r;
  asm("v_cvt_i32_f32 %0, %1" : "=v"(r) : "v"(a - b));
  return r;
}
__device__ __forceinline__ float rdl(unsigned v, int l) { return __uint_as_float(__builtin_amdgcn_readlane(v, l)); }

#define STAMP(i) do {} while (0)
#define FLUSH() do {} while (0)

__device__ __forceinline__ void diag_gemv(const u32x4 (&wr)[16], const u32x4 (&xp)[2][2], const unsigned (&mask)[4], f32x4 (&acc)[2]) {
#pragma unroll
  for (int i = 0; i < 16; i++) {
#pragma unroll
    for (int s = 0; s < 2; s++) {
      const unsigned src = xp[s][i >> 3][(i >> 1) & 3];
      const unsigned dup = __builtin_amdgcn_perm(src, src, (i & 1) ? 0x03020302u : 0x01000100u);
      const u32x4 a = {dup & mask[0], dup & mask[1], dup & mask[2], dup & mask[3]};
      acc[s] = mfma16(a, wr[i], acc[s]);
    }
  }
}

__device__ __forceinline__ void poll32(char* ws, int off, int lane, const Tag& tg) {
  GAS u64* f = (GAS u64*)(ws + off + (lane & 31) * 128);
  for (unsigned spins = 0;; spins++) {
    const u64 v = __hip_atomic_load(f, RLX_AGENT);
    if (__all(v == tg.e) || spins > SPIN_CAP) break;
    __builtin_amdgcn_s_sleep(PSL);
  }
  __builtin_amdgcn_fence(__ATOMIC_ACQUIRE, "wavefront");
}

constexpr int slot_off(int i) { return (i >> 2) * 65536 + (i & 3) * 1024; }
struct Acc { double o[4]; double l; float R; };
__device__ __forceinline__ u32x4 ldnt(rsrc_t r, int voff, int so) { return __builtin_amdgcn_raw_buffer_load_b128(r, voff, so, AUX_NT); }
__device__ __forceinline__ u32x4 ldsc(rsrc_t r, int voff, int so) { return __builtin_amdgcn_raw_buffer_load_b128(r, voff, so, AUX_SC1); }
__device__ __forceinline__ void stsc(u32x4 v, rsrc_t r, int voff, int so) { __builtin_amdgcn_raw_buffer_store_b128(v, r, voff, so, AUX_SC1); }
__device__ __forceinline__ float dot8(u32x4 k, u32x4 q) { return dot2(k[3], q[3], dot2(k[2], q[2], dot2(k[1], q[1], dot2(k[0], q[0], 0.f)))); }
#define TR_STEP(HALF, CTRL, BIT) _Pragma("unroll") for (int h = 0; h < (HALF); ++h) { \
    const float t1 = x[h] + dpp<CTRL>(x[h]), t2 = x[h + (HALF)] + dpp<CTRL>(x[h + (HALF)]); x[h] = (c & (BIT)) ? t2 : t1; }
__device__ __forceinline__ float k_step(u32x4 (&ring)[16], u32x4 q, int c, rsrc_t rn, int voff, int so) {
  float x[16];
#pragma unroll
  for (int i = 0; i < 16; i++) {
    x[i] = dot8(ring[i], q);
    asm volatile("s_waitcnt vmcnt(%1)" : "+v"(x[i]) : "n"(WTH - 1));
    ring[i] = ldnt(rn, voff, so + slot_off(i));
    SB();
  }
  TR_STEP(8, XOR8, 8) TR_STEP(4, XOR7, 4) TR_STEP(2, XOR2, 2) TR_STEP(1, XOR1, 1)
  return rbf(rbf(x[0]) * SCALE);
}
template <int BC> __device__ __forceinline__ void pv(u32x4 v, unsigned pk, const unsigned (&mask)[4], f32x4& acc) {
  const unsigned d = dppu<0x150 + BC>(pk);
  acc = mfma16(u32x4{d & mask[0], d & mask[1], d & mask[2], d & mask[3]}, v, acc);
}
template <int I, int MODE> __device__ __forceinline__ void v_step(u32x4 (&ring)[16], unsigned pk, const unsigned (&mask)[4], f32x4& acc,
                                                                  rsrc_t rk, rsrc_t rv, int voff, int so) {
  pv<I>(ring[I], pk, mask, acc);
  if (MODE == 1 || (MODE == 2 && I < 8)) {
    asm volatile("s_waitcnt vmcnt(%1)" : "+v"(acc) : "n"(WTH - 1));
    ring[I] = MODE == 1 ? ldnt(rv, voff, so + slot_off(I)) : ldnt(I < 4 ? rk : rv, voff, so + (I & 3) * 1024);
  } else {
    asm volatile("" : "+v"(acc));
  }
  SB();
  if constexpr (I < 15) v_step<I + 1, MODE>(ring, pk, mask, acc, rk, rv, voff, so);
}
__device__ __forceinline__ float exp_ref(float mx, float& f) {
  const float M = __builtin_ceilf(mx * LOG2E);
  f = __builtin_amdgcn_fmed3f(fmaf(mx, LOG2E, -M), -1.f, 0.f);
  return M;
}
__device__ __forceinline__ unsigned lds_inc(LAS unsigned* p, int lane) {
  unsigned t = 0, z = 0;
  asm volatile("" : "+v"(z));
  if (lane == 0) t = __hip_atomic_fetch_add(p + z, 1u, __ATOMIC_RELAXED, __HIP_MEMORY_SCOPE_WORKGROUP);
  return t;
}
struct Take { unsigned unit, pend; bool fetch; };
__device__ __forceinline__ void lds_add(LAS unsigned* p, unsigned v) { __hip_atomic_fetch_add(p, v, __ATOMIC_RELAXED, __HIP_MEMORY_SCOPE_WORKGROUP); }
__device__ __forceinline__ Take take(LAS unsigned* ctl, unsigned kv, rsrc_t rws, int cntoff, int lane) {
  const unsigned k = __builtin_amdgcn_readfirstlane(kv);
  unsigned b, s;
  for (unsigned spins = 0;; spins++) {
    const unsigned vs = ((volatile LAS unsigned*)ctl)[5], vb = ((volatile LAS unsigned*)ctl)[8 + (k >> 2)];
    s = __builtin_amdgcn_readfirstlane(vs); b = __builtin_amdgcn_readfirstlane(vb);
    if (b != ~0u || (s >= 0x10000u && (s & 0xffffu) == 0u) || spins > SPIN_CAP * 16) break;
    __builtin_amdgcn_s_sleep(1);
  }
  Take t;
  t.unit = min(b, (unsigned)NUNIT) + (k & 3);
  t.fetch = (k & 3) == 0 && b < NUNIT && s < 0x10000u;
  const bool go = lane == 0 && t.fetch;
  if (go) lds_add(ctl + 5, 1u);
  asm volatile("s_waitcnt lgkmcnt(0)" ::: "memory");
  t.pend = __builtin_amdgcn_raw_ptr_buffer_atomic_add_i32(4, rws, go ? cntoff : OOB, 0, AUX_SC1);
  SB();
  return t;
}
__device__ __forceinline__ void give(LAS unsigned* ctl, const Take& t, int lane) {
  if (t.fetch && lane == 0) {
    unsigned z = 0;
    asm volatile("" : "+v"(z));
    if (t.pend < NUNIT) {
      const unsigned i = __hip_atomic_fetch_add(ctl + 4 + z, 1u, __ATOMIC_RELAXED, __HIP_MEMORY_SCOPE_WORKGROUP);
      ((volatile LAS unsigned*)ctl)[8 + i] = t.pend;
    }
    asm volatile("" ::: "memory");
    lds_add(ctl + 5 + z, t.pend >= NUNIT - 4 ? 0xffffu : ~0u);
  }
}
__device__ __forceinline__ int unit_off(unsigned t) { return (int)min(t, (unsigned)NUNIT) * 4096; }

__device__ __forceinline__ void b_units(u32x4 (&ra)[16], rsrc_t rk, rsrc_t rv, unsigned cur, u32x4 q, int lane, const unsigned (&mask)[4],
                                        LAS unsigned* ctl, rsrc_t rws, int cntoff, Acc& A) {
  const int c = lane & 15, voff = lane * 16;
  unsigned nxt;
  {
    const Take t = take(ctl, lds_inc(ctl, lane), rws, cntoff, lane);
    give(ctl, t, lane);
    nxt = t.unit;
  }
#pragma unroll 1
  while (cur < NUNIT) {
    const int so = unit_off(nxt);
    float x[4];
#pragma unroll
    for (int i = 0; i < 4; i++) {
      x[i] = dot8(ra[i], q);
      asm volatile("" : "+v"(x[i]));
      ra[i] = ldnt(rk, voff, so + i * 1024);
      SB();
    }
    const unsigned kv = lds_inc(ctl, lane);
    TR_STEP(2, XOR8, 8) TR_STEP(1, XOR7, 4)
    x[0] += dpp<XOR2>(x[0]); x[0] += dpp<XOR1>(x[0]);
    const float s = rbf(rbf(x[0]) * SCALE);
    float mx = fmaxf(s, dpp<XOR8>(s));
    mx = xrow_max(fmaxf(mx, dpp<ROR4>(mx)));
    float f;
    const float M = exp_ref(mx, f);
    const float p = fexp2(fmaf(s - mx, LOG2E, f));
    unsigned pk = cvtpk(p, p);
    asm volatile("" : "+v"(pk));
    const Take t = take(ctl, kv, rws, cntoff, lane);
    f32x4 acc = {0.f, 0.f, 0.f, 0.f};
    pv<0>(ra[4], pk, mask, acc);  asm volatile("" : "+v"(acc)); ra[4] = ldnt(rv, voff, so);        SB();
    pv<4>(ra[5], pk, mask, acc);  asm volatile("" : "+v"(acc)); ra[5] = ldnt(rv, voff, so + 1024); SB();
    pv<8>(ra[6], pk, mask, acc);  asm volatile("" : "+v"(acc)); ra[6] = ldnt(rv, voff, so + 2048); SB();
    pv<12>(ra[7], pk, mask, acc); asm volatile("" : "+v"(acc)); ra[7] = ldnt(rv, voff, so + 3072); SB();
    const float Rn = fmaxf(A.R, M);
    const int d0 = edif(A.R, Rn), d1 = edif(M, Rn);
#pragma unroll
    for (int j = 0; j < 4; j++) A.o[j] = __builtin_ldexp(A.o[j], d0) + __builtin_ldexp((double)acc[j], d1);
    A.l = __builtin_ldexp(A.l, d0) + __builtin_ldexp((double)(0.25f * bf_lo(pk)), d1);
    A.R = Rn;
    SB();
    give(ctl, t, lane);
    cur = nxt; nxt = t.unit;
  }
}
#undef TR_STEP
template <int CTRL> __device__ __forceinline__ double dppd(double x) {
  const u32x2 u = __builtin_bit_cast(u32x2, x);
  return __builtin_bit_cast(double, u32x2{dppu<CTRL>(u[0]), dppu<CTRL>(u[1])});
}
__device__ __forceinline__ double rowsum_d(double l) {
  l += dppd<XOR8>(l); l += dppd<ROR4>(l); l += dppd<XOR2>(l); l += dppd<XOR1>(l);
  return l;
}

template <bool WIDE>
__device__ __forceinline__ void b_merge(LAS char* mrg, LAS unsigned* arrive, int w, int lane, const Acc& A, rsrc_t rws, int roff, int hoff, const Tag& tg) {
  const int c = lane & 15, grp = lane >> 4;
  const double l = rowsum_d(A.l);
  if (grp < 2) {
    LAS f64x2* d = (LAS f64x2*)(mrg + w * 1024 + (8 * c + 4 * grp) * 8);
    d[0] = f64x2{A.o[0], A.o[1]}; d[1] = f64x2{A.o[2], A.o[3]};
  }
  if (c == 0) { *(LAS double*)(mrg + 8192 + w * 32 + grp * 8) = l; *(LAS float*)(mrg + 8448 + w * 4) = A.R; }
  asm volatile("s_waitcnt lgkmcnt(0)" ::: "memory");
  const unsigned n = __builtin_amdgcn_readfirstlane(lds_inc(arrive, lane));
  asm volatile("" ::: "memory");
  if (n == 7) {
    f64x2 o[8];
#pragma unroll
    for (int i = 0; i < 8; i++)
      o[i] = WIDE ? f64x2{*(LAS double*)(mrg + i * 1024 + lane * 8), *(LAS double*)(mrg + i * 1024 + 512 + lane * 8)} : *(LAS f64x2*)(mrg + i * 1024 + lane * 16);
    const f32x4 Ra = *(LAS f32x4*)(mrg + 8448), Rb = *(LAS f32x4*)(mrg + 8464);
    const double lw = *(LAS double*)(mrg + 8192 + (lane & 31) * 8);
    const float Rw = *(LAS float*)(mrg + 8448 + ((lane >> 2) & 7) * 4);
    const float R = fmaxf(fmaxf(fmaxf(Ra[0], Ra[1]), fmaxf(Ra[2], Ra[3])), fmaxf(fmaxf(Rb[0], Rb[1]), fmaxf(Rb[2], Rb[3])));
    double v0 = 0.0, v1 = 0.0;
#pragma unroll
    for (int i = 0; i < 8; i++) {
      const int d = edif(i < 4 ? Ra[i & 3] : Rb[i & 3], R);
      v0 += __builtin_ldexp(o[i][0], d); v1 += __builtin_ldexp(o[i][1], d);
    }
    if (WIDE) {
      stsc(hilo(v0, tg), rws, roff + lane * 16, 0);
      stsc(hilo(v1, tg), rws, roff + 1024 + lane * 16, 0);
    } else {
      stsc(gpair((float)v0, (float)v1, tg), rws, roff + lane * 16, 0);
    }
    const double lr = rowsum_d(__builtin_ldexp(lw, edif(Rw, R)));
    const u32x2 lu = __builtin_bit_cast(u32x2, lr);
    const double L = __builtin_bit_cast(double, u32x2{(unsigned)__builtin_amdgcn_readlane(lu[0], 0), (unsigned)__builtin_amdgcn_readlane(lu[1], 0)}) +
                     __builtin_bit_cast(double, u32x2{(unsigned)__builtin_amdgcn_readlane(lu[0], 16), (unsigned)__builtin_amdgcn_readlane(lu[1], 16)});
    u32x4 hd = hilo(L, tg);
    if (lane == 0) { hd[0] = __float_as_uint(R); hd[2] = 0u; }
    stsc(hd, rws, lane < 2 ? hoff + lane * 16 : OOB, 0);
  }
}

template <int NR>
__device__ __forceinline__ void dma_rows(rsrc_t rs, int srcoff, LAS char* img, int r0, int lane) {
#pragma unroll
  for (int u = 0; u < NR; u++) {
    const int r = r0 + u;
    unsigned keep;
    asm volatile("s_mov_b32 %0, m0\n\ts_mov_b32 m0, %1\n\ts_nop 0\n\tbuffer_load_dwordx4 %2, %3, %4 offen nt lds\n\ts_mov_b32 m0, %0"
                 : "=&s"(keep) : "s"((unsigned)(uintptr_t)img + r * 1024), "v"((lane ^ swz(r)) * 16), "s"(rs), "s"(srcoff + r * 1024) : "memory");
  }
  SB();
}

__device__ __forceinline__ void wo_frags(LAS char* img, int w, int lane, u32x4 (&wf)[4][4]) {
  const int g = lane >> 4, q = (lane & 15) >> 2, p = lane & 3;
#pragma unroll
  for (int ks = 0; ks < 4; ks++)
#pragma unroll
    for (int tl = 0; tl < 4; tl++) {
      const int ch = 8 * w + 2 * tl + (p >> 1);
      const int r0 = 32 * ks + 8 * g + q, r1 = r0 + 4;
      const u32x2 a = trread(img + r0 * 1024 + ((ch ^ swz(r0)) * 16) + 8 * (p & 1));
      const u32x2 b = trread(img + r1 * 1024 + ((ch ^ swz(r1)) * 16) + 8 * (p & 1));
      wf[tl][ks] = u32x4{a[0], a[1], b[0], b[1]};
    }
}
__device__ __forceinline__ f32x4 wo_calc(const u32x4 (&wf)[4][4], LAS char* cb, int lane) {
  const int n = lane & 15, grp = lane >> 4;
  u32x4 bf[4];
#pragma unroll
  for (int ks = 0; ks < 4; ks++) bf[ks] = *(LAS u32x4*)(cb + (n & 3) * 256 + ks * 64 + grp * 16);
  f32x4 acc[4];
#pragma unroll
  for (int tl = 0; tl < 4; tl++) acc[tl] = f32x4{0.f, 0.f, 0.f, 0.f};
#pragma unroll
  for (int ks = 0; ks < 4; ks++)
#pragma unroll
    for (int tl = 0; tl < 4; tl++) acc[tl] = mfma16(wf[tl][ks], bf[ks], acc[tl]);
  const f32x4 lo = (n & 4) ? acc[1] : acc[0], hi = (n & 4) ? acc[3] : acc[2];
  return (n & 8) ? hi : lo;
}
__device__ __forceinline__ void y_store(f32x4 y, int w, int lane, int head, rsrc_t rws, int yoff, const Tag& tg) {
  const int off = yoff + (((lane & 3) * 8 + w) * 32 + head) * 512 + ((lane & 12) + (lane >> 4)) * 16;
  stsc(gpair(y[0], y[1], tg), rws, off, 0);
  stsc(gpair(y[2], y[3], tg), rws, off + 256, 0);
}

__global__ __launch_bounds__(512, 2) void k_all(const char* __restrict__ xA, const char* __restrict__ ckv, const char* __restrict__ wqA,
                                                const char* __restrict__ wuk, const char* __restrict__ wuv, const char* __restrict__ woA,
                                                const char* __restrict__ xB, const char* __restrict__ kc, const char* __restrict__ vc,
                                                const char* __restrict__ wqB, const char* __restrict__ woB, char* ws, unsigned* out) {
  __shared__ __attribute__((aligned(16))) char smem[LDS_BYTES];
  LAS char* lds = (LAS char*)smem;
  LAS char* scr = lds + LDS_SCR;
  LAS unsigned* ctl = (LAS unsigned*)scr;
  const int tid = threadIdx.x, lane = tid & 63, w = __builtin_amdgcn_readfirstlane(tid >> 6);
  const int blk = blockIdx.x, head = blk >> 3, prod = (blk + head) & 7;
  const int sub = (0x54372106u >> (4 * prod)) & 7;
  unsigned stv = 0;
  STAMP(0);
  unsigned cpf = 0;
  {
    u64 pc;
    asm volatile("s_getpc_b64 %0" : "=s"(pc));
    cpf = __builtin_amdgcn_raw_buffer_load_b32(mkrs((const void*)(pc & ~255ull), CPF), w == 7 - (CPFW) ? head * 2048 + lane * 32 : OOB, 0, 0);
    SB();
  }
  if (tid < 96) ctl[tid] = tid == 4 ? DCH : tid < 8 ? 0u : tid < 8 + DCH ? 48 + 4 * (DCH * sub + tid - 8) : ~0u;
  asm volatile("" :: "s"(xA), "s"(ckv), "s"(wqA), "s"(wuk), "s"(wuv), "s"(woA), "s"(xB), "s"(kc), "s"(vc), "s"(wqB), "s"(woB), "s"(ws), "s"(out));
  const u64 e0 = *(const __attribute__((address_space(4))) u64*)(ws + WS_EPOCH);
  SB();
  const rsrc_t rws = mkrs(ws, WS_END);
  unsigned mask[4];
#pragma unroll
  for (int v = 0; v < 4; v++) mask[v] = (((lane & 7) >> 1) == v) ? ((lane & 1) ? 0xffff0000u : 0x0000ffffu) : 0u;
  const int l16 = lane & 15, grp = lane >> 4;
  const int b4 = head & 3, cntoff = WS_CNT + head * CSTR;

  if (sub >= 6) {
    if ((ABL & 1) && sub == 7) { FLUSH(); return; }
    __builtin_amdgcn_raw_buffer_store_b32(48 + 24 * DCH, rws, (sub == 6 && tid == 0) ? cntoff : OOB, 0, AUX_SC1);
    const char* x = sub == 6 ? xB : xA;
    const rsrc_t rw = mkrs(sub == 6 ? wqB : wqA, 4u << 20);
    if (sub == 7) {
      const rsrc_t rk = mkrs(wuk, 4u << 20);
#pragma unroll
      for (int i = 0; i < 16; i++) {
        unsigned keep;
        asm volatile("s_mov_b32 %0, m0\n\ts_mov_b32 m0, %1\n\ts_nop 0\n\tbuffer_load_dwordx4 %2, %3, %4 offen nt lds\n\ts_mov_b32 m0, %0"
                     : "=&s"(keep) : "s"((unsigned)(uintptr_t)lds + w * 16384 + i * 1024), "v"((64 * w + grp) * 8192 + head * 256 + ((l16 ^ (4 * (i & 3) + grp)) * 16)),
                       "s"(rk), "s"(i * 4 * 8192) : "memory");
      }
      SB();
    }
    u32x4 xp[2][2], wq[16];
#pragma unroll
    for (int s = 0; s < 2; s++) {
      const u32x4* xs = (const u32x4*)(x + (2 * s + (l16 >> 3)) * 1024 + (64 * w + 16 * grp) * 2);
      xp[s][0] = xs[0]; xp[s][1] = xs[1];
    }
    SB();
#pragma unroll
    for (int i = 0; i < 16; i++) wq[i] = ldnt(rw, (64 * w + 16 * grp) * 8192 + head * 256 + l16 * 16, i * 8192);
    SB();
    f32x4 acc[2] = {{0.f, 0.f, 0.f, 0.f}, {0.f, 0.f, 0.f, 0.f}};
    diag_gemv(wq, xp, mask, acc);
    LAS float* red = (LAS float*)(scr + 8192);
#pragma unroll
    for (int s = 0; s < 2; s++) *(LAS f32x4*)(red + w * 512 + (2 * s + (grp >> 1)) * 128 + 8 * l16 + 4 * (grp & 1)) = acc[s];
    asm volatile("" :: "v"(cpf));
    drain();
    bar();
    if (tid < 256) {
      float a = 0.f, b = 0.f;
#pragma unroll
      for (int k = 0; k < 8; k++) { const f32x2 v = *(LAS f32x2*)(red + k * 512 + 2 * tid); a += v[0]; b += v[1]; }
      const unsigned pk = cvtpk(a, b);
      const Tag tg = mktag(e0);
      const unsigned th16 = (unsigned)tg.e & 0xffff0000u;
      stsc(u32x4{(pk & 0xffffu) | th16, tg.th, (pk >> 16) | th16, tg.th}, rws, sub == 6 ? WS_QB + head * 4096 + tid * 16 : OOB, 0);
      ((LAS unsigned*)(scr + 1024))[tid] = pk;
    }
    STAMP(1);
  }

  if (sub == 6) {
    if (ABL & 1) { drain(); FLUSH(); return; }
    const int b = head >> 3;
    const rsrc_t rc = mkrs(ckv + (size_t)head * 524288, 524288);
    dma_rows<8>(rc, 0, lds, 8 * w, lane);
    dma_rows<8>(rc, 65536, lds + 65536, 8 * w, lane);
    const Tag tg = mktag(e0);
    if (w == 4) poll32(ws, WS_F1, lane, tg);
    bar();
    STAMP(2);
    {
      u32x4 t[4];
#pragma unroll
      for (int i = 0; i < 4; i++) t[i] = ldsc(rws, WS_QABS + b * 32768 + (8 * i + w) * 1024 + lane * 16, 0);
#pragma unroll
      for (int i = 0; i < 4; i++) *(LAS u32x4*)(scr + (8 * i + w) * 1024 + ((lane ^ swz(8 * i + w)) * 16)) = t[i];
    }
    bar();
    const int kb = w & 3, hb = w >> 2;
    u32x4 qf[16];
    {
      const int row = 16 * hb + l16;
#pragma unroll
      for (int ks = 0; ks < 16; ks++) qf[ks] = *(LAS u32x4*)(scr + row * 1024 + (((4 * ks + grp) ^ swz(row)) * 16));
    }
    bar();
    STAMP(3);
    LAS float* mx = (LAS float*)scr;
    LAS char* pl = scr + 1024;
    LAS float* lr = (LAS float*)(scr + 8192);
    f32x16 o0, o1;
#pragma unroll
    for (int j = 0; j < 16; j++) { o0[j] = 0.f; o1[j] = 0.f; }
    float m1 = -INFINITY, m2 = -INFINITY, l1 = 0.f;
    const int h1 = 16 * hb + l16, h2 = lane & 31, hh = lane >> 5;
    const int arow = 16 * kb + l16;
    const int tq = (lane & 15) >> 2, tp = lane & 3, tblk = (lane >> 4) & 1;
#pragma unroll 1
    for (int t = 0; t < 8; t++) {
      if (t < 7) asm volatile("s_waitcnt vmcnt(8)" ::: "memory"); else asm volatile("s_waitcnt vmcnt(0)" ::: "memory");
      bar();
      STAMP(6 + t);
      LAS char* img = lds + (t & 1) * 65536;
      f32x4 s = {0.f, 0.f, 0.f, 0.f};
#pragma unroll
      for (int ks = 0; ks < 16; ks++) {
        const u32x4 a = *(LAS u32x4*)(img + arow * 1024 + (((4 * ks + grp) ^ swz(arow)) * 16));
        s = mfma16(a, qf[ks], s);
      }
      float x[4];
#pragma unroll
      for (int j = 0; j < 4; j++) x[j] = rbf(rbf(s[j]) * SCALE);
      const float lm = xrow_max(fmaxf(fmaxf(x[0], x[1]), fmaxf(x[2], x[3])));
      if (grp == 0) mx[h1 * 4 + kb] = lm;
      bar();
      const f32x4 ma = *(LAS f32x4*)(mx + h1 * 4), mb = *(LAS f32x4*)(mx + h2 * 4);
      const float n1 = fmaxf(m1, fmaxf(fmaxf(ma[0], ma[1]), fmaxf(ma[2], ma[3])));
      const float n2 = fmaxf(m2, fmaxf(fmaxf(mb[0], mb[1]), fmaxf(mb[2], mb[3])));
      const float al1 = fexp2((m1 - n1) * LOG2E), al2 = fexp2((m2 - n2) * LOG2E);
      m1 = n1; m2 = n2;
      const u32x2 pp = {cvtpk(fexp2((x[0] - n1) * LOG2E), fexp2((x[1] - n1) * LOG2E)), cvtpk(fexp2((x[2] - n1) * LOG2E), fexp2((x[3] - n1) * LOG2E))};
      l1 = fmaf(l1, al1, (bf_lo(pp[0]) + bf_hi(pp[0])) + (bf_lo(pp[1]) + bf_hi(pp[1])));
      *(LAS u32x2*)(pl + h1 * 144 + (16 * kb + 4 * grp) * 2) = pp;
#pragma unroll
      for (int j = 0; j < 16; j++) { o0[j] *= al2; o1[j] *= al2; }
      bar();
#pragma unroll
      for (int ks = 0; ks < 4; ks++) {
        const u32x4 pb = *(LAS u32x4*)(pl + h2 * 144 + (16 * ks + 8 * hh) * 2);
        const int r0 = 16 * ks + 8 * hh + tq, r1 = r0 + 4;
        const int ch0 = 8 * w + 2 * tblk + (tp >> 1), ch1 = ch0 + 4;
        const u32x2 a0 = trread(img + r0 * 1024 + ((ch0 ^ swz(r0)) * 16) + 8 * (tp & 1));
        const u32x2 a1 = trread(img + r1 * 1024 + ((ch0 ^ swz(r1)) * 16) + 8 * (tp & 1));
        const u32x2 b0 = trread(img + r0 * 1024 + ((ch1 ^ swz(r0)) * 16) + 8 * (tp & 1));
        const u32x2 b1 = trread(img + r1 * 1024 + ((ch1 ^ swz(r1)) * 16) + 8 * (tp & 1));
        o0 = mfma32(u32x4{a0[0], a0[1], a1[0], a1[1]}, pb, o0);
        o1 = mfma32(u32x4{b0[0], b0[1], b1[0], b1[1]}, pb, o1);
      }
      bar();
      if (t < 6) dma_rows<8>(rc, (t + 2) * 65536, img, 8 * w, lane);
    }
    STAMP(4);
    {
      float am = 0.f;
#pragma unroll
      for (int j = 0; j < 16; j++) am = fmaxf(am, fmaxf(fabsf(o0[j]), fabsf(o1[j])));
      auto sw = __builtin_amdgcn_permlane32_swap(__float_as_uint(am), __float_as_uint(am), false, false);
      am = fmaxf(__uint_as_float(sw[0]), __uint_as_float(sw[1]));
      const int e = max(__builtin_amdgcn_frexp_expf(am), -127);
      LAS char* tp = lds + h2 * 1040 + (64 * w + 4 * hh) * 2;
      auto pkn = [&](float a, float b) { return __builtin_bit_cast(unsigned, __builtin_amdgcn_cvt_pknorm_i16(__builtin_ldexpf(a, -e), __builtin_ldexpf(b, -e))); };
#pragma unroll
      for (int j4 = 0; j4 < 4; j4++) {
        *(LAS u32x2*)(tp + j4 * 16) = u32x2{pkn(o0[4 * j4], o0[4 * j4 + 1]), pkn(o0[4 * j4 + 2], o0[4 * j4 + 3])};
        *(LAS u32x2*)(tp + 64 + j4 * 16) = u32x2{pkn(o1[4 * j4], o1[4 * j4 + 1]), pkn(o1[4 * j4 + 2], o1[4 * j4 + 3])};
      }
      if (hh == 0) *(LAS unsigned char*)(scr + 9216 + h2 * 8 + w) = (unsigned char)(e + 127);
      const float ls = xrow_sum(l1);
      if (grp == 0) lr[kb * 32 + h1] = ls;
      bar();
#pragma unroll
      for (int i = 0; i < 4; i++) {
        const u32x4 v = *(LAS u32x4*)(lds + (4 * w + i) * 1040 + lane * 16);
        stsc(v, rws, WS_PO + (head * 32 + 4 * w) * 1024 + lane * 16, i * 1024);
      }
      if (tid < 32) {
        const u32x2 eb = *(LAS u32x2*)(scr + 9216 + tid * 8);
        const float l = (lr[tid] + lr[32 + tid]) + (lr[64 + tid] + lr[96 + tid]);
        stsc(u32x4{__float_as_uint(m2), __float_as_uint(l), eb[0], eb[1]}, rws, WS_PML + (head * 32 + tid) * 16, 0);
      }
      drain();
      bar();
      if (tid == 0) __hip_atomic_store((GAS u64*)(ws + WS_F2 + head * 128), tg.e, RLX_AGENT);
      if (w == 0) {
        const unsigned t = __builtin_amdgcn_readfirstlane(__builtin_amdgcn_raw_ptr_buffer_atomic_add_i32(8 + 4 * DCH, rws, lane == 0 ? cntoff : OOB, 0, AUX_SC1));
        const unsigned bs = t + 8 + 4 * (lane - 8);
        ctl[lane] = lane == 3 ? t : lane == 4 ? DCH : lane == 5 ? (t + 8 + 4 * DCH >= NUNIT ? 0x10000u : 0u) : lane < 8 ? 0u : (lane < 8 + DCH && bs < NUNIT) ? bs : ~0u;
        if (lane < 32) ctl[64 + lane] = ~0u;
      }
      bar();
    }
    STAMP(5);
  }
  if (sub < 6 + HELP) {
    const size_t base4 = (size_t)(b4 * 32 + head) << 20;
    const rsrc_t rk4 = mkrs(kc + base4, 1u << 20), rv4 = mkrs(vc + base4, 1u << 20);
    const int voff = lane * 16;
    LAS char* qst = scr + 1024 + w * 512;
    u32x4 ra[16];
    Acc A;
    unsigned cur;
    auto qwait = [&](u32x4& g0, u32x4& g1, int o0, int o1, const Tag& tg) {
      if (!__all(qok(g0, tg) & qok(g1, tg))) {
        for (unsigned spins = 0;; spins++) {
          asm volatile("" ::: "memory");
          g0 = ldsc(rws, o0, 0); g1 = ldsc(rws, o1, 0);
          if (__all(qok(g0, tg) & qok(g1, tg)) || spins > SPIN_CAP) break;
          __builtin_amdgcn_s_sleep(2);
        }
      }
      *(LAS unsigned*)(qst + lane * 4) = __builtin_amdgcn_perm(g0[2], g0[0], 0x05040100u);
      *(LAS unsigned*)(qst + 256 + lane * 4) = __builtin_amdgcn_perm(g1[2], g1[0], 0x05040100u);
    };
    const int qo4 = WS_QB + (head * 4 + b4) * 1024 + voff;
    if (sub < 6) {
      const int jm = sub >= 3 ? sub - 3 : sub, bm = (b4 + 1 + jm) & 3;
      const size_t base = (size_t)(bm * 32 + head) << 20;
      const rsrc_t rk = mkrs(kc + base, 1u << 20), rv = mkrs(vc + base, 1u << 20);
      const int so = ((sub >= 3 ? 8 : 0) + w) * 4096, qom = WS_QB + (head * 4 + bm) * 1024 + voff;
      u32x4 rb[16], g0, g1;
      if (DELAY0) __builtin_amdgcn_s_sleep(DELAY0);
#pragma unroll
      for (int i = 0; i < 32; i++) {
        if (i == WF0) bar();
        if (i >= WF0 && i < NF0) vmwait<WF0 - 1>();
        if (i >= WFILL && i >= NF0) vmwait<WFILL - 1>();
        if (i == 32 - WFILL) { g0 = ldsc(rws, qom, 0); g1 = ldsc(rws, qo4, 0); }
        const u32x4 v = ldnt(rk, voff, so + slot_off(i));
        if (i < 16) ra[i] = v; else rb[i - 16] = v;
        SB();
      }
      STAMP(12);
      asm volatile("" :: "v"(cpf));
      const Tag tg = mktag(e0);
      qwait(g0, g1, qom, qo4, tg);
      const u32x4 q = *(LAS u32x4*)(qst + l16 * 16);
      STAMP(6);
      float sc[4] = {0.f, 0.f, 0.f, 0.f};
#pragma unroll 1
      for (int it = 0; it < 2; it++) {
        const rsrc_t rn = it ? rv : rk;
        sc[2] = sc[0]; sc[3] = sc[1];
        sc[1] = k_step(ra, q, l16, rn, voff, so + (it ? 3 : 2) * slot_off(16));
        sc[0] = k_step(rb, q, l16, rn, voff, so + (it ? 2 : 3) * slot_off(16));
      }
      STAMP(3);
      const float mx = xrow_max(row_max(fmaxf(fmaxf(sc[0], sc[1]), fmaxf(sc[2], sc[3]))));
      float f, l = 0.f;
      const float M = exp_ref(mx, f);
      unsigned pk[4];
#pragma unroll
      for (int k = 0; k < 4; k++) {
        const float p = fexp2(fmaf(sc[k] - mx, LOG2E, f));
        pk[k] = cvtpk(p, p);
        l += bf_lo(pk[k]);
      }
      cur = 8 * sub + w;
      f32x4 acc = {0.f, 0.f, 0.f, 0.f};
      v_step<0, 1>(ra, pk[0], mask, acc, rk, rv, voff, so + slot_off(16));
      v_step<0, 1>(rb, pk[1], mask, acc, rk, rv, voff, so);
      v_step<0, 2>(ra, pk[2], mask, acc, rk4, rv4, voff, cur * 4096);
      v_step<0, 0>(rb, pk[3], mask, acc, rk, rv, voff, 0);
#pragma unroll
      for (int j = 0; j < 4; j++) A.o[j] = acc[j];
      A.l = l; A.R = M;
      b_merge<false>(scr + 8192, ctl + 1, w, lane, A, rws, WS_RS + (head * 8 + sub) * 1024, WS_HS + (head * 8 + sub) * 32, tg);
    } else {
      u32x4 g0 = ldsc(rws, qo4, 0), g1 = g0;
      cur = __builtin_amdgcn_readfirstlane(ctl[3]) + w;
#pragma unroll
      for (int i = 0; i < 8; i++) ra[i] = ldnt(i < 4 ? rk4 : rv4, voff, unit_off(cur) + (i & 3) * 1024);
      SB();
      qwait(g0, g1, qo4, qo4, mktag(e0));
    }
    STAMP(7);
    const Tag tg = mktag(e0);
    const u32x4 q4 = *(LAS u32x4*)(qst + 256 + l16 * 16);
    A.o[0] = A.o[1] = A.o[2] = A.o[3] = A.l = 0.0; A.R = -INFINITY;
    b_units(ra, rk4, rv4, cur, q4, lane, mask, ctl, rws, cntoff, A);
    STAMP(8);
    b_merge<true>(scr + 8192 + 8704, ctl + 2, w, lane, A, rws, WS_RD + (head * 8 + sub) * 2048, WS_HD + (head * 8 + sub) * 32, tg);
    STAMP(9);
  } else if (sub == 7) {
    const Tag tg = mktag(e0);
    bar();
    STAMP(2);
    {
      u32x4 bf[4];
#pragma unroll
      for (int ks = 0; ks < 4; ks++) bf[ks] = *(LAS u32x4*)(scr + 1024 + (l16 & 3) * 256 + ks * 64 + grp * 16);
      LAS char* tt = scr + 2048 + w * 512;
#pragma unroll
      for (int rt = 0; rt < 4; rt++) {
        f32x4 acc = {0.f, 0.f, 0.f, 0.f};
#pragma unroll
        for (int ks = 0; ks < 4; ks++) acc = mfma16(*(LAS u32x4*)(lds + (64 * w + 16 * rt + l16) * 256 + (((4 * ks + grp) ^ l16) * 16)), bf[ks], acc);
        if (l16 < 4) *(LAS u32x2*)(tt + l16 * 128 + (16 * rt + 4 * grp) * 2) = u32x2{cvtpk(acc[0], acc[1]), cvtpk(acc[2], acc[3])};
      }
      asm volatile("s_waitcnt lgkmcnt(0)" ::: "memory");
      const u32x4 v = *(LAS u32x4*)(tt + (lane & 31) * 16);
      stsc(v, rws, lane < 32 ? WS_QABS + ((lane >> 3) * 32 + head) * 1024 + w * 128 + (lane & 7) * 16 : OOB, 0);
    }
    drain();
    bar();
    if (tid == 0) __hip_atomic_store((GAS u64*)(ws + WS_F1 + head * 128), tg.e, RLX_AGENT);
    STAMP(3);
    u32x4 wf[4][4], wr[16];
    {
      const rsrc_t ro = mkrs(woA + (size_t)head * 131072, 131072);
      dma_rows<16>(ro, 0, lds, 16 * w, lane);
      const rsrc_t rv = mkrs(wuv, 4u << 20);
      const int voff = (64 * w + 16 * grp) * 8192 + head * 256 + l16 * 16;
#pragma unroll
      for (int i = 0; i < 16; i++) wr[i] = ldnt(rv, voff, i * 8192);
      SB();
      asm volatile("s_waitcnt vmcnt(16)" ::: "memory");
      bar();
      wo_frags(lds, w, lane, wf);
      bar();
      const rsrc_t rb = mkrs(woB + (size_t)head * 131072, 131072);
      dma_rows<16>(rb, 0, lds, 16 * w, lane);
    }
    STAMP(4);
    if (w == 0) poll32(ws, WS_F2, lane, tg);
    bar();
    STAMP(5);
    {
      const int b = tid >> 7, c4 = tid & 127, es = (c4 >> 4) & 3;
      u32x2 po[8]; u32x4 ml[8];
#pragma unroll
      for (int s = 0; s < 8; s++) {
        ml[s] = ldsc(rws, WS_PML + ((8 * b + s) * 32 + head) * 16, 0);
        po[s] = __builtin_amdgcn_raw_buffer_load_b64(rws, WS_PO + ((8 * b + s) * 32 + head) * 1024 + c4 * 8, 0, AUX_SC1);
      }
      float M = -INFINITY;
#pragma unroll
      for (int s = 0; s < 8; s++) M = fmaxf(M, __uint_as_float(ml[s][0]));
      float L = 0.f;
      f32x4 a = {0.f, 0.f, 0.f, 0.f};
#pragma unroll
      for (int s = 0; s < 8; s++) {
        const float f = fexp2((__uint_as_float(ml[s][0]) - M) * LOG2E);
        L = fmaf(f, __uint_as_float(ml[s][1]), L);
        const int e = (int)(((w & 1) ? ml[s][3] : ml[s][2]) >> (8 * es)) & 255;
        const float g = __builtin_ldexpf(f, e - 142);
        a += g * f32x4{(float)(short)po[s][0], (float)((int)po[s][0] >> 16), (float)(short)po[s][1], (float)((int)po[s][1] >> 16)};
      }
      const float inv = (32768.f / 32767.f) / L;
      const u32x2 v = {cvtpk(a[0] * inv, a[1] * inv), cvtpk(a[2] * inv, a[3] * inv)};
      *(LAS u32x2*)(scr + b * 1024 + c4 * 8) = v;
    }
    bar();
    {
      u32x4 xp[2][2];
#pragma unroll
      for (int s = 0; s < 2; s++) {
        LAS u32x4* xs = (LAS u32x4*)(scr + (2 * s + (l16 >> 3)) * 1024 + (64 * w + 16 * grp) * 2);
        xp[s][0] = xs[0]; xp[s][1] = xs[1];
      }
      f32x4 acc[2] = {{0.f, 0.f, 0.f, 0.f}, {0.f, 0.f, 0.f, 0.f}};
      diag_gemv(wr, xp, mask, acc);
      LAS float* red = (LAS float*)(scr + 8192);
#pragma unroll
      for (int s = 0; s < 2; s++) *(LAS f32x4*)(red + w * 512 + (2 * s + (grp >> 1)) * 128 + 8 * l16 + 4 * (grp & 1)) = acc[s];
      bar();
      if (tid < 256) {
        float a = 0.f, b = 0.f;
#pragma unroll
        for (int k = 0; k < 8; k++) { const f32x2 v = *(LAS f32x2*)(red + k * 512 + 2 * tid); a += v[0]; b += v[1]; }
        ((LAS unsigned*)(scr + 4096))[tid] = cvtpk(a, b);
      }
      bar();
    }
    const f32x4 yA = wo_calc(wf, scr + 4096, lane);
    STAMP(6);
    drain();
    bar();
    wo_frags(lds, w, lane, wf);
    if (w < 3) {
      const int hoff = WS_HS + (head * 8 + w + (lane & 2 ? 3 : 0)) * 32 + (lane & 1) * 16;
      const int off = WS_RS + (head * 8 + w) * 1024 + lane * 16;
      u32x4 hd, g0, g1;
      for (unsigned spins = 0;; spins++) {
        asm volatile("" ::: "memory");
        hd = ldsc(rws, hoff, 0); g0 = ldsc(rws, off, 0); g1 = ldsc(rws, off, 3072);
        if (__all(hok(hd, tg) & gok(g0, tg) & gok(g1, tg)) || spins > SPIN_CAP) break;
        __builtin_amdgcn_s_sleep(16);
      }
      const float R0 = rdl(hd[0], 0), R1 = rdl(hd[0], 2), R = fmaxf(R0, R1);
      const int d0 = edif(R0, R), d1 = edif(R1, R);
      const float inv = 1.f / (__builtin_ldexpf(rdl(hd[0], 1), d0) + __builtin_ldexpf(rdl(hd[0], 3), d1));
      const float c0 = (__builtin_ldexpf(gval(g0[0]), d0) + __builtin_ldexpf(gval(g1[0]), d1)) * inv;
      const float c1 = (__builtin_ldexpf(gval(g0[2]), d0) + __builtin_ldexpf(gval(g1[2]), d1)) * inv;
      ((LAS unsigned*)(scr + 5120))[((b4 + 1 + w) & 3) * 64 + lane] = cvtpk(c0, c1);
      STAMP(7);
    }
    const int hoff = WS_HD + head * 256 + (l16 < 2 * NSLOT ? l16 : 0) * 16;
    if (w == 4) {
      for (unsigned spins = 0;; spins++) {
        asm volatile("" ::: "memory");
        if (__any(hok(ldsc(rws, hoff, 0), tg)) || spins > SPIN_CAP) break;
        __builtin_amdgcn_s_sleep(4);
      }
    }
    bar();
    STAMP(8);
    y_store(yA, w, lane, head, rws, WS_Y, tg);
    if ((w >> 1) == 2) {
      const int d = tid & 127;
      const int off = WS_RD + head * 16384 + d * 16;
      u32x4 hd, g[NSLOT];
      for (unsigned spins = 0;; spins++) {
        asm volatile("" ::: "memory");
        hd = ldsc(rws, hoff, 0);
#pragma unroll
        for (int s = 0; s < NSLOT; s++) g[s] = ldsc(rws, off, s * 2048);
        bool ok = hok(hd, tg);
#pragma unroll
        for (int s = 0; s < NSLOT; s++) ok &= hok(g[s], tg);
        if (__all(ok) || spins > SPIN_CAP) break;
      }
      float R = -INFINITY;
#pragma unroll
      for (int s = 0; s < NSLOT; s++) R = fmaxf(R, rdl(hd[0], 2 * s));
      double S = 0.0, L = 0.0;
#pragma unroll
      for (int s = 0; s < NSLOT; s++) {
        const int e = edif(rdl(hd[0], 2 * s), R);
        L += __builtin_ldexp((double)rdl(hd[0], 2 * s + 1) + (double)rdl(hd[2], 2 * s + 1), e);
        S += __builtin_ldexp(hval(g[s]), e);
      }
      const float cx = (float)(S / L);
      ((LAS unsigned short*)(scr + 5120))[b4 * 128 + d] = (unsigned short)cvtpk(cx, cx);
    }
    bar();
    STAMP(9);
    y_store(wo_calc(wf, scr + 5120, lane), w, lane, head, rws, WS_Y + 524288, tg);
    {
      const int off = WS_Y + head * 16384 + (tid >> 4) * 512 + (tid & 15) * 16;
      u32x4 v[4];
      for (unsigned spins = 0;; spins++) {
        asm volatile("" ::: "memory");
#pragma unroll
        for (int i = 0; i < 4; i++) v[i] = ldsc(rws, off + (i & 1) * 256, (i >> 1) * 524288);
        if (__all(gok(v[0], tg) & gok(v[1], tg) & gok(v[2], tg) & gok(v[3], tg)) || spins > SPIN_CAP) break;
      }
      LAS float* red = (LAS float*)(scr + 8192);
#pragma unroll
      for (int p = 0; p < 2; p++) {
        const f32x4 s = {xrow_sum(gval(v[2 * p][0])), xrow_sum(gval(v[2 * p][2])), xrow_sum(gval(v[2 * p + 1][0])), xrow_sum(gval(v[2 * p + 1][2]))};
        if (lane < 16) *(LAS f32x4*)(red + p * 512 + w * 64 + lane * 4) = s;
      }
      bar();
      if (tid < 64) {
        float a = 0.f, b = 0.f;
#pragma unroll
        for (int j = 0; j < 8; j++) { const f32x2 t = *(LAS f32x2*)(red + (tid >> 5) * 512 + j * 64 + (tid & 31) * 2); a += t[0]; b += t[1]; }
        out[(head >> 3) * 512 + (tid >> 5) * 256 + (head & 7) * 32 + (tid & 31)] = cvtpk(a, b);
      }
    }
    if (head == 0 && tid == 0) __hip_atomic_store((GAS u64*)(ws + WS_EPOCH), tg.e, RLX_AGENT);
    STAMP(10);
  }
  FLUSH();
}

extern "C" void kernel_launch(void* const* d_in, const int* in_sizes, int n_in, void* d_out, int out_size, void* d_ws, size_t ws_size,
                              hipStream_t stream) {
  k_all<<<256, 512, 0, stream>>>((const char*)d_in[0], (const char*)d_in[1], (const char*)d_in[2], (const char*)d_in[3], (const char*)d_in[4],
                                 (const char*)d_in[5], (const char*)d_in[6], (const char*)d_in[7], (const char*)d_in[8], (const char*)d_in[9],
                                 (const char*)d_in[10], (char*)d_ws, (unsigned*)d_out);
}
```

```cpp
#include <hip/hip_runtime.h>
#include <hip/hip_bf16.h>
#include <stdint.h>

typedef __attribute__((ext_vector_type(4))) unsigned u32x4;
typedef __attribute__((ext_vector_type(2))) unsigned u32x2;
typedef __attribute__((ext_vector_type(8))) unsigned u32x8;
typedef __attribute__((ext_vector_type(16))) unsigned u32x16;
typedef __attribute__((ext_vector_type(4))) float f32x4;
typedef __attribute__((ext_vector_type(2))) float f32x2;
typedef __attribute__((ext_vector_type(2))) double f64x2;
typedef __attribute__((ext_vector_type(16))) float f32x16;
typedef __attribute__((ext_vector_type(8))) __bf16 bf16x8;
typedef __attribute__((ext_vector_type(2))) __bf16 bf16x2;
typedef __attribute__((ext_vector_type(4))) short s16x4;
typedef unsigned long long u64;
typedef __amdgpu_buffer_rsrc_t rsrc_t;
#define LAS __attribute__((address_space(3)))
#define GAS __attribute__((address_space(1)))
#define RLX_AGENT __ATOMIC_RELAXED, __HIP_MEMORY_SCOPE_AGENT

#define STAMPS 0
#define WTH 6
#define WFILL 6
#define HELP 1
#define DCH 3
#define CPF 31232
#define CPFW 0
#define WF0 WFILL
#define NF0 8
#define PSL 16
#define QSL 4
#define QS0 90
#define PCAP 16
#define NP2 3
#define PGAP2 8
#define FSL 24
#define EARLY 1
#define FPH0 0
#define H1DMA 0
#define AALL 1
#define ABL 0
constexpr int AUX_NT = 2, AUX_SC1 = 16, OOB = 0x40000000;
constexpr float SCALE = 0.08838834764831845f;
constexpr float LOG2E = 1.4426950408889634f;
constexpr unsigned SPIN_CAP = 1u << 17;
constexpr int NSLOT = 6 + HELP, NUNIT = 256;

constexpr int WS_EPOCH = 0;
constexpr int WS_F2   = 4096;
constexpr int CSTR    = 4352;
constexpr int WS_CNT  = WS_F2 + 4096;
constexpr int WS_QB   = WS_CNT + 32 * CSTR;
constexpr int WS_QABS = WS_QB + 32 * 2048;
constexpr int WS_PML  = WS_QABS + 4 * 32 * 2048;
constexpr int WS_PO   = WS_PML + 32 * 32 * 16;
constexpr int WS_RS   = WS_PO + 32 * 32 * 1024;
constexpr int WS_HS   = WS_RS + 32 * 8 * 1024;
constexpr int WS_RD   = WS_HS + 32 * 256;
constexpr int WS_HD   = WS_RD + 32 * 8 * 2048;
constexpr int WS_Y    = WS_HD + 32 * 256;
constexpr int WS_END  = WS_Y + 2 * 4 * 8 * 32 * 512;
constexpr int WS_STAMP = 32 << 20;

constexpr int LDS_BYTES = 163840;
constexpr int LDS_SCR = 131072;

__device__ __forceinline__ unsigned cvtpk(float lo, float hi) {
  f32x2 v = {lo, hi};
  return __builtin_bit_cast(unsigned, __builtin_convertvector(v, bf16x2));
}
__device__ __forceinline__ float bf_lo(unsigned u) { return __uint_as_float(u << 16); }
__device__ __forceinline__ float bf_hi(unsigned u) { return __uint_as_float(u & 0xffff0000u); }
__device__ __forceinline__ float rbf(float x) { return __uint_as_float(cvtpk(x, x) << 16); }
__device__ __forceinline__ float dot2(unsigned k, unsigned q, float acc) {
  return __builtin_amdgcn_fdot2_f32_bf16(__builtin_bit_cast(bf16x2, k), __builtin_bit_cast(bf16x2, q), acc, false);
}
template <int CTRL> __device__ __forceinline__ float dpp(float x) {
  return __builtin_bit_cast(float, __builtin_amdgcn_mov_dpp(__builtin_bit_cast(int, x), CTRL, 0xf, 0xf, true));
}
template <int CTRL> __device__ __forceinline__ unsigned dppu(unsigned x) {
  return (unsigned)__builtin_amdgcn_mov_dpp((int)x, CTRL, 0xf, 0xf, true);
}
constexpr int XOR1 = 0xB1, XOR2 = 0x4E, XOR7 = 0x141, XOR8 = 0x128, ROR4 = 0x124;
__device__ __forceinline__ float xrow_max(float x) {
  auto s = __builtin_amdgcn_permlane16_swap(__float_as_uint(x), __float_as_uint(x), false, false);
  x = fmaxf(__uint_as_float(s[0]), __uint_as_float(s[1]));
  auto t = __builtin_amdgcn_permlane32_swap(__float_as_uint(x), __float_as_uint(x), false, false);
  return fmaxf(__uint_as_float(t[0]), __uint_as_float(t[1]));
}
__device__ __forceinline__ float xrow_sum(float x) {
  auto s = __builtin_amdgcn_permlane16_swap(__float_as_uint(x), __float_as_uint(x), false, false);
  x = __uint_as_float(s[0]) + __uint_as_float(s[1]);
  auto t = __builtin_amdgcn_permlane32_swap(__float_as_uint(x), __float_as_uint(x), false, false);
  return __uint_as_float(t[0]) + __uint_as_float(t[1]);
}
__device__ __forceinline__ float row_max(float x) {
  x = fmaxf(x, dpp<XOR8>(x)); x = fmaxf(x, dpp<ROR4>(x)); x = fmaxf(x, dpp<XOR2>(x)); x = fmaxf(x, dpp<XOR1>(x));
  return x;
}
__device__ __forceinline__ float row_sum(float x) {
  x += dpp<XOR8>(x); x += dpp<ROR4>(x); x += dpp<XOR2>(x); x += dpp<XOR1>(x);
  return x;
}
__device__ __forceinline__ float fexp2(float x) { return __builtin_amdgcn_exp2f(x); }
__device__ __forceinline__ f32x4 mfma16(u32x4 a, u32x4 b, f32x4 c) {
  return __builtin_amdgcn_mfma_f32_16x16x32_bf16(__builtin_bit_cast(bf16x8, a), __builtin_bit_cast(bf16x8, b), c, 0, 0, 0);
}
__device__ __forceinline__ f32x16 mfma32(u32x4 a, u32x4 b, f32x16 c) {
  return __builtin_amdgcn_mfma_f32_32x32x16_bf16(__builtin_bit_cast(bf16x8, a), __builtin_bit_cast(bf16x8, b), c, 0, 0, 0);
}
__device__ __forceinline__ rsrc_t mkrs(const GAS char* p, unsigned bytes) {
  return __builtin_amdgcn_make_buffer_rsrc((void*)p, 0, bytes, 0x00020000);
}
__device__ __forceinline__ void bar() { asm volatile("s_waitcnt lgkmcnt(0)\n\ts_barrier" ::: "memory"); }
__device__ __forceinline__ void drain() { asm volatile("s_waitcnt vmcnt(0)" ::: "memory"); }
#define SB() __builtin_amdgcn_sched_barrier(0)
template <int N> __device__ __forceinline__ void vmwait() { asm volatile("s_waitcnt vmcnt(%0)" :: "n"(N) : "memory"); }
__device__ __forceinline__ unsigned swz(unsigned row) { return ((row & 3) << 2) | ((row >> 2) & 2); }
__device__ __forceinline__ u32x2 trread(LAS char* p) {
  return __builtin_bit_cast(u32x2, __builtin_amdgcn_ds_read_tr16_b64_v4i16((LAS s16x4*)p));
}

struct Tag { unsigned tl, th; u64 e; };
__device__ __forceinline__ Tag mktag(u64 e0) {
  Tag t;
  asm volatile("" : "+s"(e0));
  t.e = e0 * 6364136223846793005ull + 1442695040888963407ull;
  t.th = (unsigned)(t.e >> 32);
  t.tl = (unsigned)t.e & 0xff000000u;
  return t;
}
__device__ __forceinline__ unsigned gpack(float v, const Tag& t) { return ((__float_as_uint(v) + 0x80u) >> 8) | t.tl; }
__device__ __forceinline__ u32x4 gpair(float a, float b, const Tag& t) { return u32x4{gpack(a, t), t.th, gpack(b, t), t.th}; }
__device__ __forceinline__ bool gok(u32x4 g, const Tag& t) {
  return ((((g[0] ^ t.tl) | (g[2] ^ t.tl)) >> 24) | (g[1] ^ t.th) | (g[3] ^ t.th)) == 0u;
}
__device__ __forceinline__ float gval(unsigned lo) { return __uint_as_float(lo << 8); }
__device__ __forceinline__ bool qok(u32x4 g, const Tag& t) { return ((g[1] ^ t.th) | (g[3] ^ t.th)) == 0u; }
__device__ __forceinline__ u32x4 hilo(double v, const Tag& t) {
  const u32x2 u = __builtin_bit_cast(u32x2, v);
  return u32x4{u[0], t.th, u[1], (unsigned)t.e};
}
__device__ __forceinline__ bool hok(u32x4 g, const Tag& t) { return ((g[1] ^ t.th) | (g[3] ^ (unsigned)t.e)) == 0u; }
__device__ __forceinline__ double mkd(unsigned lo, unsigned hi) { return __builtin_bit_cast(double, u32x2{lo, hi}); }
__device__ __forceinline__ double hval(u32x4 g) { return mkd(g[0], g[2]); }
__device__ __forceinline__ int edif(float a, float b) {
  int r;
  asm("v_cvt_i32_f32 %0, %1" : "=v"(r) : "v"(a - b));
  return r;
}
__device__ __forceinline__ unsigned p2hi(float a, float b) { return (unsigned)max(edif(a, b) + 1023, 0) << 20; }
__device__ __forceinline__ unsigned rdu(unsigned v, int l) { return __builtin_amdgcn_readlane(v, l); }
__device__ __forceinline__ float rdl(unsigned v, int l) { return __uint_as_float(__builtin_amdgcn_readlane(v, l)); }

#define STAMP(i) do {} while (0)
#define FLUSH() do {} while (0)

__device__ __forceinline__ void diag_gemv(const u32x4 (&wr)[16], const u32x4 (&xp)[2][2], const unsigned (&mask)[4], f32x4 (&acc)[2]) {
#pragma unroll
  for (int i = 0; i < 16; i++) {
#pragma unroll
    for (int s = 0; s < 2; s++) {
      const unsigned src = xp[s][i >> 3][(i >> 1) & 3];
      const unsigned dup = __builtin_amdgcn_perm(src, src, (i & 1) ? 0x03020302u : 0x01000100u);
      const u32x4 a = {dup & mask[0], dup & mask[1], dup & mask[2], dup & mask[3]};
      acc[s] = mfma16(a, wr[i], acc[s]);
    }
  }
}

__device__ __forceinline__ void poll32(GAS char* ws, int off, int lane, const Tag& tg) {
  GAS u64* f = (GAS u64*)(ws + off + (lane & 31) * 128);
  for (unsigned spins = 0;; spins++) {
    const u64 v = __hip_atomic_load(f, RLX_AGENT);
    if (__all(v == tg.e) || spins > SPIN_CAP) break;
    __builtin_amdgcn_s_sleep(PSL);
  }
  __builtin_amdgcn_fence(__ATOMIC_ACQUIRE, "wavefront");
}

constexpr int CH = 4 * 65536;
struct Acc { double o[2]; double l; float R; };
__device__ __forceinline__ u32x4 ldnt(rsrc_t r, int voff, int so) { return __builtin_amdgcn_raw_buffer_load_b128(r, voff, so, AUX_NT); }
__device__ __forceinline__ u32x4 ldslot(rsrc_t r, int voff, int so, int i) { return ldnt(r, voff + (i & 3) * 1024, so + (i >> 2) * 65536); }
__device__ __forceinline__ u32x4 ldsc(rsrc_t r, int voff, int so) { return __builtin_amdgcn_raw_buffer_load_b128(r, voff, so, AUX_SC1); }
__device__ __forceinline__ void stsc(u32x4 v, rsrc_t r, int voff, int so) { __builtin_amdgcn_raw_buffer_store_b128(v, r, voff, so, AUX_SC1); }
__device__ __forceinline__ float dot8(u32x4 k, u32x4 q) { return dot2(k[3], q[3], dot2(k[2], q[2], dot2(k[1], q[1], dot2(k[0], q[0], 0.f)))); }
#define TR_STEP(HALF, CTRL, BIT) _Pragma("unroll") for (int h = 0; h < (HALF); ++h) { \
    const float t1 = x[h] + dpp<CTRL>(x[h]), t2 = x[h + (HALF)] + dpp<CTRL>(x[h + (HALF)]); x[h] = (c & (BIT)) ? t2 : t1; }
__device__ __forceinline__ float k_step(u32x4 (&ring)[16], u32x4 q, int c, rsrc_t rn, int voff, int so) {
  float x[16];
#pragma unroll
  for (int i = 0; i < 16; i++) {
    x[i] = dot8(ring[i], q);
    asm volatile("s_waitcnt vmcnt(%1)" : "+v"(x[i]) : "n"(WTH - 1));
    ring[i] = ldslot(rn, voff, so, i);
    SB();
  }
  TR_STEP(8, XOR8, 8) TR_STEP(4, XOR7, 4) TR_STEP(2, XOR2, 2) TR_STEP(1, XOR1, 1)
  return rbf(rbf(x[0]) * SCALE);
}
template <int BC> __device__ __forceinline__ void pv(u32x4 v, unsigned pk, const unsigned (&mask)[4], f32x4& acc) {
  const unsigned d = dppu<0x150 + BC>(pk);
  acc = mfma16(u32x4{d & mask[0], d & mask[1], d & mask[2], d & mask[3]}, v, acc);
}
template <int I, int MODE> __device__ __forceinline__ void v_step(u32x4 (&ring)[16], unsigned pk, const unsigned (&mask)[4], f32x4& acc,
                                                                  rsrc_t rk, rsrc_t rv, int voff, int so) {
  pv<I>(ring[I], pk, mask, acc);
  if (MODE == 1 || (MODE == 2 && I < 8)) {
    asm volatile("s_waitcnt vmcnt(%1)" : "+v"(acc) : "n"(WTH - 1));
    ring[I] = MODE == 1 ? ldslot(rv, voff, so, I) : ldslot(I < 4 ? rk : rv, voff, so, I & 3);
  } else {
    asm volatile("" : "+v"(acc));
  }
  SB();
  if constexpr (I < 15) v_step<I + 1, MODE>(ring, pk, mask, acc, rk, rv, voff, so);
}
__device__ __forceinline__ float exp_ref(float mx, float& f) {
  const float M = __builtin_ceilf(mx * LOG2E);
  f = __builtin_amdgcn_fmed3f(fmaf(mx, LOG2E, -M), -1.f, 0.f);
  return M;
}
__device__ __forceinline__ unsigned lds_inc(LAS unsigned* p, int lane) {
  unsigned t = 0, z = 0;
  asm volatile("" : "+v"(z));
  if (lane == 0) t = __hip_atomic_fetch_add(p + z, 1u, __ATOMIC_RELAXED, __HIP_MEMORY_SCOPE_WORKGROUP);
  return t;
}
struct Take { unsigned unit, pend; bool fetch; };
__device__ __forceinline__ void lds_add(LAS unsigned* p, unsigned v) { __hip_atomic_fetch_add(p, v, __ATOMIC_RELAXED, __HIP_MEMORY_SCOPE_WORKGROUP); }
__device__ __forceinline__ Take take(LAS unsigned* ctl, unsigned kv, rsrc_t rws, int cntoff, int lane) {
  const unsigned k = __builtin_amdgcn_readfirstlane(kv);
  unsigned b, s;
  for (unsigned spins = 0;; spins++) {
    const unsigned vs = ((volatile LAS unsigned*)ctl)[5], vb = ((volatile LAS unsigned*)ctl)[8 + (k >> 2)];
    s = __builtin_amdgcn_readfirstlane(vs); b = __builtin_amdgcn_readfirstlane(vb);
    if (b != ~0u || (s >= 0x10000u && (s & 0xffffu) == 0u) || spins > SPIN_CAP * 16) break;
    __builtin_amdgcn_s_sleep(1);
  }
  Take t;
  t.unit = min(b, (unsigned)NUNIT) + (k & 3);
  t.fetch = (k & 3) == 0 && b < NUNIT && s < 0x10000u;
  const bool go = lane == 0 && t.fetch;
  if (go) lds_add(ctl + 5, 1u);
  asm volatile("s_waitcnt lgkmcnt(0)" ::: "memory");
  t.pend = __builtin_amdgcn_raw_ptr_buffer_atomic_add_i32(4, rws, go ? cntoff : OOB, 0, AUX_SC1);
  SB();
  return t;
}
__device__ __forceinline__ void give(LAS unsigned* ctl, const Take& t, int lane) {
  if (t.fetch && lane == 0) {
    unsigned z = 0;
    asm volatile("" : "+v"(z));
    if (t.pend < NUNIT) {
      const unsigned i = __hip_atomic_fetch_add(ctl + 4 + z, 1u, __ATOMIC_RELAXED, __HIP_MEMORY_SCOPE_WORKGROUP);
      ((volatile LAS unsigned*)ctl)[8 + i] = t.pend;
    }
    asm volatile("" ::: "memory");
    lds_add(ctl + 5 + z, t.pend >= NUNIT - 4 ? 0xffffu : ~0u);
  }
}
__device__ __forceinline__ int unit_off(unsigned t) { return (int)min(t, (unsigned)NUNIT) * 4096; }

__device__ __forceinline__ void b_units(u32x4 (&ra)[16], rsrc_t rk, rsrc_t rv, unsigned cur, u32x4 q, int lane, const unsigned (&mask)[4],
                                        LAS unsigned* ctl, rsrc_t rws, int cntoff, Acc& A) {
  const int c = lane & 15, voff = lane * 16;
  unsigned nxt;
  {
    const Take t = take(ctl, lds_inc(ctl, lane), rws, cntoff, lane);
    give(ctl, t, lane);
    nxt = t.unit;
  }
#pragma unroll 1
  while (cur < NUNIT) {
    const int so = unit_off(nxt);
    float x[4];
#pragma unroll
    for (int i = 0; i < 4; i++) {
      x[i] = dot8(ra[i], q);
      asm volatile("" : "+v"(x[i]));
      ra[i] = ldslot(rk, voff, so, i);
      SB();
    }
    const unsigned kv = lds_inc(ctl, lane);
    TR_STEP(2, XOR8, 8) TR_STEP(1, XOR7, 4)
    x[0] += dpp<XOR2>(x[0]); x[0] += dpp<XOR1>(x[0]);
    const float s = rbf(rbf(x[0]) * SCALE);
    float mx = fmaxf(s, dpp<XOR8>(s));
    mx = xrow_max(fmaxf(mx, dpp<ROR4>(mx)));
    float f;
    const float M = exp_ref(mx, f);
    const float p = fexp2(fmaf(s - mx, LOG2E, f));
    unsigned pk = cvtpk(p, p);
    asm volatile("" : "+v"(pk));
    const Take t = take(ctl, kv, rws, cntoff, lane);
    float ls = bf_lo(pk);
    ls += dpp<XOR8>(ls);
    ls = xrow_sum(ls + dpp<ROR4>(ls));
    const float Rn = fmaxf(A.R, M);
    const double s0 = mkd(0u, p2hi(A.R, Rn)), s1 = mkd(0u, p2hi(M, Rn));
    A.o[0] *= s0; A.o[1] *= s0;
    A.l = __builtin_fma((double)ls, s1, A.l * s0);
    A.R = Rn;
    asm volatile("" : "+v"(A.o[0]), "+v"(A.o[1]), "+v"(A.l));
    SB();
    f32x4 acc = {0.f, 0.f, 0.f, 0.f};
    pv<0>(ra[4], pk, mask, acc);  asm volatile("" : "+v"(acc)); ra[4] = ldslot(rv, voff, so, 0); SB();
    pv<4>(ra[5], pk, mask, acc);  asm volatile("" : "+v"(acc)); ra[5] = ldslot(rv, voff, so, 1); SB();
    pv<8>(ra[6], pk, mask, acc);  asm volatile("" : "+v"(acc)); ra[6] = ldslot(rv, voff, so, 2); SB();
    pv<12>(ra[7], pk, mask, acc); asm volatile("" : "+v"(acc)); ra[7] = ldslot(rv, voff, so, 3); SB();
    A.o[0] = __builtin_fma((double)(lane < 32 ? acc[0] : acc[2]), s1, A.o[0]);
    A.o[1] = __builtin_fma((double)(lane < 32 ? acc[1] : acc[3]), s1, A.o[1]);
    asm volatile("" : "+v"(A.o[0]), "+v"(A.o[1]));
    SB();
    give(ctl, t, lane);
    cur = nxt; nxt = t.unit;
  }
}
#undef TR_STEP
__device__ __forceinline__ int rec_col(int lane) { return 8 * (lane & 15) + 4 * ((lane >> 4) & 1) + 2 * (lane >> 5); }
__device__ __forceinline__ void b_merge(bool wide, LAS char* mrg, LAS unsigned* arrive, int w, int lane, const Acc& A, rsrc_t rws, int roff, int hoff, const Tag& tg, unsigned& stv) {
  *(volatile LAS f64x2*)(mrg + w * 1024 + lane * 16) = f64x2{A.o[0], A.o[1]};
  *(volatile LAS double*)(mrg + 8192 + w * 8) = A.l;
  *(volatile LAS float*)(mrg + 8256 + w * 4) = A.R;
  asm volatile("" ::: "memory");
  const unsigned nv = lds_inc(arrive, lane);
  asm volatile("" ::: "memory");
  const f32x4 Ra = *(volatile LAS f32x4*)(mrg + 8256), Rb = *(volatile LAS f32x4*)(mrg + 8272);
  const float Rw = *(volatile LAS float*)(mrg + 8256 + (lane & 7) * 4);
  f64x2 o[8], lw[4];
#pragma unroll
  for (int i = 0; i < 8; i++) o[i] = *(volatile LAS f64x2*)(mrg + i * 1024 + lane * 16);
#pragma unroll
  for (int i = 0; i < 4; i++) lw[i] = *(volatile LAS f64x2*)(mrg + 8192 + i * 16);
  SB();
  if (__builtin_amdgcn_readfirstlane(nv) == 7) {
    const float R = fmaxf(fmaxf(fmaxf(Ra[0], Ra[1]), fmaxf(Ra[2], Ra[3])), fmaxf(fmaxf(Rb[0], Rb[1]), fmaxf(Rb[2], Rb[3])));
    const unsigned sw = p2hi(Rw, R);
    double v0[2] = {0.0, 0.0}, v1[2] = {0.0, 0.0}, L[2] = {0.0, 0.0};
#pragma unroll
    for (int i = 0; i < 8; i++) {
      const double sc = mkd(0u, rdu(sw, i));
      v0[i & 1] = __builtin_fma(o[i][0], sc, v0[i & 1]); v1[i & 1] = __builtin_fma(o[i][1], sc, v1[i & 1]);
      L[i & 1] = __builtin_fma(lw[i >> 1][i & 1], sc, L[i & 1]);
    }
    const double s0 = v0[0] + v0[1], s1 = v1[0] + v1[1];
    stsc(wide ? hilo(s0, tg) : gpair((float)s0, (float)s1, tg), rws, roff + lane * 16, 0);
    stsc(hilo(s1, tg), rws, wide ? roff + 1024 + lane * 16 : OOB, 0);
    u32x4 hd = hilo(L[0] + L[1], tg);
    if (lane == 0) { hd[0] = __float_as_uint(R); hd[2] = 0u; }
    stsc(hd, rws, lane < 2 ? hoff + lane * 16 : OOB, 0);
  }
}

__device__ __forceinline__ unsigned fz(unsigned row) { return ((row & 2) << 1) | ((row >> 1) & 2); }
__device__ __forceinline__ void dma1(rsrc_t rs, int voff, int so, unsigned ldsaddr, unsigned dep) {
  unsigned keep;
  asm volatile("s_mov_b32 %0, m0\n\ts_mov_b32 m0, %1\n\ts_nop 0\n\tbuffer_load_dwordx4 %2, %3, %4 offen nt lds\n\ts_mov_b32 m0, %0"
               : "=&s"(keep) : "s"(ldsaddr), "v"(voff), "s"(rs), "s"(so) : "memory");
}
__device__ __forceinline__ void dmasc(rsrc_t rs, int voff, int so, unsigned ldsaddr) {
  unsigned keep;
  asm volatile("s_mov_b32 %0, m0\n\ts_mov_b32 m0, %1\n\ts_nop 0\n\tbuffer_load_dwordx4 %2, %3, %4 offen sc1 lds\n\ts_mov_b32 m0, %0"
               : "=&s"(keep) : "s"(ldsaddr), "v"(voff), "s"(rs), "s"(so) : "memory");
}
template <int NR>
__device__ __forceinline__ void dma_rows(rsrc_t rs, int srcoff, LAS char* img, int r0, int lane) {
#pragma unroll
  for (int u = 0; u < NR; u++) {
    const int r = r0 + u;
    unsigned keep;
    asm volatile("s_mov_b32 %0, m0\n\ts_mov_b32 m0, %1\n\ts_nop 0\n\tbuffer_load_dwordx4 %2, %3, %4 offen nt lds\n\ts_mov_b32 m0, %0"
                 : "=&s"(keep) : "s"((unsigned)(uintptr_t)img + r * 1024), "v"((lane ^ swz(r)) * 16), "s"(rs), "s"(srcoff + r * 1024) : "memory");
  }
  SB();
}

__device__ __forceinline__ void wo_frags(LAS char* img, int w, int lane, u32x4 (&wf)[4][4]) {
  const int g = lane >> 4, q = (lane & 15) >> 2, p = lane & 3;
#pragma unroll
  for (int ks = 0; ks < 4; ks++)
#pragma unroll
    for (int tl = 0; tl < 4; tl++) {
      const int ch = 8 * w + 2 * tl + (p >> 1);
      const int r0 = 32 * ks + 8 * g + q, r1 = r0 + 4;
      const u32x2 a = trread(img + r0 * 1024 + ((ch ^ swz(r0)) * 16) + 8 * (p & 1));
      const u32x2 b = trread(img + r1 * 1024 + ((ch ^ swz(r1)) * 16) + 8 * (p & 1));
      wf[tl][ks] = u32x4{a[0], a[1], b[0], b[1]};
    }
}
__device__ __forceinline__ f32x4 wo_calc(const u32x4 (&wf)[4][4], LAS char* cb, int lane) {
  const int n = lane & 15, grp = lane >> 4;
  u32x4 bf[4];
#pragma unroll
  for (int ks = 0; ks < 4; ks++) bf[ks] = *(LAS u32x4*)(cb + (n & 3) * 256 + ks * 64 + grp * 16);
  f32x4 acc[4];
#pragma unroll
  for (int tl = 0; tl < 4; tl++) acc[tl] = f32x4{0.f, 0.f, 0.f, 0.f};
#pragma unroll
  for (int ks = 0; ks < 4; ks++)
#pragma unroll
    for (int tl = 0; tl < 4; tl++) acc[tl] = mfma16(wf[tl][ks], bf[ks], acc[tl]);
  const f32x4 lo = (n & 4) ? acc[1] : acc[0], hi = (n & 4) ? acc[3] : acc[2];
  return (n & 8) ? hi : lo;
}
__device__ __forceinline__ void y_store(f32x4 y, int w, int lane, int slot, rsrc_t rws, int yoff, bool on, const Tag& tg) {
  const int off = on ? yoff + (((lane & 3) * 8 + w) * 32 + slot) * 512 + ((lane & 12) + (lane >> 4)) * 16 : OOB;
  stsc(gpair(y[0], y[1], tg), rws, off, 0);
  stsc(gpair(y[2], y[3], tg), rws, off, 256);
}
__device__ __forceinline__ void ld4(u32x4 (&x)[4], rsrc_t rs, int voff) {
  asm volatile("buffer_load_dwordx4 %0, %4, %5, 0 offen sc1\n\tbuffer_load_dwordx4 %1, %4, %5, 0 offen offset:1024 sc1\n\t"
               "buffer_load_dwordx4 %2, %4, %5, 0 offen offset:2048 sc1\n\tbuffer_load_dwordx4 %3, %4, %5, 0 offen offset:3072 sc1"
               : "=&v"(x[0]), "=&v"(x[1]), "=&v"(x[2]), "=&v"(x[3]) : "v"(voff), "s"(rs) : "memory");
}
template <int N> __device__ __forceinline__ void ld4_wait(u32x4 (&x)[4]) {
  asm volatile("s_waitcnt vmcnt(%4)" : "+v"(x[0]), "+v"(x[1]), "+v"(x[2]), "+v"(x[3]) : "n"(N) : "memory");
}
__device__ __forceinline__ bool ld4_ok(const u32x4 (&x)[4], const Tag& t) { return gok(x[0], t) & gok(x[1], t) & gok(x[2], t) & gok(x[3], t); }
__device__ __forceinline__ f32x2 ld4_sum(const u32x4 (&x)[4]) {
  return f32x2{(gval(x[0][0]) + gval(x[1][0])) + (gval(x[2][0]) + gval(x[3][0])), (gval(x[0][2]) + gval(x[1][2])) + (gval(x[2][2]) + gval(x[3][2]))};
}
__device__ __forceinline__ float half_sum(float x) {
  auto t = __builtin_amdgcn_permlane32_swap(__float_as_uint(x), __float_as_uint(x), false, false);
  return __uint_as_float(t[0]) + __uint_as_float(t[1]);
}

#define LH_LD(a, b, c, d, e) "buffer_load_dwordx4 v[" #a ":" #b "-1], %2, %3, 0 offen sc1\n\tbuffer_load_dwordx4 v[" #b ":" #c "-1], %2, %3, 0 offen offset:1024 sc1\n\t" \
  "buffer_load_dwordx4 v[" #c ":" #d "-1], %2, %3, 0 offen offset:2048 sc1\n\tbuffer_load_dwordx4 v[" #d ":" #e "-1], %2, %3, 0 offen offset:3072 sc1\n\t"
#define LH_X(t, B, i, s) "v_bitop3_b32 v" #t ", v[" #B "+" #i "], v[" #B "+" #i "+2], " s " bitop3:0x7e\n\t"
#define LH_CK(B, L) "s_waitcnt vmcnt(8)\n\t" LH_X(112, B, 0, "%4") LH_X(113, B, 4, "%4") LH_X(114, B, 8, "%4") LH_X(115, B, 12, "%4") \
  "v_or3_b32 v112, v112, v113, v114\n\t" LH_X(116, B, 1, "%5") "v_or_b32 v112, v112, v115\n\t" LH_X(117, B, 5, "%5") "v_lshrrev_b32 v112, 24, v112\n\t" \
  LH_X(113, B, 9, "%5") LH_X(114, B, 13, "%5") "v_or3_b32 v112, v112, v116, v117\n\tv_or3_b32 v112, v112, v113, v114\n\t" \
  "v_cmp_ne_u32 vcc, 0, v112\n\ts_cbranch_vccz " L "\n\t"
#define LH_S(t, B, i) "v_lshlrev_b32 v" #t ", 8, v[" #B "+" #i "]\n\t"
#define LH_TK(B) LH_S(112, B, 0) LH_S(113, B, 2) LH_S(114, B, 4) LH_S(115, B, 6) LH_S(116, B, 8) LH_S(117, B, 10) LH_S(118, B, 12) LH_S(119, B, 14)
#define LH_STR2(x) #x
#define LH_STR(x) LH_STR2(x)
__device__ __forceinline__ void last_hop(rsrc_t rws, int off, const Tag& tg, unsigned red, unsigned cnt, rsrc_t rout, int ooff, int eoff) {
  unsigned n, c;
  asm volatile(
      "s_mov_b32 %0, 0x10000\n\t" LH_LD(64, 68, 72, 76, 80) "s_sleep " LH_STR(PGAP2) "\n\t" LH_LD(80, 84, 88, 92, 96) "s_sleep " LH_STR(PGAP2) "\n\t" LH_LD(96, 100, 104, 108, 112)
      "1:\n\t" LH_CK(64, "2f") LH_LD(64, 68, 72, 76, 80) LH_CK(80, "3f") LH_LD(80, 84, 88, 92, 96) LH_CK(96, "4f") LH_LD(96, 100, 104, 108, 112)
      "s_sub_u32 %0, %0, 1\n\ts_cbranch_scc0 1b\n\ts_waitcnt vmcnt(0)\n\t"
      "2:\n\t" LH_TK(64) "s_branch 5f\n\t3:\n\t" LH_TK(80) "s_branch 5f\n\t4:\n\t" LH_TK(96)
      "5:\n\ts_mov_b32 %0, 0x100000\n\t"
      "v_pk_add_f32 v[112:113], v[112:113], v[114:115]\n\tv_pk_add_f32 v[116:117], v[116:117], v[118:119]\n\t"
      "6:\n\tds_read_b32 v114, %7\n\tds_read_b64 v[118:119], %6 offset:256\n\tds_read_b64 v[120:121], %6 offset:512\n\tds_read_b64 v[122:123], %6 offset:768\n\t"
      "s_sub_u32 %0, %0, 1\n\ts_cbranch_scc1 7f\n\ts_waitcnt lgkmcnt(3)\n\tv_readfirstlane_b32 %1, v114\n\ts_cmp_lg_u32 %1, 2\n\ts_cbranch_scc1 6b\n\t"
      "7:\n\tv_pk_add_f32 v[112:113], v[112:113], v[116:117]\n\ts_nop 0\n\tv_mov_b32 v114, v112\n\tv_mov_b32 v115, v113\n\ts_nop 1\n\t"
      "v_permlane32_swap_b32 v112, v114\n\tv_permlane32_swap_b32 v113, v115\n\ts_waitcnt lgkmcnt(0)\n\t"
      "v_pk_add_f32 v[118:119], v[118:119], v[120:121]\n\tv_pk_add_f32 v[112:113], v[112:113], v[114:115]\n\tv_pk_add_f32 v[118:119], v[118:119], v[122:123]\n\ts_nop 0\n\t"
      "v_pk_add_f32 v[112:113], v[112:113], v[118:119]\n\ts_nop 0\n\tv_cvt_pk_bf16_f32 v112, v112, v113\n\ts_nop 0\n\t"
      "buffer_store_dword v112, %9, %8, 0 offen\n\tbuffer_store_dwordx2 %10, %11, %3, 0 offen sc1"
      : "=&s"(n), "=&s"(c)
      : "v"(off), "s"(rws), "s"(tg.tl), "s"(tg.th), "v"(red), "v"(cnt), "s"(rout), "v"(ooff), "v"(tg.e), "v"(eoff)
      : "memory", "vcc", "scc", "v64", "v65", "v66", "v67", "v68", "v69", "v70", "v71", "v72", "v73", "v74", "v75", "v76", "v77", "v78", "v79", "v80", "v81", "v82",
        "v83", "v84", "v85", "v86", "v87", "v88", "v89", "v90", "v91", "v92", "v93", "v94", "v95", "v96", "v97", "v98", "v99", "v100", "v101", "v102", "v103",
        "v104", "v105", "v106", "v107", "v108", "v109", "v110", "v111", "v112", "v113", "v114", "v115", "v116", "v117", "v118", "v119", "v120", "v121",
        "v122", "v123");
}

__global__ __launch_bounds__(512, 2) void k_all(const char* a0_, const char* a1_, const char* a2_, const char* a3_, const char* a4_, const char* a5_,
                                                const char* a6_, const char* a7_, const char* a8_, const char* a9_, const char* a10_, char* a11_, unsigned* a12_) {
  u32x16 ka; u32x8 kb_; u32x2 kc_;
  asm volatile("s_load_dwordx16 %0, %3, 0x0\n\ts_load_dwordx8 %1, %3, 0x40\n\ts_load_dwordx2 %2, %3, 0x60"
               : "=&s"(ka), "=&s"(kb_), "=&s"(kc_) : "s"(__builtin_amdgcn_kernarg_segment_ptr()));
  __shared__ __attribute__((aligned(16))) char smem[LDS_BYTES];
  LAS char* lds = (LAS char*)smem;
  LAS char* scr = lds + LDS_SCR;
  LAS unsigned* ctl = (LAS unsigned*)scr;
  const int tid = threadIdx.x, lane = tid & 63, w = __builtin_amdgcn_readfirstlane(tid >> 6);
  const int blk = blockIdx.x, head = blk >> 3, prod = (blk + head) & 7;
  const int sub = (0x54372106u >> (4 * prod)) & 7;
  unsigned stv = 0;
  STAMP(0);
  unsigned cpf = 0;
  {
    u64 pc;
    asm volatile("s_getpc_b64 %0" : "=s"(pc));
    cpf = __builtin_amdgcn_raw_buffer_load_b32(mkrs((const GAS char*)(pc & ~255ull), CPF), w == 7 - (CPFW) ? head * 2048 + lane * 32 : OOB, 0, 0);
    SB();
  }
  {
    unsigned c = 16 + 4 * (DCH * sub + tid);
    asm volatile("" : "+v"(c));
    c = tid < 8 + DCH ? c : ~0u;
    c = tid < 8 ? 0u : c;
    ctl[tid] = tid == 4 ? DCH : c;
  }
  const int l16 = lane & 15, grp = lane >> 4;
  const int b4 = head & 3, cntoff = WS_CNT + head * CSTR;
  const int bm = (b4 + 1 + (sub >= 3 ? sub - 3 : sub)) & 3;
  const size_t base = (size_t)(bm * 32 + head) << 20;
  const int so = ((sub >= 3 ? 8 : 0) + w) * 4096;
  int v0 = l16 * 16 + grp * (sub < 6 ? 256 : 131072) + (sub < 6 ? so : w * 524288 + head * 256);
  unsigned boff = sub < 6 ? (unsigned)base : 0u, bsz = sub < 6 ? 1u << 20 : 4u << 20;
  int s0[WF0];
#pragma unroll
  for (int i = 0; i < WF0; i++) {
    s0[i] = ((i & 3) << (sub < 6 ? 10 : 13)) + (i >> 2) * (sub < 6 ? 65536 : 32768);
    asm volatile("" : "+s"(s0[i]));
  }
  asm volatile("" : "+v"(v0), "+s"(boff), "+s"(bsz));
  SB();
  asm volatile("s_waitcnt lgkmcnt(0)" : "+s"(ka), "+s"(kb_), "+s"(kc_));
  auto ptr = [](unsigned lo, unsigned hi) { return (GAS char*)(((u64)hi << 32) | lo); };
  GAS char *const xA = ptr(ka[0], ka[1]), *const ckv = ptr(ka[2], ka[3]), *const wqA = ptr(ka[4], ka[5]), *const wuk = ptr(ka[6], ka[7]),
           *const wuv = ptr(ka[8], ka[9]), *const woA = ptr(ka[10], ka[11]), *const xB = ptr(ka[12], ka[13]), *const kc = ptr(ka[14], ka[15]),
           *const vc = ptr(kb_[0], kb_[1]), *const wqB = ptr(kb_[2], kb_[3]), *const woB = ptr(kb_[4], kb_[5]), *const ws = ptr(kb_[6], kb_[7]),
           *const out = ptr(kc_[0], kc_[1]);
  const u64 e0 = *(const __attribute__((address_space(4))) u64*)(u64)(ws + WS_EPOCH);
  const rsrc_t r0 = mkrs((sub < 6 ? kc : sub == 6 ? wqB : wqA) + boff, bsz);
  SB();
  u32x4 ra[16];
#pragma unroll
  for (int i = 0; i < WF0; i++) ra[i] = ldnt(r0, v0, s0[i]);
  SB();
  u32x4 xp[2][2];
  if (sub >= 6) {
    const GAS char* x = sub == 6 ? xB : xA;
#pragma unroll
    for (int s = 0; s < 2; s++) {
      const GAS u32x4* xs = (const GAS u32x4*)(x + (2 * s + (l16 >> 3)) * 1024 + (64 * w + 16 * grp) * 2);
      xp[s][0] = xs[0]; xp[s][1] = xs[1];
    }
#pragma unroll
    for (int i = WF0; i < 16; i++) ra[i] = ldnt(r0, v0, i * 8192);
    asm volatile("" ::: "memory");
    SB();
  }
  const rsrc_t rws = mkrs(ws, WS_END);
  unsigned mask[4];
#pragma unroll
  for (int v = 0; v < 4; v++) mask[v] = (((lane & 7) >> 1) == v) ? ((lane & 1) ? 0xffff0000u : 0x0000ffffu) : 0u;
  const size_t base4 = (size_t)(b4 * 32 + head) << 20;
  const rsrc_t rk4 = mkrs(kc + base4, 1u << 20), rv4 = mkrs(vc + base4, 1u << 20);
  const int voff = lane * 16;
  LAS char* qst = scr + 1024 + w * 512;
  const int qo = WS_QB + head * 2048 + (lane < 32 ? bm : b4) * 512 + (lane & 31) * 16;
  auto qwait = [&](u32x4 g, const Tag& tg) {
    if (!__all(qok(g, tg))) {
      for (unsigned spins = 0;; spins++) {
        asm volatile("" ::: "memory");
        g = ldsc(rws, qo, 0);
        if (__all(qok(g, tg)) || spins > SPIN_CAP) break;
        __builtin_amdgcn_s_sleep(2);
      }
    }
    *(LAS u32x2*)(qst + lane * 8) = u32x2{g[0], g[2]};
  };
  unsigned cur;

  if (__builtin_expect(sub < 6, 1)) {
    const rsrc_t rv = mkrs(vc + base, 1u << 20);
    u32x4 rb[16], g;
#pragma unroll
    for (int i = WF0; i < 32; i++) {
      if (i == WF0) asm volatile("s_barrier" ::: "memory");
      if (i < NF0) vmwait<WF0 - 1>(); else vmwait<WFILL - 1>();
      if (i == 32 - WFILL) g = ldsc(rws, qo, 0);
      const u32x4 v = ldslot(r0, voff, so, i);
      if (i < 16) ra[i] = v; else rb[i - 16] = v;
      SB();
    }
    STAMP(12);
    asm volatile("" :: "v"(cpf));
    qwait(g, mktag(e0));
    const u32x4 q = *(LAS u32x4*)(qst + l16 * 16);
    STAMP(6);
    float sc[4] = {0.f, 0.f, 0.f, 0.f};
#pragma unroll 1
    for (int it = 0; it < 2; it++) {
      const rsrc_t rn = it ? rv : r0;
      sc[2] = sc[0]; sc[3] = sc[1];
      sc[1] = k_step(ra, q, l16, rn, voff, so + (it ? 3 : 2) * CH);
      sc[0] = k_step(rb, q, l16, rn, voff, so + (it ? 2 : 3) * CH);
    }
    STAMP(3);
    const float mx = xrow_max(row_max(fmaxf(fmaxf(sc[0], sc[1]), fmaxf(sc[2], sc[3]))));
    float f, l = 0.f;
    const float M = exp_ref(mx, f);
    unsigned pk[4];
#pragma unroll
    for (int k = 0; k < 4; k++) {
      const float p = fexp2(fmaf(sc[k] - mx, LOG2E, f));
      pk[k] = cvtpk(p, p);
      l += bf_lo(pk[k]);
    }
    l = xrow_sum(row_sum(l));
    cur = 8 * sub + w;
    f32x4 acc = {0.f, 0.f, 0.f, 0.f};
    v_step<0, 1>(ra, pk[0], mask, acc, r0, rv, voff, so + CH);
    v_step<0, 1>(rb, pk[1], mask, acc, r0, rv, voff, so);
    v_step<0, 2>(ra, pk[2], mask, acc, rk4, rv4, voff, cur * 4096);
    v_step<0, 0>(rb, pk[3], mask, acc, r0, rv, voff, 0);
    Acc A;
    A.o[0] = lane < 32 ? acc[0] : acc[2]; A.o[1] = lane < 32 ? acc[1] : acc[3];
    A.l = l; A.R = M;
    b_merge(false, scr + 8192, ctl + 1, w, lane, A, rws, WS_RS + (head * 8 + sub) * 1024, WS_HS + (head * 8 + sub) * 32, mktag(e0), stv);
  } else {
  {
    if ((ABL & 1) && sub == 7) { FLUSH(); return; }
    __builtin_amdgcn_raw_buffer_store_b32(48 + 24 * DCH, rws, (sub == 6 && tid == 0) ? cntoff : OOB, 0, AUX_SC1);
    {
      const rsrc_t rk = mkrs(wuk, sub == 7 ? 4u << 20 : 0u);
#pragma unroll
      for (int i = 0; i < 16; i++)
        __builtin_amdgcn_raw_ptr_buffer_load_lds(rk, (LAS void*)(lds + w * 16384 + i * 1024), 16,
                                                 (64 * w + grp) * 8192 + head * 256 + ((l16 ^ (4 * (i & 3) + grp)) * 16), i * 4 * 8192, 0, AUX_NT);
    }
    SB();
    f32x4 acc[2] = {{0.f, 0.f, 0.f, 0.f}, {0.f, 0.f, 0.f, 0.f}};
    diag_gemv(ra, xp, mask, acc);
    LAS float* red = (LAS float*)(scr + 8192);
#pragma unroll
    for (int s = 0; s < 2; s++) *(LAS f32x4*)(red + w * 512 + (2 * s + (grp >> 1)) * 128 + 8 * l16 + 4 * (grp & 1)) = acc[s];
    asm volatile("" :: "v"(cpf));
    __builtin_amdgcn_s_waitcnt(0x0f70);
    drain();
    bar();
    if (tid < 128) {
      f32x4 a = {0.f, 0.f, 0.f, 0.f};
#pragma unroll
      for (int k = 0; k < 8; k++) a += *(LAS f32x4*)(red + k * 512 + 4 * tid);
      const unsigned p0 = cvtpk(a[0], a[1]), p1 = cvtpk(a[2], a[3]);
      const Tag tg = mktag(e0);
      stsc(u32x4{p0, tg.th, p1, tg.th}, rws, sub == 6 ? WS_QB + head * 2048 + tid * 16 : OOB, 0);
      *(LAS u32x2*)(scr + 1024 + tid * 8) = u32x2{p0, p1};
    }
    STAMP(1);
  }

  if (sub == 6) {
    if (ABL & 1) { drain(); FLUSH(); return; }
    const int b = head >> 3;
    const rsrc_t rc = mkrs(ckv + (size_t)head * 524288, 524288);
    const int dvo = (lane >> 3) * 1024 + w * 128 + (((lane & 7) ^ fz(lane >> 3)) * 16);
    const unsigned dlo = (unsigned)(uintptr_t)lds + w * 8192;
#pragma unroll
    for (int j = 0; j < 16; j++) dma1(rc, dvo, j * 8192, dlo + (j >> 3) * 65536 + (j & 7) * 1024, 0u);
    SB();
    const Tag tg = mktag(e0);
    bar();
    {
      u32x4 t[8];
      unsigned miss = 15;
      __builtin_amdgcn_s_sleep(QS0);
      for (unsigned spins = 0;; spins++) {
        asm volatile("" ::: "memory");
#pragma unroll
        for (int i = 0; i < 4; i++)
          if (miss >> i & 1) {
            t[2 * i] = ldsc(rws, WS_QABS + (b * 32 + w) * 2048 + lane * 16, i * 16384);
            t[2 * i + 1] = ldsc(rws, WS_QABS + (b * 32 + w) * 2048 + lane * 16 + 1024, i * 16384);
          }
        SB();
#pragma unroll
        for (int i = 0; i < 4; i++) if (__all(qok(t[2 * i], tg) & qok(t[2 * i + 1], tg))) miss &= ~(1u << i);
        if (!miss || spins > SPIN_CAP) break;
        __builtin_amdgcn_s_sleep(QSL);
      }
      STAMP(2);
#pragma unroll
      for (int i = 0; i < 8; i++) {
        const int row = 8 * (i >> 1) + w;
        *(LAS u32x2*)(scr + row * 1024 + (((32 * (i & 1) + (lane >> 1)) ^ swz(row)) * 16) + (lane & 1) * 8) = u32x2{t[i][0], t[i][2]};
      }
    }
    bar();
    const int kb = w & 3, hb = w >> 2;
    u32x4 qf[16];
    {
      const int row = 16 * hb + l16;
#pragma unroll
      for (int ks = 0; ks < 16; ks++) qf[ks] = *(LAS u32x4*)(scr + row * 1024 + (((4 * ks + grp) ^ swz(row)) * 16));
    }
    STAMP(3);
    LAS float* mx = (LAS float*)scr;
    LAS char* pl = scr + 1024;
    LAS float* lr = (LAS float*)(scr + 8192);
    f32x16 o0, o1;
#pragma unroll
    for (int j = 0; j < 16; j++) { o0[j] = 0.f; o1[j] = 0.f; }
    float m1 = -INFINITY, m2 = -INFINITY, l1 = 0.f;
    const int h1 = 16 * hb + l16, h2 = lane & 31, hh = lane >> 5;
    const int arow = 16 * kb + l16;
    const int tq = (lane & 15) >> 2, tp = lane & 3, tblk = (lane >> 4) & 1;
    const int qa0 = arow * 128 + ((grp ^ fz(arow)) * 16), qa1 = qa0 ^ 64;
    const int pr = 8 * hh + tq, pc = 2 * tblk + (tp >> 1);
    const int pa0 = w * 8192 + pr * 128 + ((pc ^ fz(pr)) * 16) + 8 * (tp & 1), pa1 = w * 8192 + (pr + 4) * 128 + ((pc ^ fz(pr + 4)) * 16) + 8 * (tp & 1);
#pragma unroll 1
    for (int t = 0; t < 8; t++) {
      if (t < 7) asm volatile("s_waitcnt vmcnt(8)" ::: "memory"); else asm volatile("s_waitcnt vmcnt(0)" ::: "memory");
      bar();
      LAS char* img = lds + (t & 1) * 65536;
      f32x4 s0 = {0.f, 0.f, 0.f, 0.f}, s1 = s0;
      u32x4 ka[16];
#pragma unroll
      for (int ks = 0; ks < 16; ks++) ka[ks] = *(LAS u32x4*)(img + ((ks & 1) ? qa1 : qa0) + (ks >> 1) * 8192);
      SB();
#pragma unroll
      for (int k2 = 0; k2 < 8; k2++) {
        s0 = mfma16(ka[2 * k2], qf[2 * k2], s0);
        s1 = mfma16(ka[2 * k2 + 1], qf[2 * k2 + 1], s1);
      }
      SB();
      u32x2 ta[4][4];
#pragma unroll
      for (int ks = 0; ks < 4; ks++) {
        ta[ks][0] = trread(img + pa0 + ks * 2048); ta[ks][1] = trread(img + pa1 + ks * 2048);
        ta[ks][2] = trread(img + (pa0 ^ 64) + ks * 2048); ta[ks][3] = trread(img + (pa1 ^ 64) + ks * 2048);
      }
      SB();
      float x[4];
#pragma unroll
      for (int j = 0; j < 4; j++) x[j] = rbf(rbf(s0[j] + s1[j]) * SCALE);
      const float lm = xrow_max(fmaxf(fmaxf(x[0], x[1]), fmaxf(x[2], x[3])));
      if (grp == 0) mx[h1 * 4 + kb] = lm;
      LAS u32x2* pw = (LAS u32x2*)(pl + h1 * 144 + (16 * kb + 4 * grp) * 2);
      u32x2 pp = {cvtpk(fexp2((x[0] - m1) * LOG2E), fexp2((x[1] - m1) * LOG2E)), cvtpk(fexp2((x[2] - m1) * LOG2E), fexp2((x[3] - m1) * LOG2E))};
      *pw = pp;
      bar();
      const f32x4 ma = *(LAS f32x4*)(mx + h1 * 4), mb = *(LAS f32x4*)(mx + h2 * 4);
      u32x4 pb[4];
#pragma unroll
      for (int ks = 0; ks < 4; ks++) pb[ks] = *(LAS u32x4*)(pl + h2 * 144 + (16 * ks + 8 * hh) * 2);
      const float n1 = fmaxf(m1, fmaxf(fmaxf(ma[0], ma[1]), fmaxf(ma[2], ma[3])));
      const float n2 = fmaxf(m2, fmaxf(fmaxf(mb[0], mb[1]), fmaxf(mb[2], mb[3])));
      float al1 = fexp2((m1 - n1) * LOG2E), al2 = fexp2((m2 - n2) * LOG2E);
      asm volatile("" : "+v"(pb[0]), "+v"(pb[1]), "+v"(pb[2]), "+v"(pb[3]));
      if (__any(n2 - m2 > 40.f)) {
        bar();
        pp = u32x2{cvtpk(fexp2((x[0] - n1) * LOG2E), fexp2((x[1] - n1) * LOG2E)), cvtpk(fexp2((x[2] - n1) * LOG2E), fexp2((x[3] - n1) * LOG2E))};
        *pw = pp;
        l1 *= al1;
#pragma unroll
        for (int j = 0; j < 16; j++) { o0[j] *= al2; o1[j] *= al2; }
        al1 = al2 = 1.f;
        bar();
#pragma unroll
        for (int ks = 0; ks < 4; ks++) pb[ks] = *(LAS u32x4*)(pl + h2 * 144 + (16 * ks + 8 * hh) * 2);
      }
      m1 = n1; m2 = n2;
      const int nso = t < 6 ? (t + 2) * 65536 : OOB;
#pragma unroll
      for (int ks = 0; ks < 4; ks++) {
        o0 = mfma32(u32x4{ta[ks][0][0], ta[ks][0][1], ta[ks][1][0], ta[ks][1][1]}, pb[ks], o0);
        o1 = mfma32(u32x4{ta[ks][2][0], ta[ks][2][1], ta[ks][3][0], ta[ks][3][1]}, pb[ks], o1);
        SB();
        if (t < 6) {
          dma1(rc, dvo, nso + 2 * ks * 8192, dlo + (t & 1) * 65536 + 2 * ks * 1024, 0u);
          dma1(rc, dvo, nso + (2 * ks + 1) * 8192, dlo + (t & 1) * 65536 + (2 * ks + 1) * 1024, 0u);
        }
        SB();
      }
      l1 = (l1 + ((bf_lo(pp[0]) + bf_hi(pp[0])) + (bf_lo(pp[1]) + bf_hi(pp[1])))) * al1;
#pragma unroll
      for (int j = 0; j < 16; j++) { o0[j] *= al2; o1[j] *= al2; }
    }
    bar();
    STAMP(4);
    {
      float am = 0.f;
#pragma unroll
      for (int j = 0; j < 16; j++) am = fmaxf(am, fmaxf(fabsf(o0[j]), fabsf(o1[j])));
      auto sw = __builtin_amdgcn_permlane32_swap(__float_as_uint(am), __float_as_uint(am), false, false);
      am = fmaxf(__uint_as_float(sw[0]), __uint_as_float(sw[1]));
      const int e = max(__builtin_amdgcn_frexp_expf(am), -127);
      LAS char* tp = lds + h2 * 1040 + (64 * w + 4 * hh) * 2;
      auto pkn = [&](float a, float b) { return __builtin_bit_cast(unsigned, __builtin_amdgcn_cvt_pknorm_i16(__builtin_ldexpf(a, -e), __builtin_ldexpf(b, -e))); };
#pragma unroll
      for (int j4 = 0; j4 < 4; j4++) {
        *(LAS u32x2*)(tp + j4 * 16) = u32x2{pkn(o0[4 * j4], o0[4 * j4 + 1]), pkn(o0[4 * j4 + 2], o0[4 * j4 + 3])};
        *(LAS u32x2*)(tp + 64 + j4 * 16) = u32x2{pkn(o1[4 * j4], o1[4 * j4 + 1]), pkn(o1[4 * j4 + 2], o1[4 * j4 + 3])};
      }
      if (hh == 0) *(LAS unsigned char*)(scr + 9216 + h2 * 8 + w) = (unsigned char)(e + 127);
      const float ls = xrow_sum(l1);
      if (grp == 0) lr[kb * 32 + h1] = ls;
      bar();
#pragma unroll
      for (int i = 0; i < 4; i++) {
        const u32x4 v = *(LAS u32x4*)(lds + (4 * w + i) * 1040 + lane * 16);
        stsc(v, rws, WS_PO + (head * 32 + 4 * w) * 1024 + lane * 16, i * 1024);
      }
      if (tid < 32) {
        const u32x2 eb = *(LAS u32x2*)(scr + 9216 + tid * 8);
        const float l = (lr[tid] + lr[32 + tid]) + (lr[64 + tid] + lr[96 + tid]);
        stsc(u32x4{__float_as_uint(m2), __float_as_uint(l), eb[0], eb[1]}, rws, WS_PML + (head * 32 + tid) * 16, 0);
      }
      drain();
      bar();
      if (tid == 0) __hip_atomic_store((GAS u64*)(ws + WS_F2 + head * 128), tg.e, RLX_AGENT);
      if (w == 0) {
        const unsigned t = __builtin_amdgcn_readfirstlane(__builtin_amdgcn_raw_ptr_buffer_atomic_add_i32(8 + 4 * DCH, rws, lane == 0 ? cntoff : OOB, 0, AUX_SC1));
        const unsigned bs = t + 8 + 4 * (lane - 8);
        ctl[lane] = lane == 3 ? t : lane == 4 ? DCH : lane == 5 ? (t + 8 + 4 * DCH >= NUNIT ? 0x10000u : 0u) : lane < 8 ? 0u : (lane < 8 + DCH && bs < NUNIT) ? bs : ~0u;
        if (lane < 32) ctl[64 + lane] = ~0u;
      }
      bar();
    }
    STAMP(5);
    const u32x4 g = ldsc(rws, qo, 0);
    cur = __builtin_amdgcn_readfirstlane(ctl[3]) + w;
#pragma unroll
    for (int i = 0; i < 8; i++) ra[i] = ldslot(i < 4 ? rk4 : rv4, voff, unit_off(cur), i & 3);
    SB();
    qwait(g, mktag(e0));
  } else {
    const Tag tg = mktag(e0);
    bar();
    STAMP(2);
    {
      u32x4 bf[4];
#pragma unroll
      for (int ks = 0; ks < 4; ks++) bf[ks] = *(LAS u32x4*)(scr + 1024 + (l16 & 3) * 256 + ks * 64 + grp * 16);
      LAS char* tt = scr + 2048 + w * 512;
#pragma unroll
      for (int rt = 0; rt < 4; rt++) {
        f32x4 acc = {0.f, 0.f, 0.f, 0.f};
#pragma unroll
        for (int ks = 0; ks < 4; ks++) acc = mfma16(*(LAS u32x4*)(lds + (64 * w + 16 * rt + l16) * 256 + (((4 * ks + grp) ^ l16) * 16)), bf[ks], acc);
        if (l16 < 4) *(LAS u32x2*)(tt + l16 * 128 + (16 * rt + 4 * grp) * 2) = u32x2{cvtpk(acc[0], acc[1]), cvtpk(acc[2], acc[3])};
      }
      asm volatile("s_waitcnt lgkmcnt(0)" ::: "memory");
      const u32x2 v = *(LAS u32x2*)(tt + lane * 8);
      stsc(u32x4{v[0], tg.th, v[1], tg.th}, rws, WS_QABS + (grp * 32 + head) * 2048 + w * 256 + l16 * 16, 0);
    }
    bar();
    STAMP(3);
    u32x4 wf[4][4], wr[16];
    {
      const rsrc_t ro = mkrs(woA + (size_t)head * 131072, 131072);
      dma_rows<16>(ro, 0, lds, 16 * w, lane);
      const rsrc_t rv = mkrs(wuv, 4u << 20);
      const int voff = (64 * w + 16 * grp) * 8192 + head * 256 + l16 * 16;
#pragma unroll
      for (int i = 0; i < 16; i++) wr[i] = ldnt(rv, voff, i * 8192);
      SB();
      asm volatile("s_waitcnt vmcnt(16)" ::: "memory");
      bar();
      wo_frags(lds, w, lane, wf);
      bar();
      const rsrc_t rb = mkrs(woB + (size_t)head * 131072, 131072);
      dma_rows<16>(rb, 0, lds, 16 * w, lane);
    }
    STAMP(4);
    if (w == 0) poll32(ws, WS_F2, lane, tg);
    bar();
    STAMP(5);
    {
      const int b = tid >> 7, c4 = tid & 127, es = (c4 >> 4) & 3;
      u32x2 po[8]; u32x4 ml[8];
#pragma unroll
      for (int s = 0; s < 8; s++) {
        ml[s] = ldsc(rws, WS_PML + ((8 * b + s) * 32 + head) * 16, 0);
        po[s] = __builtin_amdgcn_raw_buffer_load_b64(rws, WS_PO + ((8 * b + s) * 32 + head) * 1024 + c4 * 8, 0, AUX_SC1);
      }
      float M = -INFINITY;
#pragma unroll
      for (int s = 0; s < 8; s++) M = fmaxf(M, __uint_as_float(ml[s][0]));
      float L = 0.f;
      f32x4 a = {0.f, 0.f, 0.f, 0.f};
#pragma unroll
      for (int s = 0; s < 8; s++) {
        const float f = fexp2((__uint_as_float(ml[s][0]) - M) * LOG2E);
        L = fmaf(f, __uint_as_float(ml[s][1]), L);
        const int e = (int)(((w & 1) ? ml[s][3] : ml[s][2]) >> (8 * es)) & 255;
        const float g = __builtin_ldexpf(f, e - 142);
        a += g * f32x4{(float)(short)po[s][0], (float)((int)po[s][0] >> 16), (float)(short)po[s][1], (float)((int)po[s][1] >> 16)};
      }
      const float inv = (32768.f / 32767.f) / L;
      const u32x2 v = {cvtpk(a[0] * inv, a[1] * inv), cvtpk(a[2] * inv, a[3] * inv)};
      *(LAS u32x2*)(scr + b * 1024 + c4 * 8) = v;
    }
    bar();
    {
      u32x4 xp[2][2];
#pragma unroll
      for (int s = 0; s < 2; s++) {
        LAS u32x4* xs = (LAS u32x4*)(scr + (2 * s + (l16 >> 3)) * 1024 + (64 * w + 16 * grp) * 2);
        xp[s][0] = xs[0]; xp[s][1] = xs[1];
      }
      f32x4 acc[2] = {{0.f, 0.f, 0.f, 0.f}, {0.f, 0.f, 0.f, 0.f}};
      diag_gemv(wr, xp, mask, acc);
      LAS float* red = (LAS float*)(scr + 8192);
#pragma unroll
      for (int s = 0; s < 2; s++) *(LAS f32x4*)(red + w * 512 + (2 * s + (grp >> 1)) * 128 + 8 * l16 + 4 * (grp & 1)) = acc[s];
      bar();
      if (tid < 256) {
        float a = 0.f, b = 0.f;
#pragma unroll
        for (int k = 0; k < 8; k++) { const f32x2 v = *(LAS f32x2*)(red + k * 512 + 2 * tid); a += v[0]; b += v[1]; }
        ((LAS unsigned*)(scr + 4096))[tid] = cvtpk(a, b);
      }
      bar();
    }
    const f32x4 yA = wo_calc(wf, scr + 4096, lane);
    STAMP(6);
    drain();
    bar();
    wo_frags(lds, w, lane, wf);
    const int hoff = WS_HD + head * 256 + (l16 < 2 * NSLOT ? l16 : 0) * 16;
    if (w < 3) {
      const int hoff = WS_HS + (head * 8 + w + (lane & 2 ? 3 : 0)) * 32 + (lane & 1) * 16;
      const int off = WS_RS + (head * 8 + w) * 1024 + lane * 16;
      u32x4 hd, g0, g1;
      for (unsigned spins = 0;; spins++) {
        asm volatile("" ::: "memory");
        hd = ldsc(rws, hoff, 0); g0 = ldsc(rws, off, 0); g1 = ldsc(rws, off, 3072);
        if (__all(hok(hd, tg) & gok(g0, tg) & gok(g1, tg)) || spins > SPIN_CAP) break;
        __builtin_amdgcn_s_sleep(16);
      }
      const float R0 = rdl(hd[0], 0), R1 = rdl(hd[0], 2), R = fmaxf(R0, R1);
      const int d0 = edif(R0, R), d1 = edif(R1, R);
      const float lf = (float)hval(hd);
      const float inv = 1.f / (__builtin_ldexpf(rdl(__float_as_uint(lf), 1), d0) + __builtin_ldexpf(rdl(__float_as_uint(lf), 3), d1));
      const float c0 = (__builtin_ldexpf(gval(g0[0]), d0) + __builtin_ldexpf(gval(g1[0]), d1)) * inv;
      const float c1 = (__builtin_ldexpf(gval(g0[2]), d0) + __builtin_ldexpf(gval(g1[2]), d1)) * inv;
      *(LAS unsigned*)(scr + 5120 + ((b4 + 1 + w) & 3) * 256 + rec_col(lane) * 2) = cvtpk(c0, c1);
      STAMP(7);
    }
    if (w == 4 && !EARLY) {
      for (unsigned spins = 0;; spins++) {
        asm volatile("" ::: "memory");
        if (__any(hok(ldsc(rws, hoff, 0), tg)) || spins > SPIN_CAP) break;
        __builtin_amdgcn_s_sleep(4);
      }
    }
    bar();
    STAMP(8);
    const bool late = (lane & 3) == b4;
    const int slot = late ? 24 + (head >> 2) : 3 * (head >> 2) + ((head - 1 - lane) & 3);
    y_store(yA, w, lane, head, rws, WS_Y, !(ABL & 2), tg);
    y_store(wo_calc(wf, scr + 5120, lane), w, lane, slot, rws, WS_Y + 524288, !late && !(ABL & 2), tg);
    if ((w >> 1) == 2) {
      LAS char* pb = scr + 8192 + (w & 1) * 8192;
      const int off = WS_RD + head * 16384 + (w & 1) * 1024 + lane * 16;
      if (EARLY) {
        for (unsigned spins = 0;; spins++) {
          asm volatile("" ::: "memory");
          if (__any(hok(ldsc(rws, hoff, 0), tg)) || spins > SPIN_CAP) break;
          __builtin_amdgcn_s_sleep(4);
        }
      }
      u32x4 g[8];
      unsigned miss = 0x80 | ((1 << NSLOT) - 1);
      for (unsigned spins = 0;; spins++) {
        asm volatile("" ::: "memory");
        g[7] = ldsc(rws, hoff, 0);
#pragma unroll
        for (int s = 0; s < NSLOT; s++) g[s] = ldsc(rws, off, s * 2048);
        bool ok = hok(g[7], tg);
#pragma unroll
        for (int s = 0; s < NSLOT; s++) ok &= hok(g[s], tg);
        if (__all(ok) || spins > SPIN_CAP) break;
      }
      float R = -INFINITY;
#pragma unroll
      for (int s = 0; s < NSLOT; s++) R = fmaxf(R, rdl(g[7][0], 2 * s));
      const unsigned sw = p2hi(__uint_as_float(g[7][0]), R);
      double S[2] = {0.0, 0.0}, L[2] = {0.0, 0.0};
#pragma unroll
      for (int s = 0; s < NSLOT; s++) {
        const double sc = mkd(0u, rdu(sw, 2 * s));
        S[s & 1] = __builtin_fma(hval(g[s]), sc, S[s & 1]);
        L[s & 1] = __builtin_fma(mkd(rdu(g[7][0], 2 * s + 1), rdu(g[7][2], 2 * s + 1)), sc, L[s & 1]);
      }
      const float cx = (float)((S[0] + S[1]) * (double)__builtin_amdgcn_rcpf((float)(L[0] + L[1])));
      ((LAS unsigned short*)(scr + 5120))[b4 * 128 + rec_col(lane) + (w & 1)] = (unsigned short)cvtpk(cx, cx);
    }
    bar();
    STAMP(9);
    y_store(wo_calc(wf, scr + 5120, lane), w, lane, slot, rws, WS_Y + 524288, late, tg);
    drain();
    STAMP(10);
    FLUSH();
    return;
  }
  }
  {
    STAMP(7);
    const u32x4 q4 = *(LAS u32x4*)(qst + 256 + l16 * 16);
    Acc A;
    A.o[0] = A.o[1] = A.l = 0.0; A.R = -INFINITY;
    b_units(ra, rk4, rv4, cur, q4, lane, mask, ctl, rws, cntoff, A);
    STAMP(8);
    const Tag tg = mktag(e0);
    b_merge(true, scr + 8192 + 8704, ctl + 2, w, lane, A, rws, WS_RD + (head * 8 + sub) * 2048, WS_HD + (head * 8 + sub) * 32, tg, stv);
    SB();
    asm volatile("" :: "v"(ra[0]), "v"(ra[1]), "v"(ra[2]), "v"(ra[3]), "v"(ra[4]), "v"(ra[5]), "v"(ra[6]), "v"(ra[7]));
    STAMP(9);
    if ((ABL & 2) && sub == 1 && w && w < 4 && lane == 0) lds_add(ctl + 88, 1u);
    if (sub < 2 && w < 4 && !(ABL & 1) && (!(ABL & 2) || (sub == 1 && w == 0))) {
      const int off = WS_Y + sub * 524288 + head * 16384 + ((w + 3) & 3) * 4096 + lane * 16;
      LAS f32x2* red = (LAS f32x2*)(scr + 6144);
      u32x4 x[1][4];
      if (w || FPH0) for (unsigned spins = 0;; spins++) {
        ld4(x[0], rws, off);
        ld4_wait<0>(x[0]);
        if (__any(gok(x[0][0], tg) | gok(x[0][1], tg) | gok(x[0][2], tg) | gok(x[0][3], tg)) || spins > SPIN_CAP) break;
        __builtin_amdgcn_s_sleep(FSL);
      }
      STAMP(4);
      if (w) {
        for (unsigned spins = 0; !__all(ld4_ok(x[0], tg)) && spins < SPIN_CAP; spins++) { ld4(x[0], rws, off); ld4_wait<0>(x[0]); }
        f32x2 r = ld4_sum(x[0]);
        r = f32x2{half_sum(r[0]), half_sum(r[1])};
        if (lane < 32) red[w * 32 + lane] = r;
        asm volatile("s_waitcnt lgkmcnt(0)" ::: "memory");
        if (lane == 0) lds_add(ctl + 88, 1u);
      } else {
        last_hop(rws, off, tg, (unsigned)(uintptr_t)(red + (lane & 31)), (unsigned)(uintptr_t)(ctl + 88), mkrs(out, 8192),
                 lane < 32 ? (head >> 3) * 2048 + sub * 1024 + (head & 7) * 128 + l16 * 8 + grp * 4 : OOB,
                 head == 0 && sub == 1 && lane == 0 ? WS_EPOCH : OOB);
      }
    }
  }
  FLUSH();
}

extern "C" void kernel_launch(void* const* d_in, const int* in_sizes, int n_in, void* d_out, int out_size, void* d_ws, size_t ws_size,
                              hipStream_t stream) {
  k_all<<<256, 512, 0, stream>>>((const char*)d_in[0], (const char*)d_in[1], (const char*)d_in[2], (const char*)d_in[3], (const char*)d_in[4],
                                 (const char*)d_in[5], (const char*)d_in[6], (const char*)d_in[7], (const char*)d_in[8], (const char*)d_in[9],
                                 (const char*)d_in[10], (char*)d_ws, (unsigned*)d_out);
}
```

```cpp
#include <hip/hip_runtime.h>
#include <hip/hip_bf16.h>
#include <stdint.h>

typedef __attribute__((ext_vector_type(4))) unsigned u32x4;
typedef __attribute__((ext_vector_type(2))) unsigned u32x2;
typedef __attribute__((ext_vector_type(8))) unsigned u32x8;
typedef __attribute__((ext_vector_type(16))) unsigned u32x16;
typedef __attribute__((ext_vector_type(4))) float f32x4;
typedef __attribute__((ext_vector_type(2))) float f32x2;
typedef __attribute__((ext_vector_type(2))) double f64x2;
typedef __attribute__((ext_vector_type(16))) float f32x16;
typedef __attribute__((ext_vector_type(8))) __bf16 bf16x8;
typedef __attribute__((ext_vector_type(2))) __bf16 bf16x2;
typedef __attribute__((ext_vector_type(4))) short s16x4;
typedef unsigned long long u64;
typedef __amdgpu_buffer_rsrc_t rsrc_t;
#define LAS __attribute__((address_space(3)))
#define GAS __attribute__((address_space(1)))
#define RLX_AGENT __ATOMIC_RELAXED, __HIP_MEMORY_SCOPE_AGENT

#define STAMPS 0
#define WTH 6
#define WFILL 6
#define HELP 1
#define DCH 3
#define CPF 31232
#define CPFW 0
#define WF0 WFILL
#define NF0 8
#define PSL 16
#define QSL 4
#define QS0 90
#define PCAP 16
#define NP2 3
#define PGAP2 8
#define FSL 24
#define EARLY 1
#define FPH0 0
#define H1DMA 0
#define AALL 1
#define ABL 0
constexpr int AUX_NT = 2, AUX_SC1 = 16, OOB = 0x40000000;
constexpr float SCALE = 0.08838834764831845f;
constexpr float LOG2E = 1.4426950408889634f;
constexpr unsigned SPIN_CAP = 1u << 17;
constexpr int NSLOT = 6 + HELP, NUNIT = 256;

constexpr int WS_EPOCH = 0;
constexpr int WS_F2   = 4096;
constexpr int CSTR    = 4352;
constexpr int WS_CNT  = WS_F2 + 4096;
constexpr int WS_QB   = WS_CNT + 32 * CSTR;
constexpr int WS_QABS = WS_QB + 32 * 2048;
constexpr int WS_PML  = WS_QABS + 4 * 32 * 2048;
constexpr int WS_PO   = WS_PML + 32 * 32 * 16;
constexpr int WS_RS   = WS_PO + 32 * 32 * 1024;
constexpr int WS_HS   = WS_RS + 32 * 8 * 1024;
constexpr int WS_RD   = WS_HS + 32 * 256;
constexpr int WS_HD   = WS_RD + 32 * 8 * 2048;
constexpr int WS_Y    = WS_HD + 32 * 256;
constexpr int WS_END  = WS_Y + 2 * 4 * 8 * 32 * 512;
constexpr int WS_STAMP = 32 << 20;

constexpr int LDS_BYTES = 163840;
constexpr int LDS_SCR = 131072;

__device__ __forceinline__ unsigned cvtpk(float lo, float hi) {
  f32x2 v = {lo, hi};
  return __builtin_bit_cast(unsigned, __builtin_convertvector(v, bf16x2));
}
__device__ __forceinline__ float bf_lo(unsigned u) { return __uint_as_float(u << 16); }
__device__ __forceinline__ float bf_hi(unsigned u) { return __uint_as_float(u & 0xffff0000u); }
__device__ __forceinline__ float rbf(float x) { return __uint_as_float(cvtpk(x, x) << 16); }
__device__ __forceinline__ float dot2(unsigned k, unsigned q, float acc) {
  return __builtin_amdgcn_fdot2_f32_bf16(__builtin_bit_cast(bf16x2, k), __builtin_bit_cast(bf16x2, q), acc, false);
}
template <int CTRL> __device__ __forceinline__ float dpp(float x) {
  return __builtin_bit_cast(float, __builtin_amdgcn_mov_dpp(__builtin_bit_cast(int, x), CTRL, 0xf, 0xf, true));
}
template <int CTRL> __device__ __forceinline__ unsigned dppu(unsigned x) {
  return (unsigned)__builtin_amdgcn_mov_dpp((int)x, CTRL, 0xf, 0xf, true);
}
constexpr int XOR1 = 0xB1, XOR2 = 0x4E, XOR7 = 0x141, XOR8 = 0x128, ROR4 = 0x124;
__device__ __forceinline__ float xrow_max(float x) {
  auto s = __builtin_amdgcn_permlane16_swap(__float_as_uint(x), __float_as_uint(x), false, false);
  x = fmaxf(__uint_as_float(s[0]), __uint_as_float(s[1]));
  auto t = __builtin_amdgcn_permlane32_swap(__float_as_uint(x), __float_as_uint(x), false, false);
  return fmaxf(__uint_as_float(t[0]), __uint_as_float(t[1]));
}
__device__ __forceinline__ float xrow_sum(float x) {
  auto s = __builtin_amdgcn_permlane16_swap(__float_as_uint(x), __float_as_uint(x), false, false);
  x = __uint_as_float(s[0]) + __uint_as_float(s[1]);
  auto t = __builtin_amdgcn_permlane32_swap(__float_as_uint(x), __float_as_uint(x), false, false);
  return __uint_as_float(t[0]) + __uint_as_float(t[1]);
}
__device__ __forceinline__ float row_max(float x) {
  x = fmaxf(x, dpp<XOR8>(x)); x = fmaxf(x, dpp<ROR4>(x)); x = fmaxf(x, dpp<XOR2>(x)); x = fmaxf(x, dpp<XOR1>(x));
  return x;
}
__device__ __forceinline__ float row_sum(float x) {
  x += dpp<XOR8>(x); x += dpp<ROR4>(x); x += dpp<XOR2>(x); x += dpp<XOR1>(x);
  return x;
}
__device__ __forceinline__ float fexp2(float x) { return __builtin_amdgcn_exp2f(x); }
__device__ __forceinline__ f32x4 mfma16(u32x4 a, u32x4 b, f32x4 c) {
  return __builtin_amdgcn_mfma_f32_16x16x32_bf16(__builtin_bit_cast(bf16x8, a), __builtin_bit_cast(bf16x8, b), c, 0, 0, 0);
}
__device__ __forceinline__ f32x16 mfma32(u32x4 a, u32x4 b, f32x16 c) {
  return __builtin_amdgcn_mfma_f32_32x32x16_bf16(__builtin_bit_cast(bf16x8, a), __builtin_bit_cast(bf16x8, b), c, 0, 0, 0);
}
__device__ __forceinline__ rsrc_t mkrs(const GAS char* p, unsigned bytes) {
  return __builtin_amdgcn_make_buffer_rsrc((void*)p, 0, bytes, 0x00020000);
}
__device__ __forceinline__ void bar() { asm volatile("s_waitcnt lgkmcnt(0)\n\ts_barrier" ::: "memory"); }
__device__ __forceinline__ void drain() { asm volatile("s_waitcnt vmcnt(0)" ::: "memory"); }
#define SB() __builtin_amdgcn_sched_barrier(0)
template <int N> __device__ __forceinline__ void vmwait() { asm volatile("s_waitcnt vmcnt(%0)" :: "n"(N) : "memory"); }
__device__ __forceinline__ unsigned swz(unsigned row) { return ((row & 3) << 2) | ((row >> 2) & 2); }
__device__ __forceinline__ u32x2 trread(LAS char* p) {
  return __builtin_bit_cast(u32x2, __builtin_amdgcn_ds_read_tr16_b64_v4i16((LAS s16x4*)p));
}

struct Tag { unsigned tl, th; u64 e; };
__device__ __forceinline__ Tag mktag(u64 e0) {
  Tag t;
  asm volatile("" : "+s"(e0));
  t.e = e0 * 6364136223846793005ull + 1442695040888963407ull;
  t.th = (unsigned)(t.e >> 32);
  t.tl = (unsigned)t.e & 0xff000000u;
  return t;
}
__device__ __forceinline__ unsigned gpack(float v, const Tag& t) { return ((__float_as_uint(v) + 0x80u) >> 8) | t.tl; }
__device__ __forceinline__ u32x4 gpair(float a, float b, const Tag& t) { return u32x4{gpack(a, t), t.th, gpack(b, t), t.th}; }
__device__ __forceinline__ bool gok(u32x4 g, const Tag& t) {
  return ((((g[0] ^ t.tl) | (g[2] ^ t.tl)) >> 24) | (g[1] ^ t.th) | (g[3] ^ t.th)) == 0u;
}
__device__ __forceinline__ float gval(unsigned lo) { return __uint_as_float(lo << 8); }
__device__ __forceinline__ bool qok(u32x4 g, const Tag& t) { return ((g[1] ^ t.th) | (g[3] ^ t.th)) == 0u; }
__device__ __forceinline__ u32x4 hilo(double v, const Tag& t) {
  const u32x2 u = __builtin_bit_cast(u32x2, v);
  return u32x4{u[0], t.th, u[1], (unsigned)t.e};
}
__device__ __forceinline__ bool hok(u32x4 g, const Tag& t) { return ((g[1] ^ t.th) | (g[3] ^ (unsigned)t.e)) == 0u; }
__device__ __forceinline__ double mkd(unsigned lo, unsigned hi) { return __builtin_bit_cast(double, u32x2{lo, hi}); }
__device__ __forceinline__ double hval(u32x4 g) { return mkd(g[0], g[2]); }
__device__ __forceinline__ int edif(float a, float b) {
  int r;
  asm("v_cvt_i32_f32 %0, %1" : "=v"(r) : "v"(a - b));
  return r;
}
__device__ __forceinline__ unsigned p2hi(float a, float b) { return (unsigned)max(edif(a, b) + 1023, 0) << 20; }
__device__ __forceinline__ unsigned rdu(unsigned v, int l) { return __builtin_amdgcn_readlane(v, l); }
__device__ __forceinline__ float rdl(unsigned v, int l) { return __uint_as_float(__builtin_amdgcn_readlane(v, l)); }

#define STAMP(i) do {} while (0)
#define FLUSH() do {} while (0)

__device__ __forceinline__ void diag_gemv(const u32x4 (&wr)[16], const u32x4 (&xp)[2][2], const unsigned (&mask)[4], f32x4 (&acc)[2]) {
#pragma unroll
  for (int i = 0; i < 16; i++) {
#pragma unroll
    for (int s = 0; s < 2; s++) {
      const unsigned src = xp[s][i >> 3][(i >> 1) & 3];
      const unsigned dup = __builtin_amdgcn_perm(src, src, (i & 1) ? 0x03020302u : 0x01000100u);
      const u32x4 a = {dup & mask[0], dup & mask[1], dup & mask[2], dup & mask[3]};
      acc[s] = mfma16(a, wr[i], acc[s]);
    }
  }
}

__device__ __forceinline__ void poll32(GAS char* ws, int off, int lane, const Tag& tg) {
  GAS u64* f = (GAS u64*)(ws + off + (lane & 31) * 128);
  for (unsigned spins = 0;; spins++) {
    const u64 v = __hip_atomic_load(f, RLX_AGENT);
    if (__all(v == tg.e) || spins > SPIN_CAP) break;
    __builtin_amdgcn_s_sleep(PSL);
  }
  __builtin_amdgcn_fence(__ATOMIC_ACQUIRE, "wavefront");
}

constexpr int CH = 4 * 65536;
struct Acc { double o[2]; double l; float R; };
__device__ __forceinline__ u32x4 ldnt(rsrc_t r, int voff, int so) { return __builtin_amdgcn_raw_buffer_load_b128(r, voff, so, AUX_NT); }
__device__ __forceinline__ u32x4 ldslot(rsrc_t r, int voff, int so, int i) { return ldnt(r, voff + (i & 3) * 1024, so + (i >> 2) * 65536); }
__device__ __forceinline__ u32x4 ldsc(rsrc_t r, int voff, int so) { return __builtin_amdgcn_raw_buffer_load_b128(r, voff, so, AUX_SC1); }
__device__ __forceinline__ void stsc(u32x4 v, rsrc_t r, int voff, int so) { __builtin_amdgcn_raw_buffer_store_b128(v, r, voff, so, AUX_SC1); }
__device__ __forceinline__ float dot8(u32x4 k, u32x4 q) { return dot2(k[3], q[3], dot2(k[2], q[2], dot2(k[1], q[1], dot2(k[0], q[0], 0.f)))); }
#define TR_STEP(HALF, CTRL, BIT) _Pragma("unroll") for (int h = 0; h < (HALF); ++h) { \
    const float t1 = x[h] + dpp<CTRL>(x[h]), t2 = x[h + (HALF)] + dpp<CTRL>(x[h + (HALF)]); x[h] = (c & (BIT)) ? t2 : t1; }
__device__ __forceinline__ float k_step(u32x4 (&ring)[16], u32x4 q, int c, rsrc_t rn, int voff, int so) {
  float x[16];
#pragma unroll
  for (int i = 0; i < 16; i++) {
    x[i] = dot8(ring[i], q);
    asm volatile("s_waitcnt vmcnt(%1)" : "+v"(x[i]) : "n"(WTH - 1));
    ring[i] = ldslot(rn, voff, so, i);
    SB();
  }
  TR_STEP(8, XOR8, 8) TR_STEP(4, XOR7, 4) TR_STEP(2, XOR2, 2) TR_STEP(1, XOR1, 1)
  return rbf(rbf(x[0]) * SCALE);
}
template <int BC> __device__ __forceinline__ void pv(u32x4 v, unsigned pk, const unsigned (&mask)[4], f32x4& acc) {
  const unsigned d = dppu<0x150 + BC>(pk);
  acc = mfma16(u32x4{d & mask[0], d & mask[1], d & mask[2], d & mask[3]}, v, acc);
}
template <int I, int MODE> __device__ __forceinline__ void v_step(u32x4 (&ring)[16], unsigned pk, const unsigned (&mask)[4], f32x4& acc,
                                                                  rsrc_t rk, rsrc_t rv, int voff, int so) {
  pv<I>(ring[I], pk, mask, acc);
  if (MODE == 1 || (MODE == 2 && I < 8)) {
    asm volatile("s_waitcnt vmcnt(%1)" : "+v"(acc) : "n"(WTH - 1));
    ring[I] = MODE == 1 ? ldslot(rv, voff, so, I) : ldslot(I < 4 ? rk : rv, voff, so, I & 3);
  } else {
    asm volatile("" : "+v"(acc));
  }
  SB();
  if constexpr (I < 15) v_step<I + 1, MODE>(ring, pk, mask, acc, rk, rv, voff, so);
}
__device__ __forceinline__ float exp_ref(float mx, float& f) {
  const float M = __builtin_ceilf(mx * LOG2E);
  f = __builtin_amdgcn_fmed3f(fmaf(mx, LOG2E, -M), -1.f, 0.f);
  return M;
}
__device__ __forceinline__ unsigned lds_inc(LAS unsigned* p, int lane) {
  unsigned t = 0, z = 0;
  asm volatile("" : "+v"(z));
  if (lane == 0) t = __hip_atomic_fetch_add(p + z, 1u, __ATOMIC_RELAXED, __HIP_MEMORY_SCOPE_WORKGROUP);
  return t;
}
struct Take { unsigned unit, pend; bool fetch; };
__device__ __forceinline__ void lds_add(LAS unsigned* p, unsigned v) { __hip_atomic_fetch_add(p, v, __ATOMIC_RELAXED, __HIP_MEMORY_SCOPE_WORKGROUP); }
__device__ __forceinline__ Take take(LAS unsigned* ctl, unsigned kv, rsrc_t rws, int cntoff, int lane) {
  const unsigned k = __builtin_amdgcn_readfirstlane(kv);
  unsigned b, s;
  for (unsigned spins = 0;; spins++) {
    const unsigned vs = ((volatile LAS unsigned*)ctl)[5], vb = ((volatile LAS unsigned*)ctl)[8 + (k >> 2)];
    s = __builtin_amdgcn_readfirstlane(vs); b = __builtin_amdgcn_readfirstlane(vb);
    if (b != ~0u || (s >= 0x10000u && (s & 0xffffu) == 0u) || spins > SPIN_CAP * 16) break;
    __builtin_amdgcn_s_sleep(1);
  }
  Take t;
  t.unit = min(b, (unsigned)NUNIT) + (k & 3);
  t.fetch = (k & 3) == 0 && b < NUNIT && s < 0x10000u;
  const bool go = lane == 0 && t.fetch;
  if (go) lds_add(ctl + 5, 1u);
  asm volatile("s_waitcnt lgkmcnt(0)" ::: "memory");
  t.pend = __builtin_amdgcn_raw_ptr_buffer_atomic_add_i32(4, rws, go ? cntoff : OOB, 0, AUX_SC1);
  SB();
  return t;
}
__device__ __forceinline__ void give(LAS unsigned* ctl, const Take& t, int lane) {
  if (t.fetch && lane == 0) {
    unsigned z = 0;
    asm volatile("" : "+v"(z));
    if (t.pend < NUNIT) {
      const unsigned i = __hip_atomic_fetch_add(ctl + 4 + z, 1u, __ATOMIC_RELAXED, __HIP_MEMORY_SCOPE_WORKGROUP);
      ((volatile LAS unsigned*)ctl)[8 + i] = t.pend;
    }
    asm volatile("" ::: "memory");
    lds_add(ctl + 5 + z, t.pend >= NUNIT - 4 ? 0xffffu : ~0u);
  }
}
__device__ __forceinline__ int unit_off(unsigned t) { return (int)min(t, (unsigned)NUNIT) * 4096; }

__device__ __forceinline__ void b_units(u32x4 (&ra)[16], rsrc_t rk, rsrc_t rv, unsigned cur, u32x4 q, int lane, const unsigned (&mask)[4],
                                        LAS unsigned* ctl, rsrc_t rws, int cntoff, Acc& A) {
  const int c = lane & 15, voff = lane * 16;
  unsigned nxt;
  {
    const Take t = take(ctl, lds_inc(ctl, lane), rws, cntoff, lane);
    give(ctl, t, lane);
    nxt = t.unit;
  }
#pragma unroll 1
  while (cur < NUNIT) {
    const int so = unit_off(nxt);
    float x[4];
#pragma unroll
    for (int i = 0; i < 4; i++) {
      x[i] = dot8(ra[i], q);
      asm volatile("" : "+v"(x[i]));
      ra[i] = ldslot(rk, voff, so, i);
      SB();
    }
    const unsigned kv = lds_inc(ctl, lane);
    TR_STEP(2, XOR8, 8) TR_STEP(1, XOR7, 4)
    x[0] += dpp<XOR2>(x[0]); x[0] += dpp<XOR1>(x[0]);
    const float s = rbf(rbf(x[0]) * SCALE);
    float mx = fmaxf(s, dpp<XOR8>(s));
    mx = xrow_max(fmaxf(mx, dpp<ROR4>(mx)));
    float f;
    const float M = exp_ref(mx, f);
    const float p = fexp2(fmaf(s - mx, LOG2E, f));
    unsigned pk = cvtpk(p, p);
    asm volatile("" : "+v"(pk));
    const Take t = take(ctl, kv, rws, cntoff, lane);
    float ls = bf_lo(pk);
    ls += dpp<XOR8>(ls);
    ls = xrow_sum(ls + dpp<ROR4>(ls));
    const float Rn = fmaxf(A.R, M);
    const double s0 = mkd(0u, p2hi(A.R, Rn)), s1 = mkd(0u, p2hi(M, Rn));
    A.o[0] *= s0; A.o[1] *= s0;
    A.l = __builtin_fma((double)ls, s1, A.l * s0);
    A.R = Rn;
    asm volatile("" : "+v"(A.o[0]), "+v"(A.o[1]), "+v"(A.l));
    SB();
    f32x4 acc = {0.f, 0.f, 0.f, 0.f};
    pv<0>(ra[4], pk, mask, acc);  asm volatile("" : "+v"(acc)); ra[4] = ldslot(rv, voff, so, 0); SB();
    pv<4>(ra[5], pk, mask, acc);  asm volatile("" : "+v"(acc)); ra[5] = ldslot(rv, voff, so, 1); SB();
    pv<8>(ra[6], pk, mask, acc);  asm volatile("" : "+v"(acc)); ra[6] = ldslot(rv, voff, so, 2); SB();
    pv<12>(ra[7], pk, mask, acc); asm volatile("" : "+v"(acc)); ra[7] = ldslot(rv, voff, so, 3); SB();
    A.o[0] = __builtin_fma((double)(lane < 32 ? acc[0] : acc[2]), s1, A.o[0]);
    A.o[1] = __builtin_fma((double)(lane < 32 ? acc[1] : acc[3]), s1, A.o[1]);
    asm volatile("" : "+v"(A.o[0]), "+v"(A.o[1]));
    SB();
    give(ctl, t, lane);
    cur = nxt; nxt = t.unit;
  }
}
#undef TR_STEP
__device__ __forceinline__ int rec_col(int lane) { return 8 * (lane & 15) + 4 * ((lane >> 4) & 1) + 2 * (lane >> 5); }
__device__ __forceinline__ void b_merge(bool wide, LAS char* mrg, LAS unsigned* arrive, int w, int lane, const Acc& A, rsrc_t rws, int roff, int hoff, const Tag& tg, unsigned& stv) {
  *(volatile LAS f64x2*)(mrg + w * 1024 + lane * 16) = f64x2{A.o[0], A.o[1]};
  *(volatile LAS double*)(mrg + 8192 + w * 8) = A.l;
  *(volatile LAS float*)(mrg + 8256 + w * 4) = A.R;
  asm volatile("" ::: "memory");
  const unsigned nv = lds_inc(arrive, lane);
  asm volatile("" ::: "memory");
  const f32x4 Ra = *(volatile LAS f32x4*)(mrg + 8256), Rb = *(volatile LAS f32x4*)(mrg + 8272);
  const float Rw = *(volatile LAS float*)(mrg + 8256 + (lane & 7) * 4);
  f64x2 o[8], lw[4];
#pragma unroll
  for (int i = 0; i < 8; i++) o[i] = *(volatile LAS f64x2*)(mrg + i * 1024 + lane * 16);
#pragma unroll
  for (int i = 0; i < 4; i++) lw[i] = *(volatile LAS f64x2*)(mrg + 8192 + i * 16);
  SB();
  if (__builtin_amdgcn_readfirstlane(nv) == 7) {
    const float R = fmaxf(fmaxf(fmaxf(Ra[0], Ra[1]), fmaxf(Ra[2], Ra[3])), fmaxf(fmaxf(Rb[0], Rb[1]), fmaxf(Rb[2], Rb[3])));
    const unsigned sw = p2hi(Rw, R);
    double v0[2] = {0.0, 0.0}, v1[2] = {0.0, 0.0}, L[2] = {0.0, 0.0};
#pragma unroll
    for (int i = 0; i < 8; i++) {
      const double sc = mkd(0u, rdu(sw, i));
      v0[i & 1] = __builtin_fma(o[i][0], sc, v0[i & 1]); v1[i & 1] = __builtin_fma(o[i][1], sc, v1[i & 1]);
      L[i & 1] = __builtin_fma(lw[i >> 1][i & 1], sc, L[i & 1]);
    }
    const double s0 = v0[0] + v0[1], s1 = v1[0] + v1[1];
    stsc(wide ? hilo(s0, tg) : gpair((float)s0, (float)s1, tg), rws, roff + lane * 16, 0);
    stsc(hilo(s1, tg), rws, wide ? roff + 1024 + lane * 16 : OOB, 0);
    u32x4 hd = hilo(L[0] + L[1], tg);
    if (lane == 0) { hd[0] = __float_as_uint(R); hd[2] = 0u; }
    stsc(hd, rws, lane < 2 ? hoff + lane * 16 : OOB, 0);
  }
}

__device__ __forceinline__ unsigned fz(unsigned row) { return ((row & 2) << 1) | ((row >> 1) & 2); }
__device__ __forceinline__ void dma1(rsrc_t rs, int voff, int so, unsigned ldsaddr, unsigned dep) {
  unsigned keep;
  asm volatile("s_mov_b32 %0, m0\n\ts_mov_b32 m0, %1\n\ts_nop 0\n\tbuffer_load_dwordx4 %2, %3, %4 offen nt lds\n\ts_mov_b32 m0, %0"
               : "=&s"(keep) : "s"(ldsaddr), "v"(voff), "s"(rs), "s"(so) : "memory");
}
__device__ __forceinline__ void dmasc(rsrc_t rs, int voff, int so, unsigned ldsaddr) {
  unsigned keep;
  asm volatile("s_mov_b32 %0, m0\n\ts_mov_b32 m0, %1\n\ts_nop 0\n\tbuffer_load_dwordx4 %2, %3, %4 offen sc1 lds\n\ts_mov_b32 m0, %0"
               : "=&s"(keep) : "s"(ldsaddr), "v"(voff), "s"(rs), "s"(so) : "memory");
}
template <int NR>
__device__ __forceinline__ void dma_rows(rsrc_t rs, int srcoff, LAS char* img, int r0, int lane) {
#pragma unroll
  for (int u = 0; u < NR; u++) {
    const int r = r0 + u;
    unsigned keep;
    asm volatile("s_mov_b32 %0, m0\n\ts_mov_b32 m0, %1\n\ts_nop 0\n\tbuffer_load_dwordx4 %2, %3, %4 offen nt lds\n\ts_mov_b32 m0, %0"
                 : "=&s"(keep) : "s"((unsigned)(uintptr_t)img + r * 1024), "v"((lane ^ swz(r)) * 16), "s"(rs), "s"(srcoff + r * 1024) : "memory");
  }
  SB();
}

__device__ __forceinline__ void wo_frags(LAS char* img, int w, int lane, u32x4 (&wf)[4][4]) {
  const int g = lane >> 4, q = (lane & 15) >> 2, p = lane & 3;
#pragma unroll
  for (int ks = 0; ks < 4; ks++)
#pragma unroll
    for (int tl = 0; tl < 4; tl++) {
      const int ch = 8 * w + 2 * tl + (p >> 1);
      const int r0 = 32 * ks + 8 * g + q, r1 = r0 + 4;
      const u32x2 a = trread(img + r0 * 1024 + ((ch ^ swz(r0)) * 16) + 8 * (p & 1));
      const u32x2 b = trread(img + r1 * 1024 + ((ch ^ swz(r1)) * 16) + 8 * (p & 1));
      wf[tl][ks] = u32x4{a[0], a[1], b[0], b[1]};
    }
}
__device__ __forceinline__ f32x4 wo_calc(const u32x4 (&wf)[4][4], LAS char* cb, int lane) {
  const int n = lane & 15, grp = lane >> 4;
  u32x4 bf[4];
#pragma unroll
  for (int ks = 0; ks < 4; ks++) bf[ks] = *(LAS u32x4*)(cb + (n & 3) * 256 + ks * 64 + grp * 16);
  f32x4 acc[4];
#pragma unroll
  for (int tl = 0; tl < 4; tl++) acc[tl] = f32x4{0.f, 0.f, 0.f, 0.f};
#pragma unroll
  for (int ks = 0; ks < 4; ks++)
#pragma unroll
    for (int tl = 0; tl < 4; tl++) acc[tl] = mfma16(wf[tl][ks], bf[ks], acc[tl]);
  const f32x4 lo = (n & 4) ? acc[1] : acc[0], hi = (n & 4) ? acc[3] : acc[2];
  return (n & 8) ? hi : lo;
}
__device__ __forceinline__ void y_store(f32x4 y, int w, int lane, int slot, rsrc_t rws, int yoff, bool on, const Tag& tg) {
  const int off = on ? yoff + (((lane & 3) * 8 + w) * 32 + slot) * 512 + ((lane & 12) + (lane >> 4)) * 16 : OOB;
  stsc(gpair(y[0], y[1], tg), rws, off, 0);
  stsc(gpair(y[2], y[3], tg), rws, off, 256);
}
__device__ __forceinline__ void ld4(u32x4 (&x)[4], rsrc_t rs, int voff) {
  asm volatile("buffer_load_dwordx4 %0, %4, %5, 0 offen sc1\n\tbuffer_load_dwordx4 %1, %4, %5, 0 offen offset:1024 sc1\n\t"
               "buffer_load_dwordx4 %2, %4, %5, 0 offen offset:2048 sc1\n\tbuffer_load_dwordx4 %3, %4, %5, 0 offen offset:3072 sc1"
               : "=&v"(x[0]), "=&v"(x[1]), "=&v"(x[2]), "=&v"(x[3]) : "v"(voff), "s"(rs) : "memory");
}
template <int N> __device__ __forceinline__ void ld4_wait(u32x4 (&x)[4]) {
  asm volatile("s_waitcnt vmcnt(%4)" : "+v"(x[0]), "+v"(x[1]), "+v"(x[2]), "+v"(x[3]) : "n"(N) : "memory");
}
__device__ __forceinline__ bool ld4_ok(const u32x4 (&x)[4], const Tag& t) { return gok(x[0], t) & gok(x[1], t) & gok(x[2], t) & gok(x[3], t); }
__device__ __forceinline__ f32x2 ld4_sum(const u32x4 (&x)[4]) {
  return f32x2{(gval(x[0][0]) + gval(x[1][0])) + (gval(x[2][0]) + gval(x[3][0])), (gval(x[0][2]) + gval(x[1][2])) + (gval(x[2][2]) + gval(x[3][2]))};
}
__device__ __forceinline__ float half_sum(float x) {
  auto t = __builtin_amdgcn_permlane32_swap(__float_as_uint(x), __float_as_uint(x), false, false);
  return __uint_as_float(t[0]) + __uint_as_float(t[1]);
}

#define LH_LD(a, b, c, d, e) "buffer_load_dwordx4 v[" #a ":" #b "-1], %2, %3, 0 offen sc1\n\tbuffer_load_dwordx4 v[" #b ":" #c "-1], %2, %3, 0 offen offset:1024 sc1\n\t" \
  "buffer_load_dwordx4 v[" #c ":" #d "-1], %2, %3, 0 offen offset:2048 sc1\n\tbuffer_load_dwordx4 v[" #d ":" #e "-1], %2, %3, 0 offen offset:3072 sc1\n\t"
#define LH_X(t, B, i, s) "v_bitop3_b32 v" #t ", v[" #B "+" #i "], v[" #B "+" #i "+2], " s " bitop3:0x7e\n\t"
#define LH_CK(B, L) "s_waitcnt vmcnt(8)\n\t" LH_X(112, B, 0, "%4") LH_X(113, B, 4, "%4") LH_X(114, B, 8, "%4") LH_X(115, B, 12, "%4") \
  "v_or3_b32 v112, v112, v113, v114\n\t" LH_X(116, B, 1, "%5") "v_or_b32 v112, v112, v115\n\t" LH_X(117, B, 5, "%5") "v_lshrrev_b32 v112, 24, v112\n\t" \
  LH_X(113, B, 9, "%5") LH_X(114, B, 13, "%5") "v_or3_b32 v112, v112, v116, v117\n\tv_or3_b32 v112, v112, v113, v114\n\t" \
  "v_cmp_ne_u32 vcc, 0, v112\n\ts_cbranch_vccz " L "\n\t"
#define LH_S(t, B, i) "v_lshlrev_b32 v" #t ", 8, v[" #B "+" #i "]\n\t"
#define LH_TK(B) LH_S(112, B, 0) LH_S(113, B, 2) LH_S(114, B, 4) LH_S(115, B, 6) LH_S(116, B, 8) LH_S(117, B, 10) LH_S(118, B, 12) LH_S(119, B, 14)
#define LH_STR2(x) #x
#define LH_STR(x) LH_STR2(x)
__device__ __forceinline__ void last_hop(rsrc_t rws, int off, const Tag& tg, unsigned red, unsigned cnt, rsrc_t rout, int ooff, int eoff) {
  unsigned n, c;
  asm volatile(
      "s_mov_b32 %0, 0x10000\n\t" LH_LD(64, 68, 72, 76, 80) "s_sleep " LH_STR(PGAP2) "\n\t" LH_LD(80, 84, 88, 92, 96) "s_sleep " LH_STR(PGAP2) "\n\t" LH_LD(96, 100, 104, 108, 112)
      "1:\n\t" LH_CK(64, "2f") LH_LD(64, 68, 72, 76, 80) LH_CK(80, "3f") LH_LD(80, 84, 88, 92, 96) LH_CK(96, "4f") LH_LD(96, 100, 104, 108, 112)
      "s_sub_u32 %0, %0, 1\n\ts_cbranch_scc0 1b\n\ts_waitcnt vmcnt(0)\n\t"
      "2:\n\t" LH_TK(64) "s_branch 5f\n\t3:\n\t" LH_TK(80) "s_branch 5f\n\t4:\n\t" LH_TK(96)
      "5:\n\ts_mov_b32 %0, 0x100000\n\t"
      "v_pk_add_f32 v[112:113], v[112:113], v[114:115]\n\tv_pk_add_f32 v[116:117], v[116:117], v[118:119]\n\t"
      "6:\n\tds_read_b32 v114, %7\n\tds_read_b64 v[118:119], %6 offset:256\n\tds_read_b64 v[120:121], %6 offset:512\n\tds_read_b64 v[122:123], %6 offset:768\n\t"
      "s_sub_u32 %0, %0, 1\n\ts_cbranch_scc1 7f\n\ts_waitcnt lgkmcnt(3)\n\tv_readfirstlane_b32 %1, v114\n\ts_cmp_lg_u32 %1, 2\n\ts_cbranch_scc1 6b\n\t"
      "7:\n\tv_pk_add_f32 v[112:113], v[112:113], v[116:117]\n\ts_nop 0\n\tv_mov_b32 v114, v112\n\tv_mov_b32 v115, v113\n\ts_nop 1\n\t"
      "v_permlane32_swap_b32 v112, v114\n\tv_permlane32_swap_b32 v113, v115\n\ts_waitcnt lgkmcnt(0)\n\t"
      "v_pk_add_f32 v[118:119], v[118:119], v[120:121]\n\tv_pk_add_f32 v[112:113], v[112:113], v[114:115]\n\tv_pk_add_f32 v[118:119], v[118:119], v[122:123]\n\ts_nop 0\n\t"
      "v_pk_add_f32 v[112:113], v[112:113], v[118:119]\n\ts_nop 0\n\tv_cvt_pk_bf16_f32 v112, v112, v113\n\ts_nop 0\n\t"
      "buffer_store_dword v112, %9, %8, 0 offen\n\tbuffer_store_dwordx2 %10, %11, %3, 0 offen sc1"
      : "=&s"(n), "=&s"(c)
      : "v"(off), "s"(rws), "s"(tg.tl), "s"(tg.th), "v"(red), "v"(cnt), "s"(rout), "v"(ooff), "v"(tg.e), "v"(eoff)
      : "memory", "vcc", "scc", "v64", "v65", "v66", "v67", "v68", "v69", "v70", "v71", "v72", "v73", "v74", "v75", "v76", "v77", "v78", "v79", "v80", "v81", "v82",
        "v83", "v84", "v85", "v86", "v87", "v88", "v89", "v90", "v91", "v92", "v93", "v94", "v95", "v96", "v97", "v98", "v99", "v100", "v101", "v102", "v103",
        "v104", "v105", "v106", "v107", "v108", "v109", "v110", "v111", "v112", "v113", "v114", "v115", "v116", "v117", "v118", "v119", "v120", "v121",
        "v122", "v123");
}

__global__ __launch_bounds__(512, 2) void k_all(const char* a0_, const char* a1_, const char* a2_, const char* a3_, const char* a4_, const char* a5_,
                                                const char* a6_, const char* a7_, const char* a8_, const char* a9_, const char* a10_, char* a11_, unsigned* a12_) {
  u32x16 ka; u32x8 kb_; u32x2 kc_;
  asm volatile("s_load_dwordx16 %0, %3, 0x0\n\ts_load_dwordx8 %1, %3, 0x40\n\ts_load_dwordx2 %2, %3, 0x60"
               : "=&s"(ka), "=&s"(kb_), "=&s"(kc_) : "s"(__builtin_amdgcn_kernarg_segment_ptr()));
  __shared__ __attribute__((aligned(16))) char smem[LDS_BYTES];
  LAS char* lds = (LAS char*)smem;
  LAS char* scr = lds + LDS_SCR;
  LAS unsigned* ctl = (LAS unsigned*)scr;
  const int tid = threadIdx.x, lane = tid & 63, w = __builtin_amdgcn_readfirstlane(tid >> 6);
  const int blk = blockIdx.x, head = blk >> 3, prod = (blk + head) & 7;
  const int sub = (0x54372106u >> (4 * prod)) & 7;
  unsigned stv = 0;
  STAMP(0);
  unsigned cpf = 0;
  {
    u64 pc;
    asm volatile("s_getpc_b64 %0" : "=s"(pc));
    cpf = __builtin_amdgcn_raw_buffer_load_b32(mkrs((const GAS char*)(pc & ~255ull), CPF), w == 7 - (CPFW) ? head * 2048 + lane * 32 : OOB, 0, 0);
    SB();
  }
  {
    unsigned c = 16 + 4 * (DCH * sub + tid);
    asm volatile("" : "+v"(c));
    c = tid < 8 + DCH ? c : ~0u;
    c = tid < 8 ? 0u : c;
    ctl[tid] = tid == 4 ? DCH : c;
  }
  const int l16 = lane & 15, grp = lane >> 4;
  const int b4 = head & 3, cntoff = WS_CNT + head * CSTR;
  const int bm = (b4 + 1 + (sub >= 3 ? sub - 3 : sub)) & 3;
  const size_t base = (size_t)(bm * 32 + head) << 20;
  const int so = ((sub >= 3 ? 8 : 0) + w) * 4096;
  int v0 = l16 * 16 + grp * (sub < 6 ? 256 : 131072) + (sub < 6 ? so : w * 524288 + head * 256);
  unsigned boff = sub < 6 ? (unsigned)base : 0u, bsz = sub < 6 ? 1u << 20 : 4u << 20;
  int s0[WF0];
#pragma unroll
  for (int i = 0; i < WF0; i++) {
    s0[i] = ((i & 3) << (sub < 6 ? 10 : 13)) + (i >> 2) * (sub < 6 ? 65536 : 32768);
    asm volatile("" : "+s"(s0[i]));
  }
  asm volatile("" : "+v"(v0), "+s"(boff), "+s"(bsz));
  SB();
  asm volatile("s_waitcnt lgkmcnt(0)" : "+s"(ka), "+s"(kb_), "+s"(kc_));
  auto ptr = [](unsigned lo, unsigned hi) { return (GAS char*)(((u64)hi << 32) | lo); };
  GAS char *const xA = ptr(ka[0], ka[1]), *const ckv = ptr(ka[2], ka[3]), *const wqA = ptr(ka[4], ka[5]), *const wuk = ptr(ka[6], ka[7]),
           *const wuv = ptr(ka[8], ka[9]), *const woA = ptr(ka[10], ka[11]), *const xB = ptr(ka[12], ka[13]), *const kc = ptr(ka[14], ka[15]),
           *const vc = ptr(kb_[0], kb_[1]), *const wqB = ptr(kb_[2], kb_[3]), *const woB = ptr(kb_[4], kb_[5]), *const ws = ptr(kb_[6], kb_[7]),
           *const out = ptr(kc_[0], kc_[1]);
  const u64 e0 = *(const __attribute__((address_space(4))) u64*)(u64)(ws + WS_EPOCH);
  const rsrc_t r0 = mkrs((sub < 6 ? kc : sub == 6 ? wqB : wqA) + boff, bsz);
  SB();
  u32x4 ra[16];
#pragma unroll
  for (int i = 0; i < WF0; i++) ra[i] = ldnt(r0, v0, s0[i]);
  SB();
  u32x4 xp[2][2];
  if (sub >= 6) {
    const GAS char* x = sub == 6 ? xB : xA;
#pragma unroll
    for (int s = 0; s < 2; s++) {
      const GAS u32x4* xs = (const GAS u32x4*)(x + (2 * s + (l16 >> 3)) * 1024 + (64 * w + 16 * grp) * 2);
      xp[s][0] = xs[0]; xp[s][1] = xs[1];
    }
#pragma unroll
    for (int i = WF0; i < 16; i++) ra[i] = ldnt(r0, v0, i * 8192);
    asm volatile("" ::: "memory");
    SB();
  }
  const rsrc_t rws = mkrs(ws, WS_END);
  unsigned mask[4];
#pragma unroll
  for (int v = 0; v < 4; v++) mask[v] = (((lane & 7) >> 1) == v) ? ((lane & 1) ? 0xffff0000u : 0x0000ffffu) : 0u;
  const size_t base4 = (size_t)(b4 * 32 + head) << 20;
  const rsrc_t rk4 = mkrs(kc + base4, 1u << 20), rv4 = mkrs(vc + base4, 1u << 20);
  const int voff = lane * 16;
  LAS char* qst = scr + 1024 + w * 512;
  const int qo = WS_QB + head * 2048 + (lane < 32 ? bm : b4) * 512 + (lane & 31) * 16;
  auto qwait = [&](u32x4 g, const Tag& tg) {
    if (!__all(qok(g, tg))) {
      for (unsigned spins = 0;; spins++) {
        asm volatile("" ::: "memory");
        g = ldsc(rws, qo, 0);
        if (__all(qok(g, tg)) || spins > SPIN_CAP) break;
        __builtin_amdgcn_s_sleep(2);
      }
    }
    *(LAS u32x2*)(qst + lane * 8) = u32x2{g[0], g[2]};
  };
  unsigned cur;

  if (__builtin_expect(sub < 6, 1)) {
    const rsrc_t rv = mkrs(vc + base, 1u << 20);
    u32x4 rb[16], g;
#pragma unroll
    for (int i = WF0; i < 32; i++) {
      if (i == WF0) asm volatile("s_barrier" ::: "memory");
      if (i < NF0) vmwait<WF0 - 1>(); else vmwait<WFILL - 1>();
      if (i == 32 - WFILL) g = ldsc(rws, qo, 0);
      const u32x4 v = ldslot(r0, voff, so, i);
      if (i < 16) ra[i] = v; else rb[i - 16] = v;
      SB();
    }
    STAMP(12);
    asm volatile("" :: "v"(cpf));
    qwait(g, mktag(e0));
    const u32x4 q = *(LAS u32x4*)(qst + l16 * 16);
    STAMP(6);
    float sc[4] = {0.f, 0.f, 0.f, 0.f};
#pragma unroll 1
    for (int it = 0; it < 2; it++) {
      const rsrc_t rn = it ? rv : r0;
      sc[2] = sc[0]; sc[3] = sc[1];
      sc[1] = k_step(ra, q, l16, rn, voff, so + (it ? 3 : 2) * CH);
      sc[0] = k_step(rb, q, l16, rn, voff, so + (it ? 2 : 3) * CH);
    }
    STAMP(3);
    const float mx = xrow_max(row_max(fmaxf(fmaxf(sc[0], sc[1]), fmaxf(sc[2], sc[3]))));
    float f, l = 0.f;
    const float M = exp_ref(mx, f);
    unsigned pk[4];
#pragma unroll
    for (int k = 0; k < 4; k++) {
      const float p = fexp2(fmaf(sc[k] - mx, LOG2E, f));
      pk[k] = cvtpk(p, p);
      l += bf_lo(pk[k]);
    }
    l = xrow_sum(row_sum(l));
    cur = 8 * sub + w;
    f32x4 acc = {0.f, 0.f, 0.f, 0.f};
    v_step<0, 1>(ra, pk[0], mask, acc, r0, rv, voff, so + CH);
    v_step<0, 1>(rb, pk[1], mask, acc, r0, rv, voff, so);
    v_step<0, 2>(ra, pk[2], mask, acc, rk4, rv4, voff, cur * 4096);
    v_step<0, 0>(rb, pk[3], mask, acc, r0, rv, voff, 0);
    Acc A;
    A.o[0] = lane < 32 ? acc[0] : acc[2]; A.o[1] = lane < 32 ? acc[1] : acc[3];
    A.l = l; A.R = M;
    b_merge(false, scr + 8192, ctl + 1, w, lane, A, rws, WS_RS + (head * 8 + sub) * 1024, WS_HS + (head * 8 + sub) * 32, mktag(e0), stv);
  } else {
  {
    if ((ABL & 1) && sub == 7) { FLUSH(); return; }
    __builtin_amdgcn_raw_buffer_store_b32(48 + 24 * DCH, rws, (sub == 6 && tid == 0) ? cntoff : OOB, 0, AUX_SC1);
    {
      const rsrc_t rk = mkrs(wuk, sub == 7 ? 4u << 20 : 0u);
#pragma unroll
      for (int i = 0; i < 16; i++)
        __builtin_amdgcn_raw_ptr_buffer_load_lds(rk, (LAS void*)(lds + w * 16384 + i * 1024), 16,
                                                 (64 * w + grp) * 8192 + head * 256 + ((l16 ^ (4 * (i & 3) + grp)) * 16), i * 4 * 8192, 0, AUX_NT);
    }
    SB();
    f32x4 acc[2] = {{0.f, 0.f, 0.f, 0.f}, {0.f, 0.f, 0.f, 0.f}};
    diag_gemv(ra, xp, mask, acc);
    LAS float* red = (LAS float*)(scr + 8192);
#pragma unroll
    for (int s = 0; s < 2; s++) *(LAS f32x4*)(red + w * 512 + (2 * s + (grp >> 1)) * 128 + 8 * l16 + 4 * (grp & 1)) = acc[s];
    asm volatile("" :: "v"(cpf));
    __builtin_amdgcn_s_waitcnt(0x0f70);
    drain();
    bar();
    if (tid < 128) {
      f32x4 a = {0.f, 0.f, 0.f, 0.f};
#pragma unroll
      for (int k = 0; k < 8; k++) a += *(LAS f32x4*)(red + k * 512 + 4 * tid);
      const unsigned p0 = cvtpk(a[0], a[1]), p1 = cvtpk(a[2], a[3]);
      const Tag tg = mktag(e0);
      stsc(u32x4{p0, tg.th, p1, tg.th}, rws, sub == 6 ? WS_QB + head * 2048 + tid * 16 : OOB, 0);
      *(LAS u32x2*)(scr + 1024 + tid * 8) = u32x2{p0, p1};
    }
    STAMP(1);
  }

  if (sub == 6) {
    if (ABL & 1) { drain(); FLUSH(); return; }
    const int b = head >> 3;
    const rsrc_t rc = mkrs(ckv + (size_t)head * 524288, 524288);
    const int dvo = (lane >> 3) * 1024 + w * 128 + (((lane & 7) ^ fz(lane >> 3)) * 16);
    const unsigned dlo = (unsigned)(uintptr_t)lds + w * 8192;
#pragma unroll
    for (int j = 0; j < 16; j++) dma1(rc, dvo, j * 8192, dlo + (j >> 3) * 65536 + (j & 7) * 1024, 0u);
    SB();
    const Tag tg = mktag(e0);
    bar();
    {
      u32x4 t[8];
      unsigned miss = 15;
      __builtin_amdgcn_s_sleep(QS0);
      for (unsigned spins = 0;; spins++) {
        asm volatile("" ::: "memory");
#pragma unroll
        for (int i = 0; i < 4; i++)
          if (miss >> i & 1) {
            t[2 * i] = ldsc(rws, WS_QABS + (b * 32 + w) * 2048 + lane * 16, i * 16384);
            t[2 * i + 1] = ldsc(rws, WS_QABS + (b * 32 + w) * 2048 + lane * 16 + 1024, i * 16384);
          }
        SB();
#pragma unroll
        for (int i = 0; i < 4; i++) if (__all(qok(t[2 * i], tg) & qok(t[2 * i + 1], tg))) miss &= ~(1u << i);
        if (!miss || spins > SPIN_CAP) break;
        __builtin_amdgcn_s_sleep(QSL);
      }
      STAMP(2);
#pragma unroll
      for (int i = 0; i < 8; i++) {
        const int row = 8 * (i >> 1) + w;
        *(LAS u32x2*)(scr + row * 1024 + (((32 * (i & 1) + (lane >> 1)) ^ swz(row)) * 16) + (lane & 1) * 8) = u32x2{t[i][0], t[i][2]};
      }
    }
    bar();
    const int kb = w & 3, hb = w >> 2;
    u32x4 qf[16];
    {
      const int row = 16 * hb + l16;
#pragma unroll
      for (int ks = 0; ks < 16; ks++) qf[ks] = *(LAS u32x4*)(scr + row * 1024 + (((4 * ks + grp) ^ swz(row)) * 16));
    }
    STAMP(3);
    LAS float* mx = (LAS float*)scr;
    LAS char* pl = scr + 1024;
    LAS float* lr = (LAS float*)(scr + 8192);
    f32x16 o0, o1;
#pragma unroll
    for (int j = 0; j < 16; j++) { o0[j] = 0.f; o1[j] = 0.f; }
    float m1 = -INFINITY, m2 = -INFINITY, l1 = 0.f;
    const int h1 = 16 * hb + l16, h2 = lane & 31, hh = lane >> 5;
    const int arow = 16 * kb + l16;
    const int tq = (lane & 15) >> 2, tp = lane & 3, tblk = (lane >> 4) & 1;
    const int qa0 = arow * 128 + ((grp ^ fz(arow)) * 16), qa1 = qa0 ^ 64;
    const int pr = 8 * hh + tq, pc = 2 * tblk + (tp >> 1);
    const int pa0 = w * 8192 + pr * 128 + ((pc ^ fz(pr)) * 16) + 8 * (tp & 1), pa1 = w * 8192 + (pr + 4) * 128 + ((pc ^ fz(pr + 4)) * 16) + 8 * (tp & 1);
#pragma unroll 1
    for (int t = 0; t < 8; t++) {
      if (t < 7) asm volatile("s_waitcnt vmcnt(8)" ::: "memory"); else asm volatile("s_waitcnt vmcnt(0)" ::: "memory");
      bar();
      LAS char* img = lds + (t & 1) * 65536;
      f32x4 s0 = {0.f, 0.f, 0.f, 0.f}, s1 = s0;
      u32x4 ka[16];
#pragma unroll
      for (int ks = 0; ks < 16; ks++) ka[ks] = *(LAS u32x4*)(img + ((ks & 1) ? qa1 : qa0) + (ks >> 1) * 8192);
      SB();
#pragma unroll
      for (int k2 = 0; k2 < 8; k2++) {
        s0 = mfma16(ka[2 * k2], qf[2 * k2], s0);
        s1 = mfma16(ka[2 * k2 + 1], qf[2 * k2 + 1], s1);
      }
      SB();
      u32x2 ta[4][4];
#pragma unroll
      for (int ks = 0; ks < 4; ks++) {
        ta[ks][0] = trread(img + pa0 + ks * 2048); ta[ks][1] = trread(img + pa1 + ks * 2048);
        ta[ks][2] = trread(img + (pa0 ^ 64) + ks * 2048); ta[ks][3] = trread(img + (pa1 ^ 64) + ks * 2048);
      }
      SB();
      float x[4];
#pragma unroll
      for (int j = 0; j < 4; j++) x[j] = rbf(rbf(s0[j] + s1[j]) * SCALE);
      const float lm = xrow_max(fmaxf(fmaxf(x[0], x[1]), fmaxf(x[2], x[3])));
      if (grp == 0) mx[h1 * 4 + kb] = lm;
      LAS u32x2* pw = (LAS u32x2*)(pl + h1 * 144 + (16 * kb + 4 * grp) * 2);
      u32x2 pp = {cvtpk(fexp2((x[0] - m1) * LOG2E), fexp2((x[1] - m1) * LOG2E)), cvtpk(fexp2((x[2] - m1) * LOG2E), fexp2((x[3] - m1) * LOG2E))};
      *pw = pp;
      bar();
      const f32x4 ma = *(LAS f32x4*)(mx + h1 * 4), mb = *(LAS f32x4*)(mx + h2 * 4);
      u32x4 pb[4];
#pragma unroll
      for (int ks = 0; ks < 4; ks++) pb[ks] = *(LAS u32x4*)(pl + h2 * 144 + (16 * ks + 8 * hh) * 2);
      const float n1 = fmaxf(m1, fmaxf(fmaxf(ma[0], ma[1]), fmaxf(ma[2], ma[3])));
      const float n2 = fmaxf(m2, fmaxf(fmaxf(mb[0], mb[1]), fmaxf(mb[2], mb[3])));
      float al1 = fexp2((m1 - n1) * LOG2E), al2 = fexp2((m2 - n2) * LOG2E);
      asm volatile("" : "+v"(pb[0]), "+v"(pb[1]), "+v"(pb[2]), "+v"(pb[3]));
      if (__any(n2 - m2 > 40.f)) {
        bar();
        pp = u32x2{cvtpk(fexp2((x[0] - n1) * LOG2E), fexp2((x[1] - n1) * LOG2E)), cvtpk(fexp2((x[2] - n1) * LOG2E), fexp2((x[3] - n1) * LOG2E))};
        *pw = pp;
        l1 *= al1;
#pragma unroll
        for (int j = 0; j < 16; j++) { o0[j] *= al2; o1[j] *= al2; }
        al1 = al2 = 1.f;
        bar();
#pragma unroll
        for (int ks = 0; ks < 4; ks++) pb[ks] = *(LAS u32x4*)(pl + h2 * 144 + (16 * ks + 8 * hh) * 2);
      }
      m1 = n1; m2 = n2;
      const int nso = t < 6 ? (t + 2) * 65536 : OOB;
#pragma unroll
      for (int ks = 0; ks < 4; ks++) {
        o0 = mfma32(u32x4{ta[ks][0][0], ta[ks][0][1], ta[ks][1][0], ta[ks][1][1]}, pb[ks], o0);
        o1 = mfma32(u32x4{ta[ks][2][0], ta[ks][2][1], ta[ks][3][0], ta[ks][3][1]}, pb[ks], o1);
        SB();
        if (t < 6) {
          dma1(rc, dvo, nso + 2 * ks * 8192, dlo + (t & 1) * 65536 + 2 * ks * 1024, 0u);
          dma1(rc, dvo, nso + (2 * ks + 1) * 8192, dlo + (t & 1) * 65536 + (2 * ks + 1) * 1024, 0u);
        }
        SB();
      }
      l1 = (l1 + ((bf_lo(pp[0]) + bf_hi(pp[0])) + (bf_lo(pp[1]) + bf_hi(pp[1])))) * al1;
#pragma unroll
      for (int j = 0; j < 16; j++) { o0[j] *= al2; o1[j] *= al2; }
    }
    bar();
    STAMP(4);
    {
      float am = 0.f;
#pragma unroll
      for (int j = 0; j < 16; j++) am = fmaxf(am, fmaxf(fabsf(o0[j]), fabsf(o1[j])));
      auto sw = __builtin_amdgcn_permlane32_swap(__float_as_uint(am), __float_as_uint(am), false, false);
      am = fmaxf(__uint_as_float(sw[0]), __uint_as_float(sw[1]));
      const int e = max(__builtin_amdgcn_frexp_expf(am), -127);
      LAS char* tp = lds + h2 * 1040 + (64 * w + 4 * hh) * 2;
      auto pkn = [&](float a, float b) { return __builtin_bit_cast(unsigned, __builtin_amdgcn_cvt_pknorm_i16(__builtin_ldexpf(a, -e), __builtin_ldexpf(b, -e))); };
#pragma unroll
      for (int j4 = 0; j4 < 4; j4++) {
        *(LAS u32x2*)(tp + j4 * 16) = u32x2{pkn(o0[4 * j4], o0[4 * j4 + 1]), pkn(o0[4 * j4 + 2], o0[4 * j4 + 3])};
        *(LAS u32x2*)(tp + 64 + j4 * 16) = u32x2{pkn(o1[4 * j4], o1[4 * j4 + 1]), pkn(o1[4 * j4 + 2], o1[4 * j4 + 3])};
      }
      if (hh == 0) *(LAS unsigned char*)(scr + 9216 + h2 * 8 + w) = (unsigned char)(e + 127);
      const float ls = xrow_sum(l1);
      if (grp == 0) lr[kb * 32 + h1] = ls;
      bar();
#pragma unroll
      for (int i = 0; i < 4; i++) {
        const u32x4 v = *(LAS u32x4*)(lds + (4 * w + i) * 1040 + lane * 16);
        stsc(v, rws, WS_PO + (head * 32 + 4 * w) * 1024 + lane * 16, i * 1024);
      }
      if (tid < 32) {
        const u32x2 eb = *(LAS u32x2*)(scr + 9216 + tid * 8);
        const float l = (lr[tid] + lr[32 + tid]) + (lr[64 + tid] + lr[96 + tid]);
        stsc(u32x4{__float_as_uint(m2), __float_as_uint(l), eb[0], eb[1]}, rws, WS_PML + (head * 32 + tid) * 16, 0);
      }
      drain();
      bar();
      if (tid == 0) __hip_atomic_store((GAS u64*)(ws + WS_F2 + head * 128), tg.e, RLX_AGENT);
      if (w == 0) {
        const unsigned t = __builtin_amdgcn_readfirstlane(__builtin_amdgcn_raw_ptr_buffer_atomic_add_i32(8 + 4 * DCH, rws, lane == 0 ? cntoff : OOB, 0, AUX_SC1));
        const unsigned bs = t + 8 + 4 * (lane - 8);
        ctl[lane] = lane == 3 ? t : lane == 4 ? DCH : lane == 5 ? (t + 8 + 4 * DCH >= NUNIT ? 0x10000u : 0u) : lane < 8 ? 0u : (lane < 8 + DCH && bs < NUNIT) ? bs : ~0u;
        if (lane < 32) ctl[64 + lane] = ~0u;
      }
      bar();
    }
    STAMP(5);
    const u32x4 g = ldsc(rws, qo, 0);
    cur = __builtin_amdgcn_readfirstlane(ctl[3]) + w;
#pragma unroll
    for (int i = 0; i < 8; i++) ra[i] = ldslot(i < 4 ? rk4 : rv4, voff, unit_off(cur), i & 3);
    SB();
    qwait(g, mktag(e0));
  } else {
    const Tag tg = mktag(e0);
    bar();
    STAMP(2);
    {
      u32x4 bf[4];
#pragma unroll
      for (int ks = 0; ks < 4; ks++) bf[ks] = *(LAS u32x4*)(scr + 1024 + (l16 & 3) * 256 + ks * 64 + grp * 16);
      LAS char* tt = scr + 2048 + w * 512;
#pragma unroll
      for (int rt = 0; rt < 4; rt++) {
        f32x4 acc = {0.f, 0.f, 0.f, 0.f};
#pragma unroll
        for (int ks = 0; ks < 4; ks++) acc = mfma16(*(LAS u32x4*)(lds + (64 * w + 16 * rt + l16) * 256 + (((4 * ks + grp) ^ l16) * 16)), bf[ks], acc);
        if (l16 < 4) *(LAS u32x2*)(tt + l16 * 128 + (16 * rt + 4 * grp) * 2) = u32x2{cvtpk(acc[0], acc[1]), cvtpk(acc[2], acc[3])};
      }
      asm volatile("s_waitcnt lgkmcnt(0)" ::: "memory");
      const u32x2 v = *(LAS u32x2*)(tt + lane * 8);
      stsc(u32x4{v[0], tg.th, v[1], tg.th}, rws, WS_QABS + (grp * 32 + head) * 2048 + w * 256 + l16 * 16, 0);
    }
    bar();
    STAMP(3);
    u32x4 wf[4][4], wr[16];
    {
      const rsrc_t ro = mkrs(woA + (size_t)head * 131072, 131072);
      dma_rows<16>(ro, 0, lds, 16 * w, lane);
      const rsrc_t rv = mkrs(wuv, 4u << 20);
      const int voff = (64 * w + 16 * grp) * 8192 + head * 256 + l16 * 16;
#pragma unroll
      for (int i = 0; i < 16; i++) wr[i] = ldnt(rv, voff, i * 8192);
      SB();
      asm volatile("s_waitcnt vmcnt(16)" ::: "memory");
      bar();
      wo_frags(lds, w, lane, wf);
      bar();
      const rsrc_t rb = mkrs(woB + (size_t)head * 131072, 131072);
      dma_rows<16>(rb, 0, lds, 16 * w, lane);
    }
    STAMP(4);
    if (w == 0) poll32(ws, WS_F2, lane, tg);
    bar();
    STAMP(5);
    {
      const int b = tid >> 7, c4 = tid & 127, es = (c4 >> 4) & 3;
      u32x2 po[8]; u32x4 ml[8];
#pragma unroll
      for (int s = 0; s < 8; s++) {
        ml[s] = ldsc(rws, WS_PML + ((8 * b + s) * 32 + head) * 16, 0);
        po[s] = __builtin_amdgcn_raw_buffer_load_b64(rws, WS_PO + ((8 * b + s) * 32 + head) * 1024 + c4 * 8, 0, AUX_SC1);
      }
      float M = -INFINITY;
#pragma unroll
      for (int s = 0; s < 8; s++) M = fmaxf(M, __uint_as_float(ml[s][0]));
      float L = 0.f;
      f32x4 a = {0.f, 0.f, 0.f, 0.f};
#pragma unroll
      for (int s = 0; s < 8; s++) {
        const float f = fexp2((__uint_as_float(ml[s][0]) - M) * LOG2E);
        L = fmaf(f, __uint_as_float(ml[s][1]), L);
        const int e = (int)(((w & 1) ? ml[s][3] : ml[s][2]) >> (8 * es)) & 255;
        const float g = __builtin_ldexpf(f, e - 142);
        a += g * f32x4{(float)(short)po[s][0], (float)((int)po[s][0] >> 16), (float)(short)po[s][1], (float)((int)po[s][1] >> 16)};
      }
      const float inv = (32768.f / 32767.f) / L;
      const u32x2 v = {cvtpk(a[0] * inv, a[1] * inv), cvtpk(a[2] * inv, a[3] * inv)};
      *(LAS u32x2*)(scr + b * 1024 + c4 * 8) = v;
    }
    bar();
    {
      u32x4 xp[2][2];
#pragma unroll
      for (int s = 0; s < 2; s++) {
        LAS u32x4* xs = (LAS u32x4*)(scr + (2 * s + (l16 >> 3)) * 1024 + (64 * w + 16 * grp) * 2);
        xp[s][0] = xs[0]; xp[s][1] = xs[1];
      }
      f32x4 acc[2] = {{0.f, 0.f, 0.f, 0.f}, {0.f, 0.f, 0.f, 0.f}};
      diag_gemv(wr, xp, mask, acc);
      LAS float* red = (LAS float*)(scr + 8192);
#pragma unroll
      for (int s = 0; s < 2; s++) *(LAS f32x4*)(red + w * 512 + (2 * s + (grp >> 1)) * 128 + 8 * l16 + 4 * (grp & 1)) = acc[s];
      bar();
      if (tid < 256) {
        float a = 0.f, b = 0.f;
#pragma unroll
        for (int k = 0; k < 8; k++) { const f32x2 v = *(LAS f32x2*)(red + k * 512 + 2 * tid); a += v[0]; b += v[1]; }
        ((LAS unsigned*)(scr + 4096))[tid] = cvtpk(a, b);
      }
      bar();
    }
    const f32x4 yA = wo_calc(wf, scr + 4096, lane);
    STAMP(6);
    drain();
    bar();
    wo_frags(lds, w, lane, wf);
    const int hoff = WS_HD + head * 256 + (l16 < 2 * NSLOT ? l16 : 0) * 16;
    if (w < 3) {
      const int hoff = WS_HS + (head * 8 + w + (lane & 2 ? 3 : 0)) * 32 + (lane & 1) * 16;
      const int off = WS_RS + (head * 8 + w) * 1024 + lane * 16;
      u32x4 hd, g0, g1;
      for (unsigned spins = 0;; spins++) {
        asm volatile("" ::: "memory");
        hd = ldsc(rws, hoff, 0); g0 = ldsc(rws, off, 0); g1 = ldsc(rws, off, 3072);
        if (__all(hok(hd, tg) & gok(g0, tg) & gok(g1, tg)) || spins > SPIN_CAP) break;
        __builtin_amdgcn_s_sleep(16);
      }
      const float R0 = rdl(hd[0], 0), R1 = rdl(hd[0], 2), R = fmaxf(R0, R1);
      const int d0 = edif(R0, R), d1 = edif(R1, R);
      const float lf = (float)hval(hd);
      const float inv = 1.f / (__builtin_ldexpf(rdl(__float_as_uint(lf), 1), d0) + __builtin_ldexpf(rdl(__float_as_uint(lf), 3), d1));
      const float c0 = (__builtin_ldexpf(gval(g0[0]), d0) + __builtin_ldexpf(gval(g1[0]), d1)) * inv;
      const float c1 = (__builtin_ldexpf(gval(g0[2]), d0) + __builtin_ldexpf(gval(g1[2]), d1)) * inv;
      *(LAS unsigned*)(scr + 5120 + ((b4 + 1 + w) & 3) * 256 + rec_col(lane) * 2) = cvtpk(c0, c1);
      STAMP(7);
    }
    if (w == 4 && !EARLY) {
      for (unsigned spins = 0;; spins++) {
        asm volatile("" ::: "memory");
        if (__any(hok(ldsc(rws, hoff, 0), tg)) || spins > SPIN_CAP) break;
        __builtin_amdgcn_s_sleep(4);
      }
    }
    bar();
    STAMP(8);
    const bool late = (lane & 3) == b4;
    const int slot = late ? 24 + (head >> 2) : 3 * (head >> 2) + ((head - 1 - lane) & 3);
    y_store(yA, w, lane, head, rws, WS_Y, !(ABL & 2), tg);
    y_store(wo_calc(wf, scr + 5120, lane), w, lane, slot, rws, WS_Y + 524288, !late && !(ABL & 2), tg);
    if ((w >> 1) == 2) {
      LAS char* pb = scr + 8192 + (w & 1) * 8192;
      const int off = WS_RD + head * 16384 + (w & 1) * 1024 + lane * 16;
      if (EARLY) {
        for (unsigned spins = 0;; spins++) {
          asm volatile("" ::: "memory");
          if (__any(hok(ldsc(rws, hoff, 0), tg)) || spins > SPIN_CAP) break;
          __builtin_amdgcn_s_sleep(4);
        }
      }
      u32x4 g[8];
      unsigned miss = 0x80 | ((1 << NSLOT) - 1);
      for (unsigned spins = 0;; spins++) {
        asm volatile("" ::: "memory");
        g[7] = ldsc(rws, hoff, 0);
#pragma unroll
        for (int s = 0; s < NSLOT; s++) g[s] = ldsc(rws, off, s * 2048);
        bool ok = hok(g[7], tg);
#pragma unroll
        for (int s = 0; s < NSLOT; s++) ok &= hok(g[s], tg);
        if (__all(ok) || spins > SPIN_CAP) break;
      }
      float R = -INFINITY;
#pragma unroll
      for (int s = 0; s < NSLOT; s++) R = fmaxf(R, rdl(g[7][0], 2 * s));
      const unsigned sw = p2hi(__uint_as_float(g[7][0]), R);
      double S[2] = {0.0, 0.0}, L[2] = {0.0, 0.0};
#pragma unroll
      for (int s = 0; s < NSLOT; s++) {
        const double sc = mkd(0u, rdu(sw, 2 * s));
        S[s & 1] = __builtin_fma(hval(g[s]), sc, S[s & 1]);
        L[s & 1] = __builtin_fma(mkd(rdu(g[7][0], 2 * s + 1), rdu(g[7][2], 2 * s + 1)), sc, L[s & 1]);
      }
      const float cx = (float)((S[0] + S[1]) * (double)__builtin_amdgcn_rcpf((float)(L[0] + L[1])));
      ((LAS unsigned short*)(scr + 5120))[b4 * 128 + rec_col(lane) + (w & 1)] = (unsigned short)cvtpk(cx, cx);
    }
    bar();
    STAMP(9);
    y_store(wo_calc(wf, scr + 5120, lane), w, lane, slot, rws, WS_Y + 524288, late, tg);
    drain();
    STAMP(10);
    FLUSH();
    return;
  }
  }
  {
    STAMP(7);
    const u32x4 q4 = *(LAS u32x4*)(qst + 256 + l16 * 16);
    Acc A;
    A.o[0] = A.o[1] = A.l = 0.0; A.R = -INFINITY;
    b_units(ra, rk4, rv4, cur, q4, lane, mask, ctl, rws, cntoff, A);
    STAMP(8);
    const Tag tg = mktag(e0);
    b_merge(true, scr + 8192 + 8704, ctl + 2, w, lane, A, rws, WS_RD + (head * 8 + sub) * 2048, WS_HD + (head * 8 + sub) * 32, tg, stv);
    SB();
    asm volatile("" :: "v"(ra[0]), "v"(ra[1]), "v"(ra[2]), "v"(ra[3]), "v"(ra[4]), "v"(ra[5]), "v"(ra[6]), "v"(ra[7]));
    STAMP(9);
    if ((ABL & 2) && sub == 1 && w && w < 4 && lane == 0) lds_add(ctl + 88, 1u);
    if (sub < 2 && w < 4 && !(ABL & 1) && (!(ABL & 2) || (sub == 1 && w == 0))) {
      const int off = WS_Y + sub * 524288 + head * 16384 + ((w + 3) & 3) * 4096 + lane * 16;
      LAS f32x2* red = (LAS f32x2*)(scr + 6144);
      u32x4 x[1][4];
      if (w || FPH0) for (unsigned spins = 0;; spins++) {
        ld4(x[0], rws, off);
        ld4_wait<0>(x[0]);
        if (__any(gok(x[0][0], tg) | gok(x[0][1], tg) | gok(x[0][2], tg) | gok(x[0][3], tg)) || spins > SPIN_CAP) break;
        __builtin_amdgcn_s_sleep(FSL);
      }
      STAMP(4);
      if (w) {
        for (unsigned spins = 0; !__all(ld4_ok(x[0], tg)) && spins < SPIN_CAP; spins++) { ld4(x[0], rws, off); ld4_wait<0>(x[0]); }
        f32x2 r = ld4_sum(x[0]);
        r = f32x2{half_sum(r[0]), half_sum(r[1])};
        if (lane < 32) red[w * 32 + lane] = r;
        asm volatile("s_waitcnt lgkmcnt(0)" ::: "memory");
        if (lane == 0) lds_add(ctl + 88, 1u);
      } else {
        last_hop(rws, off, tg, (unsigned)(uintptr_t)(red + (lane & 31)), (unsigned)(uintptr_t)(ctl + 88), mkrs(out, 8192),
                 lane < 32 ? (head >> 3) * 2048 + sub * 1024 + (head & 7) * 128 + l16 * 8 + grp * 4 : OOB,
                 head == 0 && sub == 1 && lane == 0 ? WS_EPOCH : OOB);
      }
    }
  }
  FLUSH();
}

__global__ void k_probe(const unsigned* p, unsigned* sink) {
  unsigned off = 0, acc = 0;
  for (int i = 0; i < 64; i++) {
    unsigned v = __builtin_nontemporal_load(p + off);
    asm volatile("v_and_b32 %0, 0, %0" : "+v"(v));
    off += 256 + v; acc += v;
  }
  if (acc == 0x12345) sink[0] = acc;
}
extern "C" void kernel_launch(void* const* d_in, const int* in_sizes, int n_in, void* d_out, int out_size, void* d_ws, size_t ws_size,
                              hipStream_t stream) {
  k_probe<<<1, 1, 0, stream>>>((const unsigned*)((const char*)d_ws + 67108864), (unsigned*)((char*)d_ws + (48 << 20)));
  k_all<<<256, 512, 0, stream>>>((const char*)d_in[0], (const char*)d_in[1], (const char*)d_in[2], (const char*)d_in[3], (const char*)d_in[4],
                                 (const char*)d_in[5], (const char*)d_in[6], (const char*)d_in[7], (const char*)d_in[8], (const char*)d_in[9],
                                 (const char*)d_in[10], (char*)d_ws, (unsigned*)d_out);
}
```

```cpp
#include <hip/hip_runtime.h>
#include <stdint.h>

typedef __attribute__((ext_vector_type(4))) unsigned u32x4;
typedef __attribute__((ext_vector_type(2))) unsigned u32x2;
typedef __attribute__((ext_vector_type(8))) unsigned u32x8;
typedef __attribute__((ext_vector_type(16))) unsigned u32x16;
typedef __attribute__((ext_vector_type(4))) float f32x4;
typedef __attribute__((ext_vector_type(2))) float f32x2;
typedef __attribute__((ext_vector_type(2))) double f64x2;
typedef __attribute__((ext_vector_type(16))) float f32x16;
typedef __attribute__((ext_vector_type(8))) __bf16 bf16x8;
typedef __attribute__((ext_vector_type(2))) __bf16 bf16x2;
typedef __attribute__((ext_vector_type(4))) short s16x4;
typedef unsigned long long u64;
typedef __amdgpu_buffer_rsrc_t rsrc_t;
#define LAS __attribute__((address_space(3)))
#define GAS __attribute__((address_space(1)))

#define CPF 36096
#define PAT "5c10p2ba43"
constexpr int WTH = 6;
constexpr int DCH = 3;
constexpr int NXA = 8, NXB = 8;
constexpr int NREL = 24;

constexpr int AUX_NT = 2, AUX_SC1 = 16, OOB = 0x40000000;
constexpr float SCALE = 0.08838834764831845f;
constexpr float LOG2E = 1.4426950408889634f;
constexpr unsigned SPIN_CAP = 1u << 17;
constexpr int NSLOT = 7, NUNIT = 256;

constexpr int WS_EPOCH = 0;
constexpr int CSTR    = 4352;
constexpr int WS_CNT  = 8192;
constexpr int WS_QB   = WS_CNT + 32 * CSTR;
constexpr int WS_QABS = WS_QB + 32 * 2048;
constexpr int WS_PML  = WS_QABS + 4 * 32 * 2048;
constexpr int WS_PO   = WS_PML + 32 * 32 * 16;
constexpr int WS_RS   = WS_PO + 32 * 32 * 1024;
constexpr int WS_Y    = WS_RS + 32 * 16 * 1024;
constexpr int WS_YB   = WS_Y + 4 * 8 * 32 * 512;
constexpr int WS_END  = WS_YB + 4 * 8 * 4 * 512;

constexpr int LDS_BYTES = 163840;
constexpr int LDS_SCR = 131072;

__device__ __forceinline__ unsigned cvtpk(float lo, float hi) {
  f32x2 v = {lo, hi};
  return __builtin_bit_cast(unsigned, __builtin_convertvector(v, bf16x2));
}
__device__ __forceinline__ float bf_lo(unsigned u) { return __uint_as_float(u << 16); }
__device__ __forceinline__ float bf_hi(unsigned u) { return __uint_as_float(u & 0xffff0000u); }
__device__ __forceinline__ float rbf(float x) { return __uint_as_float(cvtpk(x, x) << 16); }
__device__ __forceinline__ float dot2(unsigned k, unsigned q, float acc) {
  return __builtin_amdgcn_fdot2_f32_bf16(__builtin_bit_cast(bf16x2, k), __builtin_bit_cast(bf16x2, q), acc, false);
}
template <int CTRL> __device__ __forceinline__ float dpp(float x) {
  return __builtin_bit_cast(float, __builtin_amdgcn_mov_dpp(__builtin_bit_cast(int, x), CTRL, 0xf, 0xf, true));
}
template <int CTRL> __device__ __forceinline__ unsigned dppu(unsigned x) {
  return (unsigned)__builtin_amdgcn_mov_dpp((int)x, CTRL, 0xf, 0xf, true);
}
constexpr int XOR1 = 0xB1, XOR2 = 0x4E, XOR7 = 0x141, XOR8 = 0x128, ROR4 = 0x124;
__device__ __forceinline__ float xrow_max(float x) {
  auto s = __builtin_amdgcn_permlane16_swap(__float_as_uint(x), __float_as_uint(x), false, false);
  x = fmaxf(__uint_as_float(s[0]), __uint_as_float(s[1]));
  auto t = __builtin_amdgcn_permlane32_swap(__float_as_uint(x), __float_as_uint(x), false, false);
  return fmaxf(__uint_as_float(t[0]), __uint_as_float(t[1]));
}
__device__ __forceinline__ float xrow_sum(float x) {
  auto s = __builtin_amdgcn_permlane16_swap(__float_as_uint(x), __float_as_uint(x), false, false);
  x = __uint_as_float(s[0]) + __uint_as_float(s[1]);
  auto t = __builtin_amdgcn_permlane32_swap(__float_as_uint(x), __float_as_uint(x), false, false);
  return __uint_as_float(t[0]) + __uint_as_float(t[1]);
}
__device__ __forceinline__ float row_max(float x) {
  x = fmaxf(x, dpp<XOR8>(x)); x = fmaxf(x, dpp<ROR4>(x)); x = fmaxf(x, dpp<XOR2>(x)); x = fmaxf(x, dpp<XOR1>(x));
  return x;
}
__device__ __forceinline__ float row_sum(float x) {
  x += dpp<XOR8>(x); x += dpp<ROR4>(x); x += dpp<XOR2>(x); x += dpp<XOR1>(x);
  return x;
}
__device__ __forceinline__ float fexp2(float x) { return __builtin_amdgcn_exp2f(x); }
__device__ __forceinline__ f32x4 mfma16(u32x4 a, u32x4 b, f32x4 c) {
  return __builtin_amdgcn_mfma_f32_16x16x32_bf16(__builtin_bit_cast(bf16x8, a), __builtin_bit_cast(bf16x8, b), c, 0, 0, 0);
}
__device__ __forceinline__ f32x16 mfma32(u32x4 a, u32x4 b, f32x16 c) {
  return __builtin_amdgcn_mfma_f32_32x32x16_bf16(__builtin_bit_cast(bf16x8, a), __builtin_bit_cast(bf16x8, b), c, 0, 0, 0);
}
__device__ __forceinline__ rsrc_t mkrs(const GAS char* p, unsigned bytes) {
  return __builtin_amdgcn_make_buffer_rsrc((void*)p, 0, bytes, 0x00020000);
}
__device__ __forceinline__ void bar() { asm volatile("s_waitcnt lgkmcnt(0)\n\ts_barrier" ::: "memory"); }
__device__ __forceinline__ void drain() { asm volatile("s_waitcnt vmcnt(0)" ::: "memory"); }
#define SB() __builtin_amdgcn_sched_barrier(0)
template <int N> __device__ __forceinline__ void vmwait() { asm volatile("s_waitcnt vmcnt(%0)" :: "n"(N) : "memory"); }
__device__ __forceinline__ unsigned swz(unsigned row) { return ((row & 3) << 2) | ((row >> 2) & 2); }
__device__ __forceinline__ u32x2 trread(LAS char* p) {
  return __builtin_bit_cast(u32x2, __builtin_amdgcn_ds_read_tr16_b64_v4i16((LAS s16x4*)p));
}

struct Tag { unsigned tl, th, lo, rb; u64 e; };
__device__ __forceinline__ Tag mktag(u64 e0) {
  Tag t;
  asm volatile("" : "+s"(e0));
  t.e = e0 * 6364136223846793005ull + 1442695040888963407ull;
  t.th = (unsigned)(t.e >> 32);
  t.lo = (unsigned)t.e;
  t.tl = t.lo & 0xff000000u;
  t.rb = ((t.lo >> 9) & 255u) | (~t.lo & 256u);
  return t;
}
__device__ __forceinline__ unsigned gpack(float v, const Tag& t) { return ((__float_as_uint(v) + 0x80u) >> 8) | t.tl; }
__device__ __forceinline__ u32x4 gpair(float a, float b, const Tag& t) { return u32x4{gpack(a, t), t.th, gpack(b, t), t.th}; }
__device__ __forceinline__ bool gok(u32x4 g, const Tag& t) {
  return ((((g[0] ^ t.tl) | (g[2] ^ t.tl)) >> 24) | (g[1] ^ t.th) | (g[3] ^ t.th)) == 0u;
}
__device__ __forceinline__ float gval(unsigned lo) { return __uint_as_float(lo << 8); }
__device__ __forceinline__ bool qok(u32x4 g, const Tag& t) { return ((g[1] ^ t.th) | (g[3] ^ t.th)) == 0u; }
__device__ __forceinline__ double mkd(unsigned lo, unsigned hi) { return __builtin_bit_cast(double, u32x2{lo, hi}); }
__device__ __forceinline__ u32x4 rpack(double a, double b, double l, float r, const Tag& t) {
  const u32x2 ua = __builtin_bit_cast(u32x2, a), ub = __builtin_bit_cast(u32x2, b), ul = __builtin_bit_cast(u32x2, l);
  const u64 ml = ((u64)(unsigned)__builtin_amdgcn_readfirstlane(ul[1]) << 32) | (unsigned)__builtin_amdgcn_readfirstlane(ul[0]);
  const u64 mr = (unsigned)__builtin_amdgcn_readfirstlane(__float_as_uint(r));
  const unsigned ta = t.lo & 511u, tb = t.rb;
  return u32x4{(ua[0] & ~1023u) | (__builtin_amdgcn_inverse_ballot_w64(ml) ? ta | 512u : ta), ua[1],
               (ub[0] & ~1023u) | (__builtin_amdgcn_inverse_ballot_w64(mr) ? tb | 512u : tb), ub[1]};
}
__device__ __forceinline__ bool rok(u32x4 g, const Tag& t) { return (((g[0] ^ t.lo) | (g[2] ^ t.rb)) & 511u) == 0u; }
constexpr unsigned PMASK = 0x00030003u;
__device__ __forceinline__ unsigned ptag(const Tag& t, int k) {
  const unsigned n = k == 0 ? t.lo : k == 3 ? t.lo >> 10 : ((t.lo >> (1 + 3 * k)) & 7u) | (~t.lo & 8u);
  return (n & 3u) | ((n & 12u) << 14);
}
__device__ __forceinline__ unsigned ltag(const Tag& t) { return ((t.lo >> 16) & 127u) | (~t.lo & 128u); }
__device__ __forceinline__ bool mok(u32x4 g, const Tag& t) { return (((g[0] ^ t.lo) & 0xffffu) | ((g[1] ^ ltag(t)) & 255u)) == 0u; }
__device__ __forceinline__ unsigned bfi(unsigned m, unsigned a, unsigned b) { return (a & m) | (b & ~m); }
__device__ __forceinline__ double rec_a(u32x4 g) { return mkd(g[0] & ~1023u, g[1]); }
__device__ __forceinline__ double rec_b(u32x4 g) { return mkd(g[2] & ~1023u, g[3]); }
__device__ __forceinline__ double rec_l(u32x4 g) { return __builtin_bit_cast(double, __builtin_amdgcn_ballot_w64((g[0] & 512u) != 0u)); }
__device__ __forceinline__ float rec_r(u32x4 g) { return __uint_as_float((unsigned)__builtin_amdgcn_ballot_w64((g[2] & 512u) != 0u)); }
__device__ __forceinline__ int edif(float a, float b) {
  int r;
  asm("v_cvt_i32_f32 %0, %1" : "=v"(r) : "v"(a - b));
  return r;
}
__device__ __forceinline__ unsigned p2hi(float a, float b) { return (unsigned)max(edif(a, b) + 1023, 0) << 20; }
__device__ __forceinline__ unsigned rdu(unsigned v, int l) { return __builtin_amdgcn_readlane(v, l); }

__device__ __forceinline__ void diag_gemv(const u32x4 (&wr)[16], const u32x4 (&xp)[2][2], const unsigned (&mask)[4], f32x4 (&acc)[2]) {
#pragma unroll
  for (int i = 0; i < 16; i++) {
#pragma unroll
    for (int s = 0; s < 2; s++) {
      const unsigned src = xp[s][i >> 3][(i >> 1) & 3];
      const unsigned dup = __builtin_amdgcn_perm(src, src, (i & 1) ? 0x03020302u : 0x01000100u);
      const u32x4 a = {dup & mask[0], dup & mask[1], dup & mask[2], dup & mask[3]};
      acc[s] = mfma16(a, wr[i], acc[s]);
    }
  }
}

constexpr int bitpos(char c) { for (int i = 0; i < 10; i++) if (PAT[i] == c) return 10 + i; return 31; }
constexpr int goff(int t) { int o = 0; for (int j = 0; j < 6; j++) o |= ((t >> j) & 1) << bitpos('0' + j); return o; }
struct Acc { double o[2]; double l; float R; };
__device__ __forceinline__ u32x4 ldnt(rsrc_t r, int voff, int so) { return __builtin_amdgcn_raw_buffer_load_b128(r, voff, so, AUX_NT); }
__device__ __forceinline__ u32x4 ldslot(rsrc_t r, int voff, int so, int i) { return ldnt(r, voff + goff(i & 3), so ^ goff(i & ~3)); }
__device__ __forceinline__ u32x4 ldunit(rsrc_t r, int voff, int so, int i) { return ldnt(r, voff + i * 1024, so); }
__device__ __forceinline__ u32x4 ldsc(rsrc_t r, int voff, int so) { return __builtin_amdgcn_raw_buffer_load_b128(r, voff, so, AUX_SC1); }
__device__ __forceinline__ void stsc(u32x4 v, rsrc_t r, int voff, int so) { __builtin_amdgcn_raw_buffer_store_b128(v, r, voff + so, 0, AUX_SC1); }
__device__ __forceinline__ float dot8(u32x4 k, u32x4 q) { return dot2(k[3], q[3], dot2(k[2], q[2], dot2(k[1], q[1], dot2(k[0], q[0], 0.f)))); }
#define TR_STEP(HALF, CTRL, BIT) _Pragma("unroll") for (int h = 0; h < (HALF); ++h) { \
    const float t1 = x[h] + dpp<CTRL>(x[h]), t2 = x[h + (HALF)] + dpp<CTRL>(x[h + (HALF)]); x[h] = (c & (BIT)) ? t2 : t1; }
template <int N> __device__ __forceinline__ float k_step(u32x4 (&ring)[16], u32x4 q, int c, rsrc_t rn, int voff, int so, bool ext, LAS char* xp) {
  float x[16];
#pragma unroll
  for (int i = 0; i < 16; i++) {
    x[i] = dot8(ring[i], q);
    asm volatile("s_waitcnt vmcnt(%1)" : "+v"(x[i]) : "n"(WTH - 1));
    if (i >= 16 - N && ext) ring[i] = *(volatile LAS u32x4*)(xp + (i - (16 - N)) * 1024);
    else ring[i] = ldslot(rn, voff, so, i);
    SB();
  }
  TR_STEP(8, XOR8, 8) TR_STEP(4, XOR7, 4) TR_STEP(2, XOR2, 2) TR_STEP(1, XOR1, 1)
  return rbf(rbf(x[0]) * SCALE);
}
template <int BC> __device__ __forceinline__ void pv(u32x4 v, unsigned pk, const unsigned (&mask)[4], f32x4& acc) {
  const unsigned d = dppu<0x150 + BC>(pk);
  acc = mfma16(u32x4{d & mask[0], d & mask[1], d & mask[2], d & mask[3]}, v, acc);
}
template <int I, int MODE> __device__ __forceinline__ void v_step(u32x4 (&ring)[16], unsigned pk, const unsigned (&mask)[4], f32x4& acc,
                                                                  rsrc_t rk, rsrc_t rv, int voff, int so) {
  pv<I>(ring[I], pk, mask, acc);
  if (MODE == 1 || (MODE == 2 && I < 8)) {
    asm volatile("s_waitcnt vmcnt(%1)" : "+v"(acc) : "n"(WTH - 1));
    ring[I] = MODE == 1 ? ldslot(rv, voff, so, I) : ldunit(I < 4 ? rk : rv, voff, so, I & 3);
  } else {
    asm volatile("" : "+v"(acc));
  }
  SB();
  if constexpr (I < 15) v_step<I + 1, MODE>(ring, pk, mask, acc, rk, rv, voff, so);
}
__device__ __forceinline__ float exp_ref(float mx, float& f) {
  const float M = __builtin_ceilf(mx * LOG2E);
  f = __builtin_amdgcn_fmed3f(fmaf(mx, LOG2E, -M), -1.f, 0.f);
  return M;
}
__device__ __forceinline__ unsigned lds_inc(LAS unsigned* p, int lane) {
  unsigned t = 0, z = 0;
  asm volatile("" : "+v"(z));
  if (lane == 0) t = __hip_atomic_fetch_add(p + z, 1u, __ATOMIC_RELAXED, __HIP_MEMORY_SCOPE_WORKGROUP);
  return t;
}
struct Take { unsigned unit, pend; bool fetch; };
__device__ __forceinline__ void lds_add(LAS unsigned* p, unsigned v) { __hip_atomic_fetch_add(p, v, __ATOMIC_RELAXED, __HIP_MEMORY_SCOPE_WORKGROUP); }
__device__ __forceinline__ Take take(LAS unsigned* ctl, unsigned kv, rsrc_t rws, int cntoff, int lane) {
  const unsigned k = __builtin_amdgcn_readfirstlane(kv);
  unsigned b, s;
  for (unsigned spins = 0;; spins++) {
    const unsigned vs = ((volatile LAS unsigned*)ctl)[5], vb = ((volatile LAS unsigned*)ctl)[8 + (k >> 2)];
    s = __builtin_amdgcn_readfirstlane(vs); b = __builtin_amdgcn_readfirstlane(vb);
    if (b != ~0u || (s >= 0x10000u && (s & 0xffffu) == 0u) || spins > SPIN_CAP * 16) break;
    __builtin_amdgcn_s_sleep(1);
  }
  Take t;
  t.unit = min(b, (unsigned)NUNIT) + (k & 3);
  t.fetch = (k & 3) == 0 && b < NUNIT && s < 0x10000u;
  const bool go = lane == 0 && t.fetch;
  if (go) lds_add(ctl + 5, 1u);
  asm volatile("s_waitcnt lgkmcnt(0)" ::: "memory");
  t.pend = __builtin_amdgcn_raw_ptr_buffer_atomic_add_i32(4, rws, go ? cntoff : OOB, 0, AUX_SC1);
  SB();
  return t;
}
__device__ __forceinline__ void give(LAS unsigned* ctl, const Take& t, int lane) {
  if (t.fetch && lane == 0) {
    unsigned z = 0;
    asm volatile("" : "+v"(z));
    if (t.pend < NUNIT) {
      const unsigned i = __hip_atomic_fetch_add(ctl + 4 + z, 1u, __ATOMIC_RELAXED, __HIP_MEMORY_SCOPE_WORKGROUP);
      ((volatile LAS unsigned*)ctl)[8 + i] = t.pend;
    }
    asm volatile("" ::: "memory");
    lds_add(ctl + 5 + z, t.pend >= NUNIT - 4 ? 0xffffu : ~0u);
  }
}
__device__ __forceinline__ int unit_off(unsigned t) { return (int)min(t, (unsigned)NUNIT) * 4096; }

__device__ __forceinline__ void b_units(u32x4 (&ra)[16], rsrc_t rk, rsrc_t rv, unsigned cur, u32x4 q, int lane, const unsigned (&mask)[4],
                                        LAS unsigned* ctl, rsrc_t rws, int cntoff, Acc& A) {
  const int c = lane & 15, voff = lane * 16;
  unsigned nxt;
  {
    const Take t = take(ctl, lds_inc(ctl, lane), rws, cntoff, lane);
    give(ctl, t, lane);
    nxt = t.unit;
  }
#pragma unroll 1
  while (cur < NUNIT) {
    const int so = unit_off(nxt);
    float x[4];
#pragma unroll
    for (int i = 0; i < 4; i++) {
      x[i] = dot8(ra[i], q);
      asm volatile("" : "+v"(x[i]));
      ra[i] = ldunit(rk, voff, so, i);
      SB();
    }
    const unsigned kv = lds_inc(ctl, lane);
    TR_STEP(2, XOR8, 8) TR_STEP(1, XOR7, 4)
    x[0] += dpp<XOR2>(x[0]); x[0] += dpp<XOR1>(x[0]);
    const float s = rbf(rbf(x[0]) * SCALE);
    float mx = fmaxf(s, dpp<XOR8>(s));
    mx = xrow_max(fmaxf(mx, dpp<ROR4>(mx)));
    float f;
    const float M = exp_ref(mx, f);
    const float p = fexp2(fmaf(s - mx, LOG2E, f));
    unsigned pk = cvtpk(p, p);
    asm volatile("" : "+v"(pk));
    const Take t = take(ctl, kv, rws, cntoff, lane);
    float ls = bf_lo(pk);
    ls += dpp<XOR8>(ls);
    ls = xrow_sum(ls + dpp<ROR4>(ls));
    const float Rn = fmaxf(A.R, M);
    const double s0 = mkd(0u, p2hi(A.R, Rn)), s1 = mkd(0u, p2hi(M, Rn));
    A.o[0] *= s0; A.o[1] *= s0;
    A.l = __builtin_fma((double)ls, s1, A.l * s0);
    A.R = Rn;
    asm volatile("" : "+v"(A.o[0]), "+v"(A.o[1]), "+v"(A.l));
    SB();
    f32x4 acc = {0.f, 0.f, 0.f, 0.f};
    pv<0>(ra[4], pk, mask, acc);  asm volatile("" : "+v"(acc)); ra[4] = ldunit(rv, voff, so, 0); SB();
    pv<4>(ra[5], pk, mask, acc);  asm volatile("" : "+v"(acc)); ra[5] = ldunit(rv, voff, so, 1); SB();
    pv<8>(ra[6], pk, mask, acc);  asm volatile("" : "+v"(acc)); ra[6] = ldunit(rv, voff, so, 2); SB();
    pv<12>(ra[7], pk, mask, acc); asm volatile("" : "+v"(acc)); ra[7] = ldunit(rv, voff, so, 3); SB();
    A.o[0] = __builtin_fma((double)(lane < 32 ? acc[0] : acc[2]), s1, A.o[0]);
    A.o[1] = __builtin_fma((double)(lane < 32 ? acc[1] : acc[3]), s1, A.o[1]);
    asm volatile("" : "+v"(A.o[0]), "+v"(A.o[1]));
    SB();
    give(ctl, t, lane);
    cur = nxt; nxt = t.unit;
  }
}
#undef TR_STEP
__device__ __forceinline__ int rec_col(int lane) { return 8 * (lane & 15) + 4 * ((lane >> 4) & 1) + 2 * (lane >> 5); }
__device__ __forceinline__ void b_merge(LAS char* mrg, LAS unsigned* arrive, int w, int lane, const Acc& A, rsrc_t rws, int roff, const Tag& tg) {
  *(volatile LAS f64x2*)(mrg + w * 1024 + lane * 16) = f64x2{A.o[0], A.o[1]};
  *(volatile LAS double*)(mrg + 8192 + w * 8) = A.l;
  *(volatile LAS float*)(mrg + 8256 + w * 4) = A.R;
  asm volatile("" ::: "memory");
  const unsigned nv = lds_inc(arrive, lane);
  asm volatile("" ::: "memory");
  const f32x4 Ra = *(volatile LAS f32x4*)(mrg + 8256), Rb = *(volatile LAS f32x4*)(mrg + 8272);
  const float Rw = *(volatile LAS float*)(mrg + 8256 + (lane & 7) * 4);
  f64x2 o[8], lw[4];
#pragma unroll
  for (int i = 0; i < 8; i++) o[i] = *(volatile LAS f64x2*)(mrg + i * 1024 + lane * 16);
#pragma unroll
  for (int i = 0; i < 4; i++) lw[i] = *(volatile LAS f64x2*)(mrg + 8192 + i * 16);
  SB();
  if (__builtin_amdgcn_readfirstlane(nv) == 7) {
    const float R = fmaxf(fmaxf(fmaxf(Ra[0], Ra[1]), fmaxf(Ra[2], Ra[3])), fmaxf(fmaxf(Rb[0], Rb[1]), fmaxf(Rb[2], Rb[3])));
    const unsigned sw = p2hi(Rw, R);
    double v0[2] = {0.0, 0.0}, v1[2] = {0.0, 0.0}, L[2] = {0.0, 0.0};
#pragma unroll
    for (int i = 0; i < 8; i++) {
      const double sc = mkd(0u, rdu(sw, i));
      v0[i & 1] = __builtin_fma(o[i][0], sc, v0[i & 1]); v1[i & 1] = __builtin_fma(o[i][1], sc, v1[i & 1]);
      L[i & 1] = __builtin_fma(lw[i >> 1][i & 1], sc, L[i & 1]);
    }
    stsc(rpack(v0[0] + v0[1], v1[0] + v1[1], L[0] + L[1], R, tg), rws, roff + lane * 16, 0);
  }
}

__device__ __forceinline__ unsigned fz(unsigned row) { return ((row & 2) << 1) | ((row >> 1) & 2); }
__device__ __forceinline__ void dma1(rsrc_t rs, int voff, int so, unsigned ldsaddr) {
  unsigned keep;
  asm volatile("s_mov_b32 %0, m0\n\ts_mov_b32 m0, %1\n\ts_nop 0\n\tbuffer_load_dwordx4 %2, %3, %4 offen nt lds\n\ts_mov_b32 m0, %0"
               : "=&s"(keep) : "s"(ldsaddr), "v"(voff), "s"(rs), "s"(so) : "memory");
}
template <int NR>
__device__ __forceinline__ void dma_rows(rsrc_t rs, int srcoff, LAS char* img, int r0, int lane) {
#pragma unroll
  for (int u = 0; u < NR; u++) {
    const int r = r0 + u;
    unsigned keep;
    asm volatile("s_mov_b32 %0, m0\n\ts_mov_b32 m0, %1\n\ts_nop 0\n\tbuffer_load_dwordx4 %2, %3, %4 offen nt lds\n\ts_mov_b32 m0, %0"
                 : "=&s"(keep) : "s"((unsigned)(uintptr_t)img + r * 1024), "v"((lane ^ swz(r)) * 16), "s"(rs), "s"(srcoff + r * 1024) : "memory");
  }
  SB();
}

__device__ __forceinline__ void wo_frags(LAS char* img, int w, int lane, u32x4 (&wf)[4][4]) {
  const int g = lane >> 4, q = (lane & 15) >> 2, p = lane & 3;
#pragma unroll
  for (int ks = 0; ks < 4; ks++)
#pragma unroll
    for (int tl = 0; tl < 4; tl++) {
      const int ch = 8 * w + 2 * tl + (p >> 1);
      const int r0 = 32 * ks + 8 * g + q, r1 = r0 + 4;
      const u32x2 a = trread(img + r0 * 1024 + ((ch ^ swz(r0)) * 16) + 8 * (p & 1));
      const u32x2 b = trread(img + r1 * 1024 + ((ch ^ swz(r1)) * 16) + 8 * (p & 1));
      wf[tl][ks] = u32x4{a[0], a[1], b[0], b[1]};
    }
}
__device__ __forceinline__ f32x4 wo_calc(const u32x4 (&wf)[4][4], LAS char* cb, int lane) {
  const int n = lane & 15, grp = lane >> 4;
  u32x4 bf[4];
#pragma unroll
  for (int ks = 0; ks < 4; ks++) bf[ks] = *(LAS u32x4*)(cb + (n & 3) * 256 + ks * 64 + grp * 16);
  f32x4 acc[4];
#pragma unroll
  for (int tl = 0; tl < 4; tl++) acc[tl] = f32x4{0.f, 0.f, 0.f, 0.f};
#pragma unroll
  for (int ks = 0; ks < 4; ks++)
#pragma unroll
    for (int tl = 0; tl < 4; tl++) acc[tl] = mfma16(wf[tl][ks], bf[ks], acc[tl]);
  const f32x4 lo = (n & 4) ? acc[1] : acc[0], hi = (n & 4) ? acc[3] : acc[2];
  return (n & 8) ? hi : lo;
}
__device__ __forceinline__ void y_store(f32x4 y, int w, int lane, int slot, rsrc_t rws, int yoff, const Tag& tg) {
  const int off = yoff + (((lane & 3) * 8 + w) * 32 + slot) * 512 + ((lane & 12) + (lane >> 4)) * 16;
  stsc(gpair(y[0], y[1], tg), rws, off, 0);
  stsc(gpair(y[2], y[3], tg), rws, off, 256);
}
template <int K> __device__ __forceinline__ u32x4 bc4(unsigned p) { return u32x4{dppu<0x150 + K>(p), dppu<0x151 + K>(p), dppu<0x152 + K>(p), dppu<0x153 + K>(p)}; }
__device__ __forceinline__ void proj(unsigned pr, const u32x4 (&wf)[4][4], LAS char* row, int lane) {
  const u32x4 bf[4] = {bc4<0>(pr), bc4<4>(pr), bc4<8>(pr), bc4<12>(pr)};
  f32x4 acc[4];
#pragma unroll
  for (int tl = 0; tl < 4; tl++) acc[tl] = f32x4{0.f, 0.f, 0.f, 0.f};
#pragma unroll
  for (int ks = 0; ks < 4; ks++)
#pragma unroll
    for (int tl = 0; tl < 4; tl++) acc[tl] = mfma16(wf[tl][ks], bf[ks], acc[tl]);
  const f32x4 lo = (lane & 1) ? acc[1] : acc[0], hi = (lane & 1) ? acc[3] : acc[2];
  if ((lane & 12) == 0) *(volatile LAS f32x4*)(row + (lane & 3) * 64 + (lane >> 4) * 16) = (lane & 2) ? hi : lo;
}
template <int N> __device__ __forceinline__ f32x2 rows_sum(LAS char* p) {
  f32x2 v[N];
#pragma unroll
  for (int i = 0; i < N; i++) v[i] = *(volatile LAS f32x2*)(p + i * 256);
#pragma unroll
  for (int st = 1; st < N; st *= 2)
#pragma unroll
    for (int i = 0; i + st < N; i += 2 * st) v[i] += v[i + st];
  return v[0];
}
__device__ __forceinline__ float half_sum(float x) {
  auto t = __builtin_amdgcn_permlane32_swap(__float_as_uint(x), __float_as_uint(x), false, false);
  return __uint_as_float(t[0]) + __uint_as_float(t[1]);
}

__global__ __launch_bounds__(512, 2) void k_all(const char* a0_, const char* a1_, const char* a2_, const char* a3_, const char* a4_, const char* a5_,
                                                const char* a6_, const char* a7_, const char* a8_, const char* a9_, const char* a10_, char* a11_, unsigned* a12_) {
  u32x16 ka; u32x8 kb_; u32x2 kc_;
  asm volatile("s_load_dwordx16 %0, %3, 0x0\n\ts_load_dwordx8 %1, %3, 0x40\n\ts_load_dwordx2 %2, %3, 0x60"
               : "=&s"(ka), "=&s"(kb_), "=&s"(kc_) : "s"(__builtin_amdgcn_kernarg_segment_ptr()));
  __shared__ __attribute__((aligned(16))) char smem[LDS_BYTES];
  LAS char* lds = (LAS char*)smem;
  LAS char* scr = lds + LDS_SCR;
  LAS unsigned* ctl = (LAS unsigned*)scr;
  const int tid = threadIdx.x, lane = tid & 63, w = __builtin_amdgcn_readfirstlane(tid >> 6);
  const int blk = blockIdx.x, head = blk >> 3, prod = (blk + head) & 7;
  const int sub = (0x54372106u >> (4 * prod)) & 7;
  unsigned cpf = 0;
  {
    u64 pc;
    asm volatile("s_getpc_b64 %0" : "=s"(pc));
    cpf = __builtin_amdgcn_raw_buffer_load_b32(mkrs((const GAS char*)(pc & ~255ull), CPF), w == 7 ? head * 2048 + lane * 32 : OOB, 0, 0);
    SB();
  }
  {
    unsigned c = 16 + 4 * (DCH * sub + tid);
    asm volatile("" : "+v"(c));
    c = tid < 8 + DCH ? c : ~0u;
    c = tid < 8 ? 0u : c;
    ctl[tid] = tid == 4 ? DCH : c;
  }
  const int l16 = lane & 15, grp = lane >> 4;
  const int b4 = head & 3, cntoff = WS_CNT + head * CSTR;
  const int bm = (b4 + 1 + (sub >= 3 ? sub - 3 : sub)) & 3;
  const size_t base = (size_t)(bm * 32 + head) << 20;
  const int so = ((w & 1) << bitpos('a')) | (((w >> 1) & 1) << bitpos('b')) | ((w >> 2) << bitpos('c')) | (sub >= 3 ? 1 << bitpos('p') : 0);
  int v0 = l16 * 16 + grp * (sub < 6 ? 256 : 131072) + (sub < 6 ? so : w * 524288 + head * 256);
  unsigned boff = sub < 6 ? (unsigned)base : 0u, bsz = sub < 6 ? 1u << 20 : 4u << 20;
  int s0[WTH];
#pragma unroll
  for (int i = 0; i < WTH; i++) {
    s0[i] = sub < 6 ? goff(i) : i * 8192;
    asm volatile("" : "+s"(s0[i]));
  }
  asm volatile("" : "+v"(v0), "+s"(boff), "+s"(bsz));
  SB();
  asm volatile("s_waitcnt lgkmcnt(0)" : "+s"(ka), "+s"(kb_), "+s"(kc_));
  auto ptr = [](unsigned lo, unsigned hi) { return (GAS char*)(((u64)hi << 32) | lo); };
  GAS char *const xA = ptr(ka[0], ka[1]), *const ckv = ptr(ka[2], ka[3]), *const wqA = ptr(ka[4], ka[5]), *const wuk = ptr(ka[6], ka[7]),
           *const wuv = ptr(ka[8], ka[9]), *const woA = ptr(ka[10], ka[11]), *const xB = ptr(ka[12], ka[13]), *const kc = ptr(ka[14], ka[15]),
           *const vc = ptr(kb_[0], kb_[1]), *const wqB = ptr(kb_[2], kb_[3]), *const woB = ptr(kb_[4], kb_[5]), *const ws = ptr(kb_[6], kb_[7]),
           *const out = ptr(kc_[0], kc_[1]);
  const u64 e0 = *(const __attribute__((address_space(4))) u64*)(u64)(ws + WS_EPOCH);
  const rsrc_t r0 = mkrs((sub < 6 ? kc : sub == 6 ? wqB : wqA) + boff, bsz);
  SB();
  u32x4 ra[16];
#pragma unroll
  for (int i = 0; i < WTH; i++) ra[i] = ldnt(r0, v0, s0[i]);
  SB();
  u32x4 xp[2][2];
  if (sub >= 6) {
    const GAS char* x = sub == 6 ? xB : xA;
#pragma unroll
    for (int s = 0; s < 2; s++) {
      const GAS u32x4* xs = (const GAS u32x4*)(x + (2 * s + (l16 >> 3)) * 1024 + (64 * w + 16 * grp) * 2);
      xp[s][0] = xs[0]; xp[s][1] = xs[1];
    }
#pragma unroll
    for (int i = WTH; i < 16; i++) ra[i] = ldnt(r0, v0, i * 8192);
    asm volatile("" ::: "memory");
    SB();
  }
  const rsrc_t rws = mkrs(ws, WS_END);
  unsigned mask[4];
#pragma unroll
  for (int v = 0; v < 4; v++) mask[v] = (((lane & 7) >> 1) == v) ? ((lane & 1) ? 0xffff0000u : 0x0000ffffu) : 0u;
  const size_t base4 = (size_t)(b4 * 32 + head) << 20;
  const rsrc_t rk4 = mkrs(kc + base4, 1u << 20), rv4 = mkrs(vc + base4, 1u << 20);
  const int voff = lane * 16;
  LAS char* qst = scr + 1024 + w * 512;
  const int qo = WS_QB + head * 2048 + (lane < 32 ? bm : b4) * 512 + (lane & 31) * 16;
  auto qwait = [&](u32x4 g, const Tag& tg) {
    if (!__all(qok(g, tg))) {
      for (unsigned spins = 0;; spins++) {
        asm volatile("" ::: "memory");
        g = ldsc(rws, qo, 0);
        if (__all(qok(g, tg)) || spins > SPIN_CAP) break;
        __builtin_amdgcn_s_sleep(2);
      }
    }
    *(LAS u32x2*)(qst + lane * 8) = u32x2{g[0], g[2]};
  };
  unsigned cur;

  if (__builtin_expect(sub < 6, 1)) {
    const rsrc_t rv = mkrs(vc + base, 1u << 20);
    u32x4 rb[16], g;
#pragma unroll
    for (int i = WTH; i < 32 + NXA + NXB; i++) {
      if (i == WTH) asm volatile("s_barrier" ::: "memory");
      vmwait<WTH - 1>();
      if (i == 32 + NXA + NXB - WTH) asm volatile("buffer_load_dwordx4 %0, %1, %2, 0 offen sc1" : "=&v"(g) : "v"(qo), "s"(rws));
      if (i < 32) {
        const u32x4 v = ldslot(r0, voff, so, i);
        if (i < 16) ra[i] = v; else rb[i - 16] = v;
      } else {
        const int j = i < 32 + NXA ? i + 16 - NXA : i + 32 - NXA - NXB;
        dma1(r0, voff + goff(j & 3), so ^ goff(j & ~3), (unsigned)(uintptr_t)lds + w * 16384 + (i - 32) * 1024);
      }
      SB();
    }
    asm volatile("s_waitcnt vmcnt(%1)" : "+v"(g) : "n"(WTH), "v"(cpf));
    qwait(g, mktag(e0));
    const u32x4 q = *(LAS u32x4*)(qst + l16 * 16);
    float sc[4] = {0.f, 0.f, 0.f, 0.f};
#pragma unroll 1
    for (int it = 0; it < 2; it++) {
      const rsrc_t rn = it ? rv : r0;
      sc[2] = sc[0]; sc[3] = sc[1];
      sc[1] = k_step<NXA>(ra, q, l16, rn, voff, so ^ (it ? goff(48) : goff(32)), it == 0, lds + w * 16384 + voff);
      sc[0] = k_step<NXB>(rb, q, l16, rn, voff, so ^ (it ? goff(32) : goff(48)), it == 0, lds + w * 16384 + NXA * 1024 + voff);
    }
    const float mx = xrow_max(row_max(fmaxf(fmaxf(sc[0], sc[1]), fmaxf(sc[2], sc[3]))));
    float f, l = 0.f;
    const float M = exp_ref(mx, f);
    unsigned pk[4];
#pragma unroll
    for (int k = 0; k < 4; k++) {
      const float p = fexp2(fmaf(sc[k] - mx, LOG2E, f));
      pk[k] = cvtpk(p, p);
      l += bf_lo(pk[k]);
    }
    l = xrow_sum(row_sum(l));
    cur = 8 * sub + w;
    f32x4 acc = {0.f, 0.f, 0.f, 0.f};
    v_step<0, 1>(ra, pk[0], mask, acc, r0, rv, voff, so ^ goff(16));
    v_step<0, 1>(rb, pk[1], mask, acc, r0, rv, voff, so);
    v_step<0, 2>(ra, pk[2], mask, acc, rk4, rv4, voff, cur * 4096);
    v_step<0, 0>(rb, pk[3], mask, acc, r0, rv, voff, 0);
    Acc A;
    A.o[0] = lane < 32 ? acc[0] : acc[2]; A.o[1] = lane < 32 ? acc[1] : acc[3];
    A.l = l; A.R = M;
    b_merge(scr + 8192, ctl + 1, w, lane, A, rws, WS_RS + (head * 16 + sub) * 1024, mktag(e0));
  } else {
  {
    __builtin_amdgcn_raw_buffer_store_b32(48 + 24 * DCH, rws, (sub == 6 && tid == 0) ? cntoff : OOB, 0, AUX_SC1);
    {
      const rsrc_t rk = mkrs(wuk, sub == 7 ? 4u << 20 : 0u);
#pragma unroll
      for (int i = 0; i < 16; i++)
        __builtin_amdgcn_raw_ptr_buffer_load_lds(rk, (LAS void*)(lds + w * 16384 + i * 1024), 16,
                                                 (64 * w + grp) * 8192 + head * 256 + ((l16 ^ (4 * (i & 3) + grp)) * 16), i * 4 * 8192, 0, AUX_NT);
    }
    SB();
    f32x4 acc[2] = {{0.f, 0.f, 0.f, 0.f}, {0.f, 0.f, 0.f, 0.f}};
    diag_gemv(ra, xp, mask, acc);
    LAS float* red = (LAS float*)(scr + 8192);
#pragma unroll
    for (int s = 0; s < 2; s++) *(LAS f32x4*)(red + w * 512 + (2 * s + (grp >> 1)) * 128 + 8 * l16 + 4 * (grp & 1)) = acc[s];
    asm volatile("" :: "v"(cpf));
    __builtin_amdgcn_s_waitcnt(0x0f70);
    drain();
    bar();
    if (tid < 128) {
      f32x4 a = {0.f, 0.f, 0.f, 0.f};
#pragma unroll
      for (int k = 0; k < 8; k++) a += *(LAS f32x4*)(red + k * 512 + 4 * tid);
      const unsigned p0 = cvtpk(a[0], a[1]), p1 = cvtpk(a[2], a[3]);
      const Tag tg = mktag(e0);
      stsc(u32x4{p0, tg.th, p1, tg.th}, rws, sub == 6 ? WS_QB + head * 2048 + tid * 16 : OOB, 0);
      *(LAS u32x2*)(scr + 1024 + tid * 8) = u32x2{p0, p1};
    }
  }

  if (sub == 6) {
    const int aid = ((head & 6) << 2) | ((head & 1) << 2) | (head >> 3), b = aid >> 3;
    const rsrc_t rc = mkrs(ckv + (size_t)aid * 524288, 524288);
    const int dvo = (lane >> 3) * 1024 + w * 128 + (((lane & 7) ^ fz(lane >> 3)) * 16);
    const unsigned dlo = (unsigned)(uintptr_t)lds + w * 8192;
#pragma unroll
    for (int j = 0; j < 16; j++) dma1(rc, dvo, j * 8192, dlo + (j >> 3) * 65536 + (j & 7) * 1024);
    SB();
    const Tag tg = mktag(e0);
    bar();
    {
      u32x4 t[8];
      unsigned miss = 15;
      __builtin_amdgcn_s_sleep(90);
      for (unsigned spins = 0;; spins++) {
        asm volatile("" ::: "memory");
#pragma unroll
        for (int i = 0; i < 4; i++)
          if (miss >> i & 1) {
            t[2 * i] = ldsc(rws, WS_QABS + (b * 32 + w) * 2048 + lane * 16, i * 16384);
            t[2 * i + 1] = ldsc(rws, WS_QABS + (b * 32 + w) * 2048 + lane * 16 + 1024, i * 16384);
          }
        SB();
#pragma unroll
        for (int i = 0; i < 4; i++) if (__all(qok(t[2 * i], tg) & qok(t[2 * i + 1], tg))) miss &= ~(1u << i);
        if (!miss || spins > SPIN_CAP) break;
        __builtin_amdgcn_s_sleep(4);
      }
#pragma unroll
      for (int i = 0; i < 8; i++) {
        const int row = 8 * (i >> 1) + w;
        *(LAS u32x2*)(scr + row * 1024 + (((32 * (i & 1) + (lane >> 1)) ^ swz(row)) * 16) + (lane & 1) * 8) = u32x2{t[i][0], t[i][2]};
      }
    }
    bar();
    const int kb = w & 3, hb = w >> 2;
    u32x4 qf[16];
    {
      const int row = 16 * hb + l16;
#pragma unroll
      for (int ks = 0; ks < 16; ks++) qf[ks] = *(LAS u32x4*)(scr + row * 1024 + (((4 * ks + grp) ^ swz(row)) * 16));
    }
    LAS float* mx = (LAS float*)scr;
    LAS char* pl = scr + 1024;
    LAS float* lr = (LAS float*)(scr + 8192);
    f32x16 o0, o1;
#pragma unroll
    for (int j = 0; j < 16; j++) { o0[j] = 0.f; o1[j] = 0.f; }
    float m1 = -INFINITY, m2 = -INFINITY, l1 = 0.f;
    const int h1 = 16 * hb + l16, h2 = lane & 31, hh = lane >> 5;
    const int arow = 16 * kb + l16;
    const int tq = (lane & 15) >> 2, tp = lane & 3, tblk = (lane >> 4) & 1;
    const int qa0 = arow * 128 + ((grp ^ fz(arow)) * 16), qa1 = qa0 ^ 64;
    const int pr = 8 * hh + tq, pc = 2 * tblk + (tp >> 1);
    const int pa0 = w * 8192 + pr * 128 + ((pc ^ fz(pr)) * 16) + 8 * (tp & 1), pa1 = w * 8192 + (pr + 4) * 128 + ((pc ^ fz(pr + 4)) * 16) + 8 * (tp & 1);
#pragma unroll 1
    for (int t = 0; t < 8; t++) {
      if (t < 7) asm volatile("s_waitcnt vmcnt(8)" ::: "memory"); else asm volatile("s_waitcnt vmcnt(0)" ::: "memory");
      bar();
      LAS char* img = lds + (t & 1) * 65536;
      f32x4 s0 = {0.f, 0.f, 0.f, 0.f}, s1 = s0;
      u32x4 ka[16];
      u32x2 ta[4][4];
      const int nso = t < 6 ? (t + 2) * 65536 : OOB;
#pragma unroll
      for (int ks = 0; ks < 16; ks++) ka[ks] = *(LAS u32x4*)(img + ((ks & 1) ? qa1 : qa0) + (ks >> 1) * 8192);
      SB();
#pragma unroll
      for (int k2 = 0; k2 < 8; k2++) {
        s0 = mfma16(ka[2 * k2], qf[2 * k2], s0);
        s1 = mfma16(ka[2 * k2 + 1], qf[2 * k2 + 1], s1);
      }
      asm volatile("" : "+v"(s0), "+v"(s1));
      SB();
#pragma unroll
      for (int ks = 0; ks < 4; ks++) {
        ta[ks][0] = trread(img + pa0 + ks * 2048); ta[ks][1] = trread(img + pa1 + ks * 2048);
        ta[ks][2] = trread(img + (pa0 ^ 64) + ks * 2048); ta[ks][3] = trread(img + (pa1 ^ 64) + ks * 2048);
      }
      SB();
      float x[4];
#pragma unroll
      for (int j = 0; j < 4; j++) x[j] = rbf(rbf(s0[j] + s1[j]) * SCALE);
      const float lm = xrow_max(fmaxf(fmaxf(x[0], x[1]), fmaxf(x[2], x[3])));
      if (grp == 0) mx[h1 * 4 + kb] = lm;
      LAS u32x2* pw = (LAS u32x2*)(pl + h1 * 144 + (16 * kb + 4 * grp) * 2);
      u32x2 pp = {cvtpk(fexp2((x[0] - m1) * LOG2E), fexp2((x[1] - m1) * LOG2E)), cvtpk(fexp2((x[2] - m1) * LOG2E), fexp2((x[3] - m1) * LOG2E))};
      *pw = pp;
      bar();
      const f32x4 ma = *(LAS f32x4*)(mx + h1 * 4), mb = *(LAS f32x4*)(mx + h2 * 4);
      u32x4 pb[4];
#pragma unroll
      for (int ks = 0; ks < 4; ks++) pb[ks] = *(LAS u32x4*)(pl + h2 * 144 + (16 * ks + 8 * hh) * 2);
      const float n1 = fmaxf(m1, fmaxf(fmaxf(ma[0], ma[1]), fmaxf(ma[2], ma[3])));
      const float n2 = fmaxf(m2, fmaxf(fmaxf(mb[0], mb[1]), fmaxf(mb[2], mb[3])));
      float al1 = fexp2((m1 - n1) * LOG2E), al2 = fexp2((m2 - n2) * LOG2E);
      asm volatile("" : "+v"(pb[0]), "+v"(pb[1]), "+v"(pb[2]), "+v"(pb[3]));
      if (__any(n2 - m2 > 40.f)) {
        bar();
        pp = u32x2{cvtpk(fexp2((x[0] - n1) * LOG2E), fexp2((x[1] - n1) * LOG2E)), cvtpk(fexp2((x[2] - n1) * LOG2E), fexp2((x[3] - n1) * LOG2E))};
        *pw = pp;
        l1 *= al1;
#pragma unroll
        for (int j = 0; j < 16; j++) { o0[j] *= al2; o1[j] *= al2; }
        al1 = al2 = 1.f;
        bar();
#pragma unroll
        for (int ks = 0; ks < 4; ks++) pb[ks] = *(LAS u32x4*)(pl + h2 * 144 + (16 * ks + 8 * hh) * 2);
      }
      m1 = n1; m2 = n2;
#pragma unroll
      for (int ks = 0; ks < 4; ks++) {
        o0 = mfma32(u32x4{ta[ks][0][0], ta[ks][0][1], ta[ks][1][0], ta[ks][1][1]}, pb[ks], o0);
        o1 = mfma32(u32x4{ta[ks][2][0], ta[ks][2][1], ta[ks][3][0], ta[ks][3][1]}, pb[ks], o1);
        SB();
        if (t < 6) {
          dma1(rc, dvo, nso + 2 * ks * 8192, dlo + (t & 1) * 65536 + 2 * ks * 1024);
          dma1(rc, dvo, nso + (2 * ks + 1) * 8192, dlo + (t & 1) * 65536 + (2 * ks + 1) * 1024);
        }
        SB();
      }
      l1 = (l1 + ((bf_lo(pp[0]) + bf_hi(pp[0])) + (bf_lo(pp[1]) + bf_hi(pp[1])))) * al1;
#pragma unroll
      for (int j = 0; j < 16; j++) { o0[j] *= al2; o1[j] *= al2; }
    }
    bar();
    {
      float am = 0.f;
#pragma unroll
      for (int j = 0; j < 16; j++) am = fmaxf(am, fmaxf(fabsf(o0[j]), fabsf(o1[j])));
      auto sw = __builtin_amdgcn_permlane32_swap(__float_as_uint(am), __float_as_uint(am), false, false);
      am = fmaxf(__uint_as_float(sw[0]), __uint_as_float(sw[1]));
      const int e = max(__builtin_amdgcn_frexp_expf(am), -127);
      LAS char* tp = lds + h2 * 1040 + (64 * w + 4 * hh) * 2;
      auto pkn = [&](float a, float b) { return __builtin_bit_cast(unsigned, __builtin_amdgcn_cvt_pknorm_i16(__builtin_ldexpf(a, -e), __builtin_ldexpf(b, -e))); };
#pragma unroll
      for (int j4 = 0; j4 < 4; j4++) {
        *(LAS u32x2*)(tp + j4 * 16) = u32x2{pkn(o0[4 * j4], o0[4 * j4 + 1]), pkn(o0[4 * j4 + 2], o0[4 * j4 + 3])};
        *(LAS u32x2*)(tp + 64 + j4 * 16) = u32x2{pkn(o1[4 * j4], o1[4 * j4 + 1]), pkn(o1[4 * j4 + 2], o1[4 * j4 + 3])};
      }
      if (hh == 0) *(LAS unsigned char*)(scr + 9216 + h2 * 8 + w) = (unsigned char)(e + 127);
      const float ls = xrow_sum(l1);
      if (grp == 0) lr[kb * 32 + h1] = ls;
      bar();
#pragma unroll
      for (int i = 0; i < 4; i++) {
        u32x4 v = *(LAS u32x4*)(lds + (4 * w + i) * 1040 + lane * 16);
#pragma unroll
        for (int k = 0; k < 4; k++) v[k] = bfi(PMASK, ptag(tg, k), v[k]);
        stsc(v, rws, WS_PO + (aid * 32 + 4 * w) * 1024 + lane * 16, i * 1024);
      }
      if (tid < 32) {
        const u32x2 eb = *(LAS u32x2*)(scr + 9216 + tid * 8);
        const float l = (lr[tid] + lr[32 + tid]) + (lr[64 + tid] + lr[96 + tid]);
        stsc(u32x4{bfi(0xffffu, tg.lo, __float_as_uint(m2)), bfi(255u, ltag(tg), __float_as_uint(l)), eb[0], eb[1]}, rws, WS_PML + (aid * 32 + tid) * 16, 0);
      }
      if (w == 0) {
        const unsigned t = __builtin_amdgcn_readfirstlane(__builtin_amdgcn_raw_ptr_buffer_atomic_add_i32(8 + 4 * DCH, rws, lane == 0 ? cntoff : OOB, 0, AUX_SC1));
        const unsigned bs = t + 8 + 4 * (lane - 8);
        ctl[lane] = lane == 3 ? t : lane == 4 ? DCH : lane == 5 ? (t + 8 + 4 * DCH >= NUNIT ? 0x10000u : 0u) : lane < 8 ? 0u : (lane < 8 + DCH && bs < NUNIT) ? bs : ~0u;
        if (lane < 32) ctl[64 + lane] = ~0u;
      }
      bar();
    }
    const u32x4 g = ldsc(rws, qo, 0);
    cur = __builtin_amdgcn_readfirstlane(ctl[3]) + w;
#pragma unroll
    for (int i = 0; i < 8; i++) ra[i] = ldunit(i < 4 ? rk4 : rv4, voff, unit_off(cur), i & 3);
    SB();
    qwait(g, mktag(e0));
  } else {
    const Tag tg = mktag(e0);
    bar();
    {
      u32x4 bf[4];
#pragma unroll
      for (int ks = 0; ks < 4; ks++) bf[ks] = *(LAS u32x4*)(scr + 1024 + (l16 & 3) * 256 + ks * 64 + grp * 16);
      LAS char* tt = scr + 2048 + w * 512;
#pragma unroll
      for (int rt = 0; rt < 4; rt++) {
        f32x4 acc = {0.f, 0.f, 0.f, 0.f};
#pragma unroll
        for (int ks = 0; ks < 4; ks++) acc = mfma16(*(LAS u32x4*)(lds + (64 * w + 16 * rt + l16) * 256 + (((4 * ks + grp) ^ l16) * 16)), bf[ks], acc);
        if (l16 < 4) *(LAS u32x2*)(tt + l16 * 128 + (16 * rt + 4 * grp) * 2) = u32x2{cvtpk(acc[0], acc[1]), cvtpk(acc[2], acc[3])};
      }
      asm volatile("s_waitcnt lgkmcnt(0)" ::: "memory");
      const u32x2 v = *(LAS u32x2*)(tt + lane * 8);
      stsc(u32x4{v[0], tg.th, v[1], tg.th}, rws, WS_QABS + (grp * 32 + head) * 2048 + w * 256 + l16 * 16, 0);
    }
    bar();
    u32x4 wf[4][4], wr[16];
    {
      const rsrc_t ro = mkrs(woA + (size_t)head * 131072, 131072);
      dma_rows<16>(ro, 0, lds, 16 * w, lane);
      const rsrc_t rv = mkrs(wuv, 4u << 20);
      const int voff = (64 * w + 16 * grp) * 8192 + head * 256 + l16 * 16;
#pragma unroll
      for (int i = 0; i < 16; i++) wr[i] = ldnt(rv, voff, i * 8192);
      SB();
      asm volatile("s_waitcnt vmcnt(16)" ::: "memory");
      bar();
      wo_frags(lds, w, lane, wf);
      bar();
      const rsrc_t rb = mkrs(woB, 4u << 20);
      const int bvo = (lane >> 3) * 1024 + (((lane & 7) ^ ((lane >> 5) << 1)) * 16);
#pragma unroll
      for (int i = 0; i < 16; i++)
        dma1(rb, (i & 1) ? bvo ^ 64 : bvo, (4 * w + b4) * 131072 + (head >> 2) * 128 + i * 8192, (unsigned)(uintptr_t)lds + w * 16384 + i * 1024);
      SB();
      if (tid < 8) ((LAS unsigned*)(scr + 24576))[tid] = 0u;
    }
    const int hw = 4 * w + b4, cc = head >> 2, roff = WS_RS + hw * 16384 + lane * 16;
    u32x4 g[6];
    unsigned missS = 63;
#pragma unroll
    for (int s = 0; s < 6; s++) asm volatile("" : "=v"(g[s]));
    if (w == 0) {
      for (unsigned spins = 0;; spins++) {
        asm volatile("" ::: "memory");
        const u32x4 v = ldsc(rws, WS_PML + ((lane & 31) * 32 + head) * 16, 0);
        if (__builtin_popcount((unsigned)__builtin_amdgcn_ballot_w64(mok(v, tg))) >= NREL || spins > SPIN_CAP) break;
        __builtin_amdgcn_s_sleep(16);
      }
    }
    bar();
    {
      const int b = w >> 1, c4 = tid & 127;
      const unsigned ta = (lane & 1) ? ptag(tg, 2) : ptag(tg, 0), tb = (lane & 1) ? ptag(tg, 3) : ptag(tg, 1);
      u32x4 ml;
      u32x2 po[8];
#pragma unroll
      for (int s = 0; s < 8; s++) asm volatile("" : "=v"(po[s]));
      unsigned missP = 255;
      for (unsigned spins = 0;; spins++) {
        asm volatile("" ::: "memory");
        ml = ldsc(rws, WS_PML + ((8 * b + (lane & 7)) * 32 + head) * 16, 0);
#pragma unroll
        for (int s = 0; s < 8; s++)
          if (missP >> s & 1) po[s] = __builtin_amdgcn_raw_buffer_load_b64(rws, WS_PO + (8 * b * 32 + head) * 1024 + c4 * 8, s * 32768, AUX_SC1);
        SB();
#pragma unroll
        for (int s = 0; s < 8; s++) if (__all((((po[s][0] ^ ta) | (po[s][1] ^ tb)) & PMASK) == 0u)) missP &= ~(1u << s);
        if ((!missP && __all(mok(ml, tg))) || spins > SPIN_CAP) break;
        __builtin_amdgcn_s_sleep(2);
      }
#pragma unroll
      for (int s = 0; s < 6; s++) g[s] = ldsc(rws, roff, s * 1024);
      SB();
      ml[0] &= 0xffff0000u; ml[1] &= ~255u;
#pragma unroll
      for (int s = 0; s < 8; s++) po[s] = u32x2{bfi(PMASK, 0x00020002u, po[s][0]), bfi(PMASK, 0x00020002u, po[s][1])};
      float M = __uint_as_float(ml[0]);
      M = fmaxf(M, dpp<XOR1>(M)); M = fmaxf(M, dpp<XOR2>(M)); M = fmaxf(M, dpp<XOR7>(M));
      const float f = fexp2((__uint_as_float(ml[0]) - M) * LOG2E);
      float L = f * __uint_as_float(ml[1]);
      L += dpp<XOR1>(L); L += dpp<XOR2>(L); L += dpp<XOR7>(L);
      const float gl = __builtin_ldexpf(f, (int)((((w & 1) ? ml[3] : ml[2]) >> (8 * grp)) & 255) - 142);
      const float gs[8] = {dpp<0x150>(gl), dpp<0x151>(gl), dpp<0x152>(gl), dpp<0x153>(gl), dpp<0x154>(gl), dpp<0x155>(gl), dpp<0x156>(gl), dpp<0x157>(gl)};
      f32x4 a = {0.f, 0.f, 0.f, 0.f};
#pragma unroll
      for (int s = 0; s < 8; s++)
        a += gs[s] * f32x4{(float)(short)po[s][0], (float)((int)po[s][0] >> 16), (float)(short)po[s][1], (float)((int)po[s][1] >> 16)};
      const float inv = (32768.f / 32767.f) / L;
      const u32x2 v = {cvtpk(a[0] * inv, a[1] * inv), cvtpk(a[2] * inv, a[3] * inv)};
      *(LAS u32x2*)(scr + b * 1024 + c4 * 8) = v;
    }
    bar();
    {
      u32x4 xp[2][2];
#pragma unroll
      for (int s = 0; s < 2; s++) {
        LAS u32x4* xs = (LAS u32x4*)(scr + (2 * s + (l16 >> 3)) * 1024 + (64 * w + 16 * grp) * 2);
        xp[s][0] = xs[0]; xp[s][1] = xs[1];
      }
      f32x4 acc[2] = {{0.f, 0.f, 0.f, 0.f}, {0.f, 0.f, 0.f, 0.f}};
      diag_gemv(wr, xp, mask, acc);
      LAS float* red = (LAS float*)(scr + 8192);
#pragma unroll
      for (int s = 0; s < 2; s++) *(LAS f32x4*)(red + w * 512 + (2 * s + (grp >> 1)) * 128 + 8 * l16 + 4 * (grp & 1)) = acc[s];
      bar();
      if (tid < 256) {
        float a = 0.f, b = 0.f;
#pragma unroll
        for (int k = 0; k < 8; k++) { const f32x2 v = *(LAS f32x2*)(red + k * 512 + 2 * tid); a += v[0]; b += v[1]; }
        ((LAS unsigned*)(scr + 4096))[tid] = cvtpk(a, b);
      }
      bar();
    }
    const f32x4 yA = wo_calc(wf, scr + 4096, lane);
    drain();
    y_store(yA, w, lane, head, rws, WS_Y, tg);
    {
      const int q = l16 >> 2;
      LAS char* img = lds + w * 16384 + (8 * (q >> 1) + 4 * (grp & 1) + 2 * (grp >> 1) + (q & 1)) * 128 + 8 * (lane & 3);
      const int fl = (grp & 1) | (q & 2);
#pragma unroll
      for (int ks = 0; ks < 4; ks++)
#pragma unroll
        for (int tl = 0; tl < 4; tl++) {
          const u32x2 a = trread(img + (tl ^ fl) * 32 + ks * 4096), b = trread(img + (tl ^ fl) * 32 + ks * 4096 + 2048);
          wf[tl][ks] = u32x4{a[0], a[1], b[0], b[1]};
        }
    }
    const rsrc_t rout = mkrs(out, 8192);
    LAS unsigned* cnt = (LAS unsigned*)(scr + 24576);
    LAS char* const re = scr + 8192;
    LAS char* const rA = scr + 8192 + 6144;
    LAS char* const rl = scr + 8192 + 8192;
    const int ofa = WS_Y + (b4 * 8 + cc) * 16384 + w * 2048 + lane * 16;
    const int ofb = WS_YB + ((b4 * 8 + cc) * 4 + ((b4 + 1 + w) & 3)) * 512 + (lane & 31) * 16;
    unsigned todo = w < 3 ? 7u : 3u, missL = (1u << NSLOT) - 1;
    u32x4 x0, x1, xb, p[NSLOT];
    asm volatile("" : "=v"(x0), "=v"(x1), "=v"(xb));
#pragma unroll
    for (int s = 0; s < NSLOT; s++) asm volatile("" : "=v"(p[s]));
    double S0 = 0.0, S1 = 0.0, L = 0.0;
    float R = -INFINITY;
#pragma unroll
    for (int s = 0; s < 6; s++) if (__all(rok(g[s], tg))) missS &= ~(1u << s);
    for (unsigned spins = 0;; spins++) {
      asm volatile("" ::: "memory");
      if ((todo & 1) && !missS) {
#pragma unroll
        for (int t = 0; t < 3; t++) {
          const u32x4 g0 = g[t], g1 = g[t + 3];
          const float R0 = rec_r(g0), R1 = rec_r(g1), Rm = fmaxf(R0, R1);
          const double s0 = mkd(0u, p2hi(R0, Rm)), s1 = mkd(0u, p2hi(R1, Rm));
          const double inv = (double)__builtin_amdgcn_rcpf((float)__builtin_fma(rec_l(g0), s0, rec_l(g1) * s1));
          proj(cvtpk((float)(__builtin_fma(rec_a(g0), s0, rec_a(g1) * s1) * inv), (float)(__builtin_fma(rec_b(g0), s0, rec_b(g1) * s1) * inv)), wf,
               re + (t * 8 + w) * 256, lane);
        }
        asm volatile("" ::: "memory");
        if (__builtin_amdgcn_readfirstlane(lds_inc(cnt, lane)) == 7) {
#pragma unroll
          for (int u = 0; u < 2; u++) {
            const int t = u ? 2 : lane >> 5;
            const f32x2 v = rows_sum<8>(re + t * 2048 + (lane & 31) * 8);
            stsc(gpair(v[0], v[1], tg), rws, u && lane >= 32 ? OOB : WS_YB + ((((b4 + 1 + t) & 3) * 8 + cc) * 4 + b4) * 512 + (lane & 31) * 16, 0);
          }
        }
        todo &= ~1u;
      }
      if ((!todo && !missL) || spins > SPIN_CAP * 4) break;
      const unsigned wantL = (todo & 1) ? 0u : missL;
#pragma unroll
      for (int s = 0; s < 6; s++) if (missS >> s & 1) g[s] = ldsc(rws, roff, s * 1024);
      if (todo & 2) { x0 = ldsc(rws, ofa, 0); x1 = ldsc(rws, ofa, 1024); }
      if (todo & 4) xb = ldsc(rws, ofb, 0);
#pragma unroll
      for (int s = 0; s < NSLOT; s++) if (wantL >> s & 1) p[s] = ldsc(rws, roff, 8192 + s * 1024);
      SB();
#pragma unroll
      for (int s = 0; s < 6; s++) if ((missS >> s & 1) && __all(rok(g[s], tg))) missS &= ~(1u << s);
      if ((todo & 2) && __all(gok(x0, tg) & gok(x1, tg))) {
        const f32x2 r = {half_sum(gval(x0[0]) + gval(x1[0])), half_sum(gval(x0[2]) + gval(x1[2]))};
        if (lane < 32) *(volatile LAS f32x2*)(rA + w * 256 + lane * 8) = r;
        asm volatile("" ::: "memory");
        if (__builtin_amdgcn_readfirstlane(lds_inc(cnt + 1, lane)) == 7) {
          const f32x2 v = rows_sum<8>(rA + (lane & 31) * 8);
          __builtin_amdgcn_raw_buffer_store_b32(cvtpk(v[0], v[1]), rout, lane < 32 ? b4 * 2048 + cc * 128 + l16 * 8 + grp * 4 : OOB, 0, 0);
        }
        todo &= ~2u;
      }
      if ((todo & 4) && __all(gok(xb, tg))) {
        *(volatile LAS f32x2*)(rl + (8 + w) * 256 + (lane & 31) * 8) = f32x2{gval(xb[0]), gval(xb[2])};
        todo &= ~4u;
      }
#pragma unroll
      for (int s = 0; s < NSLOT; s++)
        if ((wantL >> s & 1) && __all(rok(p[s], tg))) {
          const float Rs = rec_r(p[s]), Rn = fmaxf(R, Rs);
          const double s0 = mkd(0u, p2hi(R, Rn)), s1 = mkd(0u, p2hi(Rs, Rn));
          L = __builtin_fma(rec_l(p[s]), s1, L * s0);
          S0 = __builtin_fma(rec_a(p[s]), s1, S0 * s0); S1 = __builtin_fma(rec_b(p[s]), s1, S1 * s0);
          R = Rn;
          missL &= ~(1u << s);
        }
      if (missL == (1u << NSLOT) - 1) __builtin_amdgcn_s_sleep(6);
    }
    {
      const double inv = (double)__builtin_amdgcn_rcpf((float)L);
      proj(cvtpk((float)(S0 * inv), (float)(S1 * inv)), wf, rl + w * 256, lane);
      asm volatile("" ::: "memory");
      const unsigned nv = lds_inc(cnt + 2, lane);
      asm volatile("" ::: "memory");
      const f32x2 v = rows_sum<11>(rl + (lane & 31) * 8);
      if (__builtin_amdgcn_readfirstlane(nv) == 7) {
        __builtin_amdgcn_raw_buffer_store_b32(cvtpk(v[0], v[1]), rout, lane < 32 ? b4 * 2048 + 1024 + cc * 128 + lane * 4 : OOB, 0, 0);
        __builtin_amdgcn_raw_buffer_store_b64(__builtin_bit_cast(u32x2, tg.e), rws, head == 0 && lane == 0 ? WS_EPOCH : OOB, 0, AUX_SC1);
      }
    }
    return;
  }
  }
  {
    const u32x4 q4 = *(LAS u32x4*)(qst + 256 + l16 * 16);
    Acc A;
    A.o[0] = A.o[1] = A.l = 0.0; A.R = -INFINITY;
    b_units(ra, rk4, rv4, cur, q4, lane, mask, ctl, rws, cntoff, A);
    const Tag tg = mktag(e0);
    b_merge(scr + 8192 + 8704, ctl + 2, w, lane, A, rws, WS_RS + (head * 16 + 8 + sub) * 1024, tg);
    SB();
    asm volatile("" :: "v"(ra[0]), "v"(ra[1]), "v"(ra[2]), "v"(ra[3]), "v"(ra[4]), "v"(ra[5]), "v"(ra[6]), "v"(ra[7]));
  }
}

extern "C" void kernel_launch(void* const* d_in, const int* in_sizes, int n_in, void* d_out, int out_size, void* d_ws, size_t ws_size,
                              hipStream_t stream) {
  k_all<<<256, 512, 0, stream>>>((const char*)d_in[0], (const char*)d_in[1], (const char*)d_in[2], (const char*)d_in[3], (const char*)d_in[4],
                                 (const char*)d_in[5], (const char*)d_in[6], (const char*)d_in[7], (const char*)d_in[8], (const char*)d_in[9],
                                 (const char*)d_in[10], (char*)d_ws, (unsigned*)d_out);
}
```

```cpp
#include <hip/hip_runtime.h>
#include <stdint.h>

typedef __attribute__((ext_vector_type(4))) unsigned u32x4;
typedef __attribute__((ext_vector_type(2))) unsigned u32x2;
typedef __attribute__((ext_vector_type(8))) unsigned u32x8;
typedef __attribute__((ext_vector_type(16))) unsigned u32x16;
typedef __attribute__((ext_vector_type(4))) float f32x4;
typedef __attribute__((ext_vector_type(2))) float f32x2;
typedef __attribute__((ext_vector_type(2))) double f64x2;
typedef __attribute__((ext_vector_type(16))) float f32x16;
typedef __attribute__((ext_vector_type(8))) __bf16 bf16x8;
typedef __attribute__((ext_vector_type(2))) __bf16 bf16x2;
typedef __attribute__((ext_vector_type(4))) short s16x4;
typedef unsigned long long u64;
typedef __amdgpu_buffer_rsrc_t rsrc_t;
#define LAS __attribute__((address_space(3)))
#define GAS __attribute__((address_space(1)))

#define CPF $
#define PAT "5c10p2ba43"
constexpr int WTH = 6;
constexpr int DCH = 3;
constexpr int NXA = 8, NXB = 8;
constexpr int NREL = 24;

constexpr int AUX_NT = 2, AUX_SC1 = 16, OOB = 0x40000000;
constexpr float SCALE = 0.08838834764831845f;
constexpr float LOG2E = 1.4426950408889634f;
constexpr unsigned SPIN_CAP = 1u << 17;
constexpr int NSLOT = 7, NUNIT = 256;

constexpr int WS_EPOCH = 0;
constexpr int CSTR    = 4352;
constexpr int WS_CNT  = 8192;
constexpr int WS_QB   = WS_CNT + 32 * CSTR;
constexpr int WS_QABS = WS_QB + 32 * 2048;
constexpr int WS_PML  = WS_QABS + 4 * 32 * 2048;
constexpr int WS_PO   = WS_PML + 32 * 32 * 16;
constexpr int WS_RS   = WS_PO + 32 * 32 * 1024;
constexpr int WS_Y    = WS_RS + 32 * 16 * 1024;
constexpr int WS_YB   = WS_Y + 4 * 8 * 32 * 512;
constexpr int WS_END  = WS_YB + 4 * 8 * 4 * 512;

constexpr int LDS_BYTES = 163840;
constexpr int LDS_SCR = 131072;

__device__ __forceinline__ unsigned cvtpk(float lo, float hi) {
  f32x2 v = {lo, hi};
  return __builtin_bit_cast(unsigned, __builtin_convertvector(v, bf16x2));
}
__device__ __forceinline__ float bf_lo(unsigned u) { return __uint_as_float(u << 16); }
__device__ __forceinline__ float bf_hi(unsigned u) { return __uint_as_float(u & 0xffff0000u); }
__device__ __forceinline__ float rbf(float x) { return __uint_as_float(cvtpk(x, x) << 16); }
__device__ __forceinline__ float dot2(unsigned k, unsigned q, float acc) {
  return __builtin_amdgcn_fdot2_f32_bf16(__builtin_bit_cast(bf16x2, k), __builtin_bit_cast(bf16x2, q), acc, false);
}
template <int CTRL> __device__ __forceinline__ float dpp(float x) {
  return __builtin_bit_cast(float, __builtin_amdgcn_mov_dpp(__builtin_bit_cast(int, x), CTRL, 0xf, 0xf, true));
}
template <int CTRL> __device__ __forceinline__ unsigned dppu(unsigned x) {
  return (unsigned)__builtin_amdgcn_mov_dpp((int)x, CTRL, 0xf, 0xf, true);
}
constexpr int XOR1 = 0xB1, XOR2 = 0x4E, XOR7 = 0x141, XOR8 = 0x128, ROR4 = 0x124;
__device__ __forceinline__ float xrow_max(float x) {
  auto s = __builtin_amdgcn_permlane16_swap(__float_as_uint(x), __float_as_uint(x), false, false);
  x = fmaxf(__uint_as_float(s[0]), __uint_as_float(s[1]));
  auto t = __builtin_amdgcn_permlane32_swap(__float_as_uint(x), __float_as_uint(x), false, false);
  return fmaxf(__uint_as_float(t[0]), __uint_as_float(t[1]));
}
__device__ __forceinline__ float xrow_sum(float x) {
  auto s = __builtin_amdgcn_permlane16_swap(__float_as_uint(x), __float_as_uint(x), false, false);
  x = __uint_as_float(s[0]) + __uint_as_float(s[1]);
  auto t = __builtin_amdgcn_permlane32_swap(__float_as_uint(x), __float_as_uint(x), false, false);
  return __uint_as_float(t[0]) + __uint_as_float(t[1]);
}
__device__ __forceinline__ float row_max(float x) {
  x = fmaxf(x, dpp<XOR8>(x)); x = fmaxf(x, dpp<ROR4>(x)); x = fmaxf(x, dpp<XOR2>(x)); x = fmaxf(x, dpp<XOR1>(x));
  return x;
}
__device__ __forceinline__ float row_sum(float x) {
  x += dpp<XOR8>(x); x += dpp<ROR4>(x); x += dpp<XOR2>(x); x += dpp<XOR1>(x);
  return x;
}
__device__ __forceinline__ float fexp2(float x) { return __builtin_amdgcn_exp2f(x); }
__device__ __forceinline__ f32x4 mfma16(u32x4 a, u32x4 b, f32x4 c) {
  return __builtin_amdgcn_mfma_f32_16x16x32_bf16(__builtin_bit_cast(bf16x8, a), __builtin_bit_cast(bf16x8, b), c, 0, 0, 0);
}
__device__ __forceinline__ f32x16 mfma32(u32x4 a, u32x4 b, f32x16 c) {
  return __builtin_amdgcn_mfma_f32_32x32x16_bf16(__builtin_bit_cast(bf16x8, a), __builtin_bit_cast(bf16x8, b), c, 0, 0, 0);
}
__device__ __forceinline__ rsrc_t mkrs(const GAS char* p, unsigned bytes) {
  return __builtin_amdgcn_make_buffer_rsrc((void*)p, 0, bytes, 0x00020000);
}
__device__ __forceinline__ void bar() { asm volatile("s_waitcnt lgkmcnt(0)\n\ts_barrier" ::: "memory"); }
__device__ __forceinline__ void drain() { asm volatile("s_waitcnt vmcnt(0)" ::: "memory"); }
#define SB() __builtin_amdgcn_sched_barrier(0)
template <int N> __device__ __forceinline__ void vmwait() { asm volatile("s_waitcnt vmcnt(%0)" :: "n"(N) : "memory"); }
__device__ __forceinline__ unsigned swz(unsigned row) { return ((row & 3) << 2) | ((row >> 2) & 2); }
__device__ __forceinline__ u32x2 trread(LAS char* p) {
  return __builtin_bit_cast(u32x2, __builtin_amdgcn_ds_read_tr16_b64_v4i16((LAS s16x4*)p));
}

struct Tag { unsigned tl, th, lo, rb; u64 e; };
__device__ __forceinline__ Tag mktag(u64 e0) {
  Tag t;
  asm volatile("" : "+s"(e0));
  t.e = e0 * 6364136223846793005ull + 1442695040888963407ull;
  t.th = (unsigned)(t.e >> 32);
  t.lo = (unsigned)t.e;
  t.tl = t.lo & 0xff000000u;
  t.rb = ((t.lo >> 9) & 255u) | (~t.lo & 256u);
  return t;
}
__device__ __forceinline__ unsigned gpack(float v, const Tag& t) { return ((__float_as_uint(v) + 0x80u) >> 8) | t.tl; }
__device__ __forceinline__ u32x4 gpair(float a, float b, const Tag& t) { return u32x4{gpack(a, t), t.th, gpack(b, t), t.th}; }
__device__ __forceinline__ bool gok(u32x4 g, const Tag& t) {
  return ((((g[0] ^ t.tl) | (g[2] ^ t.tl)) >> 24) | (g[1] ^ t.th) | (g[3] ^ t.th)) == 0u;
}
__device__ __forceinline__ float gval(unsigned lo) { return __uint_as_float(lo << 8); }
__device__ __forceinline__ bool qok(u32x4 g, const Tag& t) { return ((g[1] ^ t.th) | (g[3] ^ t.th)) == 0u; }
__device__ __forceinline__ double mkd(unsigned lo, unsigned hi) { return __builtin_bit_cast(double, u32x2{lo, hi}); }
__device__ __forceinline__ u32x4 rpack(double a, double b, double l, float r, const Tag& t) {
  const u32x2 ua = __builtin_bit_cast(u32x2, a), ub = __builtin_bit_cast(u32x2, b), ul = __builtin_bit_cast(u32x2, l);
  const u64 ml = ((u64)(unsigned)__builtin_amdgcn_readfirstlane(ul[1]) << 32) | (unsigned)__builtin_amdgcn_readfirstlane(ul[0]);
  const u64 mr = (unsigned)__builtin_amdgcn_readfirstlane(__float_as_uint(r));
  const unsigned ta = t.lo & 511u, tb = t.rb;
  return u32x4{(ua[0] & ~1023u) | (__builtin_amdgcn_inverse_ballot_w64(ml) ? ta | 512u : ta), ua[1],
               (ub[0] & ~1023u) | (__builtin_amdgcn_inverse_ballot_w64(mr) ? tb | 512u : tb), ub[1]};
}
__device__ __forceinline__ bool rok(u32x4 g, const Tag& t) { return (((g[0] ^ t.lo) | (g[2] ^ t.rb)) & 511u) == 0u; }
constexpr unsigned PMASK = 0x00030003u;
__device__ __forceinline__ unsigned ptag(const Tag& t, int k) {
  const unsigned n = k == 0 ? t.lo : k == 3 ? t.lo >> 10 : ((t.lo >> (1 + 3 * k)) & 7u) | (~t.lo & 8u);
  return (n & 3u) | ((n & 12u) << 14);
}
__device__ __forceinline__ unsigned ltag(const Tag& t) { return ((t.lo >> 16) & 127u) | (~t.lo & 128u); }
__device__ __forceinline__ bool mok(u32x4 g, const Tag& t) { return (((g[0] ^ t.lo) & 0xffffu) | ((g[1] ^ ltag(t)) & 255u)) == 0u; }
__device__ __forceinline__ unsigned bfi(unsigned m, unsigned a, unsigned b) { return (a & m) | (b & ~m); }
__device__ __forceinline__ double rec_a(u32x4 g) { return mkd(g[0] & ~1023u, g[1]); }
__device__ __forceinline__ double rec_b(u32x4 g) { return mkd(g[2] & ~1023u, g[3]); }
__device__ __forceinline__ double rec_l(u32x4 g) { return __builtin_bit_cast(double, __builtin_amdgcn_ballot_w64((g[0] & 512u) != 0u)); }
__device__ __forceinline__ float rec_r(u32x4 g) { return __uint_as_float((unsigned)__builtin_amdgcn_ballot_w64((g[2] & 512u) != 0u)); }
__device__ __forceinline__ int edif(float a, float b) {
  int r;
  asm("v_cvt_i32_f32 %0, %1" : "=v"(r) : "v"(a - b));
  return r;
}
__device__ __forceinline__ unsigned p2hi(float a, float b) { return (unsigned)max(edif(a, b) + 1023, 0) << 20; }
__device__ __forceinline__ unsigned rdu(unsigned v, int l) { return __builtin_amdgcn_readlane(v, l); }

__device__ __forceinline__ void diag_gemv(const u32x4 (&wr)[16], const u32x4 (&xp)[2][2], const unsigned (&mask)[4], f32x4 (&acc)[2]) {
#pragma unroll
  for (int i = 0; i < 16; i++) {
#pragma unroll
    for (int s = 0; s < 2; s++) {
      const unsigned src = xp[s][i >> 3][(i >> 1) & 3];
      const unsigned dup = __builtin_amdgcn_perm(src, src, (i & 1) ? 0x03020302u : 0x01000100u);
      const u32x4 a = {dup & mask[0], dup & mask[1], dup & mask[2], dup & mask[3]};
      acc[s] = mfma16(a, wr[i], acc[s]);
    }
  }
}

constexpr int bitpos(char c) { for (int i = 0; i < 10; i++) if (PAT[i] == c) return 10 + i; return 31; }
constexpr int goff(int t) { int o = 0; for (int j = 0; j < 6; j++) o |= ((t >> j) & 1) << bitpos('0' + j); return o; }
struct Acc { double o[2]; double l; float R; };
__device__ __forceinline__ u32x4 ldnt(rsrc_t r, int voff, int so) { return __builtin_amdgcn_raw_buffer_load_b128(r, voff, so, AUX_NT); }
__device__ __forceinline__ u32x4 ldslot(rsrc_t r, int voff, int so, int i) { return ldnt(r, voff + goff(i & 3), so ^ goff(i & ~3)); }
__device__ __forceinline__ u32x4 ldunit(rsrc_t r, int voff, int so, int i) { return ldnt(r, voff + i * 1024, so); }
__device__ __forceinline__ u32x4 ldsc(rsrc_t r, int voff, int so) { return __builtin_amdgcn_raw_buffer_load_b128(r, voff, so, AUX_SC1); }
__device__ __forceinline__ void stsc(u32x4 v, rsrc_t r, int voff, int so) { __builtin_amdgcn_raw_buffer_store_b128(v, r, voff + so, 0, AUX_SC1); }
__device__ __forceinline__ float dot8(u32x4 k, u32x4 q) { return dot2(k[3], q[3], dot2(k[2], q[2], dot2(k[1], q[1], dot2(k[0], q[0], 0.f)))); }
#define TR_STEP(HALF, CTRL, BIT) _Pragma("unroll") for (int h = 0; h < (HALF); ++h) { \
    const float t1 = x[h] + dpp<CTRL>(x[h]), t2 = x[h + (HALF)] + dpp<CTRL>(x[h + (HALF)]); x[h] = (c & (BIT)) ? t2 : t1; }
template <int N> __device__ __forceinline__ float k_step(u32x4 (&ring)[16], u32x4 q, int c, rsrc_t rn, int voff, int so, bool ext, LAS char* xp) {
  float x[16];
#pragma unroll
  for (int i = 0; i < 16; i++) {
    x[i] = dot8(ring[i], q);
    asm volatile("s_waitcnt vmcnt(%1)" : "+v"(x[i]) : "n"(WTH - 1));
    if (i >= 16 - N && ext) ring[i] = *(volatile LAS u32x4*)(xp + (i - (16 - N)) * 1024);
    else ring[i] = ldslot(rn, voff, so, i);
    SB();
  }
  TR_STEP(8, XOR8, 8) TR_STEP(4, XOR7, 4) TR_STEP(2, XOR2, 2) TR_STEP(1, XOR1, 1)
  return rbf(rbf(x[0]) * SCALE);
}
template <int BC> __device__ __forceinline__ void pv(u32x4 v, unsigned pk, const unsigned (&mask)[4], f32x4& acc) {
  const unsigned d = dppu<0x150 + BC>(pk);
  acc = mfma16(u32x4{d & mask[0], d & mask[1], d & mask[2], d & mask[3]}, v, acc);
}
template <int I, int MODE> __device__ __forceinline__ void v_step(u32x4 (&ring)[16], unsigned pk, const unsigned (&mask)[4], f32x4& acc,
                                                                  rsrc_t rk, rsrc_t rv, int voff, int so) {
  pv<I>(ring[I], pk, mask, acc);
  if (MODE == 1 || (MODE == 2 && I < 8)) {
    asm volatile("s_waitcnt vmcnt(%1)" : "+v"(acc) : "n"(WTH - 1));
    ring[I] = MODE == 1 ? ldslot(rv, voff, so, I) : ldunit(I < 4 ? rk : rv, voff, so, I & 3);
  } else {
    asm volatile("" : "+v"(acc));
  }
  SB();
  if constexpr (I < 15) v_step<I + 1, MODE>(ring, pk, mask, acc, rk, rv, voff, so);
}
__device__ __forceinline__ float exp_ref(float mx, float& f) {
  const float M = __builtin_ceilf(mx * LOG2E);
  f = __builtin_amdgcn_fmed3f(fmaf(mx, LOG2E, -M), -1.f, 0.f);
  return M;
}
__device__ __forceinline__ unsigned lds_inc(LAS unsigned* p, int lane) {
  unsigned t = 0, z = 0;
  asm volatile("" : "+v"(z));
  if (lane == 0) t = __hip_atomic_fetch_add(p + z, 1u, __ATOMIC_RELAXED, __HIP_MEMORY_SCOPE_WORKGROUP);
  return t;
}
struct Take { unsigned unit, pend; bool fetch; };
__device__ __forceinline__ void lds_add(LAS unsigned* p, unsigned v) { __hip_atomic_fetch_add(p, v, __ATOMIC_RELAXED, __HIP_MEMORY_SCOPE_WORKGROUP); }
__device__ __forceinline__ Take take(LAS unsigned* ctl, unsigned kv, rsrc_t rws, int cntoff, int lane) {
  const unsigned k = __builtin_amdgcn_readfirstlane(kv);
  unsigned b, s;
  for (unsigned spins = 0;; spins++) {
    const unsigned vs = ((volatile LAS unsigned*)ctl)[5], vb = ((volatile LAS unsigned*)ctl)[8 + (k >> 2)];
    s = __builtin_amdgcn_readfirstlane(vs); b = __builtin_amdgcn_readfirstlane(vb);
    if (b != ~0u || (s >= 0x10000u && (s & 0xffffu) == 0u) || spins > SPIN_CAP * 16) break;
    __builtin_amdgcn_s_sleep(1);
  }
  Take t;
  t.unit = min(b, (unsigned)NUNIT) + (k & 3);
  t.fetch = (k & 3) == 0 && b < NUNIT && s < 0x10000u;
  const bool go = lane == 0 && t.fetch;
  if (go) lds_add(ctl + 5, 1u);
  asm volatile("s_waitcnt lgkmcnt(0)" ::: "memory");
  t.pend = __builtin_amdgcn_raw_ptr_buffer_atomic_add_i32(4, rws, go ? cntoff : OOB, 0, AUX_SC1);
  SB();
  return t;
}
__device__ __forceinline__ void give(LAS unsigned* ctl, const Take& t, int lane) {
  if (t.fetch && lane == 0) {
    unsigned z = 0;
    asm volatile("" : "+v"(z));
    if (t.pend < NUNIT) {
      const unsigned i = __hip_atomic_fetch_add(ctl + 4 + z, 1u, __ATOMIC_RELAXED, __HIP_MEMORY_SCOPE_WORKGROUP);
      ((volatile LAS unsigned*)ctl)[8 + i] = t.pend;
    }
    asm volatile("" ::: "memory");
    lds_add(ctl + 5 + z, t.pend >= NUNIT - 4 ? 0xffffu : ~0u);
  }
}
__device__ __forceinline__ int unit_off(unsigned t) { return (int)min(t, (unsigned)NUNIT) * 4096; }

__device__ __forceinline__ void b_units(u32x4 (&ra)[16], rsrc_t rk, rsrc_t rv, unsigned cur, u32x4 q, int lane, const unsigned (&mask)[4],
                                        LAS unsigned* ctl, rsrc_t rws, int cntoff, Acc& A) {
  const int c = lane & 15, voff = lane * 16;
  unsigned nxt;
  {
    const Take t = take(ctl, lds_inc(ctl, lane), rws, cntoff, lane);
    give(ctl, t, lane);
    nxt = t.unit;
  }
#pragma unroll 1
  while (cur < NUNIT) {
    const int so = unit_off(nxt);
    float x[4];
#pragma unroll
    for (int i = 0; i < 4; i++) {
      x[i] = dot8(ra[i], q);
      asm volatile("" : "+v"(x[i]));
      ra[i] = ldunit(rk, voff, so, i);
      SB();
    }
    const unsigned kv = lds_inc(ctl, lane);
    TR_STEP(2, XOR8, 8) TR_STEP(1, XOR7, 4)
    x[0] += dpp<XOR2>(x[0]); x[0] += dpp<XOR1>(x[0]);
    const float s = rbf(rbf(x[0]) * SCALE);
    float mx = fmaxf(s, dpp<XOR8>(s));
    mx = xrow_max(fmaxf(mx, dpp<ROR4>(mx)));
    float f;
    const float M = exp_ref(mx, f);
    const float p = fexp2(fmaf(s - mx, LOG2E, f));
    unsigned pk = cvtpk(p, p);
    asm volatile("" : "+v"(pk));
    const Take t = take(ctl, kv, rws, cntoff, lane);
    float ls = bf_lo(pk);
    ls += dpp<XOR8>(ls);
    ls = xrow_sum(ls + dpp<ROR4>(ls));
    const float Rn = fmaxf(A.R, M);
    const double s0 = mkd(0u, p2hi(A.R, Rn)), s1 = mkd(0u, p2hi(M, Rn));
    A.o[0] *= s0; A.o[1] *= s0;
    A.l = __builtin_fma((double)ls, s1, A.l * s0);
    A.R = Rn;
    asm volatile("" : "+v"(A.o[0]), "+v"(A.o[1]), "+v"(A.l));
    SB();
    f32x4 acc = {0.f, 0.f, 0.f, 0.f};
    pv<0>(ra[4], pk, mask, acc);  asm volatile("" : "+v"(acc)); ra[4] = ldunit(rv, voff, so, 0); SB();
    pv<4>(ra[5], pk, mask, acc);  asm volatile("" : "+v"(acc)); ra[5] = ldunit(rv, voff, so, 1); SB();
    pv<8>(ra[6], pk, mask, acc);  asm volatile("" : "+v"(acc)); ra[6] = ldunit(rv, voff, so, 2); SB();
    pv<12>(ra[7], pk, mask, acc); asm volatile("" : "+v"(acc)); ra[7] = ldunit(rv, voff, so, 3); SB();
    A.o[0] = __builtin_fma((double)(lane < 32 ? acc[0] : acc[2]), s1, A.o[0]);
    A.o[1] = __builtin_fma((double)(lane < 32 ? acc[1] : acc[3]), s1, A.o[1]);
    asm volatile("" : "+v"(A.o[0]), "+v"(A.o[1]));
    SB();
    give(ctl, t, lane);
    cur = nxt; nxt = t.unit;
  }
}
#undef TR_STEP
__device__ __forceinline__ int rec_col(int lane) { return 8 * (lane & 15) + 4 * ((lane >> 4) & 1) + 2 * (lane >> 5); }
__device__ __forceinline__ void b_merge(LAS char* mrg, LAS unsigned* arrive, int w, int lane, const Acc& A, rsrc_t rws, int roff, const Tag& tg) {
  *(volatile LAS f64x2*)(mrg + w * 1024 + lane * 16) = f64x2{A.o[0], A.o[1]};
  *(volatile LAS double*)(mrg + 8192 + w * 8) = A.l;
  *(volatile LAS float*)(mrg + 8256 + w * 4) = A.R;
  asm volatile("" ::: "memory");
  const unsigned nv = lds_inc(arrive, lane);
  asm volatile("" ::: "memory");
  const f32x4 Ra = *(volatile LAS f32x4*)(mrg + 8256), Rb = *(volatile LAS f32x4*)(mrg + 8272);
  const float Rw = *(volatile LAS float*)(mrg + 8256 + (lane & 7) * 4);
  f64x2 o[8], lw[4];
#pragma unroll
  for (int i = 0; i < 8; i++) o[i] = *(volatile LAS f64x2*)(mrg + i * 1024 + lane * 16);
#pragma unroll
  for (int i = 0; i < 4; i++) lw[i] = *(volatile LAS f64x2*)(mrg + 8192 + i * 16);
  SB();
  if (__builtin_amdgcn_readfirstlane(nv) == 7) {
    const float R = fmaxf(fmaxf(fmaxf(Ra[0], Ra[1]), fmaxf(Ra[2], Ra[3])), fmaxf(fmaxf(Rb[0], Rb[1]), fmaxf(Rb[2], Rb[3])));
    const unsigned sw = p2hi(Rw, R);
    double v0[2] = {0.0, 0.0}, v1[2] = {0.0, 0.0}, L[2] = {0.0, 0.0};
#pragma unroll
    for (int i = 0; i < 8; i++) {
      const double sc = mkd(0u, rdu(sw, i));
      v0[i & 1] = __builtin_fma(o[i][0], sc, v0[i & 1]); v1[i & 1] = __builtin_fma(o[i][1], sc, v1[i & 1]);
      L[i & 1] = __builtin_fma(lw[i >> 1][i & 1], sc, L[i & 1]);
    }
    stsc(rpack(v0[0] + v0[1], v1[0] + v1[1], L[0] + L[1], R, tg), rws, roff + lane * 16, 0);
  }
}

__device__ __forceinline__ unsigned fz(unsigned row) { return ((row & 2) << 1) | ((row >> 1) & 2); }
__device__ __forceinline__ void dma1(rsrc_t rs, int voff, int so, unsigned ldsaddr) {
  unsigned keep;
  asm volatile("s_mov_b32 %0, m0\n\ts_mov_b32 m0, %1\n\ts_nop 0\n\tbuffer_load_dwordx4 %2, %3, %4 offen nt lds\n\ts_mov_b32 m0, %0"
               : "=&s"(keep) : "s"(ldsaddr), "v"(voff), "s"(rs), "s"(so) : "memory");
}
template <int NR>
__device__ __forceinline__ void dma_rows(rsrc_t rs, int srcoff, LAS char* img, int r0, int lane) {
#pragma unroll
  for (int u = 0; u < NR; u++) {
    const int r = r0 + u;
    unsigned keep;
    asm volatile("s_mov_b32 %0, m0\n\ts_mov_b32 m0, %1\n\ts_nop 0\n\tbuffer_load_dwordx4 %2, %3, %4 offen nt lds\n\ts_mov_b32 m0, %0"
                 : "=&s"(keep) : "s"((unsigned)(uintptr_t)img + r * 1024), "v"((lane ^ swz(r)) * 16), "s"(rs), "s"(srcoff + r * 1024) : "memory");
  }
  SB();
}

__device__ __forceinline__ void wo_frags(LAS char* img, int w, int lane, u32x4 (&wf)[4][4]) {
  const int g = lane >> 4, q = (lane & 15) >> 2, p = lane & 3;
#pragma unroll
  for (int ks = 0; ks < 4; ks++)
#pragma unroll
    for (int tl = 0; tl < 4; tl++) {
      const int ch = 8 * w + 2 * tl + (p >> 1);
      const int r0 = 32 * ks + 8 * g + q, r1 = r0 + 4;
      const u32x2 a = trread(img + r0 * 1024 + ((ch ^ swz(r0)) * 16) + 8 * (p & 1));
      const u32x2 b = trread(img + r1 * 1024 + ((ch ^ swz(r1)) * 16) + 8 * (p & 1));
      wf[tl][ks] = u32x4{a[0], a[1], b[0], b[1]};
    }
}
__device__ __forceinline__ f32x4 wo_calc(const u32x4 (&wf)[4][4], LAS char* cb, int lane) {
  const int n = lane & 15, grp = lane >> 4;
  u32x4 bf[4];
#pragma unroll
  for (int ks = 0; ks < 4; ks++) bf[ks] = *(LAS u32x4*)(cb + (n & 3) * 256 + ks * 64 + grp * 16);
  f32x4 acc[4];
#pragma unroll
  for (int tl = 0; tl < 4; tl++) acc[tl] = f32x4{0.f, 0.f, 0.f, 0.f};
#pragma unroll
  for (int ks = 0; ks < 4; ks++)
#pragma unroll
    for (int tl = 0; tl < 4; tl++) acc[tl] = mfma16(wf[tl][ks], bf[ks], acc[tl]);
  const f32x4 lo = (n & 4) ? acc[1] : acc[0], hi = (n & 4) ? acc[3] : acc[2];
  return (n & 8) ? hi : lo;
}
__device__ __forceinline__ void y_store(f32x4 y, int w, int lane, int slot, rsrc_t rws, int yoff, const Tag& tg) {
  const int off = yoff + (((lane & 3) * 8 + w) * 32 + slot) * 512 + ((lane & 12) + (lane >> 4)) * 16;
  stsc(gpair(y[0], y[1], tg), rws, off, 0);
  stsc(gpair(y[2], y[3], tg), rws, off, 256);
}
template <int K> __device__ __forceinline__ u32x4 bc4(unsigned p) { return u32x4{dppu<0x150 + K>(p), dppu<0x151 + K>(p), dppu<0x152 + K>(p), dppu<0x153 + K>(p)}; }
__device__ __forceinline__ void proj(unsigned pr, const u32x4 (&wf)[4][4], LAS char* row, int lane) {
  const u32x4 bf[4] = {bc4<0>(pr), bc4<4>(pr), bc4<8>(pr), bc4<12>(pr)};
  f32x4 acc[4];
#pragma unroll
  for (int tl = 0; tl < 4; tl++) acc[tl] = f32x4{0.f, 0.f, 0.f, 0.f};
#pragma unroll
  for (int ks = 0; ks < 4; ks++)
#pragma unroll
    for (int tl = 0; tl < 4; tl++) acc[tl] = mfma16(wf[tl][ks], bf[ks], acc[tl]);
  const f32x4 lo = (lane & 1) ? acc[1] : acc[0], hi = (lane & 1) ? acc[3] : acc[2];
  if ((lane & 12) == 0) *(volatile LAS f32x4*)(row + (lane & 3) * 64 + (lane >> 4) * 16) = (lane & 2) ? hi : lo;
}
template <int N> __device__ __forceinline__ f32x2 rows_sum(LAS char* p) {
  f32x2 v[N];
#pragma unroll
  for (int i = 0; i < N; i++) v[i] = *(volatile LAS f32x2*)(p + i * 256);
#pragma unroll
  for (int st = 1; st < N; st *= 2)
#pragma unroll
    for (int i = 0; i + st < N; i += 2 * st) v[i] += v[i + st];
  return v[0];
}
__device__ __forceinline__ float half_sum(float x) {
  auto t = __builtin_amdgcn_permlane32_swap(__float_as_uint(x), __float_as_uint(x), false, false);
  return __uint_as_float(t[0]) + __uint_as_float(t[1]);
}

__global__ __launch_bounds__(512, 2) void k_all(const char* a0_, const char* a1_, const char* a2_, const char* a3_, const char* a4_, const char* a5_,
                                                const char* a6_, const char* a7_, const char* a8_, const char* a9_, const char* a10_, char* a11_, unsigned* a12_) {
  u32x16 ka; u32x8 kb_; u32x2 kc_;
  asm volatile("s_load_dwordx16 %0, %3, 0x0\n\ts_load_dwordx8 %1, %3, 0x40\n\ts_load_dwordx2 %2, %3, 0x60"
               : "=&s"(ka), "=&s"(kb_), "=&s"(kc_) : "s"(__builtin_amdgcn_kernarg_segment_ptr()));
  __shared__ __attribute__((aligned(16))) char smem[LDS_BYTES];
  LAS char* lds = (LAS char*)smem;
  LAS char* scr = lds + LDS_SCR;
  LAS unsigned* ctl = (LAS unsigned*)scr;
  const int tid = threadIdx.x, lane = tid & 63, w = __builtin_amdgcn_readfirstlane(tid >> 6);
  const int blk = blockIdx.x, head = blk >> 3, prod = (blk + head) & 7;
  const int sub = (0x54372106u >> (4 * prod)) & 7;
  unsigned cpf = 0;
  {
    u64 pc;
    asm volatile("s_getpc_b64 %0" : "=s"(pc));
    cpf = __builtin_amdgcn_raw_buffer_load_b32(mkrs((const GAS char*)(pc & ~255ull), CPF), w == 7 ? head * 2048 + lane * 32 : OOB, 0, 0);
    SB();
  }
  {
    unsigned c = 16 + 4 * (DCH * sub + tid);
    asm volatile("" : "+v"(c));
    c = tid < 8 + DCH ? c : ~0u;
    c = tid < 8 ? 0u : c;
    ctl[tid] = tid == 4 ? DCH : c;
  }
  const int l16 = lane & 15, grp = lane >> 4;
  const int b4 = head & 3, cntoff = WS_CNT + head * CSTR;
  const int bm = (b4 + 1 + (sub >= 3 ? sub - 3 : sub)) & 3;
  const size_t base = (size_t)(bm * 32 + head) << 20;
  const int so = ((w & 1) << bitpos('a')) | (((w >> 1) & 1) << bitpos('b')) | ((w >> 2) << bitpos('c')) | (sub >= 3 ? 1 << bitpos('p') : 0);
  int v0 = l16 * 16 + grp * (sub < 6 ? 256 : 131072) + (sub < 6 ? so : w * 524288 + head * 256);
  unsigned boff = sub < 6 ? (unsigned)base : 0u, bsz = sub < 6 ? 1u << 20 : 4u << 20;
  int s0[WTH];
#pragma unroll
  for (int i = 0; i < WTH; i++) {
    s0[i] = sub < 6 ? goff(i) : i * 8192;
    asm volatile("" : "+s"(s0[i]));
  }
  asm volatile("" : "+v"(v0), "+s"(boff), "+s"(bsz));
  SB();
  asm volatile("s_waitcnt lgkmcnt(0)" : "+s"(ka), "+s"(kb_), "+s"(kc_));
  auto ptr = [](unsigned lo, unsigned hi) { return (GAS char*)(((u64)hi << 32) | lo); };
  GAS char *const xA = ptr(ka[0], ka[1]), *const ckv = ptr(ka[2], ka[3]), *const wqA = ptr(ka[4], ka[5]), *const wuk = ptr(ka[6], ka[7]),
           *const wuv = ptr(ka[8], ka[9]), *const woA = ptr(ka[10], ka[11]), *const xB = ptr(ka[12], ka[13]), *const kc = ptr(ka[14], ka[15]),
           *const vc = ptr(kb_[0], kb_[1]), *const wqB = ptr(kb_[2], kb_[3]), *const woB = ptr(kb_[4], kb_[5]), *const ws = ptr(kb_[6], kb_[7]),
           *const out = ptr(kc_[0], kc_[1]);
  const u64 e0 = *(const __attribute__((address_space(4))) u64*)(u64)(ws + WS_EPOCH);
  const rsrc_t r0 = mkrs((sub < 6 ? kc : sub == 6 ? wqB : wqA) + boff, bsz);
  SB();
  u32x4 ra[16];
#pragma unroll
  for (int i = 0; i < WTH; i++) ra[i] = ldnt(r0, v0, s0[i]);
  SB();
  u32x4 xp[2][2];
  if (sub >= 6) {
    const GAS char* x = sub == 6 ? xB : xA;
#pragma unroll
    for (int s = 0; s < 2; s++) {
      const GAS u32x4* xs = (const GAS u32x4*)(x + (2 * s + (l16 >> 3)) * 1024 + (64 * w + 16 * grp) * 2);
      xp[s][0] = xs[0]; xp[s][1] = xs[1];
    }
#pragma unroll
    for (int i = WTH; i < 16; i++) ra[i] = ldnt(r0, v0, i * 8192);
    asm volatile("" ::: "memory");
    SB();
  }
  const rsrc_t rws = mkrs(ws, WS_END);
  unsigned mask[4];
#pragma unroll
  for (int v = 0; v < 4; v++) mask[v] = (((lane & 7) >> 1) == v) ? ((lane & 1) ? 0xffff0000u : 0x0000ffffu) : 0u;
  const size_t base4 = (size_t)(b4 * 32 + head) << 20;
  const rsrc_t rk4 = mkrs(kc + base4, 1u << 20), rv4 = mkrs(vc + base4, 1u << 20);
  const int voff = lane * 16;
  LAS char* qst = scr + 1024 + w * 512;
  const int qo = WS_QB + head * 2048 + (lane < 32 ? bm : b4) * 512 + (lane & 31) * 16;
  auto qwait = [&](u32x4 g, const Tag& tg) {
    if (!__all(qok(g, tg))) {
      for (unsigned spins = 0;; spins++) {
        asm volatile("" ::: "memory");
        g = ldsc(rws, qo, 0);
        if (__all(qok(g, tg)) || spins > SPIN_CAP) break;
        __builtin_amdgcn_s_sleep(2);
      }
    }
    *(LAS u32x2*)(qst + lane * 8) = u32x2{g[0], g[2]};
  };
  unsigned cur;

  if (__builtin_expect(sub < 6, 1)) {
    const rsrc_t rv = mkrs(vc + base, 1u << 20);
    u32x4 rb[16], g;
#pragma unroll
    for (int i = WTH; i < 32 + NXA + NXB; i++) {
      if (i == WTH) asm volatile("s_barrier" ::: "memory");
      vmwait<WTH - 1>();
      if (i == 32 + NXA + NXB - WTH) asm volatile("buffer_load_dwordx4 %0, %1, %2, 0 offen sc1" : "=&v"(g) : "v"(qo), "s"(rws));
      if (i < 32) {
        const u32x4 v = ldslot(r0, voff, so, i);
        if (i < 16) ra[i] = v; else rb[i - 16] = v;
      } else {
        const int j = i < 32 + NXA ? i + 16 - NXA : i + 32 - NXA - NXB;
        dma1(r0, voff + goff(j & 3), so ^ goff(j & ~3), (unsigned)(uintptr_t)lds + w * 16384 + (i - 32) * 1024);
      }
      SB();
    }
    asm volatile("s_waitcnt vmcnt(%1)" : "+v"(g) : "n"(WTH), "v"(cpf));
    qwait(g, mktag(e0));
    const u32x4 q = *(LAS u32x4*)(qst + l16 * 16);
    float sc[4] = {0.f, 0.f, 0.f, 0.f};
#pragma unroll 1
    for (int it = 0; it < 2; it++) {
      const rsrc_t rn = it ? rv : r0;
      sc[2] = sc[0]; sc[3] = sc[1];
      sc[1] = k_step<NXA>(ra, q, l16, rn, voff, so ^ (it ? goff(48) : goff(32)), it == 0, lds + w * 16384 + voff);
      sc[0] = k_step<NXB>(rb, q, l16, rn, voff, so ^ (it ? goff(32) : goff(48)), it == 0, lds + w * 16384 + NXA * 1024 + voff);
    }
    const float mx = xrow_max(row_max(fmaxf(fmaxf(sc[0], sc[1]), fmaxf(sc[2], sc[3]))));
    float f, l = 0.f;
    const float M = exp_ref(mx, f);
    unsigned pk[4];
#pragma unroll
    for (int k = 0; k < 4; k++) {
      const float p = fexp2(fmaf(sc[k] - mx, LOG2E, f));
      pk[k] = cvtpk(p, p);
      l += bf_lo(pk[k]);
    }
    l = xrow_sum(row_sum(l));
    cur = 8 * sub + w;
    f32x4 acc = {0.f, 0.f, 0.f, 0.f};
    v_step<0, 1>(ra, pk[0], mask, acc, r0, rv, voff, so ^ goff(16));
    v_step<0, 1>(rb, pk[1], mask, acc, r0, rv, voff, so);
    v_step<0, 2>(ra, pk[2], mask, acc, rk4, rv4, voff, cur * 4096);
    v_step<0, 0>(rb, pk[3], mask, acc, r0, rv, voff, 0);
    Acc A;
    A.o[0] = lane < 32 ? acc[0] : acc[2]; A.o[1] = lane < 32 ? acc[1] : acc[3];
    A.l = l; A.R = M;
    b_merge(scr + 8192, ctl + 1, w, lane, A, rws, WS_RS + (head * 16 + sub) * 1024, mktag(e0));
  } else {
  {
    __builtin_amdgcn_raw_buffer_store_b32(48 + 24 * DCH, rws, (sub == 6 && tid == 0) ? cntoff : OOB, 0, AUX_SC1);
    {
      const rsrc_t rk = mkrs(wuk, sub == 7 ? 4u << 20 : 0u);
#pragma unroll
      for (int i = 0; i < 16; i++)
        __builtin_amdgcn_raw_ptr_buffer_load_lds(rk, (LAS void*)(lds + w * 16384 + i * 1024), 16,
                                                 (64 * w + grp) * 8192 + head * 256 + ((l16 ^ (4 * (i & 3) + grp)) * 16), i * 4 * 8192, 0, AUX_NT);
    }
    SB();
    f32x4 acc[2] = {{0.f, 0.f, 0.f, 0.f}, {0.f, 0.f, 0.f, 0.f}};
    diag_gemv(ra, xp, mask, acc);
    LAS float* red = (LAS float*)(scr + 8192);
#pragma unroll
    for (int s = 0; s < 2; s++) *(LAS f32x4*)(red + w * 512 + (2 * s + (grp >> 1)) * 128 + 8 * l16 + 4 * (grp & 1)) = acc[s];
    asm volatile("" :: "v"(cpf));
    __builtin_amdgcn_s_waitcnt(0x0f70);
    drain();
    bar();
    if (tid < 128) {
      f32x4 a = {0.f, 0.f, 0.f, 0.f};
#pragma unroll
      for (int k = 0; k < 8; k++) a += *(LAS f32x4*)(red + k * 512 + 4 * tid);
      const unsigned p0 = cvtpk(a[0], a[1]), p1 = cvtpk(a[2], a[3]);
      const Tag tg = mktag(e0);
      stsc(u32x4{p0, tg.th, p1, tg.th}, rws, sub == 6 ? WS_QB + head * 2048 + tid * 16 : OOB, 0);
      *(LAS u32x2*)(scr + 1024 + tid * 8) = u32x2{p0, p1};
    }
  }

  if (sub == 6) {
    const int aid = ((head & 6) << 2) | ((head & 1) << 2) | (head >> 3), b = aid >> 3;
    const rsrc_t rc = mkrs(ckv + (size_t)aid * 524288, 524288);
    const int dvo = (lane >> 3) * 1024 + w * 128 + (((lane & 7) ^ fz(lane >> 3)) * 16);
    const unsigned dlo = (unsigned)(uintptr_t)lds + w * 8192;
    const int xt = (aid & 7) << 16;
#pragma unroll
    for (int j = 0; j < 16; j++) dma1(rc, dvo, (j * 8192) ^ xt, dlo + (j >> 3) * 65536 + (j & 7) * 1024);
    SB();
    const Tag tg = mktag(e0);
    bar();
    {
      u32x4 t[8];
      unsigned miss = 15;
      __builtin_amdgcn_s_sleep(90);
      for (unsigned spins = 0;; spins++) {
        asm volatile("" ::: "memory");
#pragma unroll
        for (int i = 0; i < 4; i++)
          if (miss >> i & 1) {
            t[2 * i] = ldsc(rws, WS_QABS + (b * 32 + w) * 2048 + lane * 16, i * 16384);
            t[2 * i + 1] = ldsc(rws, WS_QABS + (b * 32 + w) * 2048 + lane * 16 + 1024, i * 16384);
          }
        SB();
#pragma unroll
        for (int i = 0; i < 4; i++) if (__all(qok(t[2 * i], tg) & qok(t[2 * i + 1], tg))) miss &= ~(1u << i);
        if (!miss || spins > SPIN_CAP) break;
        __builtin_amdgcn_s_sleep(4);
      }
#pragma unroll
      for (int i = 0; i < 8; i++) {
        const int row = 8 * (i >> 1) + w;
        *(LAS u32x2*)(scr + row * 1024 + (((32 * (i & 1) + (lane >> 1)) ^ swz(row)) * 16) + (lane & 1) * 8) = u32x2{t[i][0], t[i][2]};
      }
    }
    bar();
    const int kb = w & 3, hb = w >> 2;
    u32x4 qf[16];
    {
      const int row = 16 * hb + l16;
#pragma unroll
      for (int ks = 0; ks < 16; ks++) qf[ks] = *(LAS u32x4*)(scr + row * 1024 + (((4 * ks + grp) ^ swz(row)) * 16));
    }
    LAS float* mx = (LAS float*)scr;
    LAS char* pl = scr + 1024;
    LAS float* lr = (LAS float*)(scr + 8192);
    f32x16 o0, o1;
#pragma unroll
    for (int j = 0; j < 16; j++) { o0[j] = 0.f; o1[j] = 0.f; }
    float m1 = -INFINITY, m2 = -INFINITY, l1 = 0.f;
    const int h1 = 16 * hb + l16, h2 = lane & 31, hh = lane >> 5;
    const int arow = 16 * kb + l16;
    const int tq = (lane & 15) >> 2, tp = lane & 3, tblk = (lane >> 4) & 1;
    const int qa0 = arow * 128 + ((grp ^ fz(arow)) * 16), qa1 = qa0 ^ 64;
    const int pr = 8 * hh + tq, pc = 2 * tblk + (tp >> 1);
    const int pa0 = w * 8192 + pr * 128 + ((pc ^ fz(pr)) * 16) + 8 * (tp & 1), pa1 = w * 8192 + (pr + 4) * 128 + ((pc ^ fz(pr + 4)) * 16) + 8 * (tp & 1);
#pragma unroll 1
    for (int t = 0; t < 8; t++) {
      if (t < 7) asm volatile("s_waitcnt vmcnt(8)" ::: "memory"); else asm volatile("s_waitcnt vmcnt(0)" ::: "memory");
      bar();
      LAS char* img = lds + (t & 1) * 65536;
      f32x4 s0 = {0.f, 0.f, 0.f, 0.f}, s1 = s0;
      u32x4 ka[16];
      u32x2 ta[4][4];
      const int nso = t < 6 ? ((t + 2) * 65536) ^ xt : OOB;
#pragma unroll
      for (int ks = 0; ks < 16; ks++) ka[ks] = *(LAS u32x4*)(img + ((ks & 1) ? qa1 : qa0) + (ks >> 1) * 8192);
      SB();
#pragma unroll
      for (int k2 = 0; k2 < 8; k2++) {
        s0 = mfma16(ka[2 * k2], qf[2 * k2], s0);
        s1 = mfma16(ka[2 * k2 + 1], qf[2 * k2 + 1], s1);
      }
      asm volatile("" : "+v"(s0), "+v"(s1));
      SB();
#pragma unroll
      for (int ks = 0; ks < 4; ks++) {
        ta[ks][0] = trread(img + pa0 + ks * 2048); ta[ks][1] = trread(img + pa1 + ks * 2048);
        ta[ks][2] = trread(img + (pa0 ^ 64) + ks * 2048); ta[ks][3] = trread(img + (pa1 ^ 64) + ks * 2048);
      }
      SB();
      float x[4];
#pragma unroll
      for (int j = 0; j < 4; j++) x[j] = rbf(rbf(s0[j] + s1[j]) * SCALE);
      const float lm = xrow_max(fmaxf(fmaxf(x[0], x[1]), fmaxf(x[2], x[3])));
      if (grp == 0) mx[h1 * 4 + kb] = lm;
      LAS u32x2* pw = (LAS u32x2*)(pl + h1 * 144 + (16 * kb + 4 * grp) * 2);
      u32x2 pp = {cvtpk(fexp2((x[0] - m1) * LOG2E), fexp2((x[1] - m1) * LOG2E)), cvtpk(fexp2((x[2] - m1) * LOG2E), fexp2((x[3] - m1) * LOG2E))};
      *pw = pp;
      bar();
      const f32x4 ma = *(LAS f32x4*)(mx + h1 * 4), mb = *(LAS f32x4*)(mx + h2 * 4);
      u32x4 pb[4];
#pragma unroll
      for (int ks = 0; ks < 4; ks++) pb[ks] = *(LAS u32x4*)(pl + h2 * 144 + (16 * ks + 8 * hh) * 2);
      const float n1 = fmaxf(m1, fmaxf(fmaxf(ma[0], ma[1]), fmaxf(ma[2], ma[3])));
      const float n2 = fmaxf(m2, fmaxf(fmaxf(mb[0], mb[1]), fmaxf(mb[2], mb[3])));
      float al1 = fexp2((m1 - n1) * LOG2E), al2 = fexp2((m2 - n2) * LOG2E);
      asm volatile("" : "+v"(pb[0]), "+v"(pb[1]), "+v"(pb[2]), "+v"(pb[3]));
      if (__any(n2 - m2 > 40.f)) {
        bar();
        pp = u32x2{cvtpk(fexp2((x[0] - n1) * LOG2E), fexp2((x[1] - n1) * LOG2E)), cvtpk(fexp2((x[2] - n1) * LOG2E), fexp2((x[3] - n1) * LOG2E))};
        *pw = pp;
        l1 *= al1;
#pragma unroll
        for (int j = 0; j < 16; j++) { o0[j] *= al2; o1[j] *= al2; }
        al1 = al2 = 1.f;
        bar();
#pragma unroll
        for (int ks = 0; ks < 4; ks++) pb[ks] = *(LAS u32x4*)(pl + h2 * 144 + (16 * ks + 8 * hh) * 2);
      }
      m1 = n1; m2 = n2;
#pragma unroll
      for (int ks = 0; ks < 4; ks++) {
        o0 = mfma32(u32x4{ta[ks][0][0], ta[ks][0][1], ta[ks][1][0], ta[ks][1][1]}, pb[ks], o0);
        o1 = mfma32(u32x4{ta[ks][2][0], ta[ks][2][1], ta[ks][3][0], ta[ks][3][1]}, pb[ks], o1);
        SB();
        if (t < 6) {
          dma1(rc, dvo, nso + 2 * ks * 8192, dlo + (t & 1) * 65536 + 2 * ks * 1024);
          dma1(rc, dvo, nso + (2 * ks + 1) * 8192, dlo + (t & 1) * 65536 + (2 * ks + 1) * 1024);
        }
        SB();
      }
      l1 = (l1 + ((bf_lo(pp[0]) + bf_hi(pp[0])) + (bf_lo(pp[1]) + bf_hi(pp[1])))) * al1;
#pragma unroll
      for (int j = 0; j < 16; j++) { o0[j] *= al2; o1[j] *= al2; }
    }
    bar();
    {
      float am = 0.f;
#pragma unroll
      for (int j = 0; j < 16; j++) am = fmaxf(am, fmaxf(fabsf(o0[j]), fabsf(o1[j])));
      auto sw = __builtin_amdgcn_permlane32_swap(__float_as_uint(am), __float_as_uint(am), false, false);
      am = fmaxf(__uint_as_float(sw[0]), __uint_as_float(sw[1]));
      const int e = max(__builtin_amdgcn_frexp_expf(am), -127);
      LAS char* tp = lds + h2 * 1040 + (64 * w + 4 * hh) * 2;
      auto pkn = [&](float a, float b) { return __builtin_bit_cast(unsigned, __builtin_amdgcn_cvt_pknorm_i16(__builtin_ldexpf(a, -e), __builtin_ldexpf(b, -e))); };
#pragma unroll
      for (int j4 = 0; j4 < 4; j4++) {
        *(LAS u32x2*)(tp + j4 * 16) = u32x2{pkn(o0[4 * j4], o0[4 * j4 + 1]), pkn(o0[4 * j4 + 2], o0[4 * j4 + 3])};
        *(LAS u32x2*)(tp + 64 + j4 * 16) = u32x2{pkn(o1[4 * j4], o1[4 * j4 + 1]), pkn(o1[4 * j4 + 2], o1[4 * j4 + 3])};
      }
      if (hh == 0) *(LAS unsigned char*)(scr + 9216 + h2 * 8 + w) = (unsigned char)(e + 127);
      const float ls = xrow_sum(l1);
      if (grp == 0) lr[kb * 32 + h1] = ls;
      bar();
#pragma unroll
      for (int i = 0; i < 4; i++) {
        u32x4 v = *(LAS u32x4*)(lds + (4 * w + i) * 1040 + lane * 16);
#pragma unroll
        for (int k = 0; k < 4; k++) v[k] = bfi(PMASK, ptag(tg, k), v[k]);
        stsc(v, rws, WS_PO + (aid * 32 + 4 * w) * 1024 + lane * 16, i * 1024);
      }
      if (tid < 32) {
        const u32x2 eb = *(LAS u32x2*)(scr + 9216 + tid * 8);
        const float l = (lr[tid] + lr[32 + tid]) + (lr[64 + tid] + lr[96 + tid]);
        stsc(u32x4{bfi(0xffffu, tg.lo, __float_as_uint(m2)), bfi(255u, ltag(tg), __float_as_uint(l)), eb[0], eb[1]}, rws, WS_PML + (aid * 32 + tid) * 16, 0);
      }
      if (w == 0) {
        const unsigned t = __builtin_amdgcn_readfirstlane(__builtin_amdgcn_raw_ptr_buffer_atomic_add_i32(8 + 4 * DCH, rws, lane == 0 ? cntoff : OOB, 0, AUX_SC1));
        const unsigned bs = t + 8 + 4 * (lane - 8);
        ctl[lane] = lane == 3 ? t : lane == 4 ? DCH : lane == 5 ? (t + 8 + 4 * DCH >= NUNIT ? 0x10000u : 0u) : lane < 8 ? 0u : (lane < 8 + DCH && bs < NUNIT) ? bs : ~0u;
        if (lane < 32) ctl[64 + lane] = ~0u;
      }
      bar();
    }
    const u32x4 g = ldsc(rws, qo, 0);
    cur = __builtin_amdgcn_readfirstlane(ctl[3]) + w;
#pragma unroll
    for (int i = 0; i < 8; i++) ra[i] = ldunit(i < 4 ? rk4 : rv4, voff, unit_off(cur), i & 3);
    SB();
    qwait(g, mktag(e0));
  } else {
    const Tag tg = mktag(e0);
    bar();
    {
      u32x4 bf[4];
#pragma unroll
      for (int ks = 0; ks < 4; ks++) bf[ks] = *(LAS u32x4*)(scr + 1024 + (l16 & 3) * 256 + ks * 64 + grp * 16);
      LAS char* tt = scr + 2048 + w * 512;
#pragma unroll
      for (int rt = 0; rt < 4; rt++) {
        f32x4 acc = {0.f, 0.f, 0.f, 0.f};
#pragma unroll
        for (int ks = 0; ks < 4; ks++) acc = mfma16(*(LAS u32x4*)(lds + (64 * w + 16 * rt + l16) * 256 + (((4 * ks + grp) ^ l16) * 16)), bf[ks], acc);
        if (l16 < 4) *(LAS u32x2*)(tt + l16 * 128 + (16 * rt + 4 * grp) * 2) = u32x2{cvtpk(acc[0], acc[1]), cvtpk(acc[2], acc[3])};
      }
      asm volatile("s_waitcnt lgkmcnt(0)" ::: "memory");
      const u32x2 v = *(LAS u32x2*)(tt + lane * 8);
      stsc(u32x4{v[0], tg.th, v[1], tg.th}, rws, WS_QABS + (grp * 32 + head) * 2048 + w * 256 + l16 * 16, 0);
    }
    bar();
    u32x4 wf[4][4], wr[16];
    {
      const rsrc_t ro = mkrs(woA + (size_t)head * 131072, 131072);
      dma_rows<16>(ro, 0, lds, 16 * w, lane);
      const rsrc_t rv = mkrs(wuv, 4u << 20);
      const int voff = (64 * w + 16 * grp) * 8192 + head * 256 + l16 * 16;
#pragma unroll
      for (int i = 0; i < 16; i++) wr[i] = ldnt(rv, voff, i * 8192);
      SB();
      asm volatile("s_waitcnt vmcnt(16)" ::: "memory");
      bar();
      wo_frags(lds, w, lane, wf);
      bar();
      const rsrc_t rb = mkrs(woB, 4u << 20);
      const int bvo = (lane >> 3) * 1024 + (((lane & 7) ^ ((lane >> 5) << 1)) * 16);
#pragma unroll
      for (int i = 0; i < 16; i++)
        dma1(rb, (i & 1) ? bvo ^ 64 : bvo, (4 * w + b4) * 131072 + (head >> 2) * 128 + i * 8192, (unsigned)(uintptr_t)lds + w * 16384 + i * 1024);
      SB();
      if (tid < 8) ((LAS unsigned*)(scr + 24576))[tid] = 0u;
    }
    const int hw = 4 * w + b4, cc = head >> 2, roff = WS_RS + hw * 16384 + lane * 16;
    u32x4 g[6];
    unsigned missS = 63;
#pragma unroll
    for (int s = 0; s < 6; s++) asm volatile("" : "=v"(g[s]));
    if (w == 0) {
      for (unsigned spins = 0;; spins++) {
        asm volatile("" ::: "memory");
        const u32x4 v = ldsc(rws, WS_PML + ((lane & 31) * 32 + head) * 16, 0);
        if (__builtin_popcount((unsigned)__builtin_amdgcn_ballot_w64(mok(v, tg))) >= NREL || spins > SPIN_CAP) break;
        __builtin_amdgcn_s_sleep(16);
      }
    }
    bar();
    {
      const int b = w >> 1, c4 = tid & 127;
      const unsigned ta = (lane & 1) ? ptag(tg, 2) : ptag(tg, 0), tb = (lane & 1) ? ptag(tg, 3) : ptag(tg, 1);
      u32x4 ml;
      u32x2 po[8];
#pragma unroll
      for (int s = 0; s < 8; s++) asm volatile("" : "=v"(po[s]));
      unsigned missP = 255;
      for (unsigned spins = 0;; spins++) {
        asm volatile("" ::: "memory");
        ml = ldsc(rws, WS_PML + ((8 * b + (lane & 7)) * 32 + head) * 16, 0);
#pragma unroll
        for (int s = 0; s < 8; s++)
          if (missP >> s & 1) po[s] = __builtin_amdgcn_raw_buffer_load_b64(rws, WS_PO + (8 * b * 32 + head) * 1024 + c4 * 8, s * 32768, AUX_SC1);
        SB();
#pragma unroll
        for (int s = 0; s < 8; s++) if (__all((((po[s][0] ^ ta) | (po[s][1] ^ tb)) & PMASK) == 0u)) missP &= ~(1u << s);
        if ((!missP && __all(mok(ml, tg))) || spins > SPIN_CAP) break;
        __builtin_amdgcn_s_sleep(2);
      }
#pragma unroll
      for (int s = 0; s < 6; s++) g[s] = ldsc(rws, roff, s * 1024);
      SB();
      ml[0] &= 0xffff0000u; ml[1] &= ~255u;
#pragma unroll
      for (int s = 0; s < 8; s++) po[s] = u32x2{bfi(PMASK, 0x00020002u, po[s][0]), bfi(PMASK, 0x00020002u, po[s][1])};
      float M = __uint_as_float(ml[0]);
      M = fmaxf(M, dpp<XOR1>(M)); M = fmaxf(M, dpp<XOR2>(M)); M = fmaxf(M, dpp<XOR7>(M));
      const float f = fexp2((__uint_as_float(ml[0]) - M) * LOG2E);
      float L = f * __uint_as_float(ml[1]);
      L += dpp<XOR1>(L); L += dpp<XOR2>(L); L += dpp<XOR7>(L);
      const float gl = __builtin_ldexpf(f, (int)((((w & 1) ? ml[3] : ml[2]) >> (8 * grp)) & 255) - 142);
      const float gs[8] = {dpp<0x150>(gl), dpp<0x151>(gl), dpp<0x152>(gl), dpp<0x153>(gl), dpp<0x154>(gl), dpp<0x155>(gl), dpp<0x156>(gl), dpp<0x157>(gl)};
      f32x4 a = {0.f, 0.f, 0.f, 0.f};
#pragma unroll
      for (int s = 0; s < 8; s++)
        a += gs[s] * f32x4{(float)(short)po[s][0], (float)((int)po[s][0] >> 16), (float)(short)po[s][1], (float)((int)po[s][1] >> 16)};
      const float inv = (32768.f / 32767.f) / L;
      const u32x2 v = {cvtpk(a[0] * inv, a[1] * inv), cvtpk(a[2] * inv, a[3] * inv)};
      *(LAS u32x2*)(scr + b * 1024 + c4 * 8) = v;
    }
    bar();
    {
      u32x4 xp[2][2];
#pragma unroll
      for (int s = 0; s < 2; s++) {
        LAS u32x4* xs = (LAS u32x4*)(scr + (2 * s + (l16 >> 3)) * 1024 + (64 * w + 16 * grp) * 2);
        xp[s][0] = xs[0]; xp[s][1] = xs[1];
      }
      f32x4 acc[2] = {{0.f, 0.f, 0.f, 0.f}, {0.f, 0.f, 0.f, 0.f}};
      diag_gemv(wr, xp, mask, acc);
      LAS float* red = (LAS float*)(scr + 8192);
#pragma unroll
      for (int s = 0; s < 2; s++) *(LAS f32x4*)(red + w * 512 + (2 * s + (grp >> 1)) * 128 + 8 * l16 + 4 * (grp & 1)) = acc[s];
      bar();
      if (tid < 256) {
        float a = 0.f, b = 0.f;
#pragma unroll
        for (int k = 0; k < 8; k++) { const f32x2 v = *(LAS f32x2*)(red + k * 512 + 2 * tid); a += v[0]; b += v[1]; }
        ((LAS unsigned*)(scr + 4096))[tid] = cvtpk(a, b);
      }
      bar();
    }
    const f32x4 yA = wo_calc(wf, scr + 4096, lane);
    drain();
    y_store(yA, w, lane, head, rws, WS_Y, tg);
    {
      const int q = l16 >> 2;
      LAS char* img = lds + w * 16384 + (8 * (q >> 1) + 4 * (grp & 1) + 2 * (grp >> 1) + (q & 1)) * 128 + 8 * (lane & 3);
      const int fl = (grp & 1) | (q & 2);
#pragma unroll
      for (int ks = 0; ks < 4; ks++)
#pragma unroll
        for (int tl = 0; tl < 4; tl++) {
          const u32x2 a = trread(img + (tl ^ fl) * 32 + ks * 4096), b = trread(img + (tl ^ fl) * 32 + ks * 4096 + 2048);
          wf[tl][ks] = u32x4{a[0], a[1], b[0], b[1]};
        }
    }
    const rsrc_t rout = mkrs(out, 8192);
    LAS unsigned* cnt = (LAS unsigned*)(scr + 24576);
    LAS char* const re = scr + 8192;
    LAS char* const rA = scr + 8192 + 6144;
    LAS char* const rl = scr + 8192 + 8192;
    const int ofa = WS_Y + (b4 * 8 + cc) * 16384 + w * 2048 + lane * 16;
    const int ofb = WS_YB + ((b4 * 8 + cc) * 4 + ((b4 + 1 + w) & 3)) * 512 + (lane & 31) * 16;
    unsigned todo = w < 3 ? 7u : 3u, missL = (1u << NSLOT) - 1;
    u32x4 x0, x1, xb, p[NSLOT];
    asm volatile("" : "=v"(x0), "=v"(x1), "=v"(xb));
#pragma unroll
    for (int s = 0; s < NSLOT; s++) asm volatile("" : "=v"(p[s]));
    double S0 = 0.0, S1 = 0.0, L = 0.0;
    float R = -INFINITY;
#pragma unroll
    for (int s = 0; s < 6; s++) if (__all(rok(g[s], tg))) missS &= ~(1u << s);
    for (unsigned spins = 0;; spins++) {
      asm volatile("" ::: "memory");
      if ((todo & 1) && !missS) {
#pragma unroll
        for (int t = 0; t < 3; t++) {
          const u32x4 g0 = g[t], g1 = g[t + 3];
          const float R0 = rec_r(g0), R1 = rec_r(g1), Rm = fmaxf(R0, R1);
          const double s0 = mkd(0u, p2hi(R0, Rm)), s1 = mkd(0u, p2hi(R1, Rm));
          const double inv = (double)__builtin_amdgcn_rcpf((float)__builtin_fma(rec_l(g0), s0, rec_l(g1) * s1));
          proj(cvtpk((float)(__builtin_fma(rec_a(g0), s0, rec_a(g1) * s1) * inv), (float)(__builtin_fma(rec_b(g0), s0, rec_b(g1) * s1) * inv)), wf,
               re + (t * 8 + w) * 256, lane);
        }
        asm volatile("" ::: "memory");
        if (__builtin_amdgcn_readfirstlane(lds_inc(cnt, lane)) == 7) {
#pragma unroll
          for (int u = 0; u < 2; u++) {
            const int t = u ? 2 : lane >> 5;
            const f32x2 v = rows_sum<8>(re + t * 2048 + (lane & 31) * 8);
            stsc(gpair(v[0], v[1], tg), rws, u && lane >= 32 ? OOB : WS_YB + ((((b4 + 1 + t) & 3) * 8 + cc) * 4 + b4) * 512 + (lane & 31) * 16, 0);
          }
        }
        todo &= ~1u;
      }
      if ((!todo && !missL) || spins > SPIN_CAP * 4) break;
      const unsigned wantL = (todo & 1) ? 0u : missL;
#pragma unroll
      for (int s = 0; s < 6; s++) if (missS >> s & 1) g[s] = ldsc(rws, roff, s * 1024);
      if (todo & 2) { x0 = ldsc(rws, ofa, 0); x1 = ldsc(rws, ofa, 1024); }
      if (todo & 4) xb = ldsc(rws, ofb, 0);
#pragma unroll
      for (int s = 0; s < NSLOT; s++) if (wantL >> s & 1) p[s] = ldsc(rws, roff, 8192 + s * 1024);
      SB();
#pragma unroll
      for (int s = 0; s < 6; s++) if ((missS >> s & 1) && __all(rok(g[s], tg))) missS &= ~(1u << s);
      if ((todo & 2) && __all(gok(x0, tg) & gok(x1, tg))) {
        const f32x2 r = {half_sum(gval(x0[0]) + gval(x1[0])), half_sum(gval(x0[2]) + gval(x1[2]))};
        if (lane < 32) *(volatile LAS f32x2*)(rA + w * 256 + lane * 8) = r;
        asm volatile("" ::: "memory");
        if (__builtin_amdgcn_readfirstlane(lds_inc(cnt + 1, lane)) == 7) {
          const f32x2 v = rows_sum<8>(rA + (lane & 31) * 8);
          __builtin_amdgcn_raw_buffer_store_b32(cvtpk(v[0], v[1]), rout, lane < 32 ? b4 * 2048 + cc * 128 + l16 * 8 + grp * 4 : OOB, 0, 0);
        }
        todo &= ~2u;
      }
      if ((todo & 4) && __all(gok(xb, tg))) {
        *(volatile LAS f32x2*)(rl + (8 + w) * 256 + (lane & 31) * 8) = f32x2{gval(xb[0]), gval(xb[2])};
        todo &= ~4u;
      }
#pragma unroll
      for (int s = 0; s < NSLOT; s++)
        if ((wantL >> s & 1) && __all(rok(p[s], tg))) {
          const float Rs = rec_r(p[s]), Rn = fmaxf(R, Rs);
          const double s0 = mkd(0u, p2hi(R, Rn)), s1 = mkd(0u, p2hi(Rs, Rn));
          L = __builtin_fma(rec_l(p[s]), s1, L * s0);
          S0 = __builtin_fma(rec_a(p[s]), s1, S0 * s0); S1 = __builtin_fma(rec_b(p[s]), s1, S1 * s0);
          R = Rn;
          missL &= ~(1u << s);
        }
      if (missL == (1u << NSLOT) - 1) __builtin_amdgcn_s_sleep(6);
    }
    {
      const double inv = (double)__builtin_amdgcn_rcpf((float)L);
      proj(cvtpk((float)(S0 * inv), (float)(S1 * inv)), wf, rl + w * 256, lane);
      asm volatile("" ::: "memory");
      const unsigned nv = lds_inc(cnt + 2, lane);
      asm volatile("" ::: "memory");
      const f32x2 v = rows_sum<11>(rl + (lane & 31) * 8);
      if (__builtin_amdgcn_readfirstlane(nv) == 7) {
        __builtin_amdgcn_raw_buffer_store_b32(cvtpk(v[0], v[1]), rout, lane < 32 ? b4 * 2048 + 1024 + cc * 128 + lane * 4 : OOB, 0, 0);
        __builtin_amdgcn_raw_buffer_store_b64(__builtin_bit_cast(u32x2, tg.e), rws, head == 0 && lane == 0 ? WS_EPOCH : OOB, 0, AUX_SC1);
      }
    }
    return;
  }
  }
  {
    const u32x4 q4 = *(LAS u32x4*)(qst + 256 + l16 * 16);
    Acc A;
    A.o[0] = A.o[1] = A.l = 0.0; A.R = -INFINITY;
    b_units(ra, rk4, rv4, cur, q4, lane, mask, ctl, rws, cntoff, A);
    const Tag tg = mktag(e0);
    b_merge(scr + 8192 + 8704, ctl + 2, w, lane, A, rws, WS_RS + (head * 16 + 8 + sub) * 1024, tg);
    SB();
    asm volatile("" :: "v"(ra[0]), "v"(ra[1]), "v"(ra[2]), "v"(ra[3]), "v"(ra[4]), "v"(ra[5]), "v"(ra[6]), "v"(ra[7]));
  }
}

extern "C" void kernel_launch(void* const* d_in, const int* in_sizes, int n_in, void* d_out, int out_size, void* d_ws, size_t ws_size,
                              hipStream_t stream) {
  k_all<<<256, 512, 0, stream>>>((const char*)d_in[0], (const char*)d_in[1], (const char*)d_in[2], (const char*)d_in[3], (const char*)d_in[4],
                                 (const char*)d_in[5], (const char*)d_in[6], (const char*)d_in[7], (const char*)d_in[8], (const char*)d_in[9],
                                 (const char*)d_in[10], (char*)d_ws + ((ws_size / 2) & ~(size_t)4095), (unsigned*)d_out);
}
```

```cpp
#include <hip/hip_runtime.h>
#include <stdint.h>

typedef __attribute__((ext_vector_type(4))) unsigned u32x4;
typedef __attribute__((ext_vector_type(2))) unsigned u32x2;
typedef __attribute__((ext_vector_type(8))) unsigned u32x8;
typedef __attribute__((ext_vector_type(16))) unsigned u32x16;
typedef __attribute__((ext_vector_type(4))) float f32x4;
typedef __attribute__((ext_vector_type(2))) float f32x2;
typedef __attribute__((ext_vector_type(2))) double f64x2;
typedef __attribute__((ext_vector_type(16))) float f32x16;
typedef __attribute__((ext_vector_type(8))) __bf16 bf16x8;
typedef __attribute__((ext_vector_type(2))) __bf16 bf16x2;
typedef __attribute__((ext_vector_type(4))) short s16x4;
typedef unsigned long long u64;
typedef __amdgpu_buffer_rsrc_t rsrc_t;
#define LAS __attribute__((address_space(3)))
#define GAS __attribute__((address_space(1)))

#define CPF 35584
#define PAT "5c10p2ba43"
constexpr int WTH = 5;
constexpr int DCH = 2;
constexpr int NXA = 8, NXB = 8;
constexpr int NREL = 24;

constexpr int AUX_NT = 2, AUX_SC1 = 16, OOB = 0x40000000;
constexpr float SCALE = 0.08838834764831845f;
constexpr float LOG2E = 1.4426950408889634f;
constexpr unsigned SPIN_CAP = 1u << 17;
constexpr int NSLOT = 7, NUNIT = 256;

constexpr int WS_EPOCH = 0;
constexpr int CSTR    = 4352;
constexpr int WS_CNT  = 8192;
constexpr int WS_QB   = WS_CNT + 32 * CSTR;
constexpr int WS_QABS = WS_QB + 32 * 2048;
constexpr int WS_PML  = WS_QABS + 4 * 32 * 2048;
constexpr int WS_PO   = WS_PML + 32 * 32 * 16;
constexpr int WS_RS   = WS_PO + 32 * 32 * 1024;
constexpr int WS_Y    = WS_RS + 32 * 16 * 1024;
constexpr int WS_YB   = WS_Y + 4 * 8 * 32 * 512;
constexpr int WS_END  = WS_YB + 4 * 8 * 4 * 512;

constexpr int LDS_BYTES = 163840;
constexpr int LDS_SCR = 131072;

__device__ __forceinline__ unsigned cvtpk(float lo, float hi) {
  f32x2 v = {lo, hi};
  return __builtin_bit_cast(unsigned, __builtin_convertvector(v, bf16x2));
}
__device__ __forceinline__ float bf_lo(unsigned u) { return __uint_as_float(u << 16); }
__device__ __forceinline__ float bf_hi(unsigned u) { return __uint_as_float(u & 0xffff0000u); }
__device__ __forceinline__ float rbf(float x) { return __uint_as_float(cvtpk(x, x) << 16); }
__device__ __forceinline__ float dot2(unsigned k, unsigned q, float acc) {
  return __builtin_amdgcn_fdot2_f32_bf16(__builtin_bit_cast(bf16x2, k), __builtin_bit_cast(bf16x2, q), acc, false);
}
template <int CTRL> __device__ __forceinline__ float dpp(float x) {
  return __builtin_bit_cast(float, __builtin_amdgcn_mov_dpp(__builtin_bit_cast(int, x), CTRL, 0xf, 0xf, true));
}
template <int CTRL> __device__ __forceinline__ unsigned dppu(unsigned x) {
  return (unsigned)__builtin_amdgcn_mov_dpp((int)x, CTRL, 0xf, 0xf, true);
}
constexpr int XOR1 = 0xB1, XOR2 = 0x4E, XOR7 = 0x141, XOR8 = 0x128, ROR4 = 0x124;
__device__ __forceinline__ float xrow_max(float x) {
  auto s = __builtin_amdgcn_permlane16_swap(__float_as_uint(x), __float_as_uint(x), false, false);
  x = fmaxf(__uint_as_float(s[0]), __uint_as_float(s[1]));
  auto t = __builtin_amdgcn_permlane32_swap(__float_as_uint(x), __float_as_uint(x), false, false);
  return fmaxf(__uint_as_float(t[0]), __uint_as_float(t[1]));
}
__device__ __forceinline__ float xrow_sum(float x) {
  auto s = __builtin_amdgcn_permlane16_swap(__float_as_uint(x), __float_as_uint(x), false, false);
  x = __uint_as_float(s[0]) + __uint_as_float(s[1]);
  auto t = __builtin_amdgcn_permlane32_swap(__float_as_uint(x), __float_as_uint(x), false, false);
  return __uint_as_float(t[0]) + __uint_as_float(t[1]);
}
__device__ __forceinline__ float row_max(float x) {
  x = fmaxf(x, dpp<XOR8>(x)); x = fmaxf(x, dpp<ROR4>(x)); x = fmaxf(x, dpp<XOR2>(x)); x = fmaxf(x, dpp<XOR1>(x));
  return x;
}
__device__ __forceinline__ float row_sum(float x) {
  x += dpp<XOR8>(x); x += dpp<ROR4>(x); x += dpp<XOR2>(x); x += dpp<XOR1>(x);
  return x;
}
__device__ __forceinline__ float fexp2(float x) { return __builtin_amdgcn_exp2f(x); }
__device__ __forceinline__ f32x4 mfma16(u32x4 a, u32x4 b, f32x4 c) {
  return __builtin_amdgcn_mfma_f32_16x16x32_bf16(__builtin_bit_cast(bf16x8, a), __builtin_bit_cast(bf16x8, b), c, 0, 0, 0);
}
__device__ __forceinline__ f32x16 mfma32(u32x4 a, u32x4 b, f32x16 c) {
  return __builtin_amdgcn_mfma_f32_32x32x16_bf16(__builtin_bit_cast(bf16x8, a), __builtin_bit_cast(bf16x8, b), c, 0, 0, 0);
}
__device__ __forceinline__ rsrc_t mkrs(const GAS char* p, unsigned bytes) {
  return __builtin_amdgcn_make_buffer_rsrc((void*)p, 0, bytes, 0x00020000);
}
__device__ __forceinline__ void bar() { asm volatile("s_waitcnt lgkmcnt(0)\n\ts_barrier" ::: "memory"); }
__device__ __forceinline__ void drain() { asm volatile("s_waitcnt vmcnt(0)" ::: "memory"); }
#define SB() __builtin_amdgcn_sched_barrier(0)
template <int N> __device__ __forceinline__ void vmwait() { asm volatile("s_waitcnt vmcnt(%0)" :: "n"(N) : "memory"); }
__device__ __forceinline__ unsigned swz(unsigned row) { return ((row & 3) << 2) | ((row >> 2) & 2); }
__device__ __forceinline__ u32x2 trread(LAS char* p) {
  return __builtin_bit_cast(u32x2, __builtin_amdgcn_ds_read_tr16_b64_v4i16((LAS s16x4*)p));
}

struct Tag { unsigned tl, th, lo, rb; u64 e; };
__device__ __forceinline__ Tag mktag(u64 e0) {
  Tag t;
  asm volatile("" : "+s"(e0));
  t.e = e0 * 6364136223846793005ull + 1442695040888963407ull;
  t.th = (unsigned)(t.e >> 32);
  t.lo = (unsigned)t.e;
  t.tl = t.lo & 0xff000000u;
  t.rb = ((t.lo >> 9) & 255u) | (~t.lo & 256u);
  return t;
}
__device__ __forceinline__ unsigned gpack(float v, const Tag& t) { return ((__float_as_uint(v) + 0x80u) >> 8) | t.tl; }
__device__ __forceinline__ u32x4 gpair(float a, float b, const Tag& t) { return u32x4{gpack(a, t), t.th, gpack(b, t), t.th}; }
__device__ __forceinline__ bool gok(u32x4 g, const Tag& t) {
  return ((((g[0] ^ t.tl) | (g[2] ^ t.tl)) >> 24) | (g[1] ^ t.th) | (g[3] ^ t.th)) == 0u;
}
__device__ __forceinline__ float gval(unsigned lo) { return __uint_as_float(lo << 8); }
__device__ __forceinline__ bool qok(u32x4 g, const Tag& t) { return ((g[1] ^ t.th) | (g[3] ^ t.th)) == 0u; }
__device__ __forceinline__ double mkd(unsigned lo, unsigned hi) { return __builtin_bit_cast(double, u32x2{lo, hi}); }
__device__ __forceinline__ u32x4 rpack(double a, double b, double l, float r, const Tag& t) {
  const u32x2 ua = __builtin_bit_cast(u32x2, a), ub = __builtin_bit_cast(u32x2, b), ul = __builtin_bit_cast(u32x2, l);
  const u64 ml = ((u64)(unsigned)__builtin_amdgcn_readfirstlane(ul[1]) << 32) | (unsigned)__builtin_amdgcn_readfirstlane(ul[0]);
  const u64 mr = (unsigned)__builtin_amdgcn_readfirstlane(__float_as_uint(r));
  const unsigned ta = t.lo & 511u, tb = t.rb;
  return u32x4{(ua[0] & ~1023u) | (__builtin_amdgcn_inverse_ballot_w64(ml) ? ta | 512u : ta), ua[1],
               (ub[0] & ~1023u) | (__builtin_amdgcn_inverse_ballot_w64(mr) ? tb | 512u : tb), ub[1]};
}
__device__ __forceinline__ bool rok(u32x4 g, const Tag& t) { return (((g[0] ^ t.lo) | (g[2] ^ t.rb)) & 511u) == 0u; }
constexpr unsigned PMASK = 0x00030003u;
__device__ __forceinline__ unsigned ptag(const Tag& t, int k) {
  const unsigned n = k == 0 ? t.lo : k == 3 ? t.lo >> 10 : ((t.lo >> (1 + 3 * k)) & 7u) | (~t.lo & 8u);
  return (n & 3u) | ((n & 12u) << 14);
}
__device__ __forceinline__ unsigned ltag(const Tag& t) { return ((t.lo >> 16) & 127u) | (~t.lo & 128u); }
__device__ __forceinline__ bool mok(u32x4 g, const Tag& t) { return (((g[0] ^ t.lo) & 0xffffu) | ((g[1] ^ ltag(t)) & 255u)) == 0u; }
__device__ __forceinline__ unsigned bfi(unsigned m, unsigned a, unsigned b) { return (a & m) | (b & ~m); }
__device__ __forceinline__ double rec_a(u32x4 g) { return mkd(g[0] & ~1023u, g[1]); }
__device__ __forceinline__ double rec_b(u32x4 g) { return mkd(g[2] & ~1023u, g[3]); }
__device__ __forceinline__ double rec_l(u32x4 g) { return __builtin_bit_cast(double, __builtin_amdgcn_ballot_w64((g[0] & 512u) != 0u)); }
__device__ __forceinline__ float rec_r(u32x4 g) { return __uint_as_float((unsigned)__builtin_amdgcn_ballot_w64((g[2] & 512u) != 0u)); }
__device__ __forceinline__ int edif(float a, float b) {
  int r;
  asm("v_cvt_i32_f32 %0, %1" : "=v"(r) : "v"(a - b));
  return r;
}
__device__ __forceinline__ unsigned p2hi(float a, float b) { return (unsigned)max(edif(a, b) + 1023, 0) << 20; }
__device__ __forceinline__ unsigned rdu(unsigned v, int l) { return __builtin_amdgcn_readlane(v, l); }

__device__ __forceinline__ void diag_gemv(const u32x4 (&wr)[16], const u32x4 (&xp)[2][2], const unsigned (&mask)[4], f32x4 (&acc)[2]) {
#pragma unroll
  for (int i = 0; i < 16; i++) {
#pragma unroll
    for (int s = 0; s < 2; s++) {
      const unsigned src = xp[s][i >> 3][(i >> 1) & 3];
      const unsigned dup = __builtin_amdgcn_perm(src, src, (i & 1) ? 0x03020302u : 0x01000100u);
      const u32x4 a = {dup & mask[0], dup & mask[1], dup & mask[2], dup & mask[3]};
      acc[s] = mfma16(a, wr[i], acc[s]);
    }
  }
}

constexpr int bitpos(char c) { for (int i = 0; i < 10; i++) if (PAT[i] == c) return 10 + i; return 31; }
constexpr int goff(int t) { int o = 0; for (int j = 0; j < 6; j++) o |= ((t >> j) & 1) << bitpos('0' + j); return o; }
struct Acc { double o[2]; double l; float R; };
__device__ __forceinline__ u32x4 ldnt(rsrc_t r, int voff, int so) { return __builtin_amdgcn_raw_buffer_load_b128(r, voff, so, AUX_NT); }
__device__ __forceinline__ u32x4 ldslot(rsrc_t r, int voff, int so, int i) { return ldnt(r, voff + goff(i & 3), so ^ goff(i & ~3)); }
__device__ __forceinline__ u32x4 ldunit(rsrc_t r, int voff, int so, int i) { return ldnt(r, voff + i * 1024, so); }
__device__ __forceinline__ u32x4 ldsc(rsrc_t r, int voff, int so) { return __builtin_amdgcn_raw_buffer_load_b128(r, voff, so, AUX_SC1); }
__device__ __forceinline__ void stsc(u32x4 v, rsrc_t r, int voff, int so) { __builtin_amdgcn_raw_buffer_store_b128(v, r, voff + so, 0, AUX_SC1); }
__device__ __forceinline__ float dot8(u32x4 k, u32x4 q) { return dot2(k[3], q[3], dot2(k[2], q[2], dot2(k[1], q[1], dot2(k[0], q[0], 0.f)))); }
#define TR_STEP(HALF, CTRL, BIT) _Pragma("unroll") for (int h = 0; h < (HALF); ++h) { \
    const float t1 = x[h] + dpp<CTRL>(x[h]), t2 = x[h + (HALF)] + dpp<CTRL>(x[h + (HALF)]); x[h] = (c & (BIT)) ? t2 : t1; }
template <int N> __device__ __forceinline__ float k_step(u32x4 (&ring)[16], u32x4 q, int c, rsrc_t rn, int voff, int so, bool ext, LAS char* xp) {
  float x[16];
#pragma unroll
  for (int i = 0; i < 16; i++) {
    x[i] = dot8(ring[i], q);
    asm volatile("s_waitcnt vmcnt(%1)" : "+v"(x[i]) : "n"(WTH - 1));
    if (i >= 16 - N && ext) ring[i] = *(volatile LAS u32x4*)(xp + (i - (16 - N)) * 1024);
    else ring[i] = ldslot(rn, voff, so, i);
    SB();
  }
  TR_STEP(8, XOR8, 8) TR_STEP(4, XOR7, 4) TR_STEP(2, XOR2, 2) TR_STEP(1, XOR1, 1)
  return rbf(rbf(x[0]) * SCALE);
}
template <int BC> __device__ __forceinline__ void pv(u32x4 v, unsigned pk, const unsigned (&mask)[4], f32x4& acc) {
  const unsigned d = dppu<0x150 + BC>(pk);
  acc = mfma16(u32x4{d & mask[0], d & mask[1], d & mask[2], d & mask[3]}, v, acc);
}
template <int I, int MODE> __device__ __forceinline__ void v_step(u32x4 (&ring)[16], unsigned pk, const unsigned (&mask)[4], f32x4& acc,
                                                                  rsrc_t rk, rsrc_t rv, int voff, int so) {
  pv<I>(ring[I], pk, mask, acc);
  if (MODE == 1 || (MODE == 2 && I < 8)) {
    asm volatile("s_waitcnt vmcnt(%1)" : "+v"(acc) : "n"(WTH - 1));
    ring[I] = MODE == 1 ? ldslot(rv, voff, so, I) : ldunit(I < 4 ? rk : rv, voff, so, I & 3);
  } else {
    asm volatile("" : "+v"(acc));
  }
  SB();
  if constexpr (I < 15) v_step<I + 1, MODE>(ring, pk, mask, acc, rk, rv, voff, so);
}
__device__ __forceinline__ float exp_ref(float mx, float& f) {
  const float M = __builtin_ceilf(mx * LOG2E);
  f = __builtin_amdgcn_fmed3f(fmaf(mx, LOG2E, -M), -1.f, 0.f);
  return M;
}
__device__ __forceinline__ unsigned lds_inc(LAS unsigned* p, int lane) {
  unsigned t = 0, z = 0;
  asm volatile("" : "+v"(z));
  if (lane == 0) t = __hip_atomic_fetch_add(p + z, 1u, __ATOMIC_RELAXED, __HIP_MEMORY_SCOPE_WORKGROUP);
  return t;
}
struct Take { unsigned unit, pend; bool fetch; };
__device__ __forceinline__ void lds_add(LAS unsigned* p, unsigned v) { __hip_atomic_fetch_add(p, v, __ATOMIC_RELAXED, __HIP_MEMORY_SCOPE_WORKGROUP); }
__device__ __forceinline__ Take take(LAS unsigned* ctl, unsigned kv, rsrc_t rws, int cntoff, int lane) {
  const unsigned k = __builtin_amdgcn_readfirstlane(kv);
  unsigned b, s;
  for (unsigned spins = 0;; spins++) {
    const unsigned vs = ((volatile LAS unsigned*)ctl)[5], vb = ((volatile LAS unsigned*)ctl)[8 + (k >> 2)];
    s = __builtin_amdgcn_readfirstlane(vs); b = __builtin_amdgcn_readfirstlane(vb);
    if (b != ~0u || (s >= 0x10000u && (s & 0xffffu) == 0u) || spins > SPIN_CAP * 16) break;
    __builtin_amdgcn_s_sleep(1);
  }
  Take t;
  t.unit = min(b, (unsigned)NUNIT) + (k & 3);
  t.fetch = (k & 3) == 0 && b < NUNIT && s < 0x10000u;
  const bool go = lane == 0 && t.fetch;
  if (go) lds_add(ctl + 5, 1u);
  asm volatile("s_waitcnt lgkmcnt(0)" ::: "memory");
  t.pend = __builtin_amdgcn_raw_ptr_buffer_atomic_add_i32(4, rws, go ? cntoff : OOB, 0, AUX_SC1);
  SB();
  return t;
}
__device__ __forceinline__ void give(LAS unsigned* ctl, const Take& t, int lane) {
  if (t.fetch && lane == 0) {
    unsigned z = 0;
    asm volatile("" : "+v"(z));
    if (t.pend < NUNIT) {
      const unsigned i = __hip_atomic_fetch_add(ctl + 4 + z, 1u, __ATOMIC_RELAXED, __HIP_MEMORY_SCOPE_WORKGROUP);
      ((volatile LAS unsigned*)ctl)[8 + i] = t.pend;
    }
    asm volatile("" ::: "memory");
    lds_add(ctl + 5 + z, t.pend >= NUNIT - 4 ? 0xffffu : ~0u);
  }
}
__device__ __forceinline__ int unit_off(unsigned t) { return (int)min(t, (unsigned)NUNIT) * 4096; }

__device__ __forceinline__ void b_units(u32x4 (&ra)[16], rsrc_t rk, rsrc_t rv, unsigned cur, u32x4 q, int lane, const unsigned (&mask)[4],
                                        LAS unsigned* ctl, rsrc_t rws, int cntoff, Acc& A) {
  const int c = lane & 15, voff = lane * 16;
#pragma unroll 1
  while (cur < NUNIT) {
    const unsigned kv = lds_inc(ctl, lane);
    SB();
    float x[4];
#pragma unroll
    for (int i = 0; i < 4; i++) x[i] = dot8(ra[i], q);
    asm volatile("" : "+v"(x[0]), "+v"(x[1]), "+v"(x[2]), "+v"(x[3]));
    SB();
    const Take t = take(ctl, kv, rws, cntoff, lane);
    const int so = unit_off(t.unit);
#pragma unroll
    for (int i = 0; i < 4; i++) {
      ra[i] = ldunit(rk, voff, so, i);
      SB();
    }
    TR_STEP(2, XOR8, 8) TR_STEP(1, XOR7, 4)
    x[0] += dpp<XOR2>(x[0]); x[0] += dpp<XOR1>(x[0]);
    const float s = rbf(rbf(x[0]) * SCALE);
    float mx = fmaxf(s, dpp<XOR8>(s));
    mx = xrow_max(fmaxf(mx, dpp<ROR4>(mx)));
    float f;
    const float M = exp_ref(mx, f);
    const float p = fexp2(fmaf(s - mx, LOG2E, f));
    unsigned pk = cvtpk(p, p);
    asm volatile("" : "+v"(pk));
    float ls = bf_lo(pk);
    ls += dpp<XOR8>(ls);
    ls = xrow_sum(ls + dpp<ROR4>(ls));
    const float Rn = fmaxf(A.R, M);
    const double s0 = mkd(0u, p2hi(A.R, Rn)), s1 = mkd(0u, p2hi(M, Rn));
    A.o[0] *= s0; A.o[1] *= s0;
    A.l = __builtin_fma((double)ls, s1, A.l * s0);
    A.R = Rn;
    asm volatile("" : "+v"(A.o[0]), "+v"(A.o[1]), "+v"(A.l));
    SB();
    f32x4 acc = {0.f, 0.f, 0.f, 0.f};
    pv<0>(ra[4], pk, mask, acc);  asm volatile("" : "+v"(acc)); ra[4] = ldunit(rv, voff, so, 0); SB();
    pv<4>(ra[5], pk, mask, acc);  asm volatile("" : "+v"(acc)); ra[5] = ldunit(rv, voff, so, 1); SB();
    pv<8>(ra[6], pk, mask, acc);  asm volatile("" : "+v"(acc)); ra[6] = ldunit(rv, voff, so, 2); SB();
    pv<12>(ra[7], pk, mask, acc); asm volatile("" : "+v"(acc)); ra[7] = ldunit(rv, voff, so, 3); SB();
    A.o[0] = __builtin_fma((double)(lane < 32 ? acc[0] : acc[2]), s1, A.o[0]);
    A.o[1] = __builtin_fma((double)(lane < 32 ? acc[1] : acc[3]), s1, A.o[1]);
    asm volatile("" : "+v"(A.o[0]), "+v"(A.o[1]));
    SB();
    give(ctl, t, lane);
    cur = t.unit;
  }
}
#undef TR_STEP
__device__ __forceinline__ int rec_col(int lane) { return 8 * (lane & 15) + 4 * ((lane >> 4) & 1) + 2 * (lane >> 5); }
__device__ __forceinline__ void b_merge(LAS char* mrg, LAS unsigned* arrive, int w, int lane, const Acc& A, rsrc_t rws, int roff, const Tag& tg) {
  *(volatile LAS f64x2*)(mrg + w * 1024 + lane * 16) = f64x2{A.o[0], A.o[1]};
  *(volatile LAS double*)(mrg + 8192 + w * 8) = A.l;
  *(volatile LAS float*)(mrg + 8256 + w * 4) = A.R;
  asm volatile("" ::: "memory");
  const unsigned nv = lds_inc(arrive, lane);
  asm volatile("" ::: "memory");
  const f32x4 Ra = *(volatile LAS f32x4*)(mrg + 8256), Rb = *(volatile LAS f32x4*)(mrg + 8272);
  const float Rw = *(volatile LAS float*)(mrg + 8256 + (lane & 7) * 4);
  f64x2 o[8], lw[4];
#pragma unroll
  for (int i = 0; i < 8; i++) o[i] = *(volatile LAS f64x2*)(mrg + i * 1024 + lane * 16);
#pragma unroll
  for (int i = 0; i < 4; i++) lw[i] = *(volatile LAS f64x2*)(mrg + 8192 + i * 16);
  SB();
  if (__builtin_amdgcn_readfirstlane(nv) == 7) {
    const float R = fmaxf(fmaxf(fmaxf(Ra[0], Ra[1]), fmaxf(Ra[2], Ra[3])), fmaxf(fmaxf(Rb[0], Rb[1]), fmaxf(Rb[2], Rb[3])));
    const unsigned sw = p2hi(Rw, R);
    double v0[2] = {0.0, 0.0}, v1[2] = {0.0, 0.0}, L[2] = {0.0, 0.0};
#pragma unroll
    for (int i = 0; i < 8; i++) {
      const double sc = mkd(0u, rdu(sw, i));
      v0[i & 1] = __builtin_fma(o[i][0], sc, v0[i & 1]); v1[i & 1] = __builtin_fma(o[i][1], sc, v1[i & 1]);
      L[i & 1] = __builtin_fma(lw[i >> 1][i & 1], sc, L[i & 1]);
    }
    stsc(rpack(v0[0] + v0[1], v1[0] + v1[1], L[0] + L[1], R, tg), rws, roff + lane * 16, 0);
  }
}

__device__ __forceinline__ unsigned fz(unsigned row) { return ((row & 2) << 1) | ((row >> 1) & 2); }
__device__ __forceinline__ void dma1(rsrc_t rs, int voff, int so, unsigned ldsaddr) {
  unsigned keep;
  asm volatile("s_mov_b32 %0, m0\n\ts_mov_b32 m0, %1\n\ts_nop 0\n\tbuffer_load_dwordx4 %2, %3, %4 offen nt lds\n\ts_mov_b32 m0, %0"
               : "=&s"(keep) : "s"(ldsaddr), "v"(voff), "s"(rs), "s"(so) : "memory");
}
template <int NR>
__device__ __forceinline__ void dma_rows(rsrc_t rs, int srcoff, LAS char* img, int r0, int lane) {
#pragma unroll
  for (int u = 0; u < NR; u++) {
    const int r = r0 + u;
    unsigned keep;
    asm volatile("s_mov_b32 %0, m0\n\ts_mov_b32 m0, %1\n\ts_nop 0\n\tbuffer_load_dwordx4 %2, %3, %4 offen nt lds\n\ts_mov_b32 m0, %0"
                 : "=&s"(keep) : "s"((unsigned)(uintptr_t)img + r * 1024), "v"((lane ^ swz(r)) * 16), "s"(rs), "s"(srcoff + r * 1024) : "memory");
  }
  SB();
}

__device__ __forceinline__ void wo_frags(LAS char* img, int w, int lane, u32x4 (&wf)[4][4]) {
  const int g = lane >> 4, q = (lane & 15) >> 2, p = lane & 3;
#pragma unroll
  for (int ks = 0; ks < 4; ks++)
#pragma unroll
    for (int tl = 0; tl < 4; tl++) {
      const int ch = 8 * w + 2 * tl + (p >> 1);
      const int r0 = 32 * ks + 8 * g + q, r1 = r0 + 4;
      const u32x2 a = trread(img + r0 * 1024 + ((ch ^ swz(r0)) * 16) + 8 * (p & 1));
      const u32x2 b = trread(img + r1 * 1024 + ((ch ^ swz(r1)) * 16) + 8 * (p & 1));
      wf[tl][ks] = u32x4{a[0], a[1], b[0], b[1]};
    }
}
__device__ __forceinline__ f32x4 wo_calc(const u32x4 (&wf)[4][4], LAS char* cb, int lane) {
  const int n = lane & 15, grp = lane >> 4;
  u32x4 bf[4];
#pragma unroll
  for (int ks = 0; ks < 4; ks++) bf[ks] = *(LAS u32x4*)(cb + (n & 3) * 256 + ks * 64 + grp * 16);
  f32x4 acc[4];
#pragma unroll
  for (int tl = 0; tl < 4; tl++) acc[tl] = f32x4{0.f, 0.f, 0.f, 0.f};
#pragma unroll
  for (int ks = 0; ks < 4; ks++)
#pragma unroll
    for (int tl = 0; tl < 4; tl++) acc[tl] = mfma16(wf[tl][ks], bf[ks], acc[tl]);
  const f32x4 lo = (n & 4) ? acc[1] : acc[0], hi = (n & 4) ? acc[3] : acc[2];
  return (n & 8) ? hi : lo;
}
__device__ __forceinline__ void y_store(f32x4 y, int w, int lane, int slot, rsrc_t rws, int yoff, const Tag& tg) {
  const int off = yoff + (((lane & 3) * 8 + w) * 32 + slot) * 512 + ((lane & 12) + (lane >> 4)) * 16;
  stsc(gpair(y[0], y[1], tg), rws, off, 0);
  stsc(gpair(y[2], y[3], tg), rws, off, 256);
}
template <int K> __device__ __forceinline__ u32x4 bc4(unsigned p) { return u32x4{dppu<0x150 + K>(p), dppu<0x151 + K>(p), dppu<0x152 + K>(p), dppu<0x153 + K>(p)}; }
__device__ __forceinline__ void proj(unsigned pr, const u32x4 (&wf)[4][4], LAS char* row, int lane) {
  const u32x4 bf[4] = {bc4<0>(pr), bc4<4>(pr), bc4<8>(pr), bc4<12>(pr)};
  f32x4 acc[4];
#pragma unroll
  for (int tl = 0; tl < 4; tl++) acc[tl] = f32x4{0.f, 0.f, 0.f, 0.f};
#pragma unroll
  for (int ks = 0; ks < 4; ks++)
#pragma unroll
    for (int tl = 0; tl < 4; tl++) acc[tl] = mfma16(wf[tl][ks], bf[ks], acc[tl]);
  const f32x4 lo = (lane & 1) ? acc[1] : acc[0], hi = (lane & 1) ? acc[3] : acc[2];
  if ((lane & 12) == 0) *(volatile LAS f32x4*)(row + (lane & 3) * 64 + (lane >> 4) * 16) = (lane & 2) ? hi : lo;
}
template <int N> __device__ __forceinline__ f32x2 rows_sum(LAS char* p) {
  f32x2 v[N];
#pragma unroll
  for (int i = 0; i < N; i++) v[i] = *(volatile LAS f32x2*)(p + i * 256);
#pragma unroll
  for (int st = 1; st < N; st *= 2)
#pragma unroll
    for (int i = 0; i + st < N; i += 2 * st) v[i] += v[i + st];
  return v[0];
}
__device__ __forceinline__ float half_sum(float x) {
  auto t = __builtin_amdgcn_permlane32_swap(__float_as_uint(x), __float_as_uint(x), false, false);
  return __uint_as_float(t[0]) + __uint_as_float(t[1]);
}

__global__ __launch_bounds__(512, 2) void k_all(const char* a0_, const char* a1_, const char* a2_, const char* a3_, const char* a4_, const char* a5_,
                                                const char* a6_, const char* a7_, const char* a8_, const char* a9_, const char* a10_, char* a11_, unsigned* a12_) {
  u32x16 ka; u32x8 kb_; u32x2 kc_;
  asm volatile("s_load_dwordx16 %0, %3, 0x0\n\ts_load_dwordx8 %1, %3, 0x40\n\ts_load_dwordx2 %2, %3, 0x60"
               : "=&s"(ka), "=&s"(kb_), "=&s"(kc_) : "s"(__builtin_amdgcn_kernarg_segment_ptr()));
  __shared__ __attribute__((aligned(16))) char smem[LDS_BYTES];
  LAS char* lds = (LAS char*)smem;
  LAS char* scr = lds + LDS_SCR;
  LAS unsigned* ctl = (LAS unsigned*)scr;
  const int tid = threadIdx.x, lane = tid & 63, w = __builtin_amdgcn_readfirstlane(tid >> 6);
  const int blk = blockIdx.x, head = blk >> 3, prod = (blk + head) & 7;
  const int sub = (0x54372106u >> (4 * prod)) & 7;
  unsigned cpf = 0;
  {
    u64 pc;
    asm volatile("s_getpc_b64 %0" : "=s"(pc));
    cpf = __builtin_amdgcn_raw_buffer_load_b32(mkrs((const GAS char*)(pc & ~255ull), CPF), w == 7 ? head * 2048 + lane * 32 : OOB, 0, 0);
    SB();
  }
  {
    unsigned c = 16 + 4 * (DCH * sub + tid);
    asm volatile("" : "+v"(c));
    c = tid < 8 + DCH ? c : ~0u;
    c = tid < 8 ? 0u : c;
    ctl[tid] = tid == 4 ? DCH : c;
  }
  const int l16 = lane & 15, grp = lane >> 4;
  const int b4 = head & 3, cntoff = WS_CNT + head * CSTR;
  const int bm = (b4 + 1 + (sub >= 3 ? sub - 3 : sub)) & 3;
  const size_t base = (size_t)(bm * 32 + head) << 20;
  const int so = ((w & 1) << bitpos('a')) | (((w >> 1) & 1) << bitpos('b')) | ((w >> 2) << bitpos('c')) | (sub >= 3 ? 1 << bitpos('p') : 0);
  int v0 = l16 * 16 + grp * (sub < 6 ? 256 : 131072) + (sub < 6 ? so : w * 524288 + head * 256);
  unsigned boff = sub < 6 ? (unsigned)base : 0u, bsz = sub < 6 ? 1u << 20 : 4u << 20;
  int s0[WTH];
#pragma unroll
  for (int i = 0; i < WTH; i++) {
    s0[i] = sub < 6 ? goff(i) : i * 8192;
    asm volatile("" : "+s"(s0[i]));
  }
  asm volatile("" : "+v"(v0), "+s"(boff), "+s"(bsz));
  SB();
  asm volatile("s_waitcnt lgkmcnt(0)" : "+s"(ka), "+s"(kb_), "+s"(kc_));
  auto ptr = [](unsigned lo, unsigned hi) { return (GAS char*)(((u64)hi << 32) | lo); };
  GAS char *const xA = ptr(ka[0], ka[1]), *const ckv = ptr(ka[2], ka[3]), *const wqA = ptr(ka[4], ka[5]), *const wuk = ptr(ka[6], ka[7]),
           *const wuv = ptr(ka[8], ka[9]), *const woA = ptr(ka[10], ka[11]), *const xB = ptr(ka[12], ka[13]), *const kc = ptr(ka[14], ka[15]),
           *const vc = ptr(kb_[0], kb_[1]), *const wqB = ptr(kb_[2], kb_[3]), *const woB = ptr(kb_[4], kb_[5]), *const ws = ptr(kb_[6], kb_[7]),
           *const out = ptr(kc_[0], kc_[1]);
  const u64 e0 = *(const __attribute__((address_space(4))) u64*)(u64)(ws + WS_EPOCH);
  const rsrc_t r0 = mkrs((sub < 6 ? kc : sub == 6 ? wqB : wqA) + boff, bsz);
  SB();
  u32x4 ra[16];
#pragma unroll
  for (int i = 0; i < WTH; i++) ra[i] = ldnt(r0, v0, s0[i]);
  SB();
  u32x4 xp[2][2];
  if (sub >= 6) {
    const GAS char* x = sub == 6 ? xB : xA;
#pragma unroll
    for (int s = 0; s < 2; s++) {
      const GAS u32x4* xs = (const GAS u32x4*)(x + (2 * s + (l16 >> 3)) * 1024 + (64 * w + 16 * grp) * 2);
      xp[s][0] = xs[0]; xp[s][1] = xs[1];
    }
#pragma unroll
    for (int i = WTH; i < 16; i++) ra[i] = ldnt(r0, v0, i * 8192);
    asm volatile("" ::: "memory");
    SB();
  }
  const rsrc_t rws = mkrs(ws, WS_END);
  unsigned mask[4];
#pragma unroll
  for (int v = 0; v < 4; v++) mask[v] = (((lane & 7) >> 1) == v) ? ((lane & 1) ? 0xffff0000u : 0x0000ffffu) : 0u;
  const size_t base4 = (size_t)(b4 * 32 + head) << 20;
  const rsrc_t rk4 = mkrs(kc + base4, 1u << 20), rv4 = mkrs(vc + base4, 1u << 20);
  const int voff = lane * 16;
  LAS char* qst = scr + 1024 + w * 512;
  const int qo = WS_QB + head * 2048 + (lane < 32 ? bm : b4) * 512 + (lane & 31) * 16;
  auto qwait = [&](u32x4 g, const Tag& tg) {
    if (!__all(qok(g, tg))) {
      for (unsigned spins = 0;; spins++) {
        asm volatile("" ::: "memory");
        g = ldsc(rws, qo, 0);
        if (__all(qok(g, tg)) || spins > SPIN_CAP) break;
        __builtin_amdgcn_s_sleep(2);
      }
    }
    *(LAS u32x2*)(qst + lane * 8) = u32x2{g[0], g[2]};
  };
  unsigned cur;

  if (__builtin_expect(sub < 6, 1)) {
    const rsrc_t rv = mkrs(vc + base, 1u << 20);
    u32x4 rb[16], g;
#pragma unroll
    for (int i = WTH; i < 32 + NXA + NXB; i++) {
      if (i == WTH) asm volatile("s_barrier" ::: "memory");
      vmwait<WTH - 1>();
      if (i == 32 + NXA + NXB - WTH) asm volatile("buffer_load_dwordx4 %0, %1, %2, 0 offen sc1" : "=&v"(g) : "v"(qo), "s"(rws));
      if (i < 32) {
        const u32x4 v = ldslot(r0, voff, so, i);
        if (i < 16) ra[i] = v; else rb[i - 16] = v;
      } else {
        const int j = i < 32 + NXA ? i + 16 - NXA : i + 32 - NXA - NXB;
        dma1(r0, voff + goff(j & 3), so ^ goff(j & ~3), (unsigned)(uintptr_t)lds + w * 16384 + (i - 32) * 1024);
      }
      SB();
    }
    asm volatile("s_waitcnt vmcnt(%1)" : "+v"(g) : "n"(WTH), "v"(cpf));
    qwait(g, mktag(e0));
    const u32x4 q = *(LAS u32x4*)(qst + l16 * 16);
    float sc[4] = {0.f, 0.f, 0.f, 0.f};
#pragma unroll 1
    for (int it = 0; it < 2; it++) {
      const rsrc_t rn = it ? rv : r0;
      sc[2] = sc[0]; sc[3] = sc[1];
      sc[1] = k_step<NXA>(ra, q, l16, rn, voff, so ^ (it ? goff(48) : goff(32)), it == 0, lds + w * 16384 + voff);
      sc[0] = k_step<NXB>(rb, q, l16, rn, voff, so ^ (it ? goff(32) : goff(48)), it == 0, lds + w * 16384 + NXA * 1024 + voff);
    }
    const float mx = xrow_max(row_max(fmaxf(fmaxf(sc[0], sc[1]), fmaxf(sc[2], sc[3]))));
    float f, l = 0.f;
    const float M = exp_ref(mx, f);
    unsigned pk[4];
#pragma unroll
    for (int k = 0; k < 4; k++) {
      const float p = fexp2(fmaf(sc[k] - mx, LOG2E, f));
      pk[k] = cvtpk(p, p);
      l += bf_lo(pk[k]);
    }
    l = xrow_sum(row_sum(l));
    cur = 8 * sub + w;
    f32x4 acc = {0.f, 0.f, 0.f, 0.f};
    v_step<0, 1>(ra, pk[0], mask, acc, r0, rv, voff, so ^ goff(16));
    v_step<0, 1>(rb, pk[1], mask, acc, r0, rv, voff, so);
    v_step<0, 2>(ra, pk[2], mask, acc, rk4, rv4, voff, cur * 4096);
    v_step<0, 0>(rb, pk[3], mask, acc, r0, rv, voff, 0);
    Acc A;
    A.o[0] = lane < 32 ? acc[0] : acc[2]; A.o[1] = lane < 32 ? acc[1] : acc[3];
    A.l = l; A.R = M;
    b_merge(scr + 8192, ctl + 1, w, lane, A, rws, WS_RS + (head * 16 + sub) * 1024, mktag(e0));
  } else {
  {
    __builtin_amdgcn_raw_buffer_store_b32(48 + 24 * DCH, rws, (sub == 6 && tid == 0) ? cntoff : OOB, 0, AUX_SC1);
    {
      const rsrc_t rk = mkrs(wuk, sub == 7 ? 4u << 20 : 0u);
#pragma unroll
      for (int i = 0; i < 16; i++)
        __builtin_amdgcn_raw_ptr_buffer_load_lds(rk, (LAS void*)(lds + w * 16384 + i * 1024), 16,
                                                 (64 * w + grp) * 8192 + head * 256 + ((l16 ^ (4 * (i & 3) + grp)) * 16), i * 4 * 8192, 0, AUX_NT);
    }
    SB();
    f32x4 acc[2] = {{0.f, 0.f, 0.f, 0.f}, {0.f, 0.f, 0.f, 0.f}};
    diag_gemv(ra, xp, mask, acc);
    LAS float* red = (LAS float*)(scr + 8192);
#pragma unroll
    for (int s = 0; s < 2; s++) *(LAS f32x4*)(red + w * 512 + (2 * s + (grp >> 1)) * 128 + 8 * l16 + 4 * (grp & 1)) = acc[s];
    asm volatile("" :: "v"(cpf));
    __builtin_amdgcn_s_waitcnt(0x0f70);
    drain();
    bar();
    if (tid < 128) {
      f32x4 a = {0.f, 0.f, 0.f, 0.f};
#pragma unroll
      for (int k = 0; k < 8; k++) a += *(LAS f32x4*)(red + k * 512 + 4 * tid);
      const unsigned p0 = cvtpk(a[0], a[1]), p1 = cvtpk(a[2], a[3]);
      const Tag tg = mktag(e0);
      stsc(u32x4{p0, tg.th, p1, tg.th}, rws, sub == 6 ? WS_QB + head * 2048 + tid * 16 : OOB, 0);
      *(LAS u32x2*)(scr + 1024 + tid * 8) = u32x2{p0, p1};
    }
  }

  if (sub == 6) {
    const int aid = ((head & 6) << 2) | ((head & 1) << 2) | (head >> 3), b = aid >> 3;
    const rsrc_t rc = mkrs(ckv + (size_t)aid * 524288, 524288);
    const int dvo = (lane >> 3) * 1024 + w * 128 + (((lane & 7) ^ fz(lane >> 3)) * 16);
    const unsigned dlo = (unsigned)(uintptr_t)lds + w * 8192;
    const int xt = (aid & 7) << 16;
#pragma unroll
    for (int j = 0; j < 16; j++) dma1(rc, dvo, (j * 8192) ^ xt, dlo + (j >> 3) * 65536 + (j & 7) * 1024);
    SB();
    const Tag tg = mktag(e0);
    bar();
    {
      u32x4 t[8];
      unsigned miss = 15;
      __builtin_amdgcn_s_sleep(90);
      for (unsigned spins = 0;; spins++) {
        asm volatile("" ::: "memory");
#pragma unroll
        for (int i = 0; i < 4; i++)
          if (miss >> i & 1) {
            t[2 * i] = ldsc(rws, WS_QABS + (b * 32 + w) * 2048 + lane * 16, i * 16384);
            t[2 * i + 1] = ldsc(rws, WS_QABS + (b * 32 + w) * 2048 + lane * 16 + 1024, i * 16384);
          }
        SB();
#pragma unroll
        for (int i = 0; i < 4; i++) if (__all(qok(t[2 * i], tg) & qok(t[2 * i + 1], tg))) miss &= ~(1u << i);
        if (!miss || spins > SPIN_CAP) break;
        __builtin_amdgcn_s_sleep(4);
      }
#pragma unroll
      for (int i = 0; i < 8; i++) {
        const int row = 8 * (i >> 1) + w;
        *(LAS u32x2*)(scr + row * 1024 + (((32 * (i & 1) + (lane >> 1)) ^ swz(row)) * 16) + (lane & 1) * 8) = u32x2{t[i][0], t[i][2]};
      }
    }
    bar();
    const int kb = w & 3, hb = w >> 2;
    u32x4 qf[16];
    {
      const int row = 16 * hb + l16;
#pragma unroll
      for (int ks = 0; ks < 16; ks++) qf[ks] = *(LAS u32x4*)(scr + row * 1024 + (((4 * ks + grp) ^ swz(row)) * 16));
    }
    LAS float* mx = (LAS float*)scr;
    LAS char* pl = scr + 1024;
    LAS float* lr = (LAS float*)(scr + 8192);
    f32x16 o0, o1;
#pragma unroll
    for (int j = 0; j < 16; j++) { o0[j] = 0.f; o1[j] = 0.f; }
    float m1 = -INFINITY, m2 = -INFINITY, l1 = 0.f;
    const int h1 = 16 * hb + l16, h2 = lane & 31, hh = lane >> 5;
    const int arow = 16 * kb + l16;
    const int tq = (lane & 15) >> 2, tp = lane & 3, tblk = (lane >> 4) & 1;
    const int qa0 = arow * 128 + ((grp ^ fz(arow)) * 16), qa1 = qa0 ^ 64;
    const int pr = 8 * hh + tq, pc = 2 * tblk + (tp >> 1);
    const int pa0 = w * 8192 + pr * 128 + ((pc ^ fz(pr)) * 16) + 8 * (tp & 1), pa1 = w * 8192 + (pr + 4) * 128 + ((pc ^ fz(pr + 4)) * 16) + 8 * (tp & 1);
#pragma unroll 1
    for (int t = 0; t < 8; t++) {
      if (t < 7) asm volatile("s_waitcnt vmcnt(8)" ::: "memory"); else asm volatile("s_waitcnt vmcnt(0)" ::: "memory");
      bar();
      LAS char* img = lds + (t & 1) * 65536;
      f32x4 s0 = {0.f, 0.f, 0.f, 0.f}, s1 = s0;
      u32x4 ka[16];
      u32x2 ta[4][4];
      const int nso = t < 6 ? ((t + 2) * 65536) ^ xt : OOB;
#pragma unroll
      for (int ks = 0; ks < 16; ks++) ka[ks] = *(LAS u32x4*)(img + ((ks & 1) ? qa1 : qa0) + (ks >> 1) * 8192);
      SB();
#pragma unroll
      for (int k2 = 0; k2 < 8; k2++) {
        s0 = mfma16(ka[2 * k2], qf[2 * k2], s0);
        s1 = mfma16(ka[2 * k2 + 1], qf[2 * k2 + 1], s1);
      }
      asm volatile("" : "+v"(s0), "+v"(s1));
      SB();
#pragma unroll
      for (int ks = 0; ks < 4; ks++) {
        ta[ks][0] = trread(img + pa0 + ks * 2048); ta[ks][1] = trread(img + pa1 + ks * 2048);
        ta[ks][2] = trread(img + (pa0 ^ 64) + ks * 2048); ta[ks][3] = trread(img + (pa1 ^ 64) + ks * 2048);
      }
      SB();
      float x[4];
#pragma unroll
      for (int j = 0; j < 4; j++) x[j] = rbf(rbf(s0[j] + s1[j]) * SCALE);
      const float lm = xrow_max(fmaxf(fmaxf(x[0], x[1]), fmaxf(x[2], x[3])));
      if (grp == 0) mx[h1 * 4 + kb] = lm;
      LAS u32x2* pw = (LAS u32x2*)(pl + h1 * 144 + (16 * kb + 4 * grp) * 2);
      u32x2 pp = {cvtpk(fexp2((x[0] - m1) * LOG2E), fexp2((x[1] - m1) * LOG2E)), cvtpk(fexp2((x[2] - m1) * LOG2E), fexp2((x[3] - m1) * LOG2E))};
      *pw = pp;
      bar();
      const f32x4 ma = *(LAS f32x4*)(mx + h1 * 4), mb = *(LAS f32x4*)(mx + h2 * 4);
      u32x4 pb[4];
#pragma unroll
      for (int ks = 0; ks < 4; ks++) pb[ks] = *(LAS u32x4*)(pl + h2 * 144 + (16 * ks + 8 * hh) * 2);
      const float n1 = fmaxf(m1, fmaxf(fmaxf(ma[0], ma[1]), fmaxf(ma[2], ma[3])));
      const float n2 = fmaxf(m2, fmaxf(fmaxf(mb[0], mb[1]), fmaxf(mb[2], mb[3])));
      float al1 = fexp2((m1 - n1) * LOG2E), al2 = fexp2((m2 - n2) * LOG2E);
      asm volatile("" : "+v"(pb[0]), "+v"(pb[1]), "+v"(pb[2]), "+v"(pb[3]));
      if (__any(n2 - m2 > 40.f)) {
        bar();
        pp = u32x2{cvtpk(fexp2((x[0] - n1) * LOG2E), fexp2((x[1] - n1) * LOG2E)), cvtpk(fexp2((x[2] - n1) * LOG2E), fexp2((x[3] - n1) * LOG2E))};
        *pw = pp;
        l1 *= al1;
#pragma unroll
        for (int j = 0; j < 16; j++) { o0[j] *= al2; o1[j] *= al2; }
        al1 = al2 = 1.f;
        bar();
#pragma unroll
        for (int ks = 0; ks < 4; ks++) pb[ks] = *(LAS u32x4*)(pl + h2 * 144 + (16 * ks + 8 * hh) * 2);
      }
      m1 = n1; m2 = n2;
#pragma unroll
      for (int ks = 0; ks < 4; ks++) {
        o0 = mfma32(u32x4{ta[ks][0][0], ta[ks][0][1], ta[ks][1][0], ta[ks][1][1]}, pb[ks], o0);
        o1 = mfma32(u32x4{ta[ks][2][0], ta[ks][2][1], ta[ks][3][0], ta[ks][3][1]}, pb[ks], o1);
        SB();
        if (t < 6) {
          dma1(rc, dvo, nso + 2 * ks * 8192, dlo + (t & 1) * 65536 + 2 * ks * 1024);
          dma1(rc, dvo, nso + (2 * ks + 1) * 8192, dlo + (t & 1) * 65536 + (2 * ks + 1) * 1024);
        }
        SB();
      }
      l1 = (l1 + ((bf_lo(pp[0]) + bf_hi(pp[0])) + (bf_lo(pp[1]) + bf_hi(pp[1])))) * al1;
#pragma unroll
      for (int j = 0; j < 16; j++) { o0[j] *= al2; o1[j] *= al2; }
    }
    bar();
    {
      float am = 0.f;
#pragma unroll
      for (int j = 0; j < 16; j++) am = fmaxf(am, fmaxf(fabsf(o0[j]), fabsf(o1[j])));
      auto sw = __builtin_amdgcn_permlane32_swap(__float_as_uint(am), __float_as_uint(am), false, false);
      am = fmaxf(__uint_as_float(sw[0]), __uint_as_float(sw[1]));
      const int e = max(__builtin_amdgcn_frexp_expf(am), -127);
      LAS char* tp = lds + h2 * 1040 + (64 * w + 4 * hh) * 2;
      auto pkn = [&](float a, float b) { return __builtin_bit_cast(unsigned, __builtin_amdgcn_cvt_pknorm_i16(__builtin_ldexpf(a, -e), __builtin_ldexpf(b, -e))); };
#pragma unroll
      for (int j4 = 0; j4 < 4; j4++) {
        *(LAS u32x2*)(tp + j4 * 16) = u32x2{pkn(o0[4 * j4], o0[4 * j4 + 1]), pkn(o0[4 * j4 + 2], o0[4 * j4 + 3])};
        *(LAS u32x2*)(tp + 64 + j4 * 16) = u32x2{pkn(o1[4 * j4], o1[4 * j4 + 1]), pkn(o1[4 * j4 + 2], o1[4 * j4 + 3])};
      }
      if (hh == 0) *(LAS unsigned char*)(scr + 9216 + h2 * 8 + w) = (unsigned char)(e + 127);
      const float ls = xrow_sum(l1);
      if (grp == 0) lr[kb * 32 + h1] = ls;
      bar();
#pragma unroll
      for (int i = 0; i < 4; i++) {
        u32x4 v = *(LAS u32x4*)(lds + (4 * w + i) * 1040 + lane * 16);
#pragma unroll
        for (int k = 0; k < 4; k++) v[k] = bfi(PMASK, ptag(tg, k), v[k]);
        stsc(v, rws, WS_PO + (aid * 32 + 4 * w) * 1024 + lane * 16, i * 1024);
      }
      if (tid < 32) {
        const u32x2 eb = *(LAS u32x2*)(scr + 9216 + tid * 8);
        const float l = (lr[tid] + lr[32 + tid]) + (lr[64 + tid] + lr[96 + tid]);
        stsc(u32x4{bfi(0xffffu, tg.lo, __float_as_uint(m2)), bfi(255u, ltag(tg), __float_as_uint(l)), eb[0], eb[1]}, rws, WS_PML + (aid * 32 + tid) * 16, 0);
      }
      if (w == 0) {
        const unsigned t = __builtin_amdgcn_readfirstlane(__builtin_amdgcn_raw_ptr_buffer_atomic_add_i32(8 + 4 * DCH, rws, lane == 0 ? cntoff : OOB, 0, AUX_SC1));
        const unsigned bs = t + 8 + 4 * (lane - 8);
        ctl[lane] = lane == 3 ? t : lane == 4 ? DCH : lane == 5 ? (t + 8 + 4 * DCH >= NUNIT ? 0x10000u : 0u) : lane < 8 ? 0u : (lane < 8 + DCH && bs < NUNIT) ? bs : ~0u;
        if (lane < 32) ctl[64 + lane] = ~0u;
      }
      bar();
    }
    const u32x4 g = ldsc(rws, qo, 0);
    cur = __builtin_amdgcn_readfirstlane(ctl[3]) + w;
#pragma unroll
    for (int i = 0; i < 8; i++) ra[i] = ldunit(i < 4 ? rk4 : rv4, voff, unit_off(cur), i & 3);
    SB();
    qwait(g, mktag(e0));
  } else {
    const Tag tg = mktag(e0);
    bar();
    {
      u32x4 bf[4];
#pragma unroll
      for (int ks = 0; ks < 4; ks++) bf[ks] = *(LAS u32x4*)(scr + 1024 + (l16 & 3) * 256 + ks * 64 + grp * 16);
      LAS char* tt = scr + 2048 + w * 512;
#pragma unroll
      for (int rt = 0; rt < 4; rt++) {
        f32x4 acc = {0.f, 0.f, 0.f, 0.f};
#pragma unroll
        for (int ks = 0; ks < 4; ks++) acc = mfma16(*(LAS u32x4*)(lds + (64 * w + 16 * rt + l16) * 256 + (((4 * ks + grp) ^ l16) * 16)), bf[ks], acc);
        if (l16 < 4) *(LAS u32x2*)(tt + l16 * 128 + (16 * rt + 4 * grp) * 2) = u32x2{cvtpk(acc[0], acc[1]), cvtpk(acc[2], acc[3])};
      }
      asm volatile("s_waitcnt lgkmcnt(0)" ::: "memory");
      const u32x2 v = *(LAS u32x2*)(tt + lane * 8);
      stsc(u32x4{v[0], tg.th, v[1], tg.th}, rws, WS_QABS + (grp * 32 + head) * 2048 + w * 256 + l16 * 16, 0);
    }
    bar();
    u32x4 wf[4][4], wr[16];
    {
      const rsrc_t ro = mkrs(woA + (size_t)head * 131072, 131072);
      dma_rows<16>(ro, 0, lds, 16 * w, lane);
      const rsrc_t rv = mkrs(wuv, 4u << 20);
      const int voff = (64 * w + 16 * grp) * 8192 + head * 256 + l16 * 16;
#pragma unroll
      for (int i = 0; i < 16; i++) wr[i] = ldnt(rv, voff, i * 8192);
      SB();
      asm volatile("s_waitcnt vmcnt(16)" ::: "memory");
      bar();
      wo_frags(lds, w, lane, wf);
      bar();
      const rsrc_t rb = mkrs(woB, 4u << 20);
      const int bvo = (lane >> 3) * 1024 + (((lane & 7) ^ ((lane >> 5) << 1)) * 16);
#pragma unroll
      for (int i = 0; i < 16; i++)
        dma1(rb, (i & 1) ? bvo ^ 64 : bvo, (4 * w + b4) * 131072 + (head >> 2) * 128 + i * 8192, (unsigned)(uintptr_t)lds + w * 16384 + i * 1024);
      SB();
      if (tid < 8) ((LAS unsigned*)(scr + 24576))[tid] = 0u;
    }
    const int hw = 4 * w + b4, cc = head >> 2, roff = WS_RS + hw * 16384 + lane * 16;
    u32x4 g[6];
    unsigned missS = 63;
#pragma unroll
    for (int s = 0; s < 6; s++) asm volatile("" : "=v"(g[s]));
    if (w == 0) {
      for (unsigned spins = 0;; spins++) {
        asm volatile("" ::: "memory");
        const u32x4 v = ldsc(rws, WS_PML + ((lane & 31) * 32 + head) * 16, 0);
        if (__builtin_popcount((unsigned)__builtin_amdgcn_ballot_w64(mok(v, tg))) >= NREL || spins > SPIN_CAP) break;
        __builtin_amdgcn_s_sleep(16);
      }
    }
    bar();
    {
      const int b = w >> 1, c4 = tid & 127;
      const unsigned ta = (lane & 1) ? ptag(tg, 2) : ptag(tg, 0), tb = (lane & 1) ? ptag(tg, 3) : ptag(tg, 1);
      u32x4 ml;
      u32x2 po[8];
#pragma unroll
      for (int s = 0; s < 8; s++) asm volatile("" : "=v"(po[s]));
      unsigned missP = 255;
      for (unsigned spins = 0;; spins++) {
        asm volatile("" ::: "memory");
        ml = ldsc(rws, WS_PML + ((8 * b + (lane & 7)) * 32 + head) * 16, 0);
#pragma unroll
        for (int s = 0; s < 8; s++)
          if (missP >> s & 1) po[s] = __builtin_amdgcn_raw_buffer_load_b64(rws, WS_PO + (8 * b * 32 + head) * 1024 + c4 * 8, s * 32768, AUX_SC1);
        SB();
#pragma unroll
        for (int s = 0; s < 8; s++) if (__all((((po[s][0] ^ ta) | (po[s][1] ^ tb)) & PMASK) == 0u)) missP &= ~(1u << s);
        if ((!missP && __all(mok(ml, tg))) || spins > SPIN_CAP) break;
        __builtin_amdgcn_s_sleep(2);
      }
#pragma unroll
      for (int s = 0; s < 6; s++) g[s] = ldsc(rws, roff, s * 1024);
      SB();
      ml[0] &= 0xffff0000u; ml[1] &= ~255u;
#pragma unroll
      for (int s = 0; s < 8; s++) po[s] = u32x2{bfi(PMASK, 0x00020002u, po[s][0]), bfi(PMASK, 0x00020002u, po[s][1])};
      float M = __uint_as_float(ml[0]);
      M = fmaxf(M, dpp<XOR1>(M)); M = fmaxf(M, dpp<XOR2>(M)); M = fmaxf(M, dpp<XOR7>(M));
      const float f = fexp2((__uint_as_float(ml[0]) - M) * LOG2E);
      float L = f * __uint_as_float(ml[1]);
      L += dpp<XOR1>(L); L += dpp<XOR2>(L); L += dpp<XOR7>(L);
      const float gl = __builtin_ldexpf(f, (int)((((w & 1) ? ml[3] : ml[2]) >> (8 * grp)) & 255) - 142);
      const float gs[8] = {dpp<0x150>(gl), dpp<0x151>(gl), dpp<0x152>(gl), dpp<0x153>(gl), dpp<0x154>(gl), dpp<0x155>(gl), dpp<0x156>(gl), dpp<0x157>(gl)};
      f32x4 a = {0.f, 0.f, 0.f, 0.f};
#pragma unroll
      for (int s = 0; s < 8; s++)
        a += gs[s] * f32x4{(float)(short)po[s][0], (float)((int)po[s][0] >> 16), (float)(short)po[s][1], (float)((int)po[s][1] >> 16)};
      const float inv = (32768.f / 32767.f) / L;
      const u32x2 v = {cvtpk(a[0] * inv, a[1] * inv), cvtpk(a[2] * inv, a[3] * inv)};
      *(LAS u32x2*)(scr + b * 1024 + c4 * 8) = v;
    }
    bar();
    {
      u32x4 xp[2][2];
#pragma unroll
      for (int s = 0; s < 2; s++) {
        LAS u32x4* xs = (LAS u32x4*)(scr + (2 * s + (l16 >> 3)) * 1024 + (64 * w + 16 * grp) * 2);
        xp[s][0] = xs[0]; xp[s][1] = xs[1];
      }
      f32x4 acc[2] = {{0.f, 0.f, 0.f, 0.f}, {0.f, 0.f, 0.f, 0.f}};
      diag_gemv(wr, xp, mask, acc);
      LAS float* red = (LAS float*)(scr + 8192);
#pragma unroll
      for (int s = 0; s < 2; s++) *(LAS f32x4*)(red + w * 512 + (2 * s + (grp >> 1)) * 128 + 8 * l16 + 4 * (grp & 1)) = acc[s];
      bar();
      if (tid < 256) {
        float a = 0.f, b = 0.f;
#pragma unroll
        for (int k = 0; k < 8; k++) { const f32x2 v = *(LAS f32x2*)(red + k * 512 + 2 * tid); a += v[0]; b += v[1]; }
        ((LAS unsigned*)(scr + 4096))[tid] = cvtpk(a, b);
      }
      bar();
    }
    const f32x4 yA = wo_calc(wf, scr + 4096, lane);
    drain();
    y_store(yA, w, lane, head, rws, WS_Y, tg);
    {
      const int q = l16 >> 2;
      LAS char* img = lds + w * 16384 + (8 * (q >> 1) + 4 * (grp & 1) + 2 * (grp >> 1) + (q & 1)) * 128 + 8 * (lane & 3);
      const int fl = (grp & 1) | (q & 2);
#pragma unroll
      for (int ks = 0; ks < 4; ks++)
#pragma unroll
        for (int tl = 0; tl < 4; tl++) {
          const u32x2 a = trread(img + (tl ^ fl) * 32 + ks * 4096), b = trread(img + (tl ^ fl) * 32 + ks * 4096 + 2048);
          wf[tl][ks] = u32x4{a[0], a[1], b[0], b[1]};
        }
    }
    const rsrc_t rout = mkrs(out, 8192);
    LAS unsigned* cnt = (LAS unsigned*)(scr + 24576);
    LAS char* const re = scr + 8192;
    LAS char* const rA = scr + 8192 + 6144;
    LAS char* const rl = scr + 8192 + 8192;
    const int ofa = WS_Y + (b4 * 8 + cc) * 16384 + w * 2048 + lane * 16;
    const int ofb = WS_YB + ((b4 * 8 + cc) * 4 + ((b4 + 1 + w) & 3)) * 512 + (lane & 31) * 16;
    unsigned todo = w < 3 ? 7u : 3u, missL = (1u << NSLOT) - 1;
    u32x4 x0, x1, xb, p[NSLOT];
    asm volatile("" : "=v"(x0), "=v"(x1), "=v"(xb));
#pragma unroll
    for (int s = 0; s < NSLOT; s++) asm volatile("" : "=v"(p[s]));
    double S0 = 0.0, S1 = 0.0, L = 0.0;
    float R = -INFINITY;
#pragma unroll
    for (int s = 0; s < 6; s++) if (__all(rok(g[s], tg))) missS &= ~(1u << s);
    for (unsigned spins = 0;; spins++) {
      asm volatile("" ::: "memory");
      if ((todo & 1) && !missS) {
#pragma unroll
        for (int t = 0; t < 3; t++) {
          const u32x4 g0 = g[t], g1 = g[t + 3];
          const float R0 = rec_r(g0), R1 = rec_r(g1), Rm = fmaxf(R0, R1);
          const double s0 = mkd(0u, p2hi(R0, Rm)), s1 = mkd(0u, p2hi(R1, Rm));
          const double inv = (double)__builtin_amdgcn_rcpf((float)__builtin_fma(rec_l(g0), s0, rec_l(g1) * s1));
          proj(cvtpk((float)(__builtin_fma(rec_a(g0), s0, rec_a(g1) * s1) * inv), (float)(__builtin_fma(rec_b(g0), s0, rec_b(g1) * s1) * inv)), wf,
               re + (t * 8 + w) * 256, lane);
        }
        asm volatile("" ::: "memory");
        if (__builtin_amdgcn_readfirstlane(lds_inc(cnt, lane)) == 7) {
#pragma unroll
          for (int u = 0; u < 2; u++) {
            const int t = u ? 2 : lane >> 5;
            const f32x2 v = rows_sum<8>(re + t * 2048 + (lane & 31) * 8);
            stsc(gpair(v[0], v[1], tg), rws, u && lane >= 32 ? OOB : WS_YB + ((((b4 + 1 + t) & 3) * 8 + cc) * 4 + b4) * 512 + (lane & 31) * 16, 0);
          }
        }
        todo &= ~1u;
      }
      if ((!todo && !missL) || spins > SPIN_CAP * 4) break;
      const unsigned wantL = (todo & 1) ? 0u : missL;
#pragma unroll
      for (int s = 0; s < 6; s++) if (missS >> s & 1) g[s] = ldsc(rws, roff, s * 1024);
      if (todo & 2) { x0 = ldsc(rws, ofa, 0); x1 = ldsc(rws, ofa, 1024); }
      if (todo & 4) xb = ldsc(rws, ofb, 0);
#pragma unroll
      for (int s = 0; s < NSLOT; s++) if (wantL >> s & 1) p[s] = ldsc(rws, roff, 8192 + s * 1024);
      SB();
#pragma unroll
      for (int s = 0; s < 6; s++) if ((missS >> s & 1) && __all(rok(g[s], tg))) missS &= ~(1u << s);
      if ((todo & 2) && __all(gok(x0, tg) & gok(x1, tg))) {
        const f32x2 r = {half_sum(gval(x0[0]) + gval(x1[0])), half_sum(gval(x0[2]) + gval(x1[2]))};
        if (lane < 32) *(volatile LAS f32x2*)(rA + w * 256 + lane * 8) = r;
        asm volatile("" ::: "memory");
        if (__builtin_amdgcn_readfirstlane(lds_inc(cnt + 1, lane)) == 7) {
          const f32x2 v = rows_sum<8>(rA + (lane & 31) * 8);
          __builtin_amdgcn_raw_buffer_store_b32(cvtpk(v[0], v[1]), rout, lane < 32 ? b4 * 2048 + cc * 128 + l16 * 8 + grp * 4 : OOB, 0, 0);
        }
        todo &= ~2u;
      }
      if ((todo & 4) && __all(gok(xb, tg))) {
        *(volatile LAS f32x2*)(rl + (8 + w) * 256 + (lane & 31) * 8) = f32x2{gval(xb[0]), gval(xb[2])};
        todo &= ~4u;
      }
#pragma unroll
      for (int s = 0; s < NSLOT; s++)
        if ((wantL >> s & 1) && __all(rok(p[s], tg))) {
          const float Rs = rec_r(p[s]), Rn = fmaxf(R, Rs);
          const double s0 = mkd(0u, p2hi(R, Rn)), s1 = mkd(0u, p2hi(Rs, Rn));
          L = __builtin_fma(rec_l(p[s]), s1, L * s0);
          S0 = __builtin_fma(rec_a(p[s]), s1, S0 * s0); S1 = __builtin_fma(rec_b(p[s]), s1, S1 * s0);
          R = Rn;
          missL &= ~(1u << s);
        }
      if (missL == (1u << NSLOT) - 1) __builtin_amdgcn_s_sleep(6);
    }
    {
      const double inv = (double)__builtin_amdgcn_rcpf((float)L);
      proj(cvtpk((float)(S0 * inv), (float)(S1 * inv)), wf, rl + w * 256, lane);
      asm volatile("" ::: "memory");
      const unsigned nv = lds_inc(cnt + 2, lane);
      asm volatile("" ::: "memory");
      const f32x2 v = rows_sum<11>(rl + (lane & 31) * 8);
      if (__builtin_amdgcn_readfirstlane(nv) == 7) {
        __builtin_amdgcn_raw_buffer_store_b32(cvtpk(v[0], v[1]), rout, lane < 32 ? b4 * 2048 + 1024 + cc * 128 + lane * 4 : OOB, 0, 0);
        __builtin_amdgcn_raw_buffer_store_b64(__builtin_bit_cast(u32x2, tg.e), rws, head == 0 && lane == 0 ? WS_EPOCH : OOB, 0, AUX_SC1);
      }
    }
    return;
  }
  }
  {
    const u32x4 q4 = *(LAS u32x4*)(qst + 256 + l16 * 16);
    Acc A;
    A.o[0] = A.o[1] = A.l = 0.0; A.R = -INFINITY;
    b_units(ra, rk4, rv4, cur, q4, lane, mask, ctl, rws, cntoff, A);
    const Tag tg = mktag(e0);
    b_merge(scr + 8192 + 8704, ctl + 2, w, lane, A, rws, WS_RS + (head * 16 + 8 + sub) * 1024, tg);
    SB();
    asm volatile("" :: "v"(ra[0]), "v"(ra[1]), "v"(ra[2]), "v"(ra[3]), "v"(ra[4]), "v"(ra[5]), "v"(ra[6]), "v"(ra[7]));
  }
}

extern "C" void kernel_launch(void* const* d_in, const int* in_sizes, int n_in, void* d_out, int out_size, void* d_ws, size_t ws_size,
                              hipStream_t stream) {
  k_all<<<256, 512, 0, stream>>>((const char*)d_in[0], (const char*)d_in[1], (const char*)d_in[2], (const char*)d_in[3], (const char*)d_in[4],
                                 (const char*)d_in[5], (const char*)d_in[6], (const char*)d_in[7], (const char*)d_in[8], (const char*)d_in[9],
                                 (const char*)d_in[10], (char*)d_ws, (unsigned*)d_out);
}
```

```cpp
#include <hip/hip_runtime.h>
#include <stdint.h>

typedef __attribute__((ext_vector_type(4))) unsigned u32x4;
typedef __attribute__((ext_vector_type(2))) unsigned u32x2;
typedef __attribute__((ext_vector_type(8))) unsigned u32x8;
typedef __attribute__((ext_vector_type(16))) unsigned u32x16;
typedef __attribute__((ext_vector_type(4))) float f32x4;
typedef __attribute__((ext_vector_type(2))) float f32x2;
typedef __attribute__((ext_vector_type(2))) double f64x2;
typedef __attribute__((ext_vector_type(16))) float f32x16;
typedef __attribute__((ext_vector_type(8))) __bf16 bf16x8;
typedef __attribute__((ext_vector_type(2))) __bf16 bf16x2;
typedef __attribute__((ext_vector_type(4))) short s16x4;
typedef unsigned long long u64;
typedef __amdgpu_buffer_rsrc_t rsrc_t;
#define LAS __attribute__((address_space(3)))
#define GAS __attribute__((address_space(1)))

#define CPF 34944
#define PAT "5c10p2ba43"
constexpr int WTH = 5;
constexpr int DCH = 2;
constexpr int NXA = 8, NXB = 8;
constexpr int NREL = 24;

constexpr int AUX_NT = 2, AUX_SC1 = 16, OOB = 0x40000000;
constexpr float SCALE = 0.08838834764831845f;
constexpr float LOG2E = 1.4426950408889634f;
constexpr unsigned SPIN_CAP = 1u << 17;
constexpr int NSLOT = 7, NUNIT = 256;

constexpr int WS_EPOCH = 0;
constexpr int CSTR    = 4352;
constexpr int WS_CNT  = 8192;
constexpr int WS_QB   = WS_CNT + 32 * CSTR;
constexpr int WS_QABS = WS_QB + 32 * 2048;
constexpr int WS_PML  = WS_QABS + 4 * 32 * 2048;
constexpr int WS_PO   = WS_PML + 32 * 32 * 16;
constexpr int WS_RS   = WS_PO + 32 * 32 * 1024;
constexpr int WS_Y    = WS_RS + 32 * 16 * 1024;
constexpr int WS_YB   = WS_Y + 4 * 8 * 32 * 512;
constexpr int WS_END  = WS_YB + 4 * 8 * 4 * 512;

constexpr int LDS_BYTES = 163840;
constexpr int LDS_SCR = 131072;
constexpr int SCR_SUM = 5120, SUM_STR = 1040;

__device__ __forceinline__ unsigned cvtpk(float lo, float hi) {
  f32x2 v = {lo, hi};
  return __builtin_bit_cast(unsigned, __builtin_convertvector(v, bf16x2));
}
__device__ __forceinline__ float bf_lo(unsigned u) { return __uint_as_float(u << 16); }
__device__ __forceinline__ float bf_hi(unsigned u) { return __uint_as_float(u & 0xffff0000u); }
__device__ __forceinline__ float rbf(float x) { return __uint_as_float(cvtpk(x, x) << 16); }
__device__ __forceinline__ float dot2(unsigned k, unsigned q, float acc) {
  return __builtin_amdgcn_fdot2_f32_bf16(__builtin_bit_cast(bf16x2, k), __builtin_bit_cast(bf16x2, q), acc, false);
}
template <int CTRL> __device__ __forceinline__ float dpp(float x) {
  return __builtin_bit_cast(float, __builtin_amdgcn_mov_dpp(__builtin_bit_cast(int, x), CTRL, 0xf, 0xf, true));
}
template <int CTRL> __device__ __forceinline__ unsigned dppu(unsigned x) {
  return (unsigned)__builtin_amdgcn_mov_dpp((int)x, CTRL, 0xf, 0xf, true);
}
constexpr int XOR1 = 0xB1, XOR2 = 0x4E, XOR7 = 0x141, XOR8 = 0x128, ROR4 = 0x124;
__device__ __forceinline__ float xrow_max(float x) {
  auto s = __builtin_amdgcn_permlane16_swap(__float_as_uint(x), __float_as_uint(x), false, false);
  x = fmaxf(__uint_as_float(s[0]), __uint_as_float(s[1]));
  auto t = __builtin_amdgcn_permlane32_swap(__float_as_uint(x), __float_as_uint(x), false, false);
  return fmaxf(__uint_as_float(t[0]), __uint_as_float(t[1]));
}
__device__ __forceinline__ float xrow_sum(float x) {
  auto s = __builtin_amdgcn_permlane16_swap(__float_as_uint(x), __float_as_uint(x), false, false);
  x = __uint_as_float(s[0]) + __uint_as_float(s[1]);
  auto t = __builtin_amdgcn_permlane32_swap(__float_as_uint(x), __float_as_uint(x), false, false);
  return __uint_as_float(t[0]) + __uint_as_float(t[1]);
}
__device__ __forceinline__ float row_max(float x) {
  x = fmaxf(x, dpp<XOR8>(x)); x = fmaxf(x, dpp<ROR4>(x)); x = fmaxf(x, dpp<XOR2>(x)); x = fmaxf(x, dpp<XOR1>(x));
  return x;
}
__device__ __forceinline__ float row_sum(float x) {
  x += dpp<XOR8>(x); x += dpp<ROR4>(x); x += dpp<XOR2>(x); x += dpp<XOR1>(x);
  return x;
}
__device__ __forceinline__ float fexp2(float x) { return __builtin_amdgcn_exp2f(x); }
__device__ __forceinline__ f32x4 mfma16(u32x4 a, u32x4 b, f32x4 c) {
  return __builtin_amdgcn_mfma_f32_16x16x32_bf16(__builtin_bit_cast(bf16x8, a), __builtin_bit_cast(bf16x8, b), c, 0, 0, 0);
}
__device__ __forceinline__ f32x16 mfma32(u32x4 a, u32x4 b, f32x16 c) {
  return __builtin_amdgcn_mfma_f32_32x32x16_bf16(__builtin_bit_cast(bf16x8, a), __builtin_bit_cast(bf16x8, b), c, 0, 0, 0);
}
__device__ __forceinline__ rsrc_t mkrs(const GAS char* p, unsigned bytes) {
  return __builtin_amdgcn_make_buffer_rsrc((void*)p, 0, bytes, 0x00020000);
}
__device__ __forceinline__ void bar() { asm volatile("s_waitcnt lgkmcnt(0)\n\ts_barrier" ::: "memory"); }
__device__ __forceinline__ void drain() { asm volatile("s_waitcnt vmcnt(0)" ::: "memory"); }
#define SB() __builtin_amdgcn_sched_barrier(0)
template <int N> __device__ __forceinline__ void vmwait() { asm volatile("s_waitcnt vmcnt(%0)" :: "n"(N) : "memory"); }
__device__ __forceinline__ unsigned swz(unsigned row) { return ((row & 3) << 2) | ((row >> 2) & 2); }
__device__ __forceinline__ u32x2 trread(LAS char* p) {
  return __builtin_bit_cast(u32x2, __builtin_amdgcn_ds_read_tr16_b64_v4i16((LAS s16x4*)p));
}

struct Tag { unsigned tl, th, lo, rb; u64 e; };
__device__ __forceinline__ Tag mktag(u64 e0) {
  Tag t;
  asm volatile("" : "+s"(e0));
  t.e = e0 * 6364136223846793005ull + 1442695040888963407ull;
  t.th = (unsigned)(t.e >> 32);
  t.lo = (unsigned)t.e;
  t.tl = t.lo & 0xff000000u;
  t.rb = ((t.lo >> 9) & 255u) | (~t.lo & 256u);
  return t;
}
__device__ __forceinline__ unsigned gpack(float v, const Tag& t) { return ((__float_as_uint(v) + 0x80u) >> 8) | t.tl; }
__device__ __forceinline__ u32x4 gpair(float a, float b, const Tag& t) { return u32x4{gpack(a, t), t.th, gpack(b, t), t.th}; }
__device__ __forceinline__ bool gok(u32x4 g, const Tag& t) {
  return ((((g[0] ^ t.tl) | (g[2] ^ t.tl)) >> 24) | (g[1] ^ t.th) | (g[3] ^ t.th)) == 0u;
}
__device__ __forceinline__ float gval(unsigned lo) { return __uint_as_float(lo << 8); }
__device__ __forceinline__ bool qok(u32x4 g, const Tag& t) { return ((g[1] ^ t.th) | (g[3] ^ t.th)) == 0u; }
__device__ __forceinline__ double mkd(unsigned lo, unsigned hi) { return __builtin_bit_cast(double, u32x2{lo, hi}); }
__device__ __forceinline__ u32x4 rpack(double a, double b, double l, float r, const Tag& t) {
  const u32x2 ua = __builtin_bit_cast(u32x2, a), ub = __builtin_bit_cast(u32x2, b), ul = __builtin_bit_cast(u32x2, l);
  const u64 ml = ((u64)(unsigned)__builtin_amdgcn_readfirstlane(ul[1]) << 32) | (unsigned)__builtin_amdgcn_readfirstlane(ul[0]);
  const u64 mr = (unsigned)__builtin_amdgcn_readfirstlane(__float_as_uint(r));
  const unsigned ta = t.lo & 511u, tb = t.rb;
  return u32x4{(ua[0] & ~1023u) | (__builtin_amdgcn_inverse_ballot_w64(ml) ? ta | 512u : ta), ua[1],
               (ub[0] & ~1023u) | (__builtin_amdgcn_inverse_ballot_w64(mr) ? tb | 512u : tb), ub[1]};
}
__device__ __forceinline__ u32x4 rpack0(f64x2 o, unsigned lw, int lane, unsigned ta, unsigned tb) {
  const u32x4 u = __builtin_bit_cast(u32x4, o);
  return u32x4{(u[0] & ~1023u) | (ta | (__builtin_amdgcn_ubfe(lw, lane, 1) << 9)), u[1], (u[2] & ~1023u) | tb, u[3]};
}
__device__ __forceinline__ bool rok(u32x4 g, const Tag& t) { return (((g[0] ^ t.lo) | (g[2] ^ t.rb)) & 511u) == 0u; }
constexpr unsigned PMASK = 0x00030003u;
__device__ __forceinline__ unsigned ptag(const Tag& t, int k) {
  const unsigned n = k == 0 ? t.lo : k == 3 ? t.lo >> 10 : ((t.lo >> (1 + 3 * k)) & 7u) | (~t.lo & 8u);
  return (n & 3u) | ((n & 12u) << 14);
}
__device__ __forceinline__ unsigned ltag(const Tag& t) { return ((t.lo >> 16) & 127u) | (~t.lo & 128u); }
__device__ __forceinline__ bool mok(u32x4 g, const Tag& t) { return (((g[0] ^ t.lo) & 0xffffu) | ((g[1] ^ ltag(t)) & 255u)) == 0u; }
__device__ __forceinline__ unsigned bfi(unsigned m, unsigned a, unsigned b) { return (a & m) | (b & ~m); }
__device__ __forceinline__ double rec_a(u32x4 g) { return mkd(g[0] & ~1023u, g[1]); }
__device__ __forceinline__ double rec_b(u32x4 g) { return mkd(g[2] & ~1023u, g[3]); }
__device__ __forceinline__ double rec_l(u32x4 g) { return __builtin_bit_cast(double, __builtin_amdgcn_ballot_w64((g[0] & 512u) != 0u)); }
__device__ __forceinline__ float rec_r(u32x4 g) { return __uint_as_float((unsigned)__builtin_amdgcn_ballot_w64((g[2] & 512u) != 0u)); }
__device__ __forceinline__ int edif(float a, float b) {
  int r;
  asm("v_cvt_i32_f32 %0, %1" : "=v"(r) : "v"(a - b));
  return r;
}
__device__ __forceinline__ unsigned p2hi(float a, float b) { return (unsigned)max(edif(a, b) + 1023, 0) << 20; }
__device__ __forceinline__ float bucket(float m) { return fmaf(__builtin_rintf(m * (1.f / 512.f)), 512.f, 0.f); }
__device__ __forceinline__ float rfl(float x) { return __uint_as_float(__builtin_amdgcn_readfirstlane(__float_as_uint(x))); }
__device__ __forceinline__ unsigned rdu(unsigned v, int l) { return __builtin_amdgcn_readlane(v, l); }

__device__ __forceinline__ void diag_gemv(const u32x4 (&wr)[16], const u32x4 (&xp)[2][2], const unsigned (&mask)[4], f32x4 (&acc)[2]) {
#pragma unroll
  for (int i = 0; i < 16; i++) {
#pragma unroll
    for (int s = 0; s < 2; s++) {
      const unsigned src = xp[s][i >> 3][(i >> 1) & 3];
      const unsigned dup = __builtin_amdgcn_perm(src, src, (i & 1) ? 0x03020302u : 0x01000100u);
      const u32x4 a = {dup & mask[0], dup & mask[1], dup & mask[2], dup & mask[3]};
      acc[s] = mfma16(a, wr[i], acc[s]);
    }
  }
}

constexpr int bitpos(char c) { for (int i = 0; i < 10; i++) if (PAT[i] == c) return 10 + i; return 31; }
constexpr int goff(int t) { int o = 0; for (int j = 0; j < 6; j++) o |= ((t >> j) & 1) << bitpos('0' + j); return o; }
struct Acc { double o[2]; double l; float R; };
__device__ __forceinline__ u32x4 ldnt(rsrc_t r, int voff, int so) { return __builtin_amdgcn_raw_buffer_load_b128(r, voff, so, AUX_NT); }
__device__ __forceinline__ u32x4 ldslot(rsrc_t r, int voff, int so, int i) { return ldnt(r, voff + goff(i & 3), so ^ goff(i & ~3)); }
__device__ __forceinline__ u32x4 ldunit(rsrc_t r, int voff, int so, int i) { return ldnt(r, voff + i * 1024, so); }
__device__ __forceinline__ u32x4 ldsc(rsrc_t r, int voff, int so) { return __builtin_amdgcn_raw_buffer_load_b128(r, voff, so, AUX_SC1); }
__device__ __forceinline__ void stsc(u32x4 v, rsrc_t r, int voff, int so) { __builtin_amdgcn_raw_buffer_store_b128(v, r, voff + so, 0, AUX_SC1); }
__device__ __forceinline__ float dot8(u32x4 k, u32x4 q) { return dot2(k[3], q[3], dot2(k[2], q[2], dot2(k[1], q[1], dot2(k[0], q[0], 0.f)))); }
#define TR_STEP(HALF, CTRL, BIT) _Pragma("unroll") for (int h = 0; h < (HALF); ++h) { \
    const float t1 = x[h] + dpp<CTRL>(x[h]), t2 = x[h + (HALF)] + dpp<CTRL>(x[h + (HALF)]); x[h] = (c & (BIT)) ? t2 : t1; }
template <int N> __device__ __forceinline__ float k_step(u32x4 (&ring)[16], u32x4 q, int c, rsrc_t rn, int voff, int so, bool ext, LAS char* xp) {
  float x[16];
#pragma unroll
  for (int i = 0; i < 16; i++) {
    x[i] = dot8(ring[i], q);
    asm volatile("s_waitcnt vmcnt(%1)" : "+v"(x[i]) : "n"(WTH - 1));
    if (i >= 16 - N && ext) ring[i] = *(volatile LAS u32x4*)(xp + (i - (16 - N)) * 1024);
    else ring[i] = ldslot(rn, voff, so, i);
    SB();
  }
  TR_STEP(8, XOR8, 8) TR_STEP(4, XOR7, 4) TR_STEP(2, XOR2, 2) TR_STEP(1, XOR1, 1)
  return rbf(rbf(x[0]) * SCALE);
}
template <int BC> __device__ __forceinline__ void pv(u32x4 v, unsigned pk, const unsigned (&mask)[4], f32x4& acc) {
  const unsigned d = dppu<0x150 + BC>(pk);
  acc = mfma16(u32x4{d & mask[0], d & mask[1], d & mask[2], d & mask[3]}, v, acc);
}
template <int I, int MODE> __device__ __forceinline__ void v_step(u32x4 (&ring)[16], unsigned pk, const unsigned (&mask)[4], f32x4& acc,
                                                                  rsrc_t rk, rsrc_t rv, int voff, int so) {
  pv<I>(ring[I], pk, mask, acc);
  if (MODE == 1 || (MODE == 2 && I < 8)) {
    asm volatile("s_waitcnt vmcnt(%1)" : "+v"(acc) : "n"(WTH - 1));
    ring[I] = MODE == 1 ? ldslot(rv, voff, so, I) : ldunit(I < 4 ? rk : rv, voff, so, I & 3);
  } else {
    asm volatile("" : "+v"(acc));
  }
  SB();
  if constexpr (I < 15) v_step<I + 1, MODE>(ring, pk, mask, acc, rk, rv, voff, so);
}
__device__ __forceinline__ float exp_ref(float mx, float& f) {
  const float M = __builtin_ceilf(mx * LOG2E);
  f = __builtin_amdgcn_fmed3f(fmaf(mx, LOG2E, -M), -1.f, 0.f);
  return M;
}
__device__ __forceinline__ unsigned lds_inc(LAS unsigned* p, int lane) {
  unsigned t = 0, z = 0;
  asm volatile("" : "+v"(z));
  if (lane == 0) t = __hip_atomic_fetch_add(p + z, 1u, __ATOMIC_RELAXED, __HIP_MEMORY_SCOPE_WORKGROUP);
  return t;
}
struct Take { unsigned unit, pend; bool fetch; };
__device__ __forceinline__ void lds_add(LAS unsigned* p, unsigned v) { __hip_atomic_fetch_add(p, v, __ATOMIC_RELAXED, __HIP_MEMORY_SCOPE_WORKGROUP); }
__device__ __forceinline__ Take take(LAS unsigned* ctl, unsigned kv, rsrc_t rws, int cntoff, int lane) {
  const unsigned k = __builtin_amdgcn_readfirstlane(kv);
  unsigned b, s;
  auto look = [&]() {
    const unsigned vs = ((volatile LAS unsigned*)ctl)[5], vb = ((volatile LAS unsigned*)ctl)[8 + (k >> 2)];
    s = __builtin_amdgcn_readfirstlane(vs); b = __builtin_amdgcn_readfirstlane(vb);
    return (b == ~0u) & ((s < 0x10000u) | ((s & 0xffffu) != 0u));
  };
  if (__builtin_expect(look(), 0))
    for (unsigned spins = 0; spins < SPIN_CAP * 16; spins++) {
      __builtin_amdgcn_s_sleep(1);
      if (!look()) break;
    }
  Take t;
  t.unit = min(b, (unsigned)NUNIT) + (k & 3);
  t.fetch = ((k & 3) == 0) & (b < NUNIT) & (s < 0x10000u);
  int off = OOB;
  if (t.fetch) {
    unsigned z = 0;
    asm volatile("" : "+v"(z));
    if (lane == 0) { lds_add(ctl + 5 + z, 1u); off = cntoff; }
    asm volatile("s_waitcnt lgkmcnt(0)" ::: "memory");
  }
  t.pend = __builtin_amdgcn_raw_ptr_buffer_atomic_add_i32(4, rws, off, 0, AUX_SC1);
  SB();
  return t;
}
__device__ __forceinline__ void give(LAS unsigned* ctl, const Take& t, int lane) {
  if (t.fetch && lane == 0) {
    unsigned z = 0;
    asm volatile("" : "+v"(z));
    if (t.pend < NUNIT) {
      const unsigned i = __hip_atomic_fetch_add(ctl + 4 + z, 1u, __ATOMIC_RELAXED, __HIP_MEMORY_SCOPE_WORKGROUP);
      ((volatile LAS unsigned*)ctl)[8 + i] = t.pend;
    }
    asm volatile("" ::: "memory");
    lds_add(ctl + 5 + z, t.pend >= NUNIT - 4 ? 0xffffu : ~0u);
  }
}
__device__ __forceinline__ int unit_off(unsigned t) { return (int)min(t, (unsigned)NUNIT) * 4096; }

__device__ __forceinline__ void b_units(u32x4 (&ra)[16], rsrc_t rk, rsrc_t rv, unsigned cur, u32x4 q, int lane, const unsigned (&mask)[4],
                                        LAS unsigned* ctl, rsrc_t rws, int cntoff, Acc& A) {
  const int c = lane & 15, voff = lane * 16;
  float Mr = -INFINITY;
  double s1 = 0.0;
  A.o[0] = A.o[1] = A.l = 0.0; A.R = cur < NUNIT ? -INFINITY : 0.f;
#pragma unroll 1
  while (cur < NUNIT) {
    const Take t = take(ctl, lds_inc(ctl, lane), rws, cntoff, lane);
    const int so = unit_off(t.unit);
    SB();
    float x[4];
#pragma unroll
    for (int i = 0; i < 4; i++) x[i] = dot8(ra[i], q);
    asm volatile("" : "+v"(x[0]), "+v"(x[1]), "+v"(x[2]), "+v"(x[3]));
    SB();
#pragma unroll
    for (int i = 0; i < 4; i++) {
      ra[i] = ldunit(rk, voff, so, i);
      SB();
    }
    TR_STEP(2, XOR8, 8) TR_STEP(1, XOR7, 4)
    x[0] += dpp<XOR2>(x[0]); x[0] += dpp<XOR1>(x[0]);
    const float s = rbf(rbf(x[0]) * SCALE);
    const float n = __builtin_rintf(s * LOG2E);
    float e = fexp2(__builtin_amdgcn_fmed3f(fmaf(s, LOG2E, -n), -1.f, 1.f)), d = n - Mr;
    asm volatile("" : "+v"(e));
    if (__builtin_expect(__any(d > 57.f), 0)) {
      float mx = fmaxf(n, dpp<XOR8>(n));
      mx = xrow_max(fmaxf(mx, dpp<ROR4>(mx)));
      const float Rn = rfl(fmaxf(A.R, bucket(mx)));
      const double s0 = mkd(0u, p2hi(A.R, Rn));
      A.o[0] *= s0; A.o[1] *= s0; A.l *= s0;
      A.R = Rn; Mr = mx; d = n - mx;
      s1 = mkd(0u, p2hi(mx, Rn));
    }
    const float p = __builtin_ldexpf(e, edif(d, 0.f));
    unsigned pk = cvtpk(p, p);
    asm volatile("" : "+v"(pk));
    SB();
    f32x4 acc = {0.f, 0.f, 0.f, 0.f};
    float ls = bf_lo(pk);
    pv<0>(ra[4], pk, mask, acc);  asm volatile("" : "+v"(acc)); ra[4] = ldunit(rv, voff, so, 0); ls += dpp<XOR8>(ls); asm volatile("" : "+v"(ls)); SB();
    pv<4>(ra[5], pk, mask, acc);  asm volatile("" : "+v"(acc)); ra[5] = ldunit(rv, voff, so, 1); ls += dpp<ROR4>(ls); asm volatile("" : "+v"(ls)); SB();
    pv<8>(ra[6], pk, mask, acc);  asm volatile("" : "+v"(acc)); ra[6] = ldunit(rv, voff, so, 2); SB();
    pv<12>(ra[7], pk, mask, acc); asm volatile("" : "+v"(acc)); ra[7] = ldunit(rv, voff, so, 3); SB();
    A.l = __builtin_fma((double)xrow_sum(ls), s1, A.l);
    asm volatile("" : "+v"(A.l));
    A.o[0] = __builtin_fma((double)(lane < 32 ? acc[0] : acc[2]), s1, A.o[0]);
    A.o[1] = __builtin_fma((double)(lane < 32 ? acc[1] : acc[3]), s1, A.o[1]);
    asm volatile("" : "+v"(A.o[0]), "+v"(A.o[1]));
    SB();
    cur = t.unit;
    if (cur >= NUNIT) break;
    give(ctl, t, lane);
  }
}
#undef TR_STEP
__device__ __forceinline__ int rec_col(int lane) { return 8 * (lane & 15) + 4 * ((lane >> 4) & 1) + 2 * (lane >> 5); }
struct Mrg { unsigned s0, s4, s8, ta, tb; int off; };
__device__ __forceinline__ Mrg mkmrg(LAS char* sum, int lane, int roff, const Tag& tg) {
  Mrg m = {(unsigned)(uintptr_t)sum, (unsigned)(uintptr_t)sum + (lane >> 5) * 4, (unsigned)(uintptr_t)sum + lane * 8, tg.lo & 511u, tg.rb, roff + lane * 16};
  asm volatile("" : "+v"(m.s0), "+v"(m.s4), "+v"(m.s8), "+v"(m.ta), "+v"(m.tb), "+v"(m.off));
  return m;
}
__device__ __forceinline__ void b_merge(const Mrg& m, LAS char* mrg, int w, int lane, const Acc& A, rsrc_t rws, u64 e0) {
  unsigned inc = 1;
  double a = A.o[0], b = A.o[1], l = A.l;
  if (__builtin_expect(__float_as_uint(A.R) != 0u, 0)) {
    *(volatile LAS f64x2*)(mrg + w * 1024 + lane * 16) = f64x2{a, b};
    *(volatile LAS double*)(mrg + 8192 + w * 8) = l;
    *(volatile LAS float*)(mrg + 8256 + w * 4) = A.R;
    a = b = l = 0.0;
    inc = 1u + (256u << w);
  }
  f64x2 o;
  unsigned n, lw;
  asm volatile("ds_add_f64 %3, %5\n\tds_add_f64 %3, %6 offset:512\n\ts_mov_b64 exec, 1\n\tds_add_f64 %3, %7 offset:1024\n\tds_add_u32 %3, %8 offset:1032\n\t"
               "s_mov_b64 exec, -1\n\tds_read_b32 %1, %9 offset:1032\n\tds_read_b32 %2, %4 offset:1024\n\tds_read2st64_b64 %0, %3 offset1:1\n\ts_waitcnt lgkmcnt(0)"
               : "=&v"(o), "=&v"(n), "=&v"(lw)
               : "v"(m.s8), "v"(m.s4), "v"(a), "v"(b), "v"(l), "v"(inc), "v"(m.s0)
               : "memory");
  stsc(rpack0(o, lw, lane, m.ta, m.tb), rws, n == 8u ? m.off : OOB, 0);
  SB();
  n = __builtin_amdgcn_readfirstlane(n);
  if (__builtin_expect(n > 8u && (n & 255u) == 8u, 0)) {
    double L = *(volatile LAS double*)(uintptr_t)(m.s0 + 1024);
    float R = L != 0.0 ? 0.f : -INFINITY;
#pragma unroll 1
    for (int i = 0; i < 8; i++)
      if (n >> (8 + i) & 1) {
        const f64x2 oi = *(volatile LAS f64x2*)(mrg + i * 1024 + lane * 16);
        const float Ri = *(volatile LAS float*)(mrg + 8256 + i * 4), Rn = fmaxf(R, Ri);
        const double s0 = mkd(0u, p2hi(R, Rn)), s1 = mkd(0u, p2hi(Ri, Rn));
        o[0] = __builtin_fma(oi[0], s1, o[0] * s0); o[1] = __builtin_fma(oi[1], s1, o[1] * s0);
        L = __builtin_fma(*(volatile LAS double*)(mrg + 8192 + i * 8), s1, L * s0);
        R = Rn;
      }
    stsc(rpack(o[0], o[1], L, R, mktag(e0)), rws, m.off, 0);
  }
}

__device__ __forceinline__ unsigned fz(unsigned row) { return ((row & 2) << 1) | ((row >> 1) & 2); }
__device__ __forceinline__ void dma1(rsrc_t rs, int voff, int so, unsigned ldsaddr) {
  unsigned keep;
  asm volatile("s_mov_b32 %0, m0\n\ts_mov_b32 m0, %1\n\ts_nop 0\n\tbuffer_load_dwordx4 %2, %3, %4 offen nt lds\n\ts_mov_b32 m0, %0"
               : "=&s"(keep) : "s"(ldsaddr), "v"(voff), "s"(rs), "s"(so) : "memory");
}
template <int NR>
__device__ __forceinline__ void dma_rows(rsrc_t rs, int srcoff, LAS char* img, int r0, int lane) {
#pragma unroll
  for (int u = 0; u < NR; u++) {
    const int r = r0 + u;
    unsigned keep;
    asm volatile("s_mov_b32 %0, m0\n\ts_mov_b32 m0, %1\n\ts_nop 0\n\tbuffer_load_dwordx4 %2, %3, %4 offen nt lds\n\ts_mov_b32 m0, %0"
                 : "=&s"(keep) : "s"((unsigned)(uintptr_t)img + r * 1024), "v"((lane ^ swz(r)) * 16), "s"(rs), "s"(srcoff + r * 1024) : "memory");
  }
  SB();
}

__device__ __forceinline__ void wo_frags(LAS char* img, int w, int lane, u32x4 (&wf)[4][4]) {
  const int g = lane >> 4, q = (lane & 15) >> 2, p = lane & 3;
#pragma unroll
  for (int ks = 0; ks < 4; ks++)
#pragma unroll
    for (int tl = 0; tl < 4; tl++) {
      const int ch = 8 * w + 2 * tl + (p >> 1);
      const int r0 = 32 * ks + 8 * g + q, r1 = r0 + 4;
      const u32x2 a = trread(img + r0 * 1024 + ((ch ^ swz(r0)) * 16) + 8 * (p & 1));
      const u32x2 b = trread(img + r1 * 1024 + ((ch ^ swz(r1)) * 16) + 8 * (p & 1));
      wf[tl][ks] = u32x4{a[0], a[1], b[0], b[1]};
    }
}
__device__ __forceinline__ f32x4 wo_calc(const u32x4 (&wf)[4][4], LAS char* cb, int lane) {
  const int n = lane & 15, grp = lane >> 4;
  u32x4 bf[4];
#pragma unroll
  for (int ks = 0; ks < 4; ks++) bf[ks] = *(LAS u32x4*)(cb + (n & 3) * 256 + ks * 64 + grp * 16);
  f32x4 acc[4];
#pragma unroll
  for (int tl = 0; tl < 4; tl++) acc[tl] = f32x4{0.f, 0.f, 0.f, 0.f};
#pragma unroll
  for (int ks = 0; ks < 4; ks++)
#pragma unroll
    for (int tl = 0; tl < 4; tl++) acc[tl] = mfma16(wf[tl][ks], bf[ks], acc[tl]);
  const f32x4 lo = (n & 4) ? acc[1] : acc[0], hi = (n & 4) ? acc[3] : acc[2];
  return (n & 8) ? hi : lo;
}
__device__ __forceinline__ void y_store(f32x4 y, int w, int lane, int slot, rsrc_t rws, int yoff, const Tag& tg) {
  const int off = yoff + (((lane & 3) * 8 + w) * 32 + slot) * 512 + ((lane & 12) + (lane >> 4)) * 16;
  stsc(gpair(y[0], y[1], tg), rws, off, 0);
  stsc(gpair(y[2], y[3], tg), rws, off, 256);
}
template <int K, int N> __device__ __forceinline__ unsigned bc1(const unsigned (&p)[N]) {
  unsigned v = dppu<0x150 + K>(p[0]);
  if constexpr (N > 1) {
    v = (unsigned)__builtin_amdgcn_update_dpp((int)v, (int)p[1], 0x150 + K, 0xf, 2, false);
    v = (unsigned)__builtin_amdgcn_update_dpp((int)v, (int)p[2], 0x150 + K, 0xf, 4, false);
  }
  return v;
}
template <int K, int N> __device__ __forceinline__ u32x4 bc4(const unsigned (&p)[N]) { return u32x4{bc1<K>(p), bc1<K + 1>(p), bc1<K + 2>(p), bc1<K + 3>(p)}; }
template <int N> __device__ __forceinline__ f32x4 proj(const unsigned (&pr)[N], const u32x4 (&wf)[4][4], int lane) {
  const u32x4 bf[4] = {bc4<0>(pr), bc4<4>(pr), bc4<8>(pr), bc4<12>(pr)};
  f32x4 acc[4];
#pragma unroll
  for (int tl = 0; tl < 4; tl++) acc[tl] = f32x4{0.f, 0.f, 0.f, 0.f};
#pragma unroll
  for (int ks = 0; ks < 4; ks++)
#pragma unroll
    for (int tl = 0; tl < 4; tl++) acc[tl] = mfma16(wf[tl][ks], bf[ks], acc[tl]);
  const f32x4 lo = (lane & 1) ? acc[1] : acc[0], hi = (lane & 1) ? acc[3] : acc[2];
  f32x4 r = (lane & 2) ? hi : lo;
  asm volatile("" : "+v"(r));
  return r;
}
__device__ __forceinline__ unsigned arrive(LAS unsigned* p, int lane) {
  unsigned z = 0;
  asm volatile("" : "+v"(z) :: "memory");
  if (lane == 0) lds_add(p + z, 1u);
  asm volatile("" ::: "memory");
  return *(volatile LAS unsigned*)p;
}
template <int N> __device__ __forceinline__ f32x2 rows_sum(LAS char* p) {
  f32x2 v[N];
#pragma unroll
  for (int i = 0; i < N; i++) v[i] = *(volatile LAS f32x2*)(p + i * 256);
#pragma unroll
  for (int st = 1; st < N; st *= 2)
#pragma unroll
    for (int i = 0; i + st < N; i += 2 * st) v[i] += v[i + st];
  return v[0];
}
__device__ __forceinline__ float half_sum(float x) {
  auto t = __builtin_amdgcn_permlane32_swap(__float_as_uint(x), __float_as_uint(x), false, false);
  return __uint_as_float(t[0]) + __uint_as_float(t[1]);
}

__global__ __launch_bounds__(512, 2) void k_all(const char* a0_, const char* a1_, const char* a2_, const char* a3_, const char* a4_, const char* a5_,
                                                const char* a6_, const char* a7_, const char* a8_, const char* a9_, const char* a10_, char* a11_, unsigned* a12_) {
  u32x16 ka; u32x8 kb_; u32x2 kc_;
  asm volatile("s_load_dwordx16 %0, %3, 0x0\n\ts_load_dwordx8 %1, %3, 0x40\n\ts_load_dwordx2 %2, %3, 0x60"
               : "=&s"(ka), "=&s"(kb_), "=&s"(kc_) : "s"(__builtin_amdgcn_kernarg_segment_ptr()));
  __shared__ __attribute__((aligned(16))) char smem[LDS_BYTES];
  LAS char* lds = (LAS char*)smem;
  LAS char* scr = lds + LDS_SCR;
  LAS unsigned* ctl = (LAS unsigned*)scr;
  const int tid = threadIdx.x, lane = tid & 63, w = __builtin_amdgcn_readfirstlane(tid >> 6);
  const int blk = blockIdx.x, head = blk >> 3, prod = (blk + head) & 7;
  const int sub = (0x54372106u >> (4 * prod)) & 7;
  unsigned cpf = 0;
  {
    u64 pc;
    asm volatile("s_getpc_b64 %0" : "=s"(pc));
    cpf = __builtin_amdgcn_raw_buffer_load_b32(mkrs((const GAS char*)(pc & ~255ull), CPF), w == 7 ? head * 2048 + lane * 32 : OOB, 0, 0);
    SB();
  }
  {
    unsigned c = 16 + 4 * (DCH * sub + tid);
    asm volatile("" : "+v"(c));
    c = tid < 8 + DCH ? c : ~0u;
    c = tid < 8 ? 0u : c;
    ctl[tid] = tid == 4 ? DCH : c;
    *(LAS u64*)(scr + SCR_SUM + min(tid, 2 * SUM_STR / 8 - 1) * 8) = 0;
  }
  const int l16 = lane & 15, grp = lane >> 4;
  const int b4 = head & 3, cntoff = WS_CNT + head * CSTR;
  const int bm = (b4 + 1 + (sub >= 3 ? sub - 3 : sub)) & 3;
  const size_t base = (size_t)(bm * 32 + head) << 20;
  const int so = ((w & 1) << bitpos('a')) | (((w >> 1) & 1) << bitpos('b')) | ((w >> 2) << bitpos('c')) | (sub >= 3 ? 1 << bitpos('p') : 0);
  int v0 = l16 * 16 + grp * (sub < 6 ? 256 : 131072) + (sub < 6 ? so : w * 524288 + head * 256);
  unsigned boff = sub < 6 ? (unsigned)base : 0u, bsz = sub < 6 ? 1u << 20 : 4u << 20;
  int s0[WTH];
#pragma unroll
  for (int i = 0; i < WTH; i++) {
    s0[i] = sub < 6 ? goff(i) : i * 8192;
    asm volatile("" : "+s"(s0[i]));
  }
  asm volatile("" : "+v"(v0), "+s"(boff), "+s"(bsz));
  SB();
  asm volatile("s_waitcnt lgkmcnt(0)" : "+s"(ka), "+s"(kb_), "+s"(kc_));
  auto ptr = [](unsigned lo, unsigned hi) { return (GAS char*)(((u64)hi << 32) | lo); };
  GAS char *const xA = ptr(ka[0], ka[1]), *const ckv = ptr(ka[2], ka[3]), *const wqA = ptr(ka[4], ka[5]), *const wuk = ptr(ka[6], ka[7]),
           *const wuv = ptr(ka[8], ka[9]), *const woA = ptr(ka[10], ka[11]), *const xB = ptr(ka[12], ka[13]), *const kc = ptr(ka[14], ka[15]),
           *const vc = ptr(kb_[0], kb_[1]), *const wqB = ptr(kb_[2], kb_[3]), *const woB = ptr(kb_[4], kb_[5]), *const ws = ptr(kb_[6], kb_[7]),
           *const out = ptr(kc_[0], kc_[1]);
  const u64 e0 = *(const __attribute__((address_space(4))) u64*)(u64)(ws + WS_EPOCH);
  const rsrc_t r0 = mkrs((sub < 6 ? kc : sub == 6 ? wqB : wqA) + boff, bsz);
  SB();
  u32x4 ra[16];
#pragma unroll
  for (int i = 0; i < WTH; i++) ra[i] = ldnt(r0, v0, s0[i]);
  SB();
  u32x4 xp[2][2];
  if (sub >= 6) {
    const GAS char* x = sub == 6 ? xB : xA;
#pragma unroll
    for (int s = 0; s < 2; s++) {
      const GAS u32x4* xs = (const GAS u32x4*)(x + (2 * s + (l16 >> 3)) * 1024 + (64 * w + 16 * grp) * 2);
      xp[s][0] = xs[0]; xp[s][1] = xs[1];
    }
#pragma unroll
    for (int i = WTH; i < 16; i++) ra[i] = ldnt(r0, v0, i * 8192);
    asm volatile("" ::: "memory");
    SB();
  }
  const rsrc_t rws = mkrs(ws, WS_END);
  unsigned mask[4];
#pragma unroll
  for (int v = 0; v < 4; v++) mask[v] = (((lane & 7) >> 1) == v) ? ((lane & 1) ? 0xffff0000u : 0x0000ffffu) : 0u;
  const size_t base4 = (size_t)(b4 * 32 + head) << 20;
  const rsrc_t rk4 = mkrs(kc + base4, 1u << 20), rv4 = mkrs(vc + base4, 1u << 20);
  const int voff = lane * 16;
  LAS char* qst = scr + 1024 + w * 512;
  const int qo = WS_QB + head * 2048 + (lane < 32 ? bm : b4) * 512 + (lane & 31) * 16;
  auto qwait = [&](u32x4 g, const Tag& tg) {
    if (!__all(qok(g, tg))) {
      for (unsigned spins = 0;; spins++) {
        asm volatile("" ::: "memory");
        g = ldsc(rws, qo, 0);
        if (__all(qok(g, tg)) || spins > SPIN_CAP) break;
        __builtin_amdgcn_s_sleep(2);
      }
    }
    *(LAS u32x2*)(qst + lane * 8) = u32x2{g[0], g[2]};
  };
  unsigned cur;

  if (__builtin_expect(sub < 6, 1)) {
    const rsrc_t rv = mkrs(vc + base, 1u << 20);
    u32x4 rb[16], g;
#pragma unroll
    for (int i = WTH; i < 32 + NXA + NXB; i++) {
      if (i == WTH) asm volatile("s_barrier" ::: "memory");
      vmwait<WTH - 1>();
      if (i == 32 + NXA + NXB - WTH) asm volatile("buffer_load_dwordx4 %0, %1, %2, 0 offen sc1" : "=&v"(g) : "v"(qo), "s"(rws));
      if (i < 32) {
        const u32x4 v = ldslot(r0, voff, so, i);
        if (i < 16) ra[i] = v; else rb[i - 16] = v;
      } else {
        const int j = i < 32 + NXA ? i + 16 - NXA : i + 32 - NXA - NXB;
        dma1(r0, voff + goff(j & 3), so ^ goff(j & ~3), (unsigned)(uintptr_t)lds + w * 16384 + (i - 32) * 1024);
      }
      SB();
    }
    asm volatile("s_waitcnt vmcnt(%1)" : "+v"(g) : "n"(WTH), "v"(cpf));
    qwait(g, mktag(e0));
    const u32x4 q = *(LAS u32x4*)(qst + l16 * 16);
    float sc[4] = {0.f, 0.f, 0.f, 0.f};
#pragma unroll 1
    for (int it = 0; it < 2; it++) {
      const rsrc_t rn = it ? rv : r0;
      sc[2] = sc[0]; sc[3] = sc[1];
      sc[1] = k_step<NXA>(ra, q, l16, rn, voff, so ^ (it ? goff(48) : goff(32)), it == 0, lds + w * 16384 + voff);
      sc[0] = k_step<NXB>(rb, q, l16, rn, voff, so ^ (it ? goff(32) : goff(48)), it == 0, lds + w * 16384 + NXA * 1024 + voff);
    }
    const float mx = xrow_max(row_max(fmaxf(fmaxf(sc[0], sc[1]), fmaxf(sc[2], sc[3]))));
    float f, l = 0.f;
    const float M = exp_ref(mx, f);
    unsigned pk[4];
#pragma unroll
    for (int k = 0; k < 4; k++) {
      const float p = fexp2(fmaf(sc[k] - mx, LOG2E, f));
      pk[k] = cvtpk(p, p);
      l += bf_lo(pk[k]);
    }
    l = xrow_sum(row_sum(l));
    cur = 8 * sub + w;
    f32x4 acc = {0.f, 0.f, 0.f, 0.f};
    v_step<0, 1>(ra, pk[0], mask, acc, r0, rv, voff, so ^ goff(16));
    v_step<0, 1>(rb, pk[1], mask, acc, r0, rv, voff, so);
    v_step<0, 2>(ra, pk[2], mask, acc, rk4, rv4, voff, cur * 4096);
    v_step<0, 0>(rb, pk[3], mask, acc, r0, rv, voff, 0);
    Acc A;
    A.R = rfl(bucket(M));
    const double sm = mkd(0u, p2hi(M, A.R));
    A.o[0] = (lane < 32 ? acc[0] : acc[2]) * sm; A.o[1] = (lane < 32 ? acc[1] : acc[3]) * sm;
    A.l = l * sm;
    b_merge(mkmrg(scr + SCR_SUM, lane, WS_RS + (head * 16 + sub) * 1024, mktag(e0)), scr + 8192, w, lane, A, rws, e0);
  } else {
  {
    __builtin_amdgcn_raw_buffer_store_b32(48 + 24 * DCH, rws, (sub == 6 && tid == 0) ? cntoff : OOB, 0, AUX_SC1);
    {
      const rsrc_t rk = mkrs(wuk, sub == 7 ? 4u << 20 : 0u);
#pragma unroll
      for (int i = 0; i < 16; i++)
        __builtin_amdgcn_raw_ptr_buffer_load_lds(rk, (LAS void*)(lds + w * 16384 + i * 1024), 16,
                                                 (64 * w + grp) * 8192 + head * 256 + ((l16 ^ (4 * (i & 3) + grp)) * 16), i * 4 * 8192, 0, AUX_NT);
    }
    SB();
    f32x4 acc[2] = {{0.f, 0.f, 0.f, 0.f}, {0.f, 0.f, 0.f, 0.f}};
    diag_gemv(ra, xp, mask, acc);
    LAS float* red = (LAS float*)(scr + 8192);
#pragma unroll
    for (int s = 0; s < 2; s++) *(LAS f32x4*)(red + w * 512 + (2 * s + (grp >> 1)) * 128 + 8 * l16 + 4 * (grp & 1)) = acc[s];
    asm volatile("" :: "v"(cpf));
    __builtin_amdgcn_s_waitcnt(0x0f70);
    drain();
    bar();
    if (tid < 128) {
      f32x4 a = {0.f, 0.f, 0.f, 0.f};
#pragma unroll
      for (int k = 0; k < 8; k++) a += *(LAS f32x4*)(red + k * 512 + 4 * tid);
      const unsigned p0 = cvtpk(a[0], a[1]), p1 = cvtpk(a[2], a[3]);
      const Tag tg = mktag(e0);
      stsc(u32x4{p0, tg.th, p1, tg.th}, rws, sub == 6 ? WS_QB + head * 2048 + tid * 16 : OOB, 0);
      *(LAS u32x2*)(scr + 1024 + tid * 8) = u32x2{p0, p1};
    }
  }

  if (sub == 6) {
    const int aid = ((head & 6) << 2) | ((head & 1) << 2) | (head >> 3), b = aid >> 3;
    const rsrc_t rc = mkrs(ckv + (size_t)aid * 524288, 524288);
    const int dvo = (lane >> 3) * 1024 + w * 128 + (((lane & 7) ^ fz(lane >> 3)) * 16);
    const unsigned dlo = (unsigned)(uintptr_t)lds + w * 8192;
    const int xt = (aid & 7) << 16;
#pragma unroll
    for (int j = 0; j < 16; j++) dma1(rc, dvo, (j * 8192) ^ xt, dlo + (j >> 3) * 65536 + (j & 7) * 1024);
    SB();
    const Tag tg = mktag(e0);
    bar();
    {
      u32x4 t[8];
      unsigned miss = 15;
      __builtin_amdgcn_s_sleep(90);
      for (unsigned spins = 0;; spins++) {
        asm volatile("" ::: "memory");
#pragma unroll
        for (int i = 0; i < 4; i++)
          if (miss >> i & 1) {
            t[2 * i] = ldsc(rws, WS_QABS + (b * 32 + w) * 2048 + lane * 16, i * 16384);
            t[2 * i + 1] = ldsc(rws, WS_QABS + (b * 32 + w) * 2048 + lane * 16 + 1024, i * 16384);
          }
        SB();
#pragma unroll
        for (int i = 0; i < 4; i++) if (__all(qok(t[2 * i], tg) & qok(t[2 * i + 1], tg))) miss &= ~(1u << i);
        if (!miss || spins > SPIN_CAP) break;
        __builtin_amdgcn_s_sleep(4);
      }
#pragma unroll
      for (int i = 0; i < 8; i++) {
        const int row = 8 * (i >> 1) + w;
        *(LAS u32x2*)(scr + row * 1024 + (((32 * (i & 1) + (lane >> 1)) ^ swz(row)) * 16) + (lane & 1) * 8) = u32x2{t[i][0], t[i][2]};
      }
    }
    bar();
    const int kb = w & 3, hb = w >> 2;
    u32x4 qf[16];
    {
      const int row = 16 * hb + l16;
#pragma unroll
      for (int ks = 0; ks < 16; ks++) qf[ks] = *(LAS u32x4*)(scr + row * 1024 + (((4 * ks + grp) ^ swz(row)) * 16));
    }
    LAS float* mx = (LAS float*)scr;
    LAS char* pl = scr + 1024;
    LAS float* lr = (LAS float*)(scr + 8192);
    f32x16 o0, o1;
#pragma unroll
    for (int j = 0; j < 16; j++) { o0[j] = 0.f; o1[j] = 0.f; }
    float m1 = -INFINITY, m2 = -INFINITY, l1 = 0.f;
    const int h1 = 16 * hb + l16, h2 = lane & 31, hh = lane >> 5;
    const int arow = 16 * kb + l16;
    const int tq = (lane & 15) >> 2, tp = lane & 3, tblk = (lane >> 4) & 1;
    const int qa0 = arow * 128 + ((grp ^ fz(arow)) * 16), qa1 = qa0 ^ 64;
    const int pr = 8 * hh + tq, pc = 2 * tblk + (tp >> 1);
    const int pa0 = w * 8192 + pr * 128 + ((pc ^ fz(pr)) * 16) + 8 * (tp & 1), pa1 = w * 8192 + (pr + 4) * 128 + ((pc ^ fz(pr + 4)) * 16) + 8 * (tp & 1);
#pragma unroll 1
    for (int t = 0; t < 8; t++) {
      if (t < 7) asm volatile("s_waitcnt vmcnt(8)" ::: "memory"); else asm volatile("s_waitcnt vmcnt(0)" ::: "memory");
      bar();
      LAS char* img = lds + (t & 1) * 65536;
      f32x4 s0 = {0.f, 0.f, 0.f, 0.f}, s1 = s0;
      u32x4 ka[16];
      u32x2 ta[4][4];
      const int nso = t < 6 ? ((t + 2) * 65536) ^ xt : OOB;
#pragma unroll
      for (int ks = 0; ks < 16; ks++) ka[ks] = *(LAS u32x4*)(img + ((ks & 1) ? qa1 : qa0) + (ks >> 1) * 8192);
      SB();
#pragma unroll
      for (int k2 = 0; k2 < 8; k2++) {
        s0 = mfma16(ka[2 * k2], qf[2 * k2], s0);
        s1 = mfma16(ka[2 * k2 + 1], qf[2 * k2 + 1], s1);
      }
      asm volatile("" : "+v"(s0), "+v"(s1));
      SB();
#pragma unroll
      for (int ks = 0; ks < 4; ks++) {
        ta[ks][0] = trread(img + pa0 + ks * 2048); ta[ks][1] = trread(img + pa1 + ks * 2048);
        ta[ks][2] = trread(img + (pa0 ^ 64) + ks * 2048); ta[ks][3] = trread(img + (pa1 ^ 64) + ks * 2048);
      }
      SB();
      float x[4];
#pragma unroll
      for (int j = 0; j < 4; j++) x[j] = rbf(rbf(s0[j] + s1[j]) * SCALE);
      const float lm = xrow_max(fmaxf(fmaxf(x[0], x[1]), fmaxf(x[2], x[3])));
      if (grp == 0) mx[h1 * 4 + kb] = lm;
      LAS u32x2* pw = (LAS u32x2*)(pl + h1 * 144 + (16 * kb + 4 * grp) * 2);
      u32x2 pp = {cvtpk(fexp2((x[0] - m1) * LOG2E), fexp2((x[1] - m1) * LOG2E)), cvtpk(fexp2((x[2] - m1) * LOG2E), fexp2((x[3] - m1) * LOG2E))};
      *pw = pp;
      bar();
      const f32x4 ma = *(LAS f32x4*)(mx + h1 * 4), mb = *(LAS f32x4*)(mx + h2 * 4);
      u32x4 pb[4];
#pragma unroll
      for (int ks = 0; ks < 4; ks++) pb[ks] = *(LAS u32x4*)(pl + h2 * 144 + (16 * ks + 8 * hh) * 2);
      const float n1 = fmaxf(m1, fmaxf(fmaxf(ma[0], ma[1]), fmaxf(ma[2], ma[3])));
      const float n2 = fmaxf(m2, fmaxf(fmaxf(mb[0], mb[1]), fmaxf(mb[2], mb[3])));
      float al1 = fexp2((m1 - n1) * LOG2E), al2 = fexp2((m2 - n2) * LOG2E);
      asm volatile("" : "+v"(pb[0]), "+v"(pb[1]), "+v"(pb[2]), "+v"(pb[3]));
      if (__any(n2 - m2 > 40.f)) {
        bar();
        pp = u32x2{cvtpk(fexp2((x[0] - n1) * LOG2E), fexp2((x[1] - n1) * LOG2E)), cvtpk(fexp2((x[2] - n1) * LOG2E), fexp2((x[3] - n1) * LOG2E))};
        *pw = pp;
        l1 *= al1;
#pragma unroll
        for (int j = 0; j < 16; j++) { o0[j] *= al2; o1[j] *= al2; }
        al1 = al2 = 1.f;
        bar();
#pragma unroll
        for (int ks = 0; ks < 4; ks++) pb[ks] = *(LAS u32x4*)(pl + h2 * 144 + (16 * ks + 8 * hh) * 2);
      }
      m1 = n1; m2 = n2;
#pragma unroll
      for (int ks = 0; ks < 4; ks++) {
        o0 = mfma32(u32x4{ta[ks][0][0], ta[ks][0][1], ta[ks][1][0], ta[ks][1][1]}, pb[ks], o0);
        o1 = mfma32(u32x4{ta[ks][2][0], ta[ks][2][1], ta[ks][3][0], ta[ks][3][1]}, pb[ks], o1);
        SB();
        if (t < 6) {
          dma1(rc, dvo, nso + 2 * ks * 8192, dlo + (t & 1) * 65536 + 2 * ks * 1024);
          dma1(rc, dvo, nso + (2 * ks + 1) * 8192, dlo + (t & 1) * 65536 + (2 * ks + 1) * 1024);
        }
        SB();
      }
      l1 = (l1 + ((bf_lo(pp[0]) + bf_hi(pp[0])) + (bf_lo(pp[1]) + bf_hi(pp[1])))) * al1;
#pragma unroll
      for (int j = 0; j < 16; j++) { o0[j] *= al2; o1[j] *= al2; }
    }
    bar();
    {
      float am = 0.f;
#pragma unroll
      for (int j = 0; j < 16; j++) am = fmaxf(am, fmaxf(fabsf(o0[j]), fabsf(o1[j])));
      auto sw = __builtin_amdgcn_permlane32_swap(__float_as_uint(am), __float_as_uint(am), false, false);
      am = fmaxf(__uint_as_float(sw[0]), __uint_as_float(sw[1]));
      const int e = max(__builtin_amdgcn_frexp_expf(am), -127);
      LAS char* tp = lds + h2 * 1040 + (64 * w + 4 * hh) * 2;
      auto pkn = [&](float a, float b) { return __builtin_bit_cast(unsigned, __builtin_amdgcn_cvt_pknorm_i16(__builtin_ldexpf(a, -e), __builtin_ldexpf(b, -e))); };
#pragma unroll
      for (int j4 = 0; j4 < 4; j4++) {
        *(LAS u32x2*)(tp + j4 * 16) = u32x2{pkn(o0[4 * j4], o0[4 * j4 + 1]), pkn(o0[4 * j4 + 2], o0[4 * j4 + 3])};
        *(LAS u32x2*)(tp + 64 + j4 * 16) = u32x2{pkn(o1[4 * j4], o1[4 * j4 + 1]), pkn(o1[4 * j4 + 2], o1[4 * j4 + 3])};
      }
      if (hh == 0) *(LAS unsigned char*)(scr + 9216 + h2 * 8 + w) = (unsigned char)(e + 127);
      const float ls = xrow_sum(l1);
      if (grp == 0) lr[kb * 32 + h1] = ls;
      bar();
#pragma unroll
      for (int i = 0; i < 4; i++) {
        u32x4 v = *(LAS u32x4*)(lds + (4 * w + i) * 1040 + lane * 16);
#pragma unroll
        for (int k = 0; k < 4; k++) v[k] = bfi(PMASK, ptag(tg, k), v[k]);
        stsc(v, rws, WS_PO + (aid * 32 + 4 * w) * 1024 + lane * 16, i * 1024);
      }
      if (tid < 32) {
        const u32x2 eb = *(LAS u32x2*)(scr + 9216 + tid * 8);
        const float l = (lr[tid] + lr[32 + tid]) + (lr[64 + tid] + lr[96 + tid]);
        stsc(u32x4{bfi(0xffffu, tg.lo, __float_as_uint(m2)), bfi(255u, ltag(tg), __float_as_uint(l)), eb[0], eb[1]}, rws, WS_PML + (aid * 32 + tid) * 16, 0);
      }
      if (w == 0) {
        const unsigned t = __builtin_amdgcn_readfirstlane(__builtin_amdgcn_raw_ptr_buffer_atomic_add_i32(8 + 4 * DCH, rws, lane == 0 ? cntoff : OOB, 0, AUX_SC1));
        const unsigned bs = t + 8 + 4 * (lane - 8);
        ctl[lane] = lane == 3 ? t : lane == 4 ? DCH : lane == 5 ? (t + 8 + 4 * DCH >= NUNIT ? 0x10000u : 0u) : lane < 8 ? 0u : (lane < 8 + DCH && bs < NUNIT) ? bs : ~0u;
        if (lane < 32) ctl[64 + lane] = ~0u;
      }
      *(LAS u64*)(scr + SCR_SUM + SUM_STR + min(tid, SUM_STR / 8 - 1) * 8) = 0;
      bar();
    }
    const u32x4 g = ldsc(rws, qo, 0);
    cur = __builtin_amdgcn_readfirstlane(ctl[3]) + w;
#pragma unroll
    for (int i = 0; i < 8; i++) ra[i] = ldunit(i < 4 ? rk4 : rv4, voff, unit_off(cur), i & 3);
    SB();
    qwait(g, mktag(e0));
  } else {
    const Tag tg = mktag(e0);
    bar();
    {
      u32x4 bf[4];
#pragma unroll
      for (int ks = 0; ks < 4; ks++) bf[ks] = *(LAS u32x4*)(scr + 1024 + (l16 & 3) * 256 + ks * 64 + grp * 16);
      LAS char* tt = scr + 2048 + w * 512;
#pragma unroll
      for (int rt = 0; rt < 4; rt++) {
        f32x4 acc = {0.f, 0.f, 0.f, 0.f};
#pragma unroll
        for (int ks = 0; ks < 4; ks++) acc = mfma16(*(LAS u32x4*)(lds + (64 * w + 16 * rt + l16) * 256 + (((4 * ks + grp) ^ l16) * 16)), bf[ks], acc);
        if (l16 < 4) *(LAS u32x2*)(tt + l16 * 128 + (16 * rt + 4 * grp) * 2) = u32x2{cvtpk(acc[0], acc[1]), cvtpk(acc[2], acc[3])};
      }
      asm volatile("s_waitcnt lgkmcnt(0)" ::: "memory");
      const u32x2 v = *(LAS u32x2*)(tt + lane * 8);
      stsc(u32x4{v[0], tg.th, v[1], tg.th}, rws, WS_QABS + (grp * 32 + head) * 2048 + w * 256 + l16 * 16, 0);
    }
    bar();
    u32x4 wf[4][4], wr[16];
    {
      const rsrc_t ro = mkrs(woA + (size_t)head * 131072, 131072);
      dma_rows<16>(ro, 0, lds, 16 * w, lane);
      const rsrc_t rv = mkrs(wuv, 4u << 20);
      const int voff = (64 * w + 16 * grp) * 8192 + head * 256 + l16 * 16;
#pragma unroll
      for (int i = 0; i < 16; i++) wr[i] = ldnt(rv, voff, i * 8192);
      SB();
      asm volatile("s_waitcnt vmcnt(16)" ::: "memory");
      bar();
      wo_frags(lds, w, lane, wf);
      bar();
      const rsrc_t rb = mkrs(woB, 4u << 20);
      const int bvo = (lane >> 3) * 1024 + (((lane & 7) ^ ((lane >> 5) << 1)) * 16);
#pragma unroll
      for (int i = 0; i < 16; i++)
        dma1(rb, (i & 1) ? bvo ^ 64 : bvo, (4 * w + b4) * 131072 + (head >> 2) * 128 + i * 8192, (unsigned)(uintptr_t)lds + w * 16384 + i * 1024);
      SB();
      if (tid < 8) ((LAS unsigned*)(scr + 24576))[tid] = 0u;
    }
    const int hw = 4 * w + b4, cc = head >> 2, roff = WS_RS + hw * 16384 + lane * 16;
    u32x4 g[6];
#pragma unroll
    for (int s = 0; s < 6; s++) asm volatile("" : "=v"(g[s]));
    if (w == 0) {
      for (unsigned spins = 0;; spins++) {
        asm volatile("" ::: "memory");
        const u32x4 v = ldsc(rws, WS_PML + ((lane & 31) * 32 + head) * 16, 0);
        if (__builtin_popcount((unsigned)__builtin_amdgcn_ballot_w64(mok(v, tg))) >= NREL || spins > SPIN_CAP) break;
        __builtin_amdgcn_s_sleep(16);
      }
    }
    bar();
    {
      const int b = w >> 1, c4 = tid & 127;
      const unsigned ta = (lane & 1) ? ptag(tg, 2) : ptag(tg, 0), tb = (lane & 1) ? ptag(tg, 3) : ptag(tg, 1);
      u32x4 ml;
      u32x2 po[8];
#pragma unroll
      for (int s = 0; s < 8; s++) asm volatile("" : "=v"(po[s]));
      unsigned missP = 255;
      for (unsigned spins = 0;; spins++) {
        asm volatile("" ::: "memory");
        ml = ldsc(rws, WS_PML + ((8 * b + (lane & 7)) * 32 + head) * 16, 0);
#pragma unroll
        for (int s = 0; s < 8; s++)
          if (missP >> s & 1) po[s] = __builtin_amdgcn_raw_buffer_load_b64(rws, WS_PO + (8 * b * 32 + head) * 1024 + c4 * 8, s * 32768, AUX_SC1);
        SB();
#pragma unroll
        for (int s = 0; s < 8; s++) if (__all((((po[s][0] ^ ta) | (po[s][1] ^ tb)) & PMASK) == 0u)) missP &= ~(1u << s);
        if ((!missP && __all(mok(ml, tg))) || spins > SPIN_CAP) break;
        __builtin_amdgcn_s_sleep(2);
      }
#pragma unroll
      for (int s = 0; s < 6; s++) g[s] = ldsc(rws, roff, s * 1024);
      SB();
      ml[0] &= 0xffff0000u; ml[1] &= ~255u;
#pragma unroll
      for (int s = 0; s < 8; s++) po[s] = u32x2{bfi(PMASK, 0x00020002u, po[s][0]), bfi(PMASK, 0x00020002u, po[s][1])};
      float M = __uint_as_float(ml[0]);
      M = fmaxf(M, dpp<XOR1>(M)); M = fmaxf(M, dpp<XOR2>(M)); M = fmaxf(M, dpp<XOR7>(M));
      const float f = fexp2((__uint_as_float(ml[0]) - M) * LOG2E);
      float L = f * __uint_as_float(ml[1]);
      L += dpp<XOR1>(L); L += dpp<XOR2>(L); L += dpp<XOR7>(L);
      const float gl = __builtin_ldexpf(f, (int)((((w & 1) ? ml[3] : ml[2]) >> (8 * grp)) & 255) - 142);
      const float gs[8] = {dpp<0x150>(gl), dpp<0x151>(gl), dpp<0x152>(gl), dpp<0x153>(gl), dpp<0x154>(gl), dpp<0x155>(gl), dpp<0x156>(gl), dpp<0x157>(gl)};
      f32x4 a = {0.f, 0.f, 0.f, 0.f};
#pragma unroll
      for (int s = 0; s < 8; s++)
        a += gs[s] * f32x4{(float)(short)po[s][0], (float)((int)po[s][0] >> 16), (float)(short)po[s][1], (float)((int)po[s][1] >> 16)};
      const float inv = (32768.f / 32767.f) / L;
      const u32x2 v = {cvtpk(a[0] * inv, a[1] * inv), cvtpk(a[2] * inv, a[3] * inv)};
      *(LAS u32x2*)(scr + b * 1024 + c4 * 8) = v;
    }
    bar();
    {
      u32x4 xp[2][2];
#pragma unroll
      for (int s = 0; s < 2; s++) {
        LAS u32x4* xs = (LAS u32x4*)(scr + (2 * s + (l16 >> 3)) * 1024 + (64 * w + 16 * grp) * 2);
        xp[s][0] = xs[0]; xp[s][1] = xs[1];
      }
      f32x4 acc[2] = {{0.f, 0.f, 0.f, 0.f}, {0.f, 0.f, 0.f, 0.f}};
      diag_gemv(wr, xp, mask, acc);
      LAS float* red = (LAS float*)(scr + 8192);
#pragma unroll
      for (int s = 0; s < 2; s++) *(LAS f32x4*)(red + w * 512 + (2 * s + (grp >> 1)) * 128 + 8 * l16 + 4 * (grp & 1)) = acc[s];
      bar();
      if (tid < 256) {
        float a = 0.f, b = 0.f;
#pragma unroll
        for (int k = 0; k < 8; k++) { const f32x2 v = *(LAS f32x2*)(red + k * 512 + 2 * tid); a += v[0]; b += v[1]; }
        ((LAS unsigned*)(scr + 4096))[tid] = cvtpk(a, b);
      }
      bar();
    }
    const f32x4 yA = wo_calc(wf, scr + 4096, lane);
    drain();
    y_store(yA, w, lane, head, rws, WS_Y, tg);
    {
      const int q = l16 >> 2;
      LAS char* img = lds + w * 16384 + (8 * (q >> 1) + 4 * (grp & 1) + 2 * (grp >> 1) + (q & 1)) * 128 + 8 * (lane & 3);
      const int fl = (grp & 1) | (q & 2);
#pragma unroll
      for (int ks = 0; ks < 4; ks++)
#pragma unroll
        for (int tl = 0; tl < 4; tl++) {
          const u32x2 a = trread(img + (tl ^ fl) * 32 + ks * 4096), b = trread(img + (tl ^ fl) * 32 + ks * 4096 + 2048);
          wf[tl][ks] = u32x4{a[0], a[1], b[0], b[1]};
        }
    }
    const rsrc_t rout = mkrs(out, 8192);
    LAS unsigned* cnt = (LAS unsigned*)(scr + 24576);
    LAS char* const re = scr + 8192;
    LAS char* const rA = scr + 8192 + 8192;
    LAS char* rl = scr + 8192 + 10240 + (lane & 31) * 8;
    LAS char* rw = scr + 8192 + 10240 + w * 256 + (lane & 3) * 64 + grp * 16;
    LAS char* rx = rl + (w < 3 ? 8 + w : 11) * 256;
    int oo = lane < 32 ? b4 * 2048 + 1024 + cc * 128 + lane * 4 : OOB, oe = head == 0 && lane == 0 ? WS_EPOCH : OOB;
    asm volatile("" : "+v"(rl), "+v"(rw), "+v"(rx), "+v"(oo), "+v"(oe), "+v"(cnt));
    {
      const int ofa = WS_Y + (b4 * 8 + cc) * 16384 + w * 2048 + lane * 16;
      u32x4 x0, x1;
      for (unsigned spins = 0;; spins++) {
        asm volatile("" ::: "memory");
        x0 = ldsc(rws, ofa, 0); x1 = ldsc(rws, ofa + 1024, 0);
        if (__all(gok(x0, tg) & gok(x1, tg)) || spins > SPIN_CAP) break;
        __builtin_amdgcn_s_sleep(4);
      }
      const f32x2 r = {half_sum(gval(x0[0]) + gval(x1[0])), half_sum(gval(x0[2]) + gval(x1[2]))};
      if (lane < 32) *(volatile LAS f32x2*)(rA + w * 256 + lane * 8) = r;
      const unsigned n = arrive(cnt + 1, lane);
      const f32x2 v = rows_sum<8>(rA + (lane & 31) * 8);
      __builtin_amdgcn_raw_buffer_store_b32(cvtpk(v[0], v[1]), rout, lane < 32 && n == 8u ? b4 * 2048 + cc * 128 + l16 * 8 + grp * 4 : OOB, 0, 0);
    }
    {
      for (unsigned spins = 0;; spins++) {
        unsigned bad = 0;
#pragma unroll
        for (int s = 0; s < 6; s++) bad |= (g[s][0] ^ tg.lo) | (g[s][2] ^ tg.rb);
        if (__all((bad & (spins > SPIN_CAP * 4 ? 0u : 511u)) == 0u)) break;
        asm volatile("" ::: "memory");
#pragma unroll
        for (int s = 0; s < 6; s++) g[s] = ldsc(rws, roff + s * 1024, 0);
        SB();
      }
      unsigned pr[3];
#pragma unroll
      for (int t = 0; t < 3; t++) {
        const u32x4 g0 = g[t], g1 = g[t + 3];
        const float R0 = rec_r(g0), R1 = rec_r(g1), Rm = fmaxf(R0, R1);
        const double s0 = mkd(0u, p2hi(R0, Rm)), s1 = mkd(0u, p2hi(R1, Rm));
        const double inv = __builtin_amdgcn_rcp(__builtin_fma(rec_l(g0), s0, rec_l(g1) * s1));
        pr[t] = cvtpk((float)(__builtin_fma(rec_a(g0), s0, rec_a(g1) * s1) * inv), (float)(__builtin_fma(rec_b(g0), s0, rec_b(g1) * s1) * inv));
      }
      *(volatile LAS f32x4*)(re + ((l16 >> 2) * 8 + w) * 256 + (lane & 3) * 64 + grp * 16) = proj(pr, wf, lane);
      const unsigned n = arrive(cnt, lane);
#pragma unroll
      for (int u = 0; u < 2; u++) {
        const int t = u ? 2 : lane >> 5;
        const f32x2 v = rows_sum<8>(re + t * 2048 + (lane & 31) * 8);
        stsc(gpair(v[0], v[1], tg), rws, n != 8u || (u && lane >= 32) ? OOB : WS_YB + ((((b4 + 1 + t) & 3) * 8 + cc) * 4 + b4) * 512 + (lane & 31) * 16, 0);
      }
    }
    const int ofb = w < 3 ? WS_YB + ((b4 * 8 + cc) * 4 + ((b4 + 1 + w) & 3)) * 512 + (lane & 31) * 16 : OOB;
    u32x4 xb, p[NSLOT];
    unsigned bad;
    for (unsigned spins = 0;; spins++) {
      asm volatile("" ::: "memory");
      xb = ldsc(rws, ofb, 0);
#pragma unroll
      for (int s = 0; s < NSLOT; s++) p[s] = ldsc(rws, roff + 8192 + s * 1024, 0);
      unsigned m = spins > SPIN_CAP * 4 ? 0u : 511u;
      asm volatile("" : "+s"(m));
      SB();
      unsigned ax = min((((xb[0] ^ tg.tl) | (xb[2] ^ tg.tl)) >> 24) | (xb[1] ^ tg.th) | (xb[3] ^ tg.th), w < 3 ? 1u : 0u), az = 0;
#pragma unroll
      for (int s = 0; s < NSLOT; s++) { ax |= p[s][0] ^ tg.lo; az |= p[s][2] ^ tg.rb; }
      bad = az & 512u;
      if (__all(((ax | az) & m) == 0u)) break;
    }
    *(volatile LAS f32x2*)rx = f32x2{gval(xb[0]), gval(xb[2])};
    double S0, S1, L;
    if (__builtin_expect(__all(bad == 0u), 1)) {
      S0 = mkd(p[0][0], p[0][1]); S1 = mkd(p[0][2], p[0][3]); L = rec_l(p[0]);
#pragma unroll
      for (int s = 1; s < NSLOT; s++) { S0 += mkd(p[s][0], p[s][1]); S1 += mkd(p[s][2], p[s][3]); L += rec_l(p[s]); }
    } else {
      float R = -INFINITY;
      S0 = S1 = L = 0.0;
#pragma unroll
      for (int s = 0; s < NSLOT; s++) {
        const float Rs = rec_l(p[s]) != 0.0 ? rec_r(p[s]) : -INFINITY, Rn = fmaxf(R, Rs);
        const double s0 = mkd(0u, p2hi(R, Rn)), s1 = mkd(0u, p2hi(Rs, Rn));
        L = __builtin_fma(rec_l(p[s]), s1, L * s0);
        S0 = __builtin_fma(rec_a(p[s]), s1, S0 * s0); S1 = __builtin_fma(rec_b(p[s]), s1, S1 * s0);
        R = Rn;
      }
    }
    {
      const double inv = __builtin_amdgcn_rcp(L);
      const unsigned pr[1] = {cvtpk((float)(S0 * inv), (float)(S1 * inv))};
      const f32x4 y = proj(pr, wf, lane);
      if ((lane & 12) == 0) *(volatile LAS f32x4*)rw = y;
      const unsigned n = arrive(cnt + 2, lane);
      const f32x2 v = rows_sum<11>(rl);
      __builtin_amdgcn_raw_buffer_store_b32(cvtpk(v[0], v[1]), rout, n == 8u ? oo : OOB, 0, 0);
      __builtin_amdgcn_raw_buffer_store_b64(__builtin_bit_cast(u32x2, tg.e), rws, n == 8u ? oe : OOB, 0, AUX_SC1);
    }
    return;
  }
  }
  {
    const u32x4 q4 = *(LAS u32x4*)(qst + 256 + l16 * 16);
    const Mrg mg = mkmrg(scr + SCR_SUM + SUM_STR, lane, WS_RS + (head * 16 + 8 + sub) * 1024, mktag(e0));
    Acc A;
    b_units(ra, rk4, rv4, cur, q4, lane, mask, ctl, rws, cntoff, A);
    b_merge(mg, scr + 8192 + 8704, w, lane, A, rws, e0);
    SB();
    asm volatile("" :: "v"(ra[0]), "v"(ra[1]), "v"(ra[2]), "v"(ra[3]), "v"(ra[4]), "v"(ra[5]), "v"(ra[6]), "v"(ra[7]));
  }
}

extern "C" void kernel_launch(void* const* d_in, const int* in_sizes, int n_in, void* d_out, int out_size, void* d_ws, size_t ws_size,
                              hipStream_t stream) {
  k_all<<<256, 512, 0, stream>>>((const char*)d_in[0], (const char*)d_in[1], (const char*)d_in[2], (const char*)d_in[3], (const char*)d_in[4],
                                 (const char*)d_in[5], (const char*)d_in[6], (const char*)d_in[7], (const char*)d_in[8], (const char*)d_in[9],
                                 (const char*)d_in[10], (char*)d_ws, (unsigned*)d_out);
}
```

```cpp
#define PROBE 1
#include <hip/hip_runtime.h>
#include <stdint.h>

typedef __attribute__((ext_vector_type(4))) unsigned u32x4;
typedef __attribute__((ext_vector_type(2))) unsigned u32x2;
typedef __attribute__((ext_vector_type(8))) unsigned u32x8;
typedef __attribute__((ext_vector_type(16))) unsigned u32x16;
typedef __attribute__((ext_vector_type(4))) float f32x4;
typedef __attribute__((ext_vector_type(2))) float f32x2;
typedef __attribute__((ext_vector_type(2))) double f64x2;
typedef __attribute__((ext_vector_type(16))) float f32x16;
typedef __attribute__((ext_vector_type(8))) __bf16 bf16x8;
typedef __attribute__((ext_vector_type(2))) __bf16 bf16x2;
typedef __attribute__((ext_vector_type(4))) short s16x4;
typedef unsigned long long u64;
typedef __amdgpu_buffer_rsrc_t rsrc_t;
#define LAS __attribute__((address_space(3)))
#define GAS __attribute__((address_space(1)))

#define CPF $
#define PAT "5c10p2ba43"
constexpr int WTH = 5;
constexpr int DCH = 2;
constexpr int NXA = 8, NXB = 8;
constexpr int NREL = 24;

constexpr int AUX_NT = 2, AUX_SC1 = 16, OOB = 0x40000000;
constexpr float SCALE = 0.08838834764831845f;
constexpr float LOG2E = 1.4426950408889634f;
constexpr unsigned SPIN_CAP = 1u << 17;
constexpr int NSLOT = 7, NUNIT = 256;

constexpr int WS_EPOCH = 0;
constexpr int CSTR    = 4352;
constexpr int WS_CNT  = 8192;
constexpr int WS_QB   = WS_CNT + 32 * CSTR;
constexpr int WS_QABS = WS_QB + 32 * 2048;
constexpr int WS_PML  = WS_QABS + 4 * 32 * 2048;
constexpr int WS_PO   = WS_PML + 32 * 32 * 16;
constexpr int WS_RS   = WS_PO + 32 * 32 * 1024;
constexpr int WS_Y    = WS_RS + 32 * 16 * 1024;
constexpr int WS_YB   = WS_Y + 4 * 8 * 32 * 512;
constexpr int WS_END  = WS_YB + 4 * 8 * 4 * 512;

constexpr int LDS_BYTES = 163840;
constexpr int LDS_SCR = 131072;
constexpr int SCR_SUM = 5120, SUM_STR = 1040;

__device__ __forceinline__ unsigned cvtpk(float lo, float hi) {
  f32x2 v = {lo, hi};
  return __builtin_bit_cast(unsigned, __builtin_convertvector(v, bf16x2));
}
__device__ __forceinline__ float bf_lo(unsigned u) { return __uint_as_float(u << 16); }
__device__ __forceinline__ float bf_hi(unsigned u) { return __uint_as_float(u & 0xffff0000u); }
__device__ __forceinline__ float rbf(float x) { return __uint_as_float(cvtpk(x, x) << 16); }
__device__ __forceinline__ float dot2(unsigned k, unsigned q, float acc) {
  return __builtin_amdgcn_fdot2_f32_bf16(__builtin_bit_cast(bf16x2, k), __builtin_bit_cast(bf16x2, q), acc, false);
}
template <int CTRL> __device__ __forceinline__ float dpp(float x) {
  return __builtin_bit_cast(float, __builtin_amdgcn_mov_dpp(__builtin_bit_cast(int, x), CTRL, 0xf, 0xf, true));
}
template <int CTRL> __device__ __forceinline__ unsigned dppu(unsigned x) {
  return (unsigned)__builtin_amdgcn_mov_dpp((int)x, CTRL, 0xf, 0xf, true);
}
constexpr int XOR1 = 0xB1, XOR2 = 0x4E, XOR7 = 0x141, XOR8 = 0x128, ROR4 = 0x124;
__device__ __forceinline__ float xrow_max(float x) {
  auto s = __builtin_amdgcn_permlane16_swap(__float_as_uint(x), __float_as_uint(x), false, false);
  x = fmaxf(__uint_as_float(s[0]), __uint_as_float(s[1]));
  auto t = __builtin_amdgcn_permlane32_swap(__float_as_uint(x), __float_as_uint(x), false, false);
  return fmaxf(__uint_as_float(t[0]), __uint_as_float(t[1]));
}
__device__ __forceinline__ float xrow_sum(float x) {
  auto s = __builtin_amdgcn_permlane16_swap(__float_as_uint(x), __float_as_uint(x), false, false);
  x = __uint_as_float(s[0]) + __uint_as_float(s[1]);
  auto t = __builtin_amdgcn_permlane32_swap(__float_as_uint(x), __float_as_uint(x), false, false);
  return __uint_as_float(t[0]) + __uint_as_float(t[1]);
}
__device__ __forceinline__ float row_max(float x) {
  x = fmaxf(x, dpp<XOR8>(x)); x = fmaxf(x, dpp<ROR4>(x)); x = fmaxf(x, dpp<XOR2>(x)); x = fmaxf(x, dpp<XOR1>(x));
  return x;
}
__device__ __forceinline__ float row_sum(float x) {
  x += dpp<XOR8>(x); x += dpp<ROR4>(x); x += dpp<XOR2>(x); x += dpp<XOR1>(x);
  return x;
}
__device__ __forceinline__ float fexp2(float x) { return __builtin_amdgcn_exp2f(x); }
__device__ __forceinline__ f32x4 mfma16(u32x4 a, u32x4 b, f32x4 c) {
  return __builtin_amdgcn_mfma_f32_16x16x32_bf16(__builtin_bit_cast(bf16x8, a), __builtin_bit_cast(bf16x8, b), c, 0, 0, 0);
}
__device__ __forceinline__ f32x16 mfma32(u32x4 a, u32x4 b, f32x16 c) {
  return __builtin_amdgcn_mfma_f32_32x32x16_bf16(__builtin_bit_cast(bf16x8, a), __builtin_bit_cast(bf16x8, b), c, 0, 0, 0);
}
__device__ __forceinline__ rsrc_t mkrs(const GAS char* p, unsigned bytes) {
  return __builtin_amdgcn_make_buffer_rsrc((void*)p, 0, bytes, 0x00020000);
}
__device__ __forceinline__ void bar() { asm volatile("s_waitcnt lgkmcnt(0)\n\ts_barrier" ::: "memory"); }
__device__ __forceinline__ void drain() { asm volatile("s_waitcnt vmcnt(0)" ::: "memory"); }
#define SB() __builtin_amdgcn_sched_barrier(0)
template <int N> __device__ __forceinline__ void vmwait() { asm volatile("s_waitcnt vmcnt(%0)" :: "n"(N) : "memory"); }
__device__ __forceinline__ unsigned swz(unsigned row) { return ((row & 3) << 2) | ((row >> 2) & 2); }
__device__ __forceinline__ u32x2 trread(LAS char* p) {
  return __builtin_bit_cast(u32x2, __builtin_amdgcn_ds_read_tr16_b64_v4i16((LAS s16x4*)p));
}

struct Tag { unsigned tl, th, lo, rb; u64 e; };
__device__ __forceinline__ Tag mktag(u64 e0) {
  Tag t;
  asm volatile("" : "+s"(e0));
  t.e = e0 * 6364136223846793005ull + 1442695040888963407ull;
  t.th = (unsigned)(t.e >> 32);
  t.lo = (unsigned)t.e;
  t.tl = t.lo & 0xff000000u;
  t.rb = ((t.lo >> 9) & 255u) | (~t.lo & 256u);
  return t;
}
__device__ __forceinline__ unsigned gpack(float v, const Tag& t) { return ((__float_as_uint(v) + 0x80u) >> 8) | t.tl; }
__device__ __forceinline__ u32x4 gpair(float a, float b, const Tag& t) { return u32x4{gpack(a, t), t.th, gpack(b, t), t.th}; }
__device__ __forceinline__ bool gok(u32x4 g, const Tag& t) {
  return ((((g[0] ^ t.tl) | (g[2] ^ t.tl)) >> 24) | (g[1] ^ t.th) | (g[3] ^ t.th)) == 0u;
}
__device__ __forceinline__ float gval(unsigned lo) { return __uint_as_float(lo << 8); }
__device__ __forceinline__ bool qok(u32x4 g, const Tag& t) { return ((g[1] ^ t.th) | (g[3] ^ t.th)) == 0u; }
__device__ __forceinline__ double mkd(unsigned lo, unsigned hi) { return __builtin_bit_cast(double, u32x2{lo, hi}); }
__device__ __forceinline__ u32x4 rpack(double a, double b, double l, float r, const Tag& t) {
  const u32x2 ua = __builtin_bit_cast(u32x2, a), ub = __builtin_bit_cast(u32x2, b), ul = __builtin_bit_cast(u32x2, l);
  const u64 ml = ((u64)(unsigned)__builtin_amdgcn_readfirstlane(ul[1]) << 32) | (unsigned)__builtin_amdgcn_readfirstlane(ul[0]);
  const u64 mr = (unsigned)__builtin_amdgcn_readfirstlane(__float_as_uint(r));
  const unsigned ta = t.lo & 511u, tb = t.rb;
  return u32x4{(ua[0] & ~1023u) | (__builtin_amdgcn_inverse_ballot_w64(ml) ? ta | 512u : ta), ua[1],
               (ub[0] & ~1023u) | (__builtin_amdgcn_inverse_ballot_w64(mr) ? tb | 512u : tb), ub[1]};
}
__device__ __forceinline__ u32x4 rpack0(f64x2 o, unsigned lw, int lane, unsigned ta, unsigned tb) {
  const u32x4 u = __builtin_bit_cast(u32x4, o);
  return u32x4{(u[0] & ~1023u) | (ta | (__builtin_amdgcn_ubfe(lw, lane, 1) << 9)), u[1], (u[2] & ~1023u) | tb, u[3]};
}
__device__ __forceinline__ bool rok(u32x4 g, const Tag& t) { return (((g[0] ^ t.lo) | (g[2] ^ t.rb)) & 511u) == 0u; }
constexpr unsigned PMASK = 0x00030003u;
__device__ __forceinline__ unsigned ptag(const Tag& t, int k) {
  const unsigned n = k == 0 ? t.lo : k == 3 ? t.lo >> 10 : ((t.lo >> (1 + 3 * k)) & 7u) | (~t.lo & 8u);
  return (n & 3u) | ((n & 12u) << 14);
}
__device__ __forceinline__ unsigned ltag(const Tag& t) { return ((t.lo >> 16) & 127u) | (~t.lo & 128u); }
__device__ __forceinline__ bool mok(u32x4 g, const Tag& t) { return (((g[0] ^ t.lo) & 0xffffu) | ((g[1] ^ ltag(t)) & 255u)) == 0u; }
__device__ __forceinline__ unsigned bfi(unsigned m, unsigned a, unsigned b) { return (a & m) | (b & ~m); }
__device__ __forceinline__ double rec_a(u32x4 g) { return mkd(g[0] & ~1023u, g[1]); }
__device__ __forceinline__ double rec_b(u32x4 g) { return mkd(g[2] & ~1023u, g[3]); }
__device__ __forceinline__ double rec_l(u32x4 g) { return __builtin_bit_cast(double, __builtin_amdgcn_ballot_w64((g[0] & 512u) != 0u)); }
__device__ __forceinline__ float rec_r(u32x4 g) { return __uint_as_float((unsigned)__builtin_amdgcn_ballot_w64((g[2] & 512u) != 0u)); }
__device__ __forceinline__ int edif(float a, float b) {
  int r;
  asm("v_cvt_i32_f32 %0, %1" : "=v"(r) : "v"(a - b));
  return r;
}
__device__ __forceinline__ unsigned p2hi(float a, float b) { return (unsigned)max(edif(a, b) + 1023, 0) << 20; }
__device__ __forceinline__ float bucket(float m) { return fmaf(__builtin_rintf(m * (1.f / 512.f)), 512.f, 0.f); }
__device__ __forceinline__ float rfl(float x) { return __uint_as_float(__builtin_amdgcn_readfirstlane(__float_as_uint(x))); }
__device__ __forceinline__ unsigned rdu(unsigned v, int l) { return __builtin_amdgcn_readlane(v, l); }

__device__ __forceinline__ void diag_gemv(const u32x4 (&wr)[16], const u32x4 (&xp)[2][2], const unsigned (&mask)[4], f32x4 (&acc)[2]) {
#pragma unroll
  for (int i = 0; i < 16; i++) {
#pragma unroll
    for (int s = 0; s < 2; s++) {
      const unsigned src = xp[s][i >> 3][(i >> 1) & 3];
      const unsigned dup = __builtin_amdgcn_perm(src, src, (i & 1) ? 0x03020302u : 0x01000100u);
      const u32x4 a = {dup & mask[0], dup & mask[1], dup & mask[2], dup & mask[3]};
      acc[s] = mfma16(a, wr[i], acc[s]);
    }
  }
}

constexpr int bitpos(char c) { for (int i = 0; i < 10; i++) if (PAT[i] == c) return 10 + i; return 31; }
constexpr int goff(int t) { int o = 0; for (int j = 0; j < 6; j++) o |= ((t >> j) & 1) << bitpos('0' + j); return o; }
struct Acc { double o[2]; double l; float R; };
__device__ __forceinline__ u32x4 ldnt(rsrc_t r, int voff, int so) { return __builtin_amdgcn_raw_buffer_load_b128(r, voff, so, AUX_NT); }
__device__ __forceinline__ u32x4 ldslot(rsrc_t r, int voff, int so, int i) { return ldnt(r, voff + goff(i & 3), so ^ goff(i & ~3)); }
__device__ __forceinline__ u32x4 ldunit(rsrc_t r, int voff, int so, int i) { return ldnt(r, voff + i * 1024, so); }
__device__ __forceinline__ u32x4 ldsc(rsrc_t r, int voff, int so) { return __builtin_amdgcn_raw_buffer_load_b128(r, voff, so, AUX_SC1); }
__device__ __forceinline__ void stsc(u32x4 v, rsrc_t r, int voff, int so) { __builtin_amdgcn_raw_buffer_store_b128(v, r, voff + so, 0, AUX_SC1); }
__device__ __forceinline__ float dot8(u32x4 k, u32x4 q) { return dot2(k[3], q[3], dot2(k[2], q[2], dot2(k[1], q[1], dot2(k[0], q[0], 0.f)))); }
#define TR_STEP(HALF, CTRL, BIT) _Pragma("unroll") for (int h = 0; h < (HALF); ++h) { \
    const float t1 = x[h] + dpp<CTRL>(x[h]), t2 = x[h + (HALF)] + dpp<CTRL>(x[h + (HALF)]); x[h] = (c & (BIT)) ? t2 : t1; }
template <int N> __device__ __forceinline__ float k_step(u32x4 (&ring)[16], u32x4 q, int c, rsrc_t rn, int voff, int so, bool ext, LAS char* xp) {
  float x[16];
#pragma unroll
  for (int i = 0; i < 16; i++) {
    x[i] = dot8(ring[i], q);
    asm volatile("s_waitcnt vmcnt(%1)" : "+v"(x[i]) : "n"(WTH - 1));
    if (i >= 16 - N && ext) ring[i] = *(volatile LAS u32x4*)(xp + (i - (16 - N)) * 1024);
    else ring[i] = ldslot(rn, voff, so, i);
    SB();
  }
  TR_STEP(8, XOR8, 8) TR_STEP(4, XOR7, 4) TR_STEP(2, XOR2, 2) TR_STEP(1, XOR1, 1)
  return rbf(rbf(x[0]) * SCALE);
}
template <int BC> __device__ __forceinline__ void pv(u32x4 v, unsigned pk, const unsigned (&mask)[4], f32x4& acc) {
  const unsigned d = dppu<0x150 + BC>(pk);
  acc = mfma16(u32x4{d & mask[0], d & mask[1], d & mask[2], d & mask[3]}, v, acc);
}
template <int I, int MODE> __device__ __forceinline__ void v_step(u32x4 (&ring)[16], unsigned pk, const unsigned (&mask)[4], f32x4& acc,
                                                                  rsrc_t rk, rsrc_t rv, int voff, int so) {
  pv<I>(ring[I], pk, mask, acc);
  if (MODE == 1 || (MODE == 2 && I < 8)) {
    asm volatile("s_waitcnt vmcnt(%1)" : "+v"(acc) : "n"(WTH - 1));
    ring[I] = MODE == 1 ? ldslot(rv, voff, so, I) : ldunit(I < 4 ? rk : rv, voff, so, I & 3);
  } else {
    asm volatile("" : "+v"(acc));
  }
  SB();
  if constexpr (I < 15) v_step<I + 1, MODE>(ring, pk, mask, acc, rk, rv, voff, so);
}
__device__ __forceinline__ float exp_ref(float mx, float& f) {
  const float M = __builtin_ceilf(mx * LOG2E);
  f = __builtin_amdgcn_fmed3f(fmaf(mx, LOG2E, -M), -1.f, 0.f);
  return M;
}
__device__ __forceinline__ unsigned lds_inc(LAS unsigned* p, int lane) {
  unsigned t = 0, z = 0;
  asm volatile("" : "+v"(z));
  if (lane == 0) t = __hip_atomic_fetch_add(p + z, 1u, __ATOMIC_RELAXED, __HIP_MEMORY_SCOPE_WORKGROUP);
  return t;
}
struct Take { unsigned unit, pend; bool fetch; };
__device__ __forceinline__ void lds_add(LAS unsigned* p, unsigned v) { __hip_atomic_fetch_add(p, v, __ATOMIC_RELAXED, __HIP_MEMORY_SCOPE_WORKGROUP); }
__device__ __forceinline__ Take take(LAS unsigned* ctl, unsigned kv, rsrc_t rws, int cntoff, int lane) {
  const unsigned k = __builtin_amdgcn_readfirstlane(kv);
  unsigned b, s;
  auto look = [&]() {
    const unsigned vs = ((volatile LAS unsigned*)ctl)[5], vb = ((volatile LAS unsigned*)ctl)[8 + (k >> 2)];
    s = __builtin_amdgcn_readfirstlane(vs); b = __builtin_amdgcn_readfirstlane(vb);
    return (b == ~0u) & ((s < 0x10000u) | ((s & 0xffffu) != 0u));
  };
  if (__builtin_expect(look(), 0))
    for (unsigned spins = 0; spins < SPIN_CAP * 16; spins++) {
      __builtin_amdgcn_s_sleep(1);
      if (!look()) break;
    }
  Take t;
  t.unit = min(b, (unsigned)NUNIT) + (k & 3);
  t.fetch = ((k & 3) == 0) & (b < NUNIT) & (s < 0x10000u);
  int off = OOB;
  if (t.fetch) {
    unsigned z = 0;
    asm volatile("" : "+v"(z));
    if (lane == 0) { lds_add(ctl + 5 + z, 1u); off = cntoff; }
    asm volatile("s_waitcnt lgkmcnt(0)" ::: "memory");
  }
  t.pend = __builtin_amdgcn_raw_ptr_buffer_atomic_add_i32(4, rws, off, 0, AUX_SC1);
  SB();
  return t;
}
__device__ __forceinline__ void give(LAS unsigned* ctl, const Take& t, int lane) {
  if (t.fetch && lane == 0) {
    unsigned z = 0;
    asm volatile("" : "+v"(z));
    if (t.pend < NUNIT) {
      const unsigned i = __hip_atomic_fetch_add(ctl + 4 + z, 1u, __ATOMIC_RELAXED, __HIP_MEMORY_SCOPE_WORKGROUP);
      ((volatile LAS unsigned*)ctl)[8 + i] = t.pend;
    }
    asm volatile("" ::: "memory");
    lds_add(ctl + 5 + z, t.pend >= NUNIT - 4 ? 0xffffu : ~0u);
  }
}
__device__ __forceinline__ int unit_off(unsigned t) { return (int)min(t, (unsigned)NUNIT) * 4096; }

__device__ __forceinline__ void b_units(u32x4 (&ra)[16], rsrc_t rk, rsrc_t rv, unsigned cur, u32x4 q, int lane, const unsigned (&mask)[4],
                                        LAS unsigned* ctl, rsrc_t rws, int cntoff, Acc& A) {
  const int c = lane & 15, voff = lane * 16;
  float Mr = -INFINITY;
  double s1 = 0.0;
  A.o[0] = A.o[1] = A.l = 0.0; A.R = cur < NUNIT ? -INFINITY : 0.f;
#pragma unroll 1
  while (cur < NUNIT) {
    const Take t = take(ctl, lds_inc(ctl, lane), rws, cntoff, lane);
    const int so = unit_off(t.unit);
    SB();
    float x[4];
#pragma unroll
    for (int i = 0; i < 4; i++) x[i] = dot8(ra[i], q);
    asm volatile("" : "+v"(x[0]), "+v"(x[1]), "+v"(x[2]), "+v"(x[3]));
    SB();
#pragma unroll
    for (int i = 0; i < 4; i++) {
      ra[i] = ldunit(rk, voff, so, i);
      SB();
    }
    TR_STEP(2, XOR8, 8) TR_STEP(1, XOR7, 4)
    x[0] += dpp<XOR2>(x[0]); x[0] += dpp<XOR1>(x[0]);
    const float s = rbf(rbf(x[0]) * SCALE);
    const float n = __builtin_rintf(s * LOG2E);
    float e = fexp2(__builtin_amdgcn_fmed3f(fmaf(s, LOG2E, -n), -1.f, 1.f)), d = n - Mr;
    asm volatile("" : "+v"(e));
    if (__builtin_expect(__any(d > 57.f), 0)) {
      float mx = fmaxf(n, dpp<XOR8>(n));
      mx = xrow_max(fmaxf(mx, dpp<ROR4>(mx)));
      const float Rn = rfl(fmaxf(A.R, bucket(mx)));
      const double s0 = mkd(0u, p2hi(A.R, Rn));
      A.o[0] *= s0; A.o[1] *= s0; A.l *= s0;
      A.R = Rn; Mr = mx; d = n - mx;
      s1 = mkd(0u, p2hi(mx, Rn));
    }
    const float p = __builtin_ldexpf(e, edif(d, 0.f));
    unsigned pk = cvtpk(p, p);
    asm volatile("" : "+v"(pk));
    SB();
    f32x4 acc = {0.f, 0.f, 0.f, 0.f};
    float ls = bf_lo(pk);
    pv<0>(ra[4], pk, mask, acc);  asm volatile("" : "+v"(acc)); ra[4] = ldunit(rv, voff, so, 0); ls += dpp<XOR8>(ls); asm volatile("" : "+v"(ls)); SB();
    pv<4>(ra[5], pk, mask, acc);  asm volatile("" : "+v"(acc)); ra[5] = ldunit(rv, voff, so, 1); ls += dpp<ROR4>(ls); asm volatile("" : "+v"(ls)); SB();
    pv<8>(ra[6], pk, mask, acc);  asm volatile("" : "+v"(acc)); ra[6] = ldunit(rv, voff, so, 2); SB();
    pv<12>(ra[7], pk, mask, acc); asm volatile("" : "+v"(acc)); ra[7] = ldunit(rv, voff, so, 3); SB();
    A.l = __builtin_fma((double)xrow_sum(ls), s1, A.l);
    asm volatile("" : "+v"(A.l));
    A.o[0] = __builtin_fma((double)(lane < 32 ? acc[0] : acc[2]), s1, A.o[0]);
    A.o[1] = __builtin_fma((double)(lane < 32 ? acc[1] : acc[3]), s1, A.o[1]);
    asm volatile("" : "+v"(A.o[0]), "+v"(A.o[1]));
    SB();
    cur = t.unit;
    if (cur >= NUNIT) break;
    give(ctl, t, lane);
  }
}
#undef TR_STEP
__device__ __forceinline__ int rec_col(int lane) { return 8 * (lane & 15) + 4 * ((lane >> 4) & 1) + 2 * (lane >> 5); }
struct Mrg { unsigned s0, s4, s8, ta, tb; int off; };
__device__ __forceinline__ Mrg mkmrg(LAS char* sum, int lane, int roff, const Tag& tg) {
  Mrg m = {(unsigned)(uintptr_t)sum, (unsigned)(uintptr_t)sum + (lane >> 5) * 4, (unsigned)(uintptr_t)sum + lane * 8, tg.lo & 511u, tg.rb, roff + lane * 16};
  asm volatile("" : "+v"(m.s0), "+v"(m.s4), "+v"(m.s8), "+v"(m.ta), "+v"(m.tb), "+v"(m.off));
  return m;
}
__device__ __forceinline__ void b_merge(const Mrg& m, LAS char* mrg, int w, int lane, const Acc& A, rsrc_t rws, u64 e0) {
  unsigned inc = 1;
  double a = A.o[0], b = A.o[1], l = A.l;
  if (__builtin_expect(__float_as_uint(A.R) != 0u, 0)) {
    *(volatile LAS f64x2*)(mrg + w * 1024 + lane * 16) = f64x2{a, b};
    *(volatile LAS double*)(mrg + 8192 + w * 8) = l;
    *(volatile LAS float*)(mrg + 8256 + w * 4) = A.R;
    a = b = l = 0.0;
    inc = 1u + (256u << w);
  }
  f64x2 o;
  unsigned n, lw;
  asm volatile("ds_add_f64 %3, %5\n\tds_add_f64 %3, %6 offset:512\n\ts_mov_b64 exec, 1\n\tds_add_f64 %3, %7 offset:1024\n\tds_add_u32 %3, %8 offset:1032\n\t"
               "s_mov_b64 exec, -1\n\tds_read_b32 %1, %9 offset:1032\n\tds_read_b32 %2, %4 offset:1024\n\tds_read2st64_b64 %0, %3 offset1:1\n\ts_waitcnt lgkmcnt(0)"
               : "=&v"(o), "=&v"(n), "=&v"(lw)
               : "v"(m.s8), "v"(m.s4), "v"(a), "v"(b), "v"(l), "v"(inc), "v"(m.s0)
               : "memory");
  stsc(rpack0(o, lw, lane, m.ta, m.tb), rws, n == 8u ? m.off : OOB, 0);
  SB();
  n = __builtin_amdgcn_readfirstlane(n);
  if (__builtin_expect(n > 8u && (n & 255u) == 8u, 0)) {
    double L = *(volatile LAS double*)(uintptr_t)(m.s0 + 1024);
    float R = L != 0.0 ? 0.f : -INFINITY;
#pragma unroll 1
    for (int i = 0; i < 8; i++)
      if (n >> (8 + i) & 1) {
        const f64x2 oi = *(volatile LAS f64x2*)(mrg + i * 1024 + lane * 16);
        const float Ri = *(volatile LAS float*)(mrg + 8256 + i * 4), Rn = fmaxf(R, Ri);
        const double s0 = mkd(0u, p2hi(R, Rn)), s1 = mkd(0u, p2hi(Ri, Rn));
        o[0] = __builtin_fma(oi[0], s1, o[0] * s0); o[1] = __builtin_fma(oi[1], s1, o[1] * s0);
        L = __builtin_fma(*(volatile LAS double*)(mrg + 8192 + i * 8), s1, L * s0);
        R = Rn;
      }
    stsc(rpack(o[0], o[1], L, R, mktag(e0)), rws, m.off, 0);
  }
}

__device__ __forceinline__ unsigned fz(unsigned row) { return ((row & 2) << 1) | ((row >> 1) & 2); }
__device__ __forceinline__ void dma1(rsrc_t rs, int voff, int so, unsigned ldsaddr) {
  unsigned keep;
  asm volatile("s_mov_b32 %0, m0\n\ts_mov_b32 m0, %1\n\ts_nop 0\n\tbuffer_load_dwordx4 %2, %3, %4 offen nt lds\n\ts_mov_b32 m0, %0"
               : "=&s"(keep) : "s"(ldsaddr), "v"(voff), "s"(rs), "s"(so) : "memory");
}
template <int NR>
__device__ __forceinline__ void dma_rows(rsrc_t rs, int srcoff, LAS char* img, int r0, int lane) {
#pragma unroll
  for (int u = 0; u < NR; u++) {
    const int r = r0 + u;
    unsigned keep;
    asm volatile("s_mov_b32 %0, m0\n\ts_mov_b32 m0, %1\n\ts_nop 0\n\tbuffer_load_dwordx4 %2, %3, %4 offen nt lds\n\ts_mov_b32 m0, %0"
                 : "=&s"(keep) : "s"((unsigned)(uintptr_t)img + r * 1024), "v"((lane ^ swz(r)) * 16), "s"(rs), "s"(srcoff + r * 1024) : "memory");
  }
  SB();
}

__device__ __forceinline__ void wo_frags(LAS char* img, int w, int lane, u32x4 (&wf)[4][4]) {
  const int g = lane >> 4, q = (lane & 15) >> 2, p = lane & 3;
#pragma unroll
  for (int ks = 0; ks < 4; ks++)
#pragma unroll
    for (int tl = 0; tl < 4; tl++) {
      const int ch = 8 * w + 2 * tl + (p >> 1);
      const int r0 = 32 * ks + 8 * g + q, r1 = r0 + 4;
      const u32x2 a = trread(img + r0 * 1024 + ((ch ^ swz(r0)) * 16) + 8 * (p & 1));
      const u32x2 b = trread(img + r1 * 1024 + ((ch ^ swz(r1)) * 16) + 8 * (p & 1));
      wf[tl][ks] = u32x4{a[0], a[1], b[0], b[1]};
    }
}
__device__ __forceinline__ f32x4 wo_calc(const u32x4 (&wf)[4][4], LAS char* cb, int lane) {
  const int n = lane & 15, grp = lane >> 4;
  u32x4 bf[4];
#pragma unroll
  for (int ks = 0; ks < 4; ks++) bf[ks] = *(LAS u32x4*)(cb + (n & 3) * 256 + ks * 64 + grp * 16);
  f32x4 acc[4];
#pragma unroll
  for (int tl = 0; tl < 4; tl++) acc[tl] = f32x4{0.f, 0.f, 0.f, 0.f};
#pragma unroll
  for (int ks = 0; ks < 4; ks++)
#pragma unroll
    for (int tl = 0; tl < 4; tl++) acc[tl] = mfma16(wf[tl][ks], bf[ks], acc[tl]);
  const f32x4 lo = (n & 4) ? acc[1] : acc[0], hi = (n & 4) ? acc[3] : acc[2];
  return (n & 8) ? hi : lo;
}
__device__ __forceinline__ void y_store(f32x4 y, int w, int lane, int slot, rsrc_t rws, int yoff, const Tag& tg) {
  const int off = yoff + (((lane & 3) * 8 + w) * 32 + slot) * 512 + ((lane & 12) + (lane >> 4)) * 16;
  stsc(gpair(y[0], y[1], tg), rws, off, 0);
  stsc(gpair(y[2], y[3], tg), rws, off, 256);
}
template <int K, int N> __device__ __forceinline__ unsigned bc1(const unsigned (&p)[N]) {
  unsigned v = dppu<0x150 + K>(p[0]);
  if constexpr (N > 1) {
    v = (unsigned)__builtin_amdgcn_update_dpp((int)v, (int)p[1], 0x150 + K, 0xf, 2, false);
    v = (unsigned)__builtin_amdgcn_update_dpp((int)v, (int)p[2], 0x150 + K, 0xf, 4, false);
  }
  return v;
}
template <int K, int N> __device__ __forceinline__ u32x4 bc4(const unsigned (&p)[N]) { return u32x4{bc1<K>(p), bc1<K + 1>(p), bc1<K + 2>(p), bc1<K + 3>(p)}; }
template <int N> __device__ __forceinline__ f32x4 proj(const unsigned (&pr)[N], const u32x4 (&wf)[4][4], int lane) {
  const u32x4 bf[4] = {bc4<0>(pr), bc4<4>(pr), bc4<8>(pr), bc4<12>(pr)};
  f32x4 acc[4];
#pragma unroll
  for (int tl = 0; tl < 4; tl++) acc[tl] = f32x4{0.f, 0.f, 0.f, 0.f};
#pragma unroll
  for (int ks = 0; ks < 4; ks++)
#pragma unroll
    for (int tl = 0; tl < 4; tl++) acc[tl] = mfma16(wf[tl][ks], bf[ks], acc[tl]);
  const f32x4 lo = (lane & 1) ? acc[1] : acc[0], hi = (lane & 1) ? acc[3] : acc[2];
  f32x4 r = (lane & 2) ? hi : lo;
  asm volatile("" : "+v"(r));
  return r;
}
__device__ __forceinline__ unsigned arrive(LAS unsigned* p, int lane) {
  unsigned z = 0;
  asm volatile("" : "+v"(z) :: "memory");
  if (lane == 0) lds_add(p + z, 1u);
  asm volatile("" ::: "memory");
  return *(volatile LAS unsigned*)p;
}
template <int N> __device__ __forceinline__ f32x2 rows_sum(LAS char* p) {
  f32x2 v[N];
#pragma unroll
  for (int i = 0; i < N; i++) v[i] = *(volatile LAS f32x2*)(p + i * 256);
#pragma unroll
  for (int st = 1; st < N; st *= 2)
#pragma unroll
    for (int i = 0; i + st < N; i += 2 * st) v[i] += v[i + st];
  return v[0];
}
__device__ __forceinline__ float half_sum(float x) {
  auto t = __builtin_amdgcn_permlane32_swap(__float_as_uint(x), __float_as_uint(x), false, false);
  return __uint_as_float(t[0]) + __uint_as_float(t[1]);
}

__global__ __launch_bounds__(512, 2) void k_all(const char* a0_, const char* a1_, const char* a2_, const char* a3_, const char* a4_, const char* a5_,
                                                const char* a6_, const char* a7_, const char* a8_, const char* a9_, const char* a10_, char* a11_, unsigned* a12_) {
  u32x16 ka; u32x8 kb_; u32x2 kc_;
  asm volatile("s_load_dwordx16 %0, %3, 0x0\n\ts_load_dwordx8 %1, %3, 0x40\n\ts_load_dwordx2 %2, %3, 0x60"
               : "=&s"(ka), "=&s"(kb_), "=&s"(kc_) : "s"(__builtin_amdgcn_kernarg_segment_ptr()));
  __shared__ __attribute__((aligned(16))) char smem[LDS_BYTES];
  LAS char* lds = (LAS char*)smem;
  LAS char* scr = lds + LDS_SCR;
  LAS unsigned* ctl = (LAS unsigned*)scr;
  const int tid = threadIdx.x, lane = tid & 63, w = __builtin_amdgcn_readfirstlane(tid >> 6);
  const int blk = blockIdx.x, head = blk >> 3, prod = (blk + head) & 7;
  const int sub = (0x54372106u >> (4 * prod)) & 7;
  const unsigned tst = (unsigned)__builtin_amdgcn_s_memrealtime();
  unsigned cpf = 0;
  {
    u64 pc;
    asm volatile("s_getpc_b64 %0" : "=s"(pc));
    cpf = __builtin_amdgcn_raw_buffer_load_b32(mkrs((const GAS char*)(pc & ~255ull), CPF), w == 7 ? head * 2048 + lane * 32 : OOB, 0, 0);
    SB();
  }
  {
    unsigned c = 16 + 4 * (DCH * sub + tid);
    asm volatile("" : "+v"(c));
    c = tid < 8 + DCH ? c : ~0u;
    c = tid < 8 ? 0u : c;
    ctl[tid] = tid == 4 ? DCH : c;
    *(LAS u64*)(scr + SCR_SUM + min(tid, 2 * SUM_STR / 8 - 1) * 8) = 0;
  }
  const int l16 = lane & 15, grp = lane >> 4;
  const int b4 = head & 3, cntoff = WS_CNT + head * CSTR;
  const int bm = (b4 + 1 + (sub >= 3 ? sub - 3 : sub)) & 3;
  const size_t base = (size_t)(bm * 32 + head) << 20;
  const int so = ((w & 1) << bitpos('a')) | (((w >> 1) & 1) << bitpos('b')) | ((w >> 2) << bitpos('c')) | (sub >= 3 ? 1 << bitpos('p') : 0);
  int v0 = l16 * 16 + grp * (sub < 6 ? 256 : 131072) + (sub < 6 ? so : w * 524288 + head * 256);
  unsigned boff = sub < 6 ? (unsigned)base : 0u, bsz = sub < 6 ? 1u << 20 : 4u << 20;
  int s0[WTH];
#pragma unroll
  for (int i = 0; i < WTH; i++) {
    s0[i] = sub < 6 ? goff(i) : i * 8192;
    asm volatile("" : "+s"(s0[i]));
  }
  asm volatile("" : "+v"(v0), "+s"(boff), "+s"(bsz));
  SB();
  asm volatile("s_waitcnt lgkmcnt(0)" : "+s"(ka), "+s"(kb_), "+s"(kc_));
  auto ptr = [](unsigned lo, unsigned hi) { return (GAS char*)(((u64)hi << 32) | lo); };
  GAS char *const xA = ptr(ka[0], ka[1]), *const ckv = ptr(ka[2], ka[3]), *const wqA = ptr(ka[4], ka[5]), *const wuk = ptr(ka[6], ka[7]),
           *const wuv = ptr(ka[8], ka[9]), *const woA = ptr(ka[10], ka[11]), *const xB = ptr(ka[12], ka[13]), *const kc = ptr(ka[14], ka[15]),
           *const vc = ptr(kb_[0], kb_[1]), *const wqB = ptr(kb_[2], kb_[3]), *const woB = ptr(kb_[4], kb_[5]), *const ws = ptr(kb_[6], kb_[7]),
           *const out = ptr(kc_[0], kc_[1]);
  const u64 e0 = *(const __attribute__((address_space(4))) u64*)(u64)(ws + WS_EPOCH);
  const rsrc_t r0 = mkrs((sub < 6 ? kc : sub == 6 ? wqB : wqA) + boff, bsz);
  SB();
  u32x4 ra[16];
#pragma unroll
  for (int i = 0; i < WTH; i++) ra[i] = ldnt(r0, v0, s0[i]);
  SB();
  u32x4 xp[2][2];
  if (sub >= 6) {
    const GAS char* x = sub == 6 ? xB : xA;
#pragma unroll
    for (int s = 0; s < 2; s++) {
      const GAS u32x4* xs = (const GAS u32x4*)(x + (2 * s + (l16 >> 3)) * 1024 + (64 * w + 16 * grp) * 2);
      xp[s][0] = xs[0]; xp[s][1] = xs[1];
    }
#pragma unroll
    for (int i = WTH; i < 16; i++) ra[i] = ldnt(r0, v0, i * 8192);
    asm volatile("" ::: "memory");
    SB();
  }
  const rsrc_t rws = mkrs(ws, WS_END);
  __builtin_amdgcn_raw_buffer_store_b32(tst, mkrs(ws + (8 << 20), 4096), tid == 0 ? blk * 4 : OOB, 0, AUX_SC1);
  unsigned mask[4];
#pragma unroll
  for (int v = 0; v < 4; v++) mask[v] = (((lane & 7) >> 1) == v) ? ((lane & 1) ? 0xffff0000u : 0x0000ffffu) : 0u;
  const size_t base4 = (size_t)(b4 * 32 + head) << 20;
  const rsrc_t rk4 = mkrs(kc + base4, 1u << 20), rv4 = mkrs(vc + base4, 1u << 20);
  const int voff = lane * 16;
  LAS char* qst = scr + 1024 + w * 512;
  const int qo = WS_QB + head * 2048 + (lane < 32 ? bm : b4) * 512 + (lane & 31) * 16;
  auto qwait = [&](u32x4 g, const Tag& tg) {
    if (!__all(qok(g, tg))) {
      for (unsigned spins = 0;; spins++) {
        asm volatile("" ::: "memory");
        g = ldsc(rws, qo, 0);
        if (__all(qok(g, tg)) || spins > SPIN_CAP) break;
        __builtin_amdgcn_s_sleep(2);
      }
    }
    *(LAS u32x2*)(qst + lane * 8) = u32x2{g[0], g[2]};
  };
  unsigned cur;

  if (__builtin_expect(sub < 6, 1)) {
    const rsrc_t rv = mkrs(vc + base, 1u << 20);
    u32x4 rb[16], g;
#pragma unroll
    for (int i = WTH; i < 32 + NXA + NXB; i++) {
      if (i == WTH) asm volatile("s_barrier" ::: "memory");
      vmwait<WTH - 1>();
      if (i == 32 + NXA + NXB - WTH) asm volatile("buffer_load_dwordx4 %0, %1, %2, 0 offen sc1" : "=&v"(g) : "v"(qo), "s"(rws));
      if (i < 32) {
        const u32x4 v = ldslot(r0, voff, so, i);
        if (i < 16) ra[i] = v; else rb[i - 16] = v;
      } else {
        const int j = i < 32 + NXA ? i + 16 - NXA : i + 32 - NXA - NXB;
        dma1(r0, voff + goff(j & 3), so ^ goff(j & ~3), (unsigned)(uintptr_t)lds + w * 16384 + (i - 32) * 1024);
      }
      SB();
    }
    asm volatile("s_waitcnt vmcnt(%1)" : "+v"(g) : "n"(WTH), "v"(cpf));
    qwait(g, mktag(e0));
    const u32x4 q = *(LAS u32x4*)(qst + l16 * 16);
    float sc[4] = {0.f, 0.f, 0.f, 0.f};
#pragma unroll 1
    for (int it = 0; it < 2; it++) {
      const rsrc_t rn = it ? rv : r0;
      sc[2] = sc[0]; sc[3] = sc[1];
      sc[1] = k_step<NXA>(ra, q, l16, rn, voff, so ^ (it ? goff(48) : goff(32)), it == 0, lds + w * 16384 + voff);
      sc[0] = k_step<NXB>(rb, q, l16, rn, voff, so ^ (it ? goff(32) : goff(48)), it == 0, lds + w * 16384 + NXA * 1024 + voff);
    }
    const float mx = xrow_max(row_max(fmaxf(fmaxf(sc[0], sc[1]), fmaxf(sc[2], sc[3]))));
    float f, l = 0.f;
    const float M = exp_ref(mx, f);
    unsigned pk[4];
#pragma unroll
    for (int k = 0; k < 4; k++) {
      const float p = fexp2(fmaf(sc[k] - mx, LOG2E, f));
      pk[k] = cvtpk(p, p);
      l += bf_lo(pk[k]);
    }
    l = xrow_sum(row_sum(l));
    cur = 8 * sub + w;
    f32x4 acc = {0.f, 0.f, 0.f, 0.f};
    v_step<0, 1>(ra, pk[0], mask, acc, r0, rv, voff, so ^ goff(16));
    v_step<0, 1>(rb, pk[1], mask, acc, r0, rv, voff, so);
    v_step<0, 2>(ra, pk[2], mask, acc, rk4, rv4, voff, cur * 4096);
    v_step<0, 0>(rb, pk[3], mask, acc, r0, rv, voff, 0);
    Acc A;
    A.R = rfl(bucket(M));
    const double sm = mkd(0u, p2hi(M, A.R));
    A.o[0] = (lane < 32 ? acc[0] : acc[2]) * sm; A.o[1] = (lane < 32 ? acc[1] : acc[3]) * sm;
    A.l = l * sm;
    b_merge(mkmrg(scr + SCR_SUM, lane, WS_RS + (head * 16 + sub) * 1024, mktag(e0)), scr + 8192, w, lane, A, rws, e0);
  } else {
  {
    __builtin_amdgcn_raw_buffer_store_b32(48 + 24 * DCH, rws, (sub == 6 && tid == 0) ? cntoff : OOB, 0, AUX_SC1);
    {
      const rsrc_t rk = mkrs(wuk, sub == 7 ? 4u << 20 : 0u);
#pragma unroll
      for (int i = 0; i < 16; i++)
        __builtin_amdgcn_raw_ptr_buffer_load_lds(rk, (LAS void*)(lds + w * 16384 + i * 1024), 16,
                                                 (64 * w + grp) * 8192 + head * 256 + ((l16 ^ (4 * (i & 3) + grp)) * 16), i * 4 * 8192, 0, AUX_NT);
    }
    SB();
    f32x4 acc[2] = {{0.f, 0.f, 0.f, 0.f}, {0.f, 0.f, 0.f, 0.f}};
    diag_gemv(ra, xp, mask, acc);
    LAS float* red = (LAS float*)(scr + 8192);
#pragma unroll
    for (int s = 0; s < 2; s++) *(LAS f32x4*)(red + w * 512 + (2 * s + (grp >> 1)) * 128 + 8 * l16 + 4 * (grp & 1)) = acc[s];
    asm volatile("" :: "v"(cpf));
    __builtin_amdgcn_s_waitcnt(0x0f70);
    drain();
    bar();
    if (tid < 128) {
      f32x4 a = {0.f, 0.f, 0.f, 0.f};
#pragma unroll
      for (int k = 0; k < 8; k++) a += *(LAS f32x4*)(red + k * 512 + 4 * tid);
      const unsigned p0 = cvtpk(a[0], a[1]), p1 = cvtpk(a[2], a[3]);
      const Tag tg = mktag(e0);
      stsc(u32x4{p0, tg.th, p1, tg.th}, rws, sub == 6 ? WS_QB + head * 2048 + tid * 16 : OOB, 0);
      *(LAS u32x2*)(scr + 1024 + tid * 8) = u32x2{p0, p1};
    }
  }

  if (sub == 6) {
    const int aid = ((head & 6) << 2) | ((head & 1) << 2) | (head >> 3), b = aid >> 3;
    const rsrc_t rc = mkrs(ckv + (size_t)aid * 524288, 524288);
    const int dvo = (lane >> 3) * 1024 + w * 128 + (((lane & 7) ^ fz(lane >> 3)) * 16);
    const unsigned dlo = (unsigned)(uintptr_t)lds + w * 8192;
    const int xt = (aid & 7) << 16;
#pragma unroll
    for (int j = 0; j < 16; j++) dma1(rc, dvo, (j * 8192) ^ xt, dlo + (j >> 3) * 65536 + (j & 7) * 1024);
    SB();
    const Tag tg = mktag(e0);
    bar();
    {
      u32x4 t[8];
      unsigned miss = 15;
      __builtin_amdgcn_s_sleep(90);
      for (unsigned spins = 0;; spins++) {
        asm volatile("" ::: "memory");
#pragma unroll
        for (int i = 0; i < 4; i++)
          if (miss >> i & 1) {
            t[2 * i] = ldsc(rws, WS_QABS + (b * 32 + w) * 2048 + lane * 16, i * 16384);
            t[2 * i + 1] = ldsc(rws, WS_QABS + (b * 32 + w) * 2048 + lane * 16 + 1024, i * 16384);
          }
        SB();
#pragma unroll
        for (int i = 0; i < 4; i++) if (__all(qok(t[2 * i], tg) & qok(t[2 * i + 1], tg))) miss &= ~(1u << i);
        if (!miss || spins > SPIN_CAP) break;
        __builtin_amdgcn_s_sleep(4);
      }
#pragma unroll
      for (int i = 0; i < 8; i++) {
        const int row = 8 * (i >> 1) + w;
        *(LAS u32x2*)(scr + row * 1024 + (((32 * (i & 1) + (lane >> 1)) ^ swz(row)) * 16) + (lane & 1) * 8) = u32x2{t[i][0], t[i][2]};
      }
    }
    bar();
    const int kb = w & 3, hb = w >> 2;
    u32x4 qf[16];
    {
      const int row = 16 * hb + l16;
#pragma unroll
      for (int ks = 0; ks < 16; ks++) qf[ks] = *(LAS u32x4*)(scr + row * 1024 + (((4 * ks + grp) ^ swz(row)) * 16));
    }
    LAS float* mx = (LAS float*)scr;
    LAS char* pl = scr + 1024;
    LAS float* lr = (LAS float*)(scr + 8192);
    f32x16 o0, o1;
#pragma unroll
    for (int j = 0; j < 16; j++) { o0[j] = 0.f; o1[j] = 0.f; }
    float m1 = -INFINITY, m2 = -INFINITY, l1 = 0.f;
    const int h1 = 16 * hb + l16, h2 = lane & 31, hh = lane >> 5;
    const int arow = 16 * kb + l16;
    const int tq = (lane & 15) >> 2, tp = lane & 3, tblk = (lane >> 4) & 1;
    const int qa0 = arow * 128 + ((grp ^ fz(arow)) * 16), qa1 = qa0 ^ 64;
    const int pr = 8 * hh + tq, pc = 2 * tblk + (tp >> 1);
    const int pa0 = w * 8192 + pr * 128 + ((pc ^ fz(pr)) * 16) + 8 * (tp & 1), pa1 = w * 8192 + (pr + 4) * 128 + ((pc ^ fz(pr + 4)) * 16) + 8 * (tp & 1);
#pragma unroll 1
    for (int t = 0; t < 8; t++) {
      if (t < 7) asm volatile("s_waitcnt vmcnt(8)" ::: "memory"); else asm volatile("s_waitcnt vmcnt(0)" ::: "memory");
      bar();
      LAS char* img = lds + (t & 1) * 65536;
      f32x4 s0 = {0.f, 0.f, 0.f, 0.f}, s1 = s0;
      u32x4 ka[16];
      u32x2 ta[4][4];
      const int nso = t < 6 ? ((t + 2) * 65536) ^ xt : OOB;
#pragma unroll
      for (int ks = 0; ks < 16; ks++) ka[ks] = *(LAS u32x4*)(img + ((ks & 1) ? qa1 : qa0) + (ks >> 1) * 8192);
      SB();
#pragma unroll
      for (int k2 = 0; k2 < 8; k2++) {
        s0 = mfma16(ka[2 * k2], qf[2 * k2], s0);
        s1 = mfma16(ka[2 * k2 + 1], qf[2 * k2 + 1], s1);
      }
      asm volatile("" : "+v"(s0), "+v"(s1));
      SB();
#pragma unroll
      for (int ks = 0; ks < 4; ks++) {
        ta[ks][0] = trread(img + pa0 + ks * 2048); ta[ks][1] = trread(img + pa1 + ks * 2048);
        ta[ks][2] = trread(img + (pa0 ^ 64) + ks * 2048); ta[ks][3] = trread(img + (pa1 ^ 64) + ks * 2048);
      }
      SB();
      float x[4];
#pragma unroll
      for (int j = 0; j < 4; j++) x[j] = rbf(rbf(s0[j] + s1[j]) * SCALE);
      const float lm = xrow_max(fmaxf(fmaxf(x[0], x[1]), fmaxf(x[2], x[3])));
      if (grp == 0) mx[h1 * 4 + kb] = lm;
      LAS u32x2* pw = (LAS u32x2*)(pl + h1 * 144 + (16 * kb + 4 * grp) * 2);
      u32x2 pp = {cvtpk(fexp2((x[0] - m1) * LOG2E), fexp2((x[1] - m1) * LOG2E)), cvtpk(fexp2((x[2] - m1) * LOG2E), fexp2((x[3] - m1) * LOG2E))};
      *pw = pp;
      bar();
      const f32x4 ma = *(LAS f32x4*)(mx + h1 * 4), mb = *(LAS f32x4*)(mx + h2 * 4);
      u32x4 pb[4];
#pragma unroll
      for (int ks = 0; ks < 4; ks++) pb[ks] = *(LAS u32x4*)(pl + h2 * 144 + (16 * ks + 8 * hh) * 2);
      const float n1 = fmaxf(m1, fmaxf(fmaxf(ma[0], ma[1]), fmaxf(ma[2], ma[3])));
      const float n2 = fmaxf(m2, fmaxf(fmaxf(mb[0], mb[1]), fmaxf(mb[2], mb[3])));
      float al1 = fexp2((m1 - n1) * LOG2E), al2 = fexp2((m2 - n2) * LOG2E);
      asm volatile("" : "+v"(pb[0]), "+v"(pb[1]), "+v"(pb[2]), "+v"(pb[3]));
      if (__any(n2 - m2 > 40.f)) {
        bar();
        pp = u32x2{cvtpk(fexp2((x[0] - n1) * LOG2E), fexp2((x[1] - n1) * LOG2E)), cvtpk(fexp2((x[2] - n1) * LOG2E), fexp2((x[3] - n1) * LOG2E))};
        *pw = pp;
        l1 *= al1;
#pragma unroll
        for (int j = 0; j < 16; j++) { o0[j] *= al2; o1[j] *= al2; }
        al1 = al2 = 1.f;
        bar();
#pragma unroll
        for (int ks = 0; ks < 4; ks++) pb[ks] = *(LAS u32x4*)(pl + h2 * 144 + (16 * ks + 8 * hh) * 2);
      }
      m1 = n1; m2 = n2;
#pragma unroll
      for (int ks = 0; ks < 4; ks++) {
        o0 = mfma32(u32x4{ta[ks][0][0], ta[ks][0][1], ta[ks][1][0], ta[ks][1][1]}, pb[ks], o0);
        o1 = mfma32(u32x4{ta[ks][2][0], ta[ks][2][1], ta[ks][3][0], ta[ks][3][1]}, pb[ks], o1);
        SB();
        if (t < 6) {
          dma1(rc, dvo, nso + 2 * ks * 8192, dlo + (t & 1) * 65536 + 2 * ks * 1024);
          dma1(rc, dvo, nso + (2 * ks + 1) * 8192, dlo + (t & 1) * 65536 + (2 * ks + 1) * 1024);
        }
        SB();
      }
      l1 = (l1 + ((bf_lo(pp[0]) + bf_hi(pp[0])) + (bf_lo(pp[1]) + bf_hi(pp[1])))) * al1;
#pragma unroll
      for (int j = 0; j < 16; j++) { o0[j] *= al2; o1[j] *= al2; }
    }
    bar();
    {
      float am = 0.f;
#pragma unroll
      for (int j = 0; j < 16; j++) am = fmaxf(am, fmaxf(fabsf(o0[j]), fabsf(o1[j])));
      auto sw = __builtin_amdgcn_permlane32_swap(__float_as_uint(am), __float_as_uint(am), false, false);
      am = fmaxf(__uint_as_float(sw[0]), __uint_as_float(sw[1]));
      const int e = max(__builtin_amdgcn_frexp_expf(am), -127);
      LAS char* tp = lds + h2 * 1040 + (64 * w + 4 * hh) * 2;
      auto pkn = [&](float a, float b) { return __builtin_bit_cast(unsigned, __builtin_amdgcn_cvt_pknorm_i16(__builtin_ldexpf(a, -e), __builtin_ldexpf(b, -e))); };
#pragma unroll
      for (int j4 = 0; j4 < 4; j4++) {
        *(LAS u32x2*)(tp + j4 * 16) = u32x2{pkn(o0[4 * j4], o0[4 * j4 + 1]), pkn(o0[4 * j4 + 2], o0[4 * j4 + 3])};
        *(LAS u32x2*)(tp + 64 + j4 * 16) = u32x2{pkn(o1[4 * j4], o1[4 * j4 + 1]), pkn(o1[4 * j4 + 2], o1[4 * j4 + 3])};
      }
      if (hh == 0) *(LAS unsigned char*)(scr + 9216 + h2 * 8 + w) = (unsigned char)(e + 127);
      const float ls = xrow_sum(l1);
      if (grp == 0) lr[kb * 32 + h1] = ls;
      bar();
#pragma unroll
      for (int i = 0; i < 4; i++) {
        u32x4 v = *(LAS u32x4*)(lds + (4 * w + i) * 1040 + lane * 16);
#pragma unroll
        for (int k = 0; k < 4; k++) v[k] = bfi(PMASK, ptag(tg, k), v[k]);
        stsc(v, rws, WS_PO + (aid * 32 + 4 * w) * 1024 + lane * 16, i * 1024);
      }
      if (tid < 32) {
        const u32x2 eb = *(LAS u32x2*)(scr + 9216 + tid * 8);
        const float l = (lr[tid] + lr[32 + tid]) + (lr[64 + tid] + lr[96 + tid]);
        stsc(u32x4{bfi(0xffffu, tg.lo, __float_as_uint(m2)), bfi(255u, ltag(tg), __float_as_uint(l)), eb[0], eb[1]}, rws, WS_PML + (aid * 32 + tid) * 16, 0);
      }
      if (w == 0) {
        const unsigned t = __builtin_amdgcn_readfirstlane(__builtin_amdgcn_raw_ptr_buffer_atomic_add_i32(8 + 4 * DCH, rws, lane == 0 ? cntoff : OOB, 0, AUX_SC1));
        const unsigned bs = t + 8 + 4 * (lane - 8);
        ctl[lane] = lane == 3 ? t : lane == 4 ? DCH : lane == 5 ? (t + 8 + 4 * DCH >= NUNIT ? 0x10000u : 0u) : lane < 8 ? 0u : (lane < 8 + DCH && bs < NUNIT) ? bs : ~0u;
        if (lane < 32) ctl[64 + lane] = ~0u;
      }
      *(LAS u64*)(scr + SCR_SUM + SUM_STR + min(tid, SUM_STR / 8 - 1) * 8) = 0;
      bar();
    }
    const u32x4 g = ldsc(rws, qo, 0);
    cur = __builtin_amdgcn_readfirstlane(ctl[3]) + w;
#pragma unroll
    for (int i = 0; i < 8; i++) ra[i] = ldunit(i < 4 ? rk4 : rv4, voff, unit_off(cur), i & 3);
    SB();
    qwait(g, mktag(e0));
  } else {
    const Tag tg = mktag(e0);
    bar();
    {
      u32x4 bf[4];
#pragma unroll
      for (int ks = 0; ks < 4; ks++) bf[ks] = *(LAS u32x4*)(scr + 1024 + (l16 & 3) * 256 + ks * 64 + grp * 16);
      LAS char* tt = scr + 2048 + w * 512;
#pragma unroll
      for (int rt = 0; rt < 4; rt++) {
        f32x4 acc = {0.f, 0.f, 0.f, 0.f};
#pragma unroll
        for (int ks = 0; ks < 4; ks++) acc = mfma16(*(LAS u32x4*)(lds + (64 * w + 16 * rt + l16) * 256 + (((4 * ks + grp) ^ l16) * 16)), bf[ks], acc);
        if (l16 < 4) *(LAS u32x2*)(tt + l16 * 128 + (16 * rt + 4 * grp) * 2) = u32x2{cvtpk(acc[0], acc[1]), cvtpk(acc[2], acc[3])};
      }
      asm volatile("s_waitcnt lgkmcnt(0)" ::: "memory");
      const u32x2 v = *(LAS u32x2*)(tt + lane * 8);
      stsc(u32x4{v[0], tg.th, v[1], tg.th}, rws, WS_QABS + (grp * 32 + head) * 2048 + w * 256 + l16 * 16, 0);
    }
    bar();
    u32x4 wf[4][4], wr[16];
    {
      const rsrc_t ro = mkrs(woA + (size_t)head * 131072, 131072);
      dma_rows<16>(ro, 0, lds, 16 * w, lane);
      const rsrc_t rv = mkrs(wuv, 4u << 20);
      const int voff = (64 * w + 16 * grp) * 8192 + head * 256 + l16 * 16;
#pragma unroll
      for (int i = 0; i < 16; i++) wr[i] = ldnt(rv, voff, i * 8192);
      SB();
      asm volatile("s_waitcnt vmcnt(16)" ::: "memory");
      bar();
      wo_frags(lds, w, lane, wf);
      bar();
      const rsrc_t rb = mkrs(woB, 4u << 20);
      const int bvo = (lane >> 3) * 1024 + (((lane & 7) ^ ((lane >> 5) << 1)) * 16);
#pragma unroll
      for (int i = 0; i < 16; i++)
        dma1(rb, (i & 1) ? bvo ^ 64 : bvo, (4 * w + b4) * 131072 + (head >> 2) * 128 + i * 8192, (unsigned)(uintptr_t)lds + w * 16384 + i * 1024);
      SB();
      if (tid < 8) ((LAS unsigned*)(scr + 24576))[tid] = 0u;
    }
    const int hw = 4 * w + b4, cc = head >> 2, roff = WS_RS + hw * 16384 + lane * 16;
    u32x4 g[6];
#pragma unroll
    for (int s = 0; s < 6; s++) asm volatile("" : "=v"(g[s]));
    if (w == 0) {
      for (unsigned spins = 0;; spins++) {
        asm volatile("" ::: "memory");
        const u32x4 v = ldsc(rws, WS_PML + ((lane & 31) * 32 + head) * 16, 0);
        if (__builtin_popcount((unsigned)__builtin_amdgcn_ballot_w64(mok(v, tg))) >= NREL || spins > SPIN_CAP) break;
        __builtin_amdgcn_s_sleep(16);
      }
    }
    bar();
    {
      const int b = w >> 1, c4 = tid & 127;
      const unsigned ta = (lane & 1) ? ptag(tg, 2) : ptag(tg, 0), tb = (lane & 1) ? ptag(tg, 3) : ptag(tg, 1);
      u32x4 ml;
      u32x2 po[8];
#pragma unroll
      for (int s = 0; s < 8; s++) asm volatile("" : "=v"(po[s]));
      unsigned missP = 255;
      for (unsigned spins = 0;; spins++) {
        asm volatile("" ::: "memory");
        ml = ldsc(rws, WS_PML + ((8 * b + (lane & 7)) * 32 + head) * 16, 0);
#pragma unroll
        for (int s = 0; s < 8; s++)
          if (missP >> s & 1) po[s] = __builtin_amdgcn_raw_buffer_load_b64(rws, WS_PO + (8 * b * 32 + head) * 1024 + c4 * 8, s * 32768, AUX_SC1);
        SB();
#pragma unroll
        for (int s = 0; s < 8; s++) if (__all((((po[s][0] ^ ta) | (po[s][1] ^ tb)) & PMASK) == 0u)) missP &= ~(1u << s);
        if ((!missP && __all(mok(ml, tg))) || spins > SPIN_CAP) break;
        __builtin_amdgcn_s_sleep(2);
      }
#pragma unroll
      for (int s = 0; s < 6; s++) g[s] = ldsc(rws, roff, s * 1024);
      SB();
      ml[0] &= 0xffff0000u; ml[1] &= ~255u;
#pragma unroll
      for (int s = 0; s < 8; s++) po[s] = u32x2{bfi(PMASK, 0x00020002u, po[s][0]), bfi(PMASK, 0x00020002u, po[s][1])};
      float M = __uint_as_float(ml[0]);
      M = fmaxf(M, dpp<XOR1>(M)); M = fmaxf(M, dpp<XOR2>(M)); M = fmaxf(M, dpp<XOR7>(M));
      const float f = fexp2((__uint_as_float(ml[0]) - M) * LOG2E);
      float L = f * __uint_as_float(ml[1]);
      L += dpp<XOR1>(L); L += dpp<XOR2>(L); L += dpp<XOR7>(L);
      const float gl = __builtin_ldexpf(f, (int)((((w & 1) ? ml[3] : ml[2]) >> (8 * grp)) & 255) - 142);
      const float gs[8] = {dpp<0x150>(gl), dpp<0x151>(gl), dpp<0x152>(gl), dpp<0x153>(gl), dpp<0x154>(gl), dpp<0x155>(gl), dpp<0x156>(gl), dpp<0x157>(gl)};
      f32x4 a = {0.f, 0.f, 0.f, 0.f};
#pragma unroll
      for (int s = 0; s < 8; s++)
        a += gs[s] * f32x4{(float)(short)po[s][0], (float)((int)po[s][0] >> 16), (float)(short)po[s][1], (float)((int)po[s][1] >> 16)};
      const float inv = (32768.f / 32767.f) / L;
      const u32x2 v = {cvtpk(a[0] * inv, a[1] * inv), cvtpk(a[2] * inv, a[3] * inv)};
      *(LAS u32x2*)(scr + b * 1024 + c4 * 8) = v;
    }
    bar();
    {
      u32x4 xp[2][2];
#pragma unroll
      for (int s = 0; s < 2; s++) {
        LAS u32x4* xs = (LAS u32x4*)(scr + (2 * s + (l16 >> 3)) * 1024 + (64 * w + 16 * grp) * 2);
        xp[s][0] = xs[0]; xp[s][1] = xs[1];
      }
      f32x4 acc[2] = {{0.f, 0.f, 0.f, 0.f}, {0.f, 0.f, 0.f, 0.f}};
      diag_gemv(wr, xp, mask, acc);
      LAS float* red = (LAS float*)(scr + 8192);
#pragma unroll
      for (int s = 0; s < 2; s++) *(LAS f32x4*)(red + w * 512 + (2 * s + (grp >> 1)) * 128 + 8 * l16 + 4 * (grp & 1)) = acc[s];
      bar();
      if (tid < 256) {
        float a = 0.f, b = 0.f;
#pragma unroll
        for (int k = 0; k < 8; k++) { const f32x2 v = *(LAS f32x2*)(red + k * 512 + 2 * tid); a += v[0]; b += v[1]; }
        ((LAS unsigned*)(scr + 4096))[tid] = cvtpk(a, b);
      }
      bar();
    }
    const f32x4 yA = wo_calc(wf, scr + 4096, lane);
    drain();
    y_store(yA, w, lane, head, rws, WS_Y, tg);
    {
      const int q = l16 >> 2;
      LAS char* img = lds + w * 16384 + (8 * (q >> 1) + 4 * (grp & 1) + 2 * (grp >> 1) + (q & 1)) * 128 + 8 * (lane & 3);
      const int fl = (grp & 1) | (q & 2);
#pragma unroll
      for (int ks = 0; ks < 4; ks++)
#pragma unroll
        for (int tl = 0; tl < 4; tl++) {
          const u32x2 a = trread(img + (tl ^ fl) * 32 + ks * 4096), b = trread(img + (tl ^ fl) * 32 + ks * 4096 + 2048);
          wf[tl][ks] = u32x4{a[0], a[1], b[0], b[1]};
        }
    }
    const rsrc_t rout = mkrs(out, 8192);
    LAS unsigned* cnt = (LAS unsigned*)(scr + 24576);
    LAS char* const re = scr + 8192;
    LAS char* const rA = scr + 8192 + 8192;
    LAS char* rl = scr + 8192 + 10240 + (lane & 31) * 8;
    LAS char* rw = scr + 8192 + 10240 + w * 256 + (lane & 3) * 64 + grp * 16;
    LAS char* rx = rl + (w < 3 ? 8 + w : 11) * 256;
    int oo = lane < 32 ? b4 * 2048 + 1024 + cc * 128 + lane * 4 : OOB, oe = head == 0 && lane == 0 ? WS_EPOCH : OOB;
    asm volatile("" : "+v"(rl), "+v"(rw), "+v"(rx), "+v"(oo), "+v"(oe), "+v"(cnt));
    {
      const int ofa = WS_Y + (b4 * 8 + cc) * 16384 + w * 2048 + lane * 16;
      u32x4 x0, x1;
      for (unsigned spins = 0;; spins++) {
        asm volatile("" ::: "memory");
        x0 = ldsc(rws, ofa, 0); x1 = ldsc(rws, ofa + 1024, 0);
        if (__all(gok(x0, tg) & gok(x1, tg)) || spins > SPIN_CAP) break;
        __builtin_amdgcn_s_sleep(4);
      }
      const f32x2 r = {half_sum(gval(x0[0]) + gval(x1[0])), half_sum(gval(x0[2]) + gval(x1[2]))};
      if (lane < 32) *(volatile LAS f32x2*)(rA + w * 256 + lane * 8) = r;
      const unsigned n = arrive(cnt + 1, lane);
      const f32x2 v = rows_sum<8>(rA + (lane & 31) * 8);
      __builtin_amdgcn_raw_buffer_store_b32(cvtpk(v[0], v[1]), rout, lane < 32 && n == 8u ? b4 * 2048 + cc * 128 + l16 * 8 + grp * 4 : OOB, 0, 0);
    }
    {
      for (unsigned spins = 0;; spins++) {
        unsigned bad = 0;
#pragma unroll
        for (int s = 0; s < 6; s++) bad |= (g[s][0] ^ tg.lo) | (g[s][2] ^ tg.rb);
        if (__all((bad & (spins > SPIN_CAP * 4 ? 0u : 511u)) == 0u)) break;
        asm volatile("" ::: "memory");
#pragma unroll
        for (int s = 0; s < 6; s++) g[s] = ldsc(rws, roff + s * 1024, 0);
        SB();
      }
      unsigned pr[3];
#pragma unroll
      for (int t = 0; t < 3; t++) {
        const u32x4 g0 = g[t], g1 = g[t + 3];
        const float R0 = rec_r(g0), R1 = rec_r(g1), Rm = fmaxf(R0, R1);
        const double s0 = mkd(0u, p2hi(R0, Rm)), s1 = mkd(0u, p2hi(R1, Rm));
        const double inv = __builtin_amdgcn_rcp(__builtin_fma(rec_l(g0), s0, rec_l(g1) * s1));
        pr[t] = cvtpk((float)(__builtin_fma(rec_a(g0), s0, rec_a(g1) * s1) * inv), (float)(__builtin_fma(rec_b(g0), s0, rec_b(g1) * s1) * inv));
      }
      *(volatile LAS f32x4*)(re + ((l16 >> 2) * 8 + w) * 256 + (lane & 3) * 64 + grp * 16) = proj(pr, wf, lane);
      const unsigned n = arrive(cnt, lane);
#pragma unroll
      for (int u = 0; u < 2; u++) {
        const int t = u ? 2 : lane >> 5;
        const f32x2 v = rows_sum<8>(re + t * 2048 + (lane & 31) * 8);
        stsc(gpair(v[0], v[1], tg), rws, n != 8u || (u && lane >= 32) ? OOB : WS_YB + ((((b4 + 1 + t) & 3) * 8 + cc) * 4 + b4) * 512 + (lane & 31) * 16, 0);
      }
    }
    const int ofb = w < 3 ? WS_YB + ((b4 * 8 + cc) * 4 + ((b4 + 1 + w) & 3)) * 512 + (lane & 31) * 16 : OOB;
    u32x4 xb, p[NSLOT];
    unsigned bad;
    for (unsigned spins = 0;; spins++) {
      asm volatile("" ::: "memory");
      xb = ldsc(rws, ofb, 0);
#pragma unroll
      for (int s = 0; s < NSLOT; s++) p[s] = ldsc(rws, roff + 8192 + s * 1024, 0);
      unsigned m = spins > SPIN_CAP * 4 ? 0u : 511u;
      asm volatile("" : "+s"(m));
      SB();
      unsigned ax = min((((xb[0] ^ tg.tl) | (xb[2] ^ tg.tl)) >> 24) | (xb[1] ^ tg.th) | (xb[3] ^ tg.th), w < 3 ? 1u : 0u), az = 0;
#pragma unroll
      for (int s = 0; s < NSLOT; s++) { ax |= p[s][0] ^ tg.lo; az |= p[s][2] ^ tg.rb; }
      bad = az & 512u;
      if (__all(((ax | az) & m) == 0u)) break;
    }
    *(volatile LAS f32x2*)rx = f32x2{gval(xb[0]), gval(xb[2])};
    double S0, S1, L;
    if (__builtin_expect(__all(bad == 0u), 1)) {
      S0 = mkd(p[0][0], p[0][1]); S1 = mkd(p[0][2], p[0][3]); L = rec_l(p[0]);
#pragma unroll
      for (int s = 1; s < NSLOT; s++) { S0 += mkd(p[s][0], p[s][1]); S1 += mkd(p[s][2], p[s][3]); L += rec_l(p[s]); }
    } else {
      float R = -INFINITY;
      S0 = S1 = L = 0.0;
#pragma unroll
      for (int s = 0; s < NSLOT; s++) {
        const float Rs = rec_l(p[s]) != 0.0 ? rec_r(p[s]) : -INFINITY, Rn = fmaxf(R, Rs);
        const double s0 = mkd(0u, p2hi(R, Rn)), s1 = mkd(0u, p2hi(Rs, Rn));
        L = __builtin_fma(rec_l(p[s]), s1, L * s0);
        S0 = __builtin_fma(rec_a(p[s]), s1, S0 * s0); S1 = __builtin_fma(rec_b(p[s]), s1, S1 * s0);
        R = Rn;
      }
    }
    {
      const double inv = __builtin_amdgcn_rcp(L);
      const unsigned pr[1] = {cvtpk((float)(S0 * inv), (float)(S1 * inv))};
      const f32x4 y = proj(pr, wf, lane);
      if ((lane & 12) == 0) *(volatile LAS f32x4*)rw = y;
      const unsigned n = arrive(cnt + 2, lane);
      const f32x2 v = rows_sum<11>(rl);
      __builtin_amdgcn_raw_buffer_store_b32(cvtpk(v[0], v[1]), rout, n == 8u ? oo : OOB, 0, 0);
      __builtin_amdgcn_raw_buffer_store_b64(__builtin_bit_cast(u32x2, tg.e), rws, n == 8u ? oe : OOB, 0, AUX_SC1);
      if (head == 0 && w == 0) {
        const unsigned now = (unsigned)__builtin_amdgcn_s_memrealtime();
        const u32x4 t4 = __builtin_amdgcn_raw_buffer_load_b128(mkrs(ws + (8 << 20), 4096), lane * 16, 0, AUX_SC1);
        int mn = 1 << 30, mx = -(1 << 30);
        for (int i = 0; i < 4; i++) { const int d = (int)(now - t4[i]); mn = min(mn, d); mx = max(mx, d); }
        for (int o = 1; o < 64; o *= 2) { mn = min(mn, __shfl_xor(mn, o)); mx = max(mx, __shfl_xor(mx, o)); }
        int ex = PROBE == 1 ? 10 * (mx - mn) : PROBE == 2 ? mx : mn;
        ex = min(max(ex, 0), 20000);
        ex = __builtin_amdgcn_readfirstlane(ex);
        while ((int)((unsigned)__builtin_amdgcn_s_memrealtime() - now) < ex) __builtin_amdgcn_s_sleep(2);
      }
    }
    return;
  }
  }
  {
    const u32x4 q4 = *(LAS u32x4*)(qst + 256 + l16 * 16);
    const Mrg mg = mkmrg(scr + SCR_SUM + SUM_STR, lane, WS_RS + (head * 16 + 8 + sub) * 1024, mktag(e0));
    Acc A;
    b_units(ra, rk4, rv4, cur, q4, lane, mask, ctl, rws, cntoff, A);
    b_merge(mg, scr + 8192 + 8704, w, lane, A, rws, e0);
    SB();
    asm volatile("" :: "v"(ra[0]), "v"(ra[1]), "v"(ra[2]), "v"(ra[3]), "v"(ra[4]), "v"(ra[5]), "v"(ra[6]), "v"(ra[7]));
  }
}

extern "C" void kernel_launch(void* const* d_in, const int* in_sizes, int n_in, void* d_out, int out_size, void* d_ws, size_t ws_size,
                              hipStream_t stream) {
  k_all<<<256, 512, 0, stream>>>((const char*)d_in[0], (const char*)d_in[1], (const char*)d_in[2], (const char*)d_in[3], (const char*)d_in[4],
                                 (const char*)d_in[5], (const char*)d_in[6], (const char*)d_in[7], (const char*)d_in[8], (const char*)d_in[9],
                                 (const char*)d_in[10], (char*)d_ws, (unsigned*)d_out);
}
```

```cpp
#define PROBE 1
#define OAUX 0
#include <hip/hip_runtime.h>
#include <stdint.h>

typedef __attribute__((ext_vector_type(4))) unsigned u32x4;
typedef __attribute__((ext_vector_type(2))) unsigned u32x2;
typedef __attribute__((ext_vector_type(8))) unsigned u32x8;
typedef __attribute__((ext_vector_type(16))) unsigned u32x16;
typedef __attribute__((ext_vector_type(4))) float f32x4;
typedef __attribute__((ext_vector_type(2))) float f32x2;
typedef __attribute__((ext_vector_type(2))) double f64x2;
typedef __attribute__((ext_vector_type(16))) float f32x16;
typedef __attribute__((ext_vector_type(8))) __bf16 bf16x8;
typedef __attribute__((ext_vector_type(2))) __bf16 bf16x2;
typedef __attribute__((ext_vector_type(4))) short s16x4;
typedef unsigned long long u64;
typedef __amdgpu_buffer_rsrc_t rsrc_t;
#define LAS __attribute__((address_space(3)))
#define GAS __attribute__((address_space(1)))

#define CPF $$
#define PAT "5c10p2ba43"
constexpr int WTH = 5;
constexpr int DCH = 2;
constexpr int NXA = 8, NXB = 8;
constexpr int NREL = 24;

constexpr int AUX_NT = 2, AUX_SC1 = 16, OOB = 0x40000000;
constexpr float SCALE = 0.08838834764831845f;
constexpr float LOG2E = 1.4426950408889634f;
constexpr unsigned SPIN_CAP = 1u << 17;
constexpr int NSLOT = 7, NUNIT = 256;

constexpr int WS_EPOCH = 0;
constexpr int CSTR    = 4352;
constexpr int WS_CNT  = 8192;
constexpr int WS_QB   = WS_CNT + 32 * CSTR;
constexpr int WS_QABS = WS_QB + 32 * 2048;
constexpr int WS_PML  = WS_QABS + 4 * 32 * 2048;
constexpr int WS_PO   = WS_PML + 32 * 32 * 16;
constexpr int WS_RS   = WS_PO + 32 * 32 * 1024;
constexpr int WS_Y    = WS_RS + 32 * 16 * 1024;
constexpr int WS_YB   = WS_Y + 4 * 8 * 32 * 512;
constexpr int WS_END  = WS_YB + 4 * 8 * 4 * 512;

constexpr int LDS_BYTES = 163840;
constexpr int LDS_SCR = 131072;
constexpr int SCR_SUM = 5120, SUM_STR = 1040;

__device__ __forceinline__ unsigned cvtpk(float lo, float hi) {
  f32x2 v = {lo, hi};
  return __builtin_bit_cast(unsigned, __builtin_convertvector(v, bf16x2));
}
__device__ __forceinline__ float bf_lo(unsigned u) { return __uint_as_float(u << 16); }
__device__ __forceinline__ float bf_hi(unsigned u) { return __uint_as_float(u & 0xffff0000u); }
__device__ __forceinline__ float rbf(float x) { return __uint_as_float(cvtpk(x, x) << 16); }
__device__ __forceinline__ float dot2(unsigned k, unsigned q, float acc) {
  return __builtin_amdgcn_fdot2_f32_bf16(__builtin_bit_cast(bf16x2, k), __builtin_bit_cast(bf16x2, q), acc, false);
}
template <int CTRL> __device__ __forceinline__ float dpp(float x) {
  return __builtin_bit_cast(float, __builtin_amdgcn_mov_dpp(__builtin_bit_cast(int, x), CTRL, 0xf, 0xf, true));
}
template <int CTRL> __device__ __forceinline__ unsigned dppu(unsigned x) {
  return (unsigned)__builtin_amdgcn_mov_dpp((int)x, CTRL, 0xf, 0xf, true);
}
constexpr int XOR1 = 0xB1, XOR2 = 0x4E, XOR7 = 0x141, XOR8 = 0x128, ROR4 = 0x124;
__device__ __forceinline__ float xrow_max(float x) {
  auto s = __builtin_amdgcn_permlane16_swap(__float_as_uint(x), __float_as_uint(x), false, false);
  x = fmaxf(__uint_as_float(s[0]), __uint_as_float(s[1]));
  auto t = __builtin_amdgcn_permlane32_swap(__float_as_uint(x), __float_as_uint(x), false, false);
  return fmaxf(__uint_as_float(t[0]), __uint_as_float(t[1]));
}
__device__ __forceinline__ float xrow_sum(float x) {
  auto s = __builtin_amdgcn_permlane16_swap(__float_as_uint(x), __float_as_uint(x), false, false);
  x = __uint_as_float(s[0]) + __uint_as_float(s[1]);
  auto t = __builtin_amdgcn_permlane32_swap(__float_as_uint(x), __float_as_uint(x), false, false);
  return __uint_as_float(t[0]) + __uint_as_float(t[1]);
}
__device__ __forceinline__ float row_max(float x) {
  x = fmaxf(x, dpp<XOR8>(x)); x = fmaxf(x, dpp<ROR4>(x)); x = fmaxf(x, dpp<XOR2>(x)); x = fmaxf(x, dpp<XOR1>(x));
  return x;
}
__device__ __forceinline__ float row_sum(float x) {
  x += dpp<XOR8>(x); x += dpp<ROR4>(x); x += dpp<XOR2>(x); x += dpp<XOR1>(x);
  return x;
}
__device__ __forceinline__ float fexp2(float x) { return __builtin_amdgcn_exp2f(x); }
__device__ __forceinline__ f32x4 mfma16(u32x4 a, u32x4 b, f32x4 c) {
  return __builtin_amdgcn_mfma_f32_16x16x32_bf16(__builtin_bit_cast(bf16x8, a), __builtin_bit_cast(bf16x8, b), c, 0, 0, 0);
}
__device__ __forceinline__ f32x16 mfma32(u32x4 a, u32x4 b, f32x16 c) {
  return __builtin_amdgcn_mfma_f32_32x32x16_bf16(__builtin_bit_cast(bf16x8, a), __builtin_bit_cast(bf16x8, b), c, 0, 0, 0);
}
__device__ __forceinline__ rsrc_t mkrs(const GAS char* p, unsigned bytes) {
  return __builtin_amdgcn_make_buffer_rsrc((void*)p, 0, bytes, 0x00020000);
}
__device__ __forceinline__ void bar() { asm volatile("s_waitcnt lgkmcnt(0)\n\ts_barrier" ::: "memory"); }
__device__ __forceinline__ void drain() { asm volatile("s_waitcnt vmcnt(0)" ::: "memory"); }
#define SB() __builtin_amdgcn_sched_barrier(0)
template <int N> __device__ __forceinline__ void vmwait() { asm volatile("s_waitcnt vmcnt(%0)" :: "n"(N) : "memory"); }
__device__ __forceinline__ unsigned swz(unsigned row) { return ((row & 3) << 2) | ((row >> 2) & 2); }
__device__ __forceinline__ u32x2 trread(LAS char* p) {
  return __builtin_bit_cast(u32x2, __builtin_amdgcn_ds_read_tr16_b64_v4i16((LAS s16x4*)p));
}

struct Tag { unsigned tl, th, lo, rb; u64 e; };
__device__ __forceinline__ Tag mktag(u64 e0) {
  Tag t;
  asm volatile("" : "+s"(e0));
  t.e = e0 * 6364136223846793005ull + 1442695040888963407ull;
  t.th = (unsigned)(t.e >> 32);
  t.lo = (unsigned)t.e;
  t.tl = t.lo & 0xff000000u;
  t.rb = ((t.lo >> 9) & 255u) | (~t.lo & 256u);
  return t;
}
__device__ __forceinline__ unsigned gpack(float v, const Tag& t) { return ((__float_as_uint(v) + 0x80u) >> 8) | t.tl; }
__device__ __forceinline__ u32x4 gpair(float a, float b, const Tag& t) { return u32x4{gpack(a, t), t.th, gpack(b, t), t.th}; }
__device__ __forceinline__ bool gok(u32x4 g, const Tag& t) {
  return ((((g[0] ^ t.tl) | (g[2] ^ t.tl)) >> 24) | (g[1] ^ t.th) | (g[3] ^ t.th)) == 0u;
}
__device__ __forceinline__ float gval(unsigned lo) { return __uint_as_float(lo << 8); }
__device__ __forceinline__ bool qok(u32x4 g, const Tag& t) { return ((g[1] ^ t.th) | (g[3] ^ t.th)) == 0u; }
__device__ __forceinline__ double mkd(unsigned lo, unsigned hi) { return __builtin_bit_cast(double, u32x2{lo, hi}); }
__device__ __forceinline__ u32x4 rpack(double a, double b, double l, float r, const Tag& t) {
  const u32x2 ua = __builtin_bit_cast(u32x2, a), ub = __builtin_bit_cast(u32x2, b), ul = __builtin_bit_cast(u32x2, l);
  const u64 ml = ((u64)(unsigned)__builtin_amdgcn_readfirstlane(ul[1]) << 32) | (unsigned)__builtin_amdgcn_readfirstlane(ul[0]);
  const u64 mr = (unsigned)__builtin_amdgcn_readfirstlane(__float_as_uint(r));
  const unsigned ta = t.lo & 511u, tb = t.rb;
  return u32x4{(ua[0] & ~1023u) | (__builtin_amdgcn_inverse_ballot_w64(ml) ? ta | 512u : ta), ua[1],
               (ub[0] & ~1023u) | (__builtin_amdgcn_inverse_ballot_w64(mr) ? tb | 512u : tb), ub[1]};
}
__device__ __forceinline__ u32x4 rpack0(f64x2 o, unsigned lw, int lane, unsigned ta, unsigned tb) {
  const u32x4 u = __builtin_bit_cast(u32x4, o);
  return u32x4{(u[0] & ~1023u) | (ta | (__builtin_amdgcn_ubfe(lw, lane, 1) << 9)), u[1], (u[2] & ~1023u) | tb, u[3]};
}
__device__ __forceinline__ bool rok(u32x4 g, const Tag& t) { return (((g[0] ^ t.lo) | (g[2] ^ t.rb)) & 511u) == 0u; }
constexpr unsigned PMASK = 0x00030003u;
__device__ __forceinline__ unsigned ptag(const Tag& t, int k) {
  const unsigned n = k == 0 ? t.lo : k == 3 ? t.lo >> 10 : ((t.lo >> (1 + 3 * k)) & 7u) | (~t.lo & 8u);
  return (n & 3u) | ((n & 12u) << 14);
}
__device__ __forceinline__ unsigned ltag(const Tag& t) { return ((t.lo >> 16) & 127u) | (~t.lo & 128u); }
__device__ __forceinline__ bool mok(u32x4 g, const Tag& t) { return (((g[0] ^ t.lo) & 0xffffu) | ((g[1] ^ ltag(t)) & 255u)) == 0u; }
__device__ __forceinline__ unsigned bfi(unsigned m, unsigned a, unsigned b) { return (a & m) | (b & ~m); }
__device__ __forceinline__ double rec_a(u32x4 g) { return mkd(g[0] & ~1023u, g[1]); }
__device__ __forceinline__ double rec_b(u32x4 g) { return mkd(g[2] & ~1023u, g[3]); }
__device__ __forceinline__ double rec_l(u32x4 g) { return __builtin_bit_cast(double, __builtin_amdgcn_ballot_w64((g[0] & 512u) != 0u)); }
__device__ __forceinline__ float rec_r(u32x4 g) { return __uint_as_float((unsigned)__builtin_amdgcn_ballot_w64((g[2] & 512u) != 0u)); }
__device__ __forceinline__ int edif(float a, float b) {
  int r;
  asm("v_cvt_i32_f32 %0, %1" : "=v"(r) : "v"(a - b));
  return r;
}
__device__ __forceinline__ unsigned p2hi(float a, float b) { return (unsigned)max(edif(a, b) + 1023, 0) << 20; }
__device__ __forceinline__ float bucket(float m) { return fmaf(__builtin_rintf(m * (1.f / 512.f)), 512.f, 0.f); }
__device__ __forceinline__ float rfl(float x) { return __uint_as_float(__builtin_amdgcn_readfirstlane(__float_as_uint(x))); }
__device__ __forceinline__ unsigned rdu(unsigned v, int l) { return __builtin_amdgcn_readlane(v, l); }

__device__ __forceinline__ void diag_gemv(const u32x4 (&wr)[16], const u32x4 (&xp)[2][2], const unsigned (&mask)[4], f32x4 (&acc)[2]) {
#pragma unroll
  for (int i = 0; i < 16; i++) {
#pragma unroll
    for (int s = 0; s < 2; s++) {
      const unsigned src = xp[s][i >> 3][(i >> 1) & 3];
      const unsigned dup = __builtin_amdgcn_perm(src, src, (i & 1) ? 0x03020302u : 0x01000100u);
      const u32x4 a = {dup & mask[0], dup & mask[1], dup & mask[2], dup & mask[3]};
      acc[s] = mfma16(a, wr[i], acc[s]);
    }
  }
}

constexpr int bitpos(char c) { for (int i = 0; i < 10; i++) if (PAT[i] == c) return 10 + i; return 31; }
constexpr int goff(int t) { int o = 0; for (int j = 0; j < 6; j++) o |= ((t >> j) & 1) << bitpos('0' + j); return o; }
struct Acc { double o[2]; double l; float R; };
__device__ __forceinline__ u32x4 ldnt(rsrc_t r, int voff, int so) { return __builtin_amdgcn_raw_buffer_load_b128(r, voff, so, AUX_NT); }
__device__ __forceinline__ u32x4 ldslot(rsrc_t r, int voff, int so, int i) { return ldnt(r, voff + goff(i & 3), so ^ goff(i & ~3)); }
__device__ __forceinline__ u32x4 ldunit(rsrc_t r, int voff, int so, int i) { return ldnt(r, voff + i * 1024, so); }
__device__ __forceinline__ u32x4 ldsc(rsrc_t r, int voff, int so) { return __builtin_amdgcn_raw_buffer_load_b128(r, voff, so, AUX_SC1); }
__device__ __forceinline__ void stsc(u32x4 v, rsrc_t r, int voff, int so) { __builtin_amdgcn_raw_buffer_store_b128(v, r, voff + so, 0, AUX_SC1); }
__device__ __forceinline__ float dot8(u32x4 k, u32x4 q) { return dot2(k[3], q[3], dot2(k[2], q[2], dot2(k[1], q[1], dot2(k[0], q[0], 0.f)))); }
#define TR_STEP(HALF, CTRL, BIT) _Pragma("unroll") for (int h = 0; h < (HALF); ++h) { \
    const float t1 = x[h] + dpp<CTRL>(x[h]), t2 = x[h + (HALF)] + dpp<CTRL>(x[h + (HALF)]); x[h] = (c & (BIT)) ? t2 : t1; }
template <int N> __device__ __forceinline__ float k_step(u32x4 (&ring)[16], u32x4 q, int c, rsrc_t rn, int voff, int so, bool ext, LAS char* xp) {
  float x[16];
#pragma unroll
  for (int i = 0; i < 16; i++) {
    x[i] = dot8(ring[i], q);
    asm volatile("s_waitcnt vmcnt(%1)" : "+v"(x[i]) : "n"(WTH - 1));
    if (i >= 16 - N && ext) ring[i] = *(volatile LAS u32x4*)(xp + (i - (16 - N)) * 1024);
    else ring[i] = ldslot(rn, voff, so, i);
    SB();
  }
  TR_STEP(8, XOR8, 8) TR_STEP(4, XOR7, 4) TR_STEP(2, XOR2, 2) TR_STEP(1, XOR1, 1)
  return rbf(rbf(x[0]) * SCALE);
}
template <int BC> __device__ __forceinline__ void pv(u32x4 v, unsigned pk, const unsigned (&mask)[4], f32x4& acc) {
  const unsigned d = dppu<0x150 + BC>(pk);
  acc = mfma16(u32x4{d & mask[0], d & mask[1], d & mask[2], d & mask[3]}, v, acc);
}
template <int I, int MODE> __device__ __forceinline__ void v_step(u32x4 (&ring)[16], unsigned pk, const unsigned (&mask)[4], f32x4& acc,
                                                                  rsrc_t rk, rsrc_t rv, int voff, int so) {
  pv<I>(ring[I], pk, mask, acc);
  if (MODE == 1 || (MODE == 2 && I < 8)) {
    asm volatile("s_waitcnt vmcnt(%1)" : "+v"(acc) : "n"(WTH - 1));
    ring[I] = MODE == 1 ? ldslot(rv, voff, so, I) : ldunit(I < 4 ? rk : rv, voff, so, I & 3);
  } else {
    asm volatile("" : "+v"(acc));
  }
  SB();
  if constexpr (I < 15) v_step<I + 1, MODE>(ring, pk, mask, acc, rk, rv, voff, so);
}
__device__ __forceinline__ float exp_ref(float mx, float& f) {
  const float M = __builtin_ceilf(mx * LOG2E);
  f = __builtin_amdgcn_fmed3f(fmaf(mx, LOG2E, -M), -1.f, 0.f);
  return M;
}
__device__ __forceinline__ unsigned lds_inc(LAS unsigned* p, int lane) {
  unsigned t = 0, z = 0;
  asm volatile("" : "+v"(z));
  if (lane == 0) t = __hip_atomic_fetch_add(p + z, 1u, __ATOMIC_RELAXED, __HIP_MEMORY_SCOPE_WORKGROUP);
  return t;
}
struct Take { unsigned unit, pend; bool fetch; };
__device__ __forceinline__ void lds_add(LAS unsigned* p, unsigned v) { __hip_atomic_fetch_add(p, v, __ATOMIC_RELAXED, __HIP_MEMORY_SCOPE_WORKGROUP); }
__device__ __forceinline__ Take take(LAS unsigned* ctl, unsigned kv, rsrc_t rws, int cntoff, int lane) {
  const unsigned k = __builtin_amdgcn_readfirstlane(kv);
  unsigned b, s;
  auto look = [&]() {
    const unsigned vs = ((volatile LAS unsigned*)ctl)[5], vb = ((volatile LAS unsigned*)ctl)[8 + (k >> 2)];
    s = __builtin_amdgcn_readfirstlane(vs); b = __builtin_amdgcn_readfirstlane(vb);
    return (b == ~0u) & ((s < 0x10000u) | ((s & 0xffffu) != 0u));
  };
  if (__builtin_expect(look(), 0))
    for (unsigned spins = 0; spins < SPIN_CAP * 16; spins++) {
      __builtin_amdgcn_s_sleep(1);
      if (!look()) break;
    }
  Take t;
  t.unit = min(b, (unsigned)NUNIT) + (k & 3);
  t.fetch = ((k & 3) == 0) & (b < NUNIT) & (s < 0x10000u);
  int off = OOB;
  if (t.fetch) {
    unsigned z = 0;
    asm volatile("" : "+v"(z));
    if (lane == 0) { lds_add(ctl + 5 + z, 1u); off = cntoff; }
    asm volatile("s_waitcnt lgkmcnt(0)" ::: "memory");
  }
  t.pend = __builtin_amdgcn_raw_ptr_buffer_atomic_add_i32(4, rws, off, 0, AUX_SC1);
  SB();
  return t;
}
__device__ __forceinline__ void give(LAS unsigned* ctl, const Take& t, int lane) {
  if (t.fetch && lane == 0) {
    unsigned z = 0;
    asm volatile("" : "+v"(z));
    if (t.pend < NUNIT) {
      const unsigned i = __hip_atomic_fetch_add(ctl + 4 + z, 1u, __ATOMIC_RELAXED, __HIP_MEMORY_SCOPE_WORKGROUP);
      ((volatile LAS unsigned*)ctl)[8 + i] = t.pend;
    }
    asm volatile("" ::: "memory");
    lds_add(ctl + 5 + z, t.pend >= NUNIT - 4 ? 0xffffu : ~0u);
  }
}
__device__ __forceinline__ int unit_off(unsigned t) { return (int)min(t, (unsigned)NUNIT) * 4096; }

__device__ __forceinline__ void b_units(u32x4 (&ra)[16], rsrc_t rk, rsrc_t rv, unsigned cur, u32x4 q, int lane, const unsigned (&mask)[4],
                                        LAS unsigned* ctl, rsrc_t rws, int cntoff, Acc& A) {
  const int c = lane & 15, voff = lane * 16;
  float Mr = -INFINITY;
  double s1 = 0.0;
  A.o[0] = A.o[1] = A.l = 0.0; A.R = cur < NUNIT ? -INFINITY : 0.f;
#pragma unroll 1
  while (cur < NUNIT) {
    const Take t = take(ctl, lds_inc(ctl, lane), rws, cntoff, lane);
    const int so = unit_off(t.unit);
    SB();
    float x[4];
#pragma unroll
    for (int i = 0; i < 4; i++) x[i] = dot8(ra[i], q);
    asm volatile("" : "+v"(x[0]), "+v"(x[1]), "+v"(x[2]), "+v"(x[3]));
    SB();
#pragma unroll
    for (int i = 0; i < 4; i++) {
      ra[i] = ldunit(rk, voff, so, i);
      SB();
    }
    TR_STEP(2, XOR8, 8) TR_STEP(1, XOR7, 4)
    x[0] += dpp<XOR2>(x[0]); x[0] += dpp<XOR1>(x[0]);
    const float s = rbf(rbf(x[0]) * SCALE);
    const float n = __builtin_rintf(s * LOG2E);
    float e = fexp2(__builtin_amdgcn_fmed3f(fmaf(s, LOG2E, -n), -1.f, 1.f)), d = n - Mr;
    asm volatile("" : "+v"(e));
    if (__builtin_expect(__any(d > 57.f), 0)) {
      float mx = fmaxf(n, dpp<XOR8>(n));
      mx = xrow_max(fmaxf(mx, dpp<ROR4>(mx)));
      const float Rn = rfl(fmaxf(A.R, bucket(mx)));
      const double s0 = mkd(0u, p2hi(A.R, Rn));
      A.o[0] *= s0; A.o[1] *= s0; A.l *= s0;
      A.R = Rn; Mr = mx; d = n - mx;
      s1 = mkd(0u, p2hi(mx, Rn));
    }
    const float p = __builtin_ldexpf(e, edif(d, 0.f));
    unsigned pk = cvtpk(p, p);
    asm volatile("" : "+v"(pk));
    SB();
    f32x4 acc = {0.f, 0.f, 0.f, 0.f};
    float ls = bf_lo(pk);
    pv<0>(ra[4], pk, mask, acc);  asm volatile("" : "+v"(acc)); ra[4] = ldunit(rv, voff, so, 0); ls += dpp<XOR8>(ls); asm volatile("" : "+v"(ls)); SB();
    pv<4>(ra[5], pk, mask, acc);  asm volatile("" : "+v"(acc)); ra[5] = ldunit(rv, voff, so, 1); ls += dpp<ROR4>(ls); asm volatile("" : "+v"(ls)); SB();
    pv<8>(ra[6], pk, mask, acc);  asm volatile("" : "+v"(acc)); ra[6] = ldunit(rv, voff, so, 2); SB();
    pv<12>(ra[7], pk, mask, acc); asm volatile("" : "+v"(acc)); ra[7] = ldunit(rv, voff, so, 3); SB();
    A.l = __builtin_fma((double)xrow_sum(ls), s1, A.l);
    asm volatile("" : "+v"(A.l));
    A.o[0] = __builtin_fma((double)(lane < 32 ? acc[0] : acc[2]), s1, A.o[0]);
    A.o[1] = __builtin_fma((double)(lane < 32 ? acc[1] : acc[3]), s1, A.o[1]);
    asm volatile("" : "+v"(A.o[0]), "+v"(A.o[1]));
    SB();
    cur = t.unit;
    if (cur >= NUNIT) break;
    give(ctl, t, lane);
  }
}
#undef TR_STEP
__device__ __forceinline__ int rec_col(int lane) { return 8 * (lane & 15) + 4 * ((lane >> 4) & 1) + 2 * (lane >> 5); }
struct Mrg { unsigned s0, s4, s8, ta, tb; int off; };
__device__ __forceinline__ Mrg mkmrg(LAS char* sum, int lane, int roff, const Tag& tg) {
  Mrg m = {(unsigned)(uintptr_t)sum, (unsigned)(uintptr_t)sum + (lane >> 5) * 4, (unsigned)(uintptr_t)sum + lane * 8, tg.lo & 511u, tg.rb, roff + lane * 16};
  asm volatile("" : "+v"(m.s0), "+v"(m.s4), "+v"(m.s8), "+v"(m.ta), "+v"(m.tb), "+v"(m.off));
  return m;
}
__device__ __forceinline__ void b_merge(const Mrg& m, LAS char* mrg, int w, int lane, const Acc& A, rsrc_t rws, u64 e0) {
  unsigned inc = 1;
  double a = A.o[0], b = A.o[1], l = A.l;
  if (__builtin_expect(__float_as_uint(A.R) != 0u, 0)) {
    *(volatile LAS f64x2*)(mrg + w * 1024 + lane * 16) = f64x2{a, b};
    *(volatile LAS double*)(mrg + 8192 + w * 8) = l;
    *(volatile LAS float*)(mrg + 8256 + w * 4) = A.R;
    a = b = l = 0.0;
    inc = 1u + (256u << w);
  }
  f64x2 o;
  unsigned n, lw;
  asm volatile("ds_add_f64 %3, %5\n\tds_add_f64 %3, %6 offset:512\n\ts_mov_b64 exec, 1\n\tds_add_f64 %3, %7 offset:1024\n\tds_add_u32 %3, %8 offset:1032\n\t"
               "s_mov_b64 exec, -1\n\tds_read_b32 %1, %9 offset:1032\n\tds_read_b32 %2, %4 offset:1024\n\tds_read2st64_b64 %0, %3 offset1:1\n\ts_waitcnt lgkmcnt(0)"
               : "=&v"(o), "=&v"(n), "=&v"(lw)
               : "v"(m.s8), "v"(m.s4), "v"(a), "v"(b), "v"(l), "v"(inc), "v"(m.s0)
               : "memory");
  stsc(rpack0(o, lw, lane, m.ta, m.tb), rws, n == 8u ? m.off : OOB, 0);
  SB();
  n = __builtin_amdgcn_readfirstlane(n);
  if (__builtin_expect(n > 8u && (n & 255u) == 8u, 0)) {
    double L = *(volatile LAS double*)(uintptr_t)(m.s0 + 1024);
    float R = L != 0.0 ? 0.f : -INFINITY;
#pragma unroll 1
    for (int i = 0; i < 8; i++)
      if (n >> (8 + i) & 1) {
        const f64x2 oi = *(volatile LAS f64x2*)(mrg + i * 1024 + lane * 16);
        const float Ri = *(volatile LAS float*)(mrg + 8256 + i * 4), Rn = fmaxf(R, Ri);
        const double s0 = mkd(0u, p2hi(R, Rn)), s1 = mkd(0u, p2hi(Ri, Rn));
        o[0] = __builtin_fma(oi[0], s1, o[0] * s0); o[1] = __builtin_fma(oi[1], s1, o[1] * s0);
        L = __builtin_fma(*(volatile LAS double*)(mrg + 8192 + i * 8), s1, L * s0);
        R = Rn;
      }
    stsc(rpack(o[0], o[1], L, R, mktag(e0)), rws, m.off, 0);
  }
}

__device__ __forceinline__ unsigned fz(unsigned row) { return ((row & 2) << 1) | ((row >> 1) & 2); }
__device__ __forceinline__ void dma1(rsrc_t rs, int voff, int so, unsigned ldsaddr) {
  unsigned keep;
  asm volatile("s_mov_b32 %0, m0\n\ts_mov_b32 m0, %1\n\ts_nop 0\n\tbuffer_load_dwordx4 %2, %3, %4 offen nt lds\n\ts_mov_b32 m0, %0"
               : "=&s"(keep) : "s"(ldsaddr), "v"(voff), "s"(rs), "s"(so) : "memory");
}
template <int NR>
__device__ __forceinline__ void dma_rows(rsrc_t rs, int srcoff, LAS char* img, int r0, int lane) {
#pragma unroll
  for (int u = 0; u < NR; u++) {
    const int r = r0 + u;
    unsigned keep;
    asm volatile("s_mov_b32 %0, m0\n\ts_mov_b32 m0, %1\n\ts_nop 0\n\tbuffer_load_dwordx4 %2, %3, %4 offen nt lds\n\ts_mov_b32 m0, %0"
                 : "=&s"(keep) : "s"((unsigned)(uintptr_t)img + r * 1024), "v"((lane ^ swz(r)) * 16), "s"(rs), "s"(srcoff + r * 1024) : "memory");
  }
  SB();
}

__device__ __forceinline__ void wo_frags(LAS char* img, int w, int lane, u32x4 (&wf)[4][4]) {
  const int g = lane >> 4, q = (lane & 15) >> 2, p = lane & 3;
#pragma unroll
  for (int ks = 0; ks < 4; ks++)
#pragma unroll
    for (int tl = 0; tl < 4; tl++) {
      const int ch = 8 * w + 2 * tl + (p >> 1);
      const int r0 = 32 * ks + 8 * g + q, r1 = r0 + 4;
      const u32x2 a = trread(img + r0 * 1024 + ((ch ^ swz(r0)) * 16) + 8 * (p & 1));
      const u32x2 b = trread(img + r1 * 1024 + ((ch ^ swz(r1)) * 16) + 8 * (p & 1));
      wf[tl][ks] = u32x4{a[0], a[1], b[0], b[1]};
    }
}
__device__ __forceinline__ f32x4 wo_calc(const u32x4 (&wf)[4][4], LAS char* cb, int lane) {
  const int n = lane & 15, grp = lane >> 4;
  u32x4 bf[4];
#pragma unroll
  for (int ks = 0; ks < 4; ks++) bf[ks] = *(LAS u32x4*)(cb + (n & 3) * 256 + ks * 64 + grp * 16);
  f32x4 acc[4];
#pragma unroll
  for (int tl = 0; tl < 4; tl++) acc[tl] = f32x4{0.f, 0.f, 0.f, 0.f};
#pragma unroll
  for (int ks = 0; ks < 4; ks++)
#pragma unroll
    for (int tl = 0; tl < 4; tl++) acc[tl] = mfma16(wf[tl][ks], bf[ks], acc[tl]);
  const f32x4 lo = (n & 4) ? acc[1] : acc[0], hi = (n & 4) ? acc[3] : acc[2];
  return (n & 8) ? hi : lo;
}
__device__ __forceinline__ void y_store(f32x4 y, int w, int lane, int slot, rsrc_t rws, int yoff, const Tag& tg) {
  const int off = yoff + (((lane & 3) * 8 + w) * 32 + slot) * 512 + ((lane & 12) + (lane >> 4)) * 16;
  stsc(gpair(y[0], y[1], tg), rws, off, 0);
  stsc(gpair(y[2], y[3], tg), rws, off, 256);
}
template <int K, int N> __device__ __forceinline__ unsigned bc1(const unsigned (&p)[N]) {
  unsigned v = dppu<0x150 + K>(p[0]);
  if constexpr (N > 1) {
    v = (unsigned)__builtin_amdgcn_update_dpp((int)v, (int)p[1], 0x150 + K, 0xf, 2, false);
    v = (unsigned)__builtin_amdgcn_update_dpp((int)v, (int)p[2], 0x150 + K, 0xf, 4, false);
  }
  return v;
}
template <int K, int N> __device__ __forceinline__ u32x4 bc4(const unsigned (&p)[N]) { return u32x4{bc1<K>(p), bc1<K + 1>(p), bc1<K + 2>(p), bc1<K + 3>(p)}; }
template <int N> __device__ __forceinline__ f32x4 proj(const unsigned (&pr)[N], const u32x4 (&wf)[4][4], int lane) {
  const u32x4 bf[4] = {bc4<0>(pr), bc4<4>(pr), bc4<8>(pr), bc4<12>(pr)};
  f32x4 acc[4];
#pragma unroll
  for (int tl = 0; tl < 4; tl++) acc[tl] = f32x4{0.f, 0.f, 0.f, 0.f};
#pragma unroll
  for (int ks = 0; ks < 4; ks++)
#pragma unroll
    for (int tl = 0; tl < 4; tl++) acc[tl] = mfma16(wf[tl][ks], bf[ks], acc[tl]);
  const f32x4 lo = (lane & 1) ? acc[1] : acc[0], hi = (lane & 1) ? acc[3] : acc[2];
  f32x4 r = (lane & 2) ? hi : lo;
  asm volatile("" : "+v"(r));
  return r;
}
__device__ __forceinline__ unsigned arrive(LAS unsigned* p, int lane) {
  unsigned z = 0;
  asm volatile("" : "+v"(z) :: "memory");
  if (lane == 0) lds_add(p + z, 1u);
  asm volatile("" ::: "memory");
  return *(volatile LAS unsigned*)p;
}
template <int N> __device__ __forceinline__ f32x2 rows_sum(LAS char* p) {
  f32x2 v[N];
#pragma unroll
  for (int i = 0; i < N; i++) v[i] = *(volatile LAS f32x2*)(p + i * 256);
#pragma unroll
  for (int st = 1; st < N; st *= 2)
#pragma unroll
    for (int i = 0; i + st < N; i += 2 * st) v[i] += v[i + st];
  return v[0];
}
__device__ __forceinline__ float half_sum(float x) {
  auto t = __builtin_amdgcn_permlane32_swap(__float_as_uint(x), __float_as_uint(x), false, false);
  return __uint_as_float(t[0]) + __uint_as_float(t[1]);
}

__global__ __launch_bounds__(512, 2) void k_all(const char* a0_, const char* a1_, const char* a2_, const char* a3_, const char* a4_, const char* a5_,
                                                const char* a6_, const char* a7_, const char* a8_, const char* a9_, const char* a10_, char* a11_, unsigned* a12_) {
  u32x16 ka; u32x8 kb_; u32x2 kc_;
  asm volatile("s_load_dwordx16 %0, %3, 0x0\n\ts_load_dwordx8 %1, %3, 0x40\n\ts_load_dwordx2 %2, %3, 0x60"
               : "=&s"(ka), "=&s"(kb_), "=&s"(kc_) : "s"(__builtin_amdgcn_kernarg_segment_ptr()));
  __shared__ __attribute__((aligned(16))) char smem[LDS_BYTES];
  LAS char* lds = (LAS char*)smem;
  LAS char* scr = lds + LDS_SCR;
  LAS unsigned* ctl = (LAS unsigned*)scr;
  const int tid = threadIdx.x, lane = tid & 63, w = __builtin_amdgcn_readfirstlane(tid >> 6);
  const int blk = blockIdx.x, head = blk >> 3, prod = (blk + head) & 7;
  const int sub = (0x54372106u >> (4 * prod)) & 7;
  const unsigned tst = (unsigned)__builtin_amdgcn_s_memrealtime();
  unsigned cpf = 0;
  {
    u64 pc;
    asm volatile("s_getpc_b64 %0" : "=s"(pc));
    cpf = __builtin_amdgcn_raw_buffer_load_b32(mkrs((const GAS char*)(pc & ~255ull), CPF), w == 7 ? head * 2048 + lane * 32 : OOB, 0, 0);
    SB();
  }
  {
    unsigned c = 16 + 4 * (DCH * sub + tid);
    asm volatile("" : "+v"(c));
    c = tid < 8 + DCH ? c : ~0u;
    c = tid < 8 ? 0u : c;
    ctl[tid] = tid == 4 ? DCH : c;
    *(LAS u64*)(scr + SCR_SUM + min(tid, 2 * SUM_STR / 8 - 1) * 8) = 0;
  }
  const int l16 = lane & 15, grp = lane >> 4;
  const int b4 = head & 3, cntoff = WS_CNT + head * CSTR;
  const int bm = (b4 + 1 + (sub >= 3 ? sub - 3 : sub)) & 3;
  const size_t base = (size_t)(bm * 32 + head) << 20;
  const int so = ((w & 1) << bitpos('a')) | (((w >> 1) & 1) << bitpos('b')) | ((w >> 2) << bitpos('c')) | (sub >= 3 ? 1 << bitpos('p') : 0);
  int v0 = l16 * 16 + grp * (sub < 6 ? 256 : 131072) + (sub < 6 ? so : w * 524288 + head * 256);
  unsigned boff = sub < 6 ? (unsigned)base : 0u, bsz = sub < 6 ? 1u << 20 : 4u << 20;
  int s0[WTH];
#pragma unroll
  for (int i = 0; i < WTH; i++) {
    s0[i] = sub < 6 ? goff(i) : i * 8192;
    asm volatile("" : "+s"(s0[i]));
  }
  asm volatile("" : "+v"(v0), "+s"(boff), "+s"(bsz));
  SB();
  asm volatile("s_waitcnt lgkmcnt(0)" : "+s"(ka), "+s"(kb_), "+s"(kc_));
  auto ptr = [](unsigned lo, unsigned hi) { return (GAS char*)(((u64)hi << 32) | lo); };
  GAS char *const xA = ptr(ka[0], ka[1]), *const ckv = ptr(ka[2], ka[3]), *const wqA = ptr(ka[4], ka[5]), *const wuk = ptr(ka[6], ka[7]),
           *const wuv = ptr(ka[8], ka[9]), *const woA = ptr(ka[10], ka[11]), *const xB = ptr(ka[12], ka[13]), *const kc = ptr(ka[14], ka[15]),
           *const vc = ptr(kb_[0], kb_[1]), *const wqB = ptr(kb_[2], kb_[3]), *const woB = ptr(kb_[4], kb_[5]), *const ws = ptr(kb_[6], kb_[7]),
           *const out = ptr(kc_[0], kc_[1]);
  const u64 e0 = *(const __attribute__((address_space(4))) u64*)(u64)(ws + WS_EPOCH);
  const rsrc_t r0 = mkrs((sub < 6 ? kc : sub == 6 ? wqB : wqA) + boff, bsz);
  SB();
  u32x4 ra[16];
#pragma unroll
  for (int i = 0; i < WTH; i++) ra[i] = ldnt(r0, v0, s0[i]);
  SB();
  u32x4 xp[2][2];
  if (sub >= 6) {
    const GAS char* x = sub == 6 ? xB : xA;
#pragma unroll
    for (int s = 0; s < 2; s++) {
      const GAS u32x4* xs = (const GAS u32x4*)(x + (2 * s + (l16 >> 3)) * 1024 + (64 * w + 16 * grp) * 2);
      xp[s][0] = xs[0]; xp[s][1] = xs[1];
    }
#pragma unroll
    for (int i = WTH; i < 16; i++) ra[i] = ldnt(r0, v0, i * 8192);
    asm volatile("" ::: "memory");
    SB();
  }
  const rsrc_t rws = mkrs(ws, WS_END);
  __builtin_amdgcn_raw_buffer_store_b32(tst, mkrs(ws + (8 << 20), 4096), tid == 0 ? blk * 4 : OOB, 0, AUX_SC1);
  unsigned mask[4];
#pragma unroll
  for (int v = 0; v < 4; v++) mask[v] = (((lane & 7) >> 1) == v) ? ((lane & 1) ? 0xffff0000u : 0x0000ffffu) : 0u;
  const size_t base4 = (size_t)(b4 * 32 + head) << 20;
  const rsrc_t rk4 = mkrs(kc + base4, 1u << 20), rv4 = mkrs(vc + base4, 1u << 20);
  const int voff = lane * 16;
  LAS char* qst = scr + 1024 + w * 512;
  const int qo = WS_QB + head * 2048 + (lane < 32 ? bm : b4) * 512 + (lane & 31) * 16;
  auto qwait = [&](u32x4 g, const Tag& tg) {
    if (!__all(qok(g, tg))) {
      for (unsigned spins = 0;; spins++) {
        asm volatile("" ::: "memory");
        g = ldsc(rws, qo, 0);
        if (__all(qok(g, tg)) || spins > SPIN_CAP) break;
        __builtin_amdgcn_s_sleep(2);
      }
    }
    *(LAS u32x2*)(qst + lane * 8) = u32x2{g[0], g[2]};
  };
  unsigned cur;

  if (__builtin_expect(sub < 6, 1)) {
    const rsrc_t rv = mkrs(vc + base, 1u << 20);
    u32x4 rb[16], g;
#pragma unroll
    for (int i = WTH; i < 32 + NXA + NXB; i++) {
      if (i == WTH) asm volatile("s_barrier" ::: "memory");
      vmwait<WTH - 1>();
      if (i == 32 + NXA + NXB - WTH) asm volatile("buffer_load_dwordx4 %0, %1, %2, 0 offen sc1" : "=&v"(g) : "v"(qo), "s"(rws));
      if (i < 32) {
        const u32x4 v = ldslot(r0, voff, so, i);
        if (i < 16) ra[i] = v; else rb[i - 16] = v;
      } else {
        const int j = i < 32 + NXA ? i + 16 - NXA : i + 32 - NXA - NXB;
        dma1(r0, voff + goff(j & 3), so ^ goff(j & ~3), (unsigned)(uintptr_t)lds + w * 16384 + (i - 32) * 1024);
      }
      SB();
    }
    asm volatile("s_waitcnt vmcnt(%1)" : "+v"(g) : "n"(WTH), "v"(cpf));
    qwait(g, mktag(e0));
    const u32x4 q = *(LAS u32x4*)(qst + l16 * 16);
    float sc[4] = {0.f, 0.f, 0.f, 0.f};
#pragma unroll 1
    for (int it = 0; it < 2; it++) {
      const rsrc_t rn = it ? rv : r0;
      sc[2] = sc[0]; sc[3] = sc[1];
      sc[1] = k_step<NXA>(ra, q, l16, rn, voff, so ^ (it ? goff(48) : goff(32)), it == 0, lds + w * 16384 + voff);
      sc[0] = k_step<NXB>(rb, q, l16, rn, voff, so ^ (it ? goff(32) : goff(48)), it == 0, lds + w * 16384 + NXA * 1024 + voff);
    }
    const float mx = xrow_max(row_max(fmaxf(fmaxf(sc[0], sc[1]), fmaxf(sc[2], sc[3]))));
    float f, l = 0.f;
    const float M = exp_ref(mx, f);
    unsigned pk[4];
#pragma unroll
    for (int k = 0; k < 4; k++) {
      const float p = fexp2(fmaf(sc[k] - mx, LOG2E, f));
      pk[k] = cvtpk(p, p);
      l += bf_lo(pk[k]);
    }
    l = xrow_sum(row_sum(l));
    cur = 8 * sub + w;
    f32x4 acc = {0.f, 0.f, 0.f, 0.f};
    v_step<0, 1>(ra, pk[0], mask, acc, r0, rv, voff, so ^ goff(16));
    v_step<0, 1>(rb, pk[1], mask, acc, r0, rv, voff, so);
    v_step<0, 2>(ra, pk[2], mask, acc, rk4, rv4, voff, cur * 4096);
    v_step<0, 0>(rb, pk[3], mask, acc, r0, rv, voff, 0);
    Acc A;
    A.R = rfl(bucket(M));
    const double sm = mkd(0u, p2hi(M, A.R));
    A.o[0] = (lane < 32 ? acc[0] : acc[2]) * sm; A.o[1] = (lane < 32 ? acc[1] : acc[3]) * sm;
    A.l = l * sm;
    b_merge(mkmrg(scr + SCR_SUM, lane, WS_RS + (head * 16 + sub) * 1024, mktag(e0)), scr + 8192, w, lane, A, rws, e0);
  } else {
  {
    __builtin_amdgcn_raw_buffer_store_b32(48 + 24 * DCH, rws, (sub == 6 && tid == 0) ? cntoff : OOB, 0, AUX_SC1);
    {
      const rsrc_t rk = mkrs(wuk, sub == 7 ? 4u << 20 : 0u);
#pragma unroll
      for (int i = 0; i < 16; i++)
        __builtin_amdgcn_raw_ptr_buffer_load_lds(rk, (LAS void*)(lds + w * 16384 + i * 1024), 16,
                                                 (64 * w + grp) * 8192 + head * 256 + ((l16 ^ (4 * (i & 3) + grp)) * 16), i * 4 * 8192, 0, AUX_NT);
    }
    SB();
    f32x4 acc[2] = {{0.f, 0.f, 0.f, 0.f}, {0.f, 0.f, 0.f, 0.f}};
    diag_gemv(ra, xp, mask, acc);
    LAS float* red = (LAS float*)(scr + 8192);
#pragma unroll
    for (int s = 0; s < 2; s++) *(LAS f32x4*)(red + w * 512 + (2 * s + (grp >> 1)) * 128 + 8 * l16 + 4 * (grp & 1)) = acc[s];
    asm volatile("" :: "v"(cpf));
    __builtin_amdgcn_s_waitcnt(0x0f70);
    drain();
    bar();
    if (tid < 128) {
      f32x4 a = {0.f, 0.f, 0.f, 0.f};
#pragma unroll
      for (int k = 0; k < 8; k++) a += *(LAS f32x4*)(red + k * 512 + 4 * tid);
      const unsigned p0 = cvtpk(a[0], a[1]), p1 = cvtpk(a[2], a[3]);
      const Tag tg = mktag(e0);
      stsc(u32x4{p0, tg.th, p1, tg.th}, rws, sub == 6 ? WS_QB + head * 2048 + tid * 16 : OOB, 0);
      *(LAS u32x2*)(scr + 1024 + tid * 8) = u32x2{p0, p1};
    }
  }

  if (sub == 6) {
    const int aid = ((head & 6) << 2) | ((head & 1) << 2) | (head >> 3), b = aid >> 3;
    const rsrc_t rc = mkrs(ckv + (size_t)aid * 524288, 524288);
    const int dvo = (lane >> 3) * 1024 + w * 128 + (((lane & 7) ^ fz(lane >> 3)) * 16);
    const unsigned dlo = (unsigned)(uintptr_t)lds + w * 8192;
    const int xt = (aid & 7) << 16;
#pragma unroll
    for (int j = 0; j < 16; j++) dma1(rc, dvo, (j * 8192) ^ xt, dlo + (j >> 3) * 65536 + (j & 7) * 1024);
    SB();
    const Tag tg = mktag(e0);
    bar();
    {
      u32x4 t[8];
      unsigned miss = 15;
      __builtin_amdgcn_s_sleep(90);
      for (unsigned spins = 0;; spins++) {
        asm volatile("" ::: "memory");
#pragma unroll
        for (int i = 0; i < 4; i++)
          if (miss >> i & 1) {
            t[2 * i] = ldsc(rws, WS_QABS + (b * 32 + w) * 2048 + lane * 16, i * 16384);
            t[2 * i + 1] = ldsc(rws, WS_QABS + (b * 32 + w) * 2048 + lane * 16 + 1024, i * 16384);
          }
        SB();
#pragma unroll
        for (int i = 0; i < 4; i++) if (__all(qok(t[2 * i], tg) & qok(t[2 * i + 1], tg))) miss &= ~(1u << i);
        if (!miss || spins > SPIN_CAP) break;
        __builtin_amdgcn_s_sleep(4);
      }
#pragma unroll
      for (int i = 0; i < 8; i++) {
        const int row = 8 * (i >> 1) + w;
        *(LAS u32x2*)(scr + row * 1024 + (((32 * (i & 1) + (lane >> 1)) ^ swz(row)) * 16) + (lane & 1) * 8) = u32x2{t[i][0], t[i][2]};
      }
    }
    bar();
    const int kb = w & 3, hb = w >> 2;
    u32x4 qf[16];
    {
      const int row = 16 * hb + l16;
#pragma unroll
      for (int ks = 0; ks < 16; ks++) qf[ks] = *(LAS u32x4*)(scr + row * 1024 + (((4 * ks + grp) ^ swz(row)) * 16));
    }
    LAS float* mx = (LAS float*)scr;
    LAS char* pl = scr + 1024;
    LAS float* lr = (LAS float*)(scr + 8192);
    f32x16 o0, o1;
#pragma unroll
    for (int j = 0; j < 16; j++) { o0[j] = 0.f; o1[j] = 0.f; }
    float m1 = -INFINITY, m2 = -INFINITY, l1 = 0.f;
    const int h1 = 16 * hb + l16, h2 = lane & 31, hh = lane >> 5;
    const int arow = 16 * kb + l16;
    const int tq = (lane & 15) >> 2, tp = lane & 3, tblk = (lane >> 4) & 1;
    const int qa0 = arow * 128 + ((grp ^ fz(arow)) * 16), qa1 = qa0 ^ 64;
    const int pr = 8 * hh + tq, pc = 2 * tblk + (tp >> 1);
    const int pa0 = w * 8192 + pr * 128 + ((pc ^ fz(pr)) * 16) + 8 * (tp & 1), pa1 = w * 8192 + (pr + 4) * 128 + ((pc ^ fz(pr + 4)) * 16) + 8 * (tp & 1);
#pragma unroll 1
    for (int t = 0; t < 8; t++) {
      if (t < 7) asm volatile("s_waitcnt vmcnt(8)" ::: "memory"); else asm volatile("s_waitcnt vmcnt(0)" ::: "memory");
      bar();
      LAS char* img = lds + (t & 1) * 65536;
      f32x4 s0 = {0.f, 0.f, 0.f, 0.f}, s1 = s0;
      u32x4 ka[16];
      u32x2 ta[4][4];
      const int nso = t < 6 ? ((t + 2) * 65536) ^ xt : OOB;
#pragma unroll
      for (int ks = 0; ks < 16; ks++) ka[ks] = *(LAS u32x4*)(img + ((ks & 1) ? qa1 : qa0) + (ks >> 1) * 8192);
      SB();
#pragma unroll
      for (int k2 = 0; k2 < 8; k2++) {
        s0 = mfma16(ka[2 * k2], qf[2 * k2], s0);
        s1 = mfma16(ka[2 * k2 + 1], qf[2 * k2 + 1], s1);
      }
      asm volatile("" : "+v"(s0), "+v"(s1));
      SB();
#pragma unroll
      for (int ks = 0; ks < 4; ks++) {
        ta[ks][0] = trread(img + pa0 + ks * 2048); ta[ks][1] = trread(img + pa1 + ks * 2048);
        ta[ks][2] = trread(img + (pa0 ^ 64) + ks * 2048); ta[ks][3] = trread(img + (pa1 ^ 64) + ks * 2048);
      }
      SB();
      float x[4];
#pragma unroll
      for (int j = 0; j < 4; j++) x[j] = rbf(rbf(s0[j] + s1[j]) * SCALE);
      const float lm = xrow_max(fmaxf(fmaxf(x[0], x[1]), fmaxf(x[2], x[3])));
      if (grp == 0) mx[h1 * 4 + kb] = lm;
      LAS u32x2* pw = (LAS u32x2*)(pl + h1 * 144 + (16 * kb + 4 * grp) * 2);
      u32x2 pp = {cvtpk(fexp2((x[0] - m1) * LOG2E), fexp2((x[1] - m1) * LOG2E)), cvtpk(fexp2((x[2] - m1) * LOG2E), fexp2((x[3] - m1) * LOG2E))};
      *pw = pp;
      bar();
      const f32x4 ma = *(LAS f32x4*)(mx + h1 * 4), mb = *(LAS f32x4*)(mx + h2 * 4);
      u32x4 pb[4];
#pragma unroll
      for (int ks = 0; ks < 4; ks++) pb[ks] = *(LAS u32x4*)(pl + h2 * 144 + (16 * ks + 8 * hh) * 2);
      const float n1 = fmaxf(m1, fmaxf(fmaxf(ma[0], ma[1]), fmaxf(ma[2], ma[3])));
      const float n2 = fmaxf(m2, fmaxf(fmaxf(mb[0], mb[1]), fmaxf(mb[2], mb[3])));
      float al1 = fexp2((m1 - n1) * LOG2E), al2 = fexp2((m2 - n2) * LOG2E);
      asm volatile("" : "+v"(pb[0]), "+v"(pb[1]), "+v"(pb[2]), "+v"(pb[3]));
      if (__any(n2 - m2 > 40.f)) {
        bar();
        pp = u32x2{cvtpk(fexp2((x[0] - n1) * LOG2E), fexp2((x[1] - n1) * LOG2E)), cvtpk(fexp2((x[2] - n1) * LOG2E), fexp2((x[3] - n1) * LOG2E))};
        *pw = pp;
        l1 *= al1;
#pragma unroll
        for (int j = 0; j < 16; j++) { o0[j] *= al2; o1[j] *= al2; }
        al1 = al2 = 1.f;
        bar();
#pragma unroll
        for (int ks = 0; ks < 4; ks++) pb[ks] = *(LAS u32x4*)(pl + h2 * 144 + (16 * ks + 8 * hh) * 2);
      }
      m1 = n1; m2 = n2;
#pragma unroll
      for (int ks = 0; ks < 4; ks++) {
        o0 = mfma32(u32x4{ta[ks][0][0], ta[ks][0][1], ta[ks][1][0], ta[ks][1][1]}, pb[ks], o0);
        o1 = mfma32(u32x4{ta[ks][2][0], ta[ks][2][1], ta[ks][3][0], ta[ks][3][1]}, pb[ks], o1);
        SB();
        if (t < 6) {
          dma1(rc, dvo, nso + 2 * ks * 8192, dlo + (t & 1) * 65536 + 2 * ks * 1024);
          dma1(rc, dvo, nso + (2 * ks + 1) * 8192, dlo + (t & 1) * 65536 + (2 * ks + 1) * 1024);
        }
        SB();
      }
      l1 = (l1 + ((bf_lo(pp[0]) + bf_hi(pp[0])) + (bf_lo(pp[1]) + bf_hi(pp[1])))) * al1;
#pragma unroll
      for (int j = 0; j < 16; j++) { o0[j] *= al2; o1[j] *= al2; }
    }
    bar();
    {
      float am = 0.f;
#pragma unroll
      for (int j = 0; j < 16; j++) am = fmaxf(am, fmaxf(fabsf(o0[j]), fabsf(o1[j])));
      auto sw = __builtin_amdgcn_permlane32_swap(__float_as_uint(am), __float_as_uint(am), false, false);
      am = fmaxf(__uint_as_float(sw[0]), __uint_as_float(sw[1]));
      const int e = max(__builtin_amdgcn_frexp_expf(am), -127);
      LAS char* tp = lds + h2 * 1040 + (64 * w + 4 * hh) * 2;
      auto pkn = [&](float a, float b) { return __builtin_bit_cast(unsigned, __builtin_amdgcn_cvt_pknorm_i16(__builtin_ldexpf(a, -e), __builtin_ldexpf(b, -e))); };
#pragma unroll
      for (int j4 = 0; j4 < 4; j4++) {
        *(LAS u32x2*)(tp + j4 * 16) = u32x2{pkn(o0[4 * j4], o0[4 * j4 + 1]), pkn(o0[4 * j4 + 2], o0[4 * j4 + 3])};
        *(LAS u32x2*)(tp + 64 + j4 * 16) = u32x2{pkn(o1[4 * j4], o1[4 * j4 + 1]), pkn(o1[4 * j4 + 2], o1[4 * j4 + 3])};
      }
      if (hh == 0) *(LAS unsigned char*)(scr + 9216 + h2 * 8 + w) = (unsigned char)(e + 127);
      const float ls = xrow_sum(l1);
      if (grp == 0) lr[kb * 32 + h1] = ls;
      bar();
#pragma unroll
      for (int i = 0; i < 4; i++) {
        u32x4 v = *(LAS u32x4*)(lds + (4 * w + i) * 1040 + lane * 16);
#pragma unroll
        for (int k = 0; k < 4; k++) v[k] = bfi(PMASK, ptag(tg, k), v[k]);
        stsc(v, rws, WS_PO + (aid * 32 + 4 * w) * 1024 + lane * 16, i * 1024);
      }
      if (tid < 32) {
        const u32x2 eb = *(LAS u32x2*)(scr + 9216 + tid * 8);
        const float l = (lr[tid] + lr[32 + tid]) + (lr[64 + tid] + lr[96 + tid]);
        stsc(u32x4{bfi(0xffffu, tg.lo, __float_as_uint(m2)), bfi(255u, ltag(tg), __float_as_uint(l)), eb[0], eb[1]}, rws, WS_PML + (aid * 32 + tid) * 16, 0);
      }
      if (w == 0) {
        const unsigned t = __builtin_amdgcn_readfirstlane(__builtin_amdgcn_raw_ptr_buffer_atomic_add_i32(8 + 4 * DCH, rws, lane == 0 ? cntoff : OOB, 0, AUX_SC1));
        const unsigned bs = t + 8 + 4 * (lane - 8);
        ctl[lane] = lane == 3 ? t : lane == 4 ? DCH : lane == 5 ? (t + 8 + 4 * DCH >= NUNIT ? 0x10000u : 0u) : lane < 8 ? 0u : (lane < 8 + DCH && bs < NUNIT) ? bs : ~0u;
        if (lane < 32) ctl[64 + lane] = ~0u;
      }
      *(LAS u64*)(scr + SCR_SUM + SUM_STR + min(tid, SUM_STR / 8 - 1) * 8) = 0;
      bar();
    }
    const u32x4 g = ldsc(rws, qo, 0);
    cur = __builtin_amdgcn_readfirstlane(ctl[3]) + w;
#pragma unroll
    for (int i = 0; i < 8; i++) ra[i] = ldunit(i < 4 ? rk4 : rv4, voff, unit_off(cur), i & 3);
    SB();
    qwait(g, mktag(e0));
  } else {
    const Tag tg = mktag(e0);
    bar();
    {
      u32x4 bf[4];
#pragma unroll
      for (int ks = 0; ks < 4; ks++) bf[ks] = *(LAS u32x4*)(scr + 1024 + (l16 & 3) * 256 + ks * 64 + grp * 16);
      LAS char* tt = scr + 2048 + w * 512;
#pragma unroll
      for (int rt = 0; rt < 4; rt++) {
        f32x4 acc = {0.f, 0.f, 0.f, 0.f};
#pragma unroll
        for (int ks = 0; ks < 4; ks++) acc = mfma16(*(LAS u32x4*)(lds + (64 * w + 16 * rt + l16) * 256 + (((4 * ks + grp) ^ l16) * 16)), bf[ks], acc);
        if (l16 < 4) *(LAS u32x2*)(tt + l16 * 128 + (16 * rt + 4 * grp) * 2) = u32x2{cvtpk(acc[0], acc[1]), cvtpk(acc[2], acc[3])};
      }
      asm volatile("s_waitcnt lgkmcnt(0)" ::: "memory");
      const u32x2 v = *(LAS u32x2*)(tt + lane * 8);
      stsc(u32x4{v[0], tg.th, v[1], tg.th}, rws, WS_QABS + (grp * 32 + head) * 2048 + w * 256 + l16 * 16, 0);
    }
    bar();
    u32x4 wf[4][4], wr[16];
    {
      const rsrc_t ro = mkrs(woA + (size_t)head * 131072, 131072);
      dma_rows<16>(ro, 0, lds, 16 * w, lane);
      const rsrc_t rv = mkrs(wuv, 4u << 20);
      const int voff = (64 * w + 16 * grp) * 8192 + head * 256 + l16 * 16;
#pragma unroll
      for (int i = 0; i < 16; i++) wr[i] = ldnt(rv, voff, i * 8192);
      SB();
      asm volatile("s_waitcnt vmcnt(16)" ::: "memory");
      bar();
      wo_frags(lds, w, lane, wf);
      bar();
      const rsrc_t rb = mkrs(woB, 4u << 20);
      const int bvo = (lane >> 3) * 1024 + (((lane & 7) ^ ((lane >> 5) << 1)) * 16);
#pragma unroll
      for (int i = 0; i < 16; i++)
        dma1(rb, (i & 1) ? bvo ^ 64 : bvo, (4 * w + b4) * 131072 + (head >> 2) * 128 + i * 8192, (unsigned)(uintptr_t)lds + w * 16384 + i * 1024);
      SB();
      if (tid < 8) ((LAS unsigned*)(scr + 24576))[tid] = 0u;
    }
    const int hw = 4 * w + b4, cc = head >> 2, roff = WS_RS + hw * 16384 + lane * 16;
    u32x4 g[6];
#pragma unroll
    for (int s = 0; s < 6; s++) asm volatile("" : "=v"(g[s]));
    if (w == 0) {
      for (unsigned spins = 0;; spins++) {
        asm volatile("" ::: "memory");
        const u32x4 v = ldsc(rws, WS_PML + ((lane & 31) * 32 + head) * 16, 0);
        if (__builtin_popcount((unsigned)__builtin_amdgcn_ballot_w64(mok(v, tg))) >= NREL || spins > SPIN_CAP) break;
        __builtin_amdgcn_s_sleep(16);
      }
    }
    bar();
    {
      const int b = w >> 1, c4 = tid & 127;
      const unsigned ta = (lane & 1) ? ptag(tg, 2) : ptag(tg, 0), tb = (lane & 1) ? ptag(tg, 3) : ptag(tg, 1);
      u32x4 ml;
      u32x2 po[8];
#pragma unroll
      for (int s = 0; s < 8; s++) asm volatile("" : "=v"(po[s]));
      unsigned missP = 255;
      for (unsigned spins = 0;; spins++) {
        asm volatile("" ::: "memory");
        ml = ldsc(rws, WS_PML + ((8 * b + (lane & 7)) * 32 + head) * 16, 0);
#pragma unroll
        for (int s = 0; s < 8; s++)
          if (missP >> s & 1) po[s] = __builtin_amdgcn_raw_buffer_load_b64(rws, WS_PO + (8 * b * 32 + head) * 1024 + c4 * 8, s * 32768, AUX_SC1);
        SB();
#pragma unroll
        for (int s = 0; s < 8; s++) if (__all((((po[s][0] ^ ta) | (po[s][1] ^ tb)) & PMASK) == 0u)) missP &= ~(1u << s);
        if ((!missP && __all(mok(ml, tg))) || spins > SPIN_CAP) break;
        __builtin_amdgcn_s_sleep(2);
      }
#pragma unroll
      for (int s = 0; s < 6; s++) g[s] = ldsc(rws, roff, s * 1024);
      SB();
      ml[0] &= 0xffff0000u; ml[1] &= ~255u;
#pragma unroll
      for (int s = 0; s < 8; s++) po[s] = u32x2{bfi(PMASK, 0x00020002u, po[s][0]), bfi(PMASK, 0x00020002u, po[s][1])};
      float M = __uint_as_float(ml[0]);
      M = fmaxf(M, dpp<XOR1>(M)); M = fmaxf(M, dpp<XOR2>(M)); M = fmaxf(M, dpp<XOR7>(M));
      const float f = fexp2((__uint_as_float(ml[0]) - M) * LOG2E);
      float L = f * __uint_as_float(ml[1]);
      L += dpp<XOR1>(L); L += dpp<XOR2>(L); L += dpp<XOR7>(L);
      const float gl = __builtin_ldexpf(f, (int)((((w & 1) ? ml[3] : ml[2]) >> (8 * grp)) & 255) - 142);
      const float gs[8] = {dpp<0x150>(gl), dpp<0x151>(gl), dpp<0x152>(gl), dpp<0x153>(gl), dpp<0x154>(gl), dpp<0x155>(gl), dpp<0x156>(gl), dpp<0x157>(gl)};
      f32x4 a = {0.f, 0.f, 0.f, 0.f};
#pragma unroll
      for (int s = 0; s < 8; s++)
        a += gs[s] * f32x4{(float)(short)po[s][0], (float)((int)po[s][0] >> 16), (float)(short)po[s][1], (float)((int)po[s][1] >> 16)};
      const float inv = (32768.f / 32767.f) / L;
      const u32x2 v = {cvtpk(a[0] * inv, a[1] * inv), cvtpk(a[2] * inv, a[3] * inv)};
      *(LAS u32x2*)(scr + b * 1024 + c4 * 8) = v;
    }
    bar();
    {
      u32x4 xp[2][2];
#pragma unroll
      for (int s = 0; s < 2; s++) {
        LAS u32x4* xs = (LAS u32x4*)(scr + (2 * s + (l16 >> 3)) * 1024 + (64 * w + 16 * grp) * 2);
        xp[s][0] = xs[0]; xp[s][1] = xs[1];
      }
      f32x4 acc[2] = {{0.f, 0.f, 0.f, 0.f}, {0.f, 0.f, 0.f, 0.f}};
      diag_gemv(wr, xp, mask, acc);
      LAS float* red = (LAS float*)(scr + 8192);
#pragma unroll
      for (int s = 0; s < 2; s++) *(LAS f32x4*)(red + w * 512 + (2 * s + (grp >> 1)) * 128 + 8 * l16 + 4 * (grp & 1)) = acc[s];
      bar();
      if (tid < 256) {
        float a = 0.f, b = 0.f;
#pragma unroll
        for (int k = 0; k < 8; k++) { const f32x2 v = *(LAS f32x2*)(red + k * 512 + 2 * tid); a += v[0]; b += v[1]; }
        ((LAS unsigned*)(scr + 4096))[tid] = cvtpk(a, b);
      }
      bar();
    }
    const f32x4 yA = wo_calc(wf, scr + 4096, lane);
    drain();
    y_store(yA, w, lane, head, rws, WS_Y, tg);
    {
      const int q = l16 >> 2;
      LAS char* img = lds + w * 16384 + (8 * (q >> 1) + 4 * (grp & 1) + 2 * (grp >> 1) + (q & 1)) * 128 + 8 * (lane & 3);
      const int fl = (grp & 1) | (q & 2);
#pragma unroll
      for (int ks = 0; ks < 4; ks++)
#pragma unroll
        for (int tl = 0; tl < 4; tl++) {
          const u32x2 a = trread(img + (tl ^ fl) * 32 + ks * 4096), b = trread(img + (tl ^ fl) * 32 + ks * 4096 + 2048);
          wf[tl][ks] = u32x4{a[0], a[1], b[0], b[1]};
        }
    }
    const rsrc_t rout = mkrs(out, 8192);
    LAS unsigned* cnt = (LAS unsigned*)(scr + 24576);
    LAS char* const re = scr + 8192;
    LAS char* const rA = scr + 8192 + 8192;
    LAS char* rl = scr + 8192 + 10240 + (lane & 31) * 8;
    LAS char* rw = scr + 8192 + 10240 + w * 256 + (lane & 3) * 64 + grp * 16;
    LAS char* rx = rl + (w < 3 ? 8 + w : 11) * 256;
    int oo = lane < 32 ? b4 * 2048 + 1024 + cc * 128 + lane * 4 : OOB, oe = head == 0 && lane == 0 ? WS_EPOCH : OOB;
    asm volatile("" : "+v"(rl), "+v"(rw), "+v"(rx), "+v"(oo), "+v"(oe), "+v"(cnt));
    {
      const int ofa = WS_Y + (b4 * 8 + cc) * 16384 + w * 2048 + lane * 16;
      u32x4 x0, x1;
      for (unsigned spins = 0;; spins++) {
        asm volatile("" ::: "memory");
        x0 = ldsc(rws, ofa, 0); x1 = ldsc(rws, ofa + 1024, 0);
        if (__all(gok(x0, tg) & gok(x1, tg)) || spins > SPIN_CAP) break;
        __builtin_amdgcn_s_sleep(4);
      }
      const f32x2 r = {half_sum(gval(x0[0]) + gval(x1[0])), half_sum(gval(x0[2]) + gval(x1[2]))};
      if (lane < 32) *(volatile LAS f32x2*)(rA + w * 256 + lane * 8) = r;
      const unsigned n = arrive(cnt + 1, lane);
      const f32x2 v = rows_sum<8>(rA + (lane & 31) * 8);
      __builtin_amdgcn_raw_buffer_store_b32(cvtpk(v[0], v[1]), rout, lane < 32 && n == 8u ? b4 * 2048 + cc * 128 + l16 * 8 + grp * 4 : OOB, 0, OAUX);
    }
    {
      for (unsigned spins = 0;; spins++) {
        unsigned bad = 0;
#pragma unroll
        for (int s = 0; s < 6; s++) bad |= (g[s][0] ^ tg.lo) | (g[s][2] ^ tg.rb);
        if (__all((bad & (spins > SPIN_CAP * 4 ? 0u : 511u)) == 0u)) break;
        asm volatile("" ::: "memory");
#pragma unroll
        for (int s = 0; s < 6; s++) g[s] = ldsc(rws, roff + s * 1024, 0);
        SB();
      }
      unsigned pr[3];
#pragma unroll
      for (int t = 0; t < 3; t++) {
        const u32x4 g0 = g[t], g1 = g[t + 3];
        const float R0 = rec_r(g0), R1 = rec_r(g1), Rm = fmaxf(R0, R1);
        const double s0 = mkd(0u, p2hi(R0, Rm)), s1 = mkd(0u, p2hi(R1, Rm));
        const double inv = __builtin_amdgcn_rcp(__builtin_fma(rec_l(g0), s0, rec_l(g1) * s1));
        pr[t] = cvtpk((float)(__builtin_fma(rec_a(g0), s0, rec_a(g1) * s1) * inv), (float)(__builtin_fma(rec_b(g0), s0, rec_b(g1) * s1) * inv));
      }
      *(volatile LAS f32x4*)(re + ((l16 >> 2) * 8 + w) * 256 + (lane & 3) * 64 + grp * 16) = proj(pr, wf, lane);
      const unsigned n = arrive(cnt, lane);
#pragma unroll
      for (int u = 0; u < 2; u++) {
        const int t = u ? 2 : lane >> 5;
        const f32x2 v = rows_sum<8>(re + t * 2048 + (lane & 31) * 8);
        stsc(gpair(v[0], v[1], tg), rws, n != 8u || (u && lane >= 32) ? OOB : WS_YB + ((((b4 + 1 + t) & 3) * 8 + cc) * 4 + b4) * 512 + (lane & 31) * 16, 0);
      }
    }
    const int ofb = w < 3 ? WS_YB + ((b4 * 8 + cc) * 4 + ((b4 + 1 + w) & 3)) * 512 + (lane & 31) * 16 : OOB;
    u32x4 xb, p[NSLOT];
    unsigned bad;
    for (unsigned spins = 0;; spins++) {
      asm volatile("" ::: "memory");
      xb = ldsc(rws, ofb, 0);
#pragma unroll
      for (int s = 0; s < NSLOT; s++) p[s] = ldsc(rws, roff + 8192 + s * 1024, 0);
      unsigned m = spins > SPIN_CAP * 4 ? 0u : 511u;
      asm volatile("" : "+s"(m));
      SB();
      unsigned ax = min((((xb[0] ^ tg.tl) | (xb[2] ^ tg.tl)) >> 24) | (xb[1] ^ tg.th) | (xb[3] ^ tg.th), w < 3 ? 1u : 0u), az = 0;
#pragma unroll
      for (int s = 0; s < NSLOT; s++) { ax |= p[s][0] ^ tg.lo; az |= p[s][2] ^ tg.rb; }
      bad = az & 512u;
      if (__all(((ax | az) & m) == 0u)) break;
    }
    *(volatile LAS f32x2*)rx = f32x2{gval(xb[0]), gval(xb[2])};
    double S0, S1, L;
    if (__builtin_expect(__all(bad == 0u), 1)) {
      S0 = mkd(p[0][0], p[0][1]); S1 = mkd(p[0][2], p[0][3]); L = rec_l(p[0]);
#pragma unroll
      for (int s = 1; s < NSLOT; s++) { S0 += mkd(p[s][0], p[s][1]); S1 += mkd(p[s][2], p[s][3]); L += rec_l(p[s]); }
    } else {
      float R = -INFINITY;
      S0 = S1 = L = 0.0;
#pragma unroll
      for (int s = 0; s < NSLOT; s++) {
        const float Rs = rec_l(p[s]) != 0.0 ? rec_r(p[s]) : -INFINITY, Rn = fmaxf(R, Rs);
        const double s0 = mkd(0u, p2hi(R, Rn)), s1 = mkd(0u, p2hi(Rs, Rn));
        L = __builtin_fma(rec_l(p[s]), s1, L * s0);
        S0 = __builtin_fma(rec_a(p[s]), s1, S0 * s0); S1 = __builtin_fma(rec_b(p[s]), s1, S1 * s0);
        R = Rn;
      }
    }
    {
      const double inv = __builtin_amdgcn_rcp(L);
      const unsigned pr[1] = {cvtpk((float)(S0 * inv), (float)(S1 * inv))};
      const f32x4 y = proj(pr, wf, lane);
      if ((lane & 12) == 0) *(volatile LAS f32x4*)rw = y;
      const unsigned n = arrive(cnt + 2, lane);
      const f32x2 v = rows_sum<11>(rl);
      __builtin_amdgcn_raw_buffer_store_b32(cvtpk(v[0], v[1]), rout, n == 8u ? oo : OOB, 0, OAUX);
      __builtin_amdgcn_raw_buffer_store_b64(__builtin_bit_cast(u32x2, tg.e), rws, n == 8u ? oe : OOB, 0, AUX_SC1);
      if (w == 0) {
        const unsigned now = (unsigned)__builtin_amdgcn_s_memrealtime();
        const u32x4 t4 = __builtin_amdgcn_raw_buffer_load_b128(mkrs(ws + (8 << 20), 4096), lane * 16, 0, AUX_SC1);
        int mn = 1 << 30, mx = -(1 << 30);
        for (int i = 0; i < 4; i++) { const int d = (int)(now - t4[i]); mn = min(mn, d); mx = max(mx, d); }
        for (int o = 1; o < 64; o *= 2) { mn = min(mn, __shfl_xor(mn, o)); mx = max(mx, __shfl_xor(mx, o)); }
        int ex = 6000 - mx;
        if (PROBE == 3) asm volatile("buffer_wbl2 sc1\n\ts_waitcnt vmcnt(0)" ::: "memory");
        ex = min(max(ex, 0), 20000);
        ex = __builtin_amdgcn_readfirstlane(ex);
        while ((int)((unsigned)__builtin_amdgcn_s_memrealtime() - now) < ex) __builtin_amdgcn_s_sleep(2);
      }
    }
    return;
  }
  }
  {
    const u32x4 q4 = *(LAS u32x4*)(qst + 256 + l16 * 16);
    const Mrg mg = mkmrg(scr + SCR_SUM + SUM_STR, lane, WS_RS + (head * 16 + 8 + sub) * 1024, mktag(e0));
    Acc A;
    b_units(ra, rk4, rv4, cur, q4, lane, mask, ctl, rws, cntoff, A);
    b_merge(mg, scr + 8192 + 8704, w, lane, A, rws, e0);
    SB();
    asm volatile("" :: "v"(ra[0]), "v"(ra[1]), "v"(ra[2]), "v"(ra[3]), "v"(ra[4]), "v"(ra[5]), "v"(ra[6]), "v"(ra[7]));
  }
}

extern "C" void kernel_launch(void* const* d_in, const int* in_sizes, int n_in, void* d_out, int out_size, void* d_ws, size_t ws_size,
                              hipStream_t stream) {
  k_all<<<256, 512, 0, stream>>>((const char*)d_in[0], (const char*)d_in[1], (const char*)d_in[2], (const char*)d_in[3], (const char*)d_in[4],
                                 (const char*)d_in[5], (const char*)d_in[6], (const char*)d_in[7], (const char*)d_in[8], (const char*)d_in[9],
                                 (const char*)d_in[10], (char*)d_ws, (unsigned*)d_out);
}
```

```cpp
#define PROBE 1
#define OAUX 17
#define OAUXB 17
#include <hip/hip_runtime.h>
#include <stdint.h>

typedef __attribute__((ext_vector_type(4))) unsigned u32x4;
typedef __attribute__((ext_vector_type(2))) unsigned u32x2;
typedef __attribute__((ext_vector_type(8))) unsigned u32x8;
typedef __attribute__((ext_vector_type(16))) unsigned u32x16;
typedef __attribute__((ext_vector_type(4))) float f32x4;
typedef __attribute__((ext_vector_type(2))) float f32x2;
typedef __attribute__((ext_vector_type(2))) double f64x2;
typedef __attribute__((ext_vector_type(16))) float f32x16;
typedef __attribute__((ext_vector_type(8))) __bf16 bf16x8;
typedef __attribute__((ext_vector_type(2))) __bf16 bf16x2;
typedef __attribute__((ext_vector_type(4))) short s16x4;
typedef unsigned long long u64;
typedef __amdgpu_buffer_rsrc_t rsrc_t;
#define LAS __attribute__((address_space(3)))
#define GAS __attribute__((address_space(1)))

#define CPF $$$
#define PAT "5c10p2ba43"
constexpr int WTH = 5;
constexpr int DCH = 2;
constexpr int NXA = 8, NXB = 8;
constexpr int NREL = 24;

constexpr int AUX_NT = 2, AUX_SC1 = 16, OOB = 0x40000000;
constexpr float SCALE = 0.08838834764831845f;
constexpr float LOG2E = 1.4426950408889634f;
constexpr unsigned SPIN_CAP = 1u << 17;
constexpr int NSLOT = 7, NUNIT = 256;

constexpr int WS_EPOCH = 0;
constexpr int CSTR    = 4352;
constexpr int WS_CNT  = 8192;
constexpr int WS_QB   = WS_CNT + 32 * CSTR;
constexpr int WS_QABS = WS_QB + 32 * 2048;
constexpr int WS_PML  = WS_QABS + 4 * 32 * 2048;
constexpr int WS_PO   = WS_PML + 32 * 32 * 16;
constexpr int WS_RS   = WS_PO + 32 * 32 * 1024;
constexpr int WS_Y    = WS_RS + 32 * 16 * 1024;
constexpr int WS_YB   = WS_Y + 4 * 8 * 32 * 512;
constexpr int WS_END  = WS_YB + 4 * 8 * 4 * 512;

constexpr int LDS_BYTES = 163840;
constexpr int LDS_SCR = 131072;
constexpr int SCR_SUM = 5120, SUM_STR = 1040;

__device__ __forceinline__ unsigned cvtpk(float lo, float hi) {
  f32x2 v = {lo, hi};
  return __builtin_bit_cast(unsigned, __builtin_convertvector(v, bf16x2));
}
__device__ __forceinline__ float bf_lo(unsigned u) { return __uint_as_float(u << 16); }
__device__ __forceinline__ float bf_hi(unsigned u) { return __uint_as_float(u & 0xffff0000u); }
__device__ __forceinline__ float rbf(float x) { return __uint_as_float(cvtpk(x, x) << 16); }
__device__ __forceinline__ float dot2(unsigned k, unsigned q, float acc) {
  return __builtin_amdgcn_fdot2_f32_bf16(__builtin_bit_cast(bf16x2, k), __builtin_bit_cast(bf16x2, q), acc, false);
}
template <int CTRL> __device__ __forceinline__ float dpp(float x) {
  return __builtin_bit_cast(float, __builtin_amdgcn_mov_dpp(__builtin_bit_cast(int, x), CTRL, 0xf, 0xf, true));
}
template <int CTRL> __device__ __forceinline__ unsigned dppu(unsigned x) {
  return (unsigned)__builtin_amdgcn_mov_dpp((int)x, CTRL, 0xf, 0xf, true);
}
constexpr int XOR1 = 0xB1, XOR2 = 0x4E, XOR7 = 0x141, XOR8 = 0x128, ROR4 = 0x124;
__device__ __forceinline__ float xrow_max(float x) {
  auto s = __builtin_amdgcn_permlane16_swap(__float_as_uint(x), __float_as_uint(x), false, false);
  x = fmaxf(__uint_as_float(s[0]), __uint_as_float(s[1]));
  auto t = __builtin_amdgcn_permlane32_swap(__float_as_uint(x), __float_as_uint(x), false, false);
  return fmaxf(__uint_as_float(t[0]), __uint_as_float(t[1]));
}
__device__ __forceinline__ float xrow_sum(float x) {
  auto s = __builtin_amdgcn_permlane16_swap(__float_as_uint(x), __float_as_uint(x), false, false);
  x = __uint_as_float(s[0]) + __uint_as_float(s[1]);
  auto t = __builtin_amdgcn_permlane32_swap(__float_as_uint(x), __float_as_uint(x), false, false);
  return __uint_as_float(t[0]) + __uint_as_float(t[1]);
}
__device__ __forceinline__ float row_max(float x) {
  x = fmaxf(x, dpp<XOR8>(x)); x = fmaxf(x, dpp<ROR4>(x)); x = fmaxf(x, dpp<XOR2>(x)); x = fmaxf(x, dpp<XOR1>(x));
  return x;
}
__device__ __forceinline__ float row_sum(float x) {
  x += dpp<XOR8>(x); x += dpp<ROR4>(x); x += dpp<XOR2>(x); x += dpp<XOR1>(x);
  return x;
}
__device__ __forceinline__ float fexp2(float x) { return __builtin_amdgcn_exp2f(x); }
__device__ __forceinline__ f32x4 mfma16(u32x4 a, u32x4 b, f32x4 c) {
  return __builtin_amdgcn_mfma_f32_16x16x32_bf16(__builtin_bit_cast(bf16x8, a), __builtin_bit_cast(bf16x8, b), c, 0, 0, 0);
}
__device__ __forceinline__ f32x16 mfma32(u32x4 a, u32x4 b, f32x16 c) {
  return __builtin_amdgcn_mfma_f32_32x32x16_bf16(__builtin_bit_cast(bf16x8, a), __builtin_bit_cast(bf16x8, b), c, 0, 0, 0);
}
__device__ __forceinline__ rsrc_t mkrs(const GAS char* p, unsigned bytes) {
  return __builtin_amdgcn_make_buffer_rsrc((void*)p, 0, bytes, 0x00020000);
}
__device__ __forceinline__ void bar() { asm volatile("s_waitcnt lgkmcnt(0)\n\ts_barrier" ::: "memory"); }
__device__ __forceinline__ void drain() { asm volatile("s_waitcnt vmcnt(0)" ::: "memory"); }
#define SB() __builtin_amdgcn_sched_barrier(0)
template <int N> __device__ __forceinline__ void vmwait() { asm volatile("s_waitcnt vmcnt(%0)" :: "n"(N) : "memory"); }
__device__ __forceinline__ unsigned swz(unsigned row) { return ((row & 3) << 2) | ((row >> 2) & 2); }
__device__ __forceinline__ u32x2 trread(LAS char* p) {
  return __builtin_bit_cast(u32x2, __builtin_amdgcn_ds_read_tr16_b64_v4i16((LAS s16x4*)p));
}

struct Tag { unsigned tl, th, lo, rb; u64 e; };
__device__ __forceinline__ Tag mktag(u64 e0) {
  Tag t;
  asm volatile("" : "+s"(e0));
  t.e = e0 * 6364136223846793005ull + 1442695040888963407ull;
  t.th = (unsigned)(t.e >> 32);
  t.lo = (unsigned)t.e;
  t.tl = t.lo & 0xff000000u;
  t.rb = ((t.lo >> 9) & 255u) | (~t.lo & 256u);
  return t;
}
__device__ __forceinline__ unsigned gpack(float v, const Tag& t) { return ((__float_as_uint(v) + 0x80u) >> 8) | t.tl; }
__device__ __forceinline__ u32x4 gpair(float a, float b, const Tag& t) { return u32x4{gpack(a, t), t.th, gpack(b, t), t.th}; }
__device__ __forceinline__ bool gok(u32x4 g, const Tag& t) {
  return ((((g[0] ^ t.tl) | (g[2] ^ t.tl)) >> 24) | (g[1] ^ t.th) | (g[3] ^ t.th)) == 0u;
}
__device__ __forceinline__ float gval(unsigned lo) { return __uint_as_float(lo << 8); }
__device__ __forceinline__ bool qok(u32x4 g, const Tag& t) { return ((g[1] ^ t.th) | (g[3] ^ t.th)) == 0u; }
__device__ __forceinline__ double mkd(unsigned lo, unsigned hi) { return __builtin_bit_cast(double, u32x2{lo, hi}); }
__device__ __forceinline__ u32x4 rpack(double a, double b, double l, float r, const Tag& t) {
  const u32x2 ua = __builtin_bit_cast(u32x2, a), ub = __builtin_bit_cast(u32x2, b), ul = __builtin_bit_cast(u32x2, l);
  const u64 ml = ((u64)(unsigned)__builtin_amdgcn_readfirstlane(ul[1]) << 32) | (unsigned)__builtin_amdgcn_readfirstlane(ul[0]);
  const u64 mr = (unsigned)__builtin_amdgcn_readfirstlane(__float_as_uint(r));
  const unsigned ta = t.lo & 511u, tb = t.rb;
  return u32x4{(ua[0] & ~1023u) | (__builtin_amdgcn_inverse_ballot_w64(ml) ? ta | 512u : ta), ua[1],
               (ub[0] & ~1023u) | (__builtin_amdgcn_inverse_ballot_w64(mr) ? tb | 512u : tb), ub[1]};
}
__device__ __forceinline__ u32x4 rpack0(f64x2 o, unsigned lw, int lane, unsigned ta, unsigned tb) {
  const u32x4 u = __builtin_bit_cast(u32x4, o);
  return u32x4{(u[0] & ~1023u) | (ta | (__builtin_amdgcn_ubfe(lw, lane, 1) << 9)), u[1], (u[2] & ~1023u) | tb, u[3]};
}
__device__ __forceinline__ bool rok(u32x4 g, const Tag& t) { return (((g[0] ^ t.lo) | (g[2] ^ t.rb)) & 511u) == 0u; }
constexpr unsigned PMASK = 0x00030003u;
__device__ __forceinline__ unsigned ptag(const Tag& t, int k) {
  const unsigned n = k == 0 ? t.lo : k == 3 ? t.lo >> 10 : ((t.lo >> (1 + 3 * k)) & 7u) | (~t.lo & 8u);
  return (n & 3u) | ((n & 12u) << 14);
}
__device__ __forceinline__ unsigned ltag(const Tag& t) { return ((t.lo >> 16) & 127u) | (~t.lo & 128u); }
__device__ __forceinline__ bool mok(u32x4 g, const Tag& t) { return (((g[0] ^ t.lo) & 0xffffu) | ((g[1] ^ ltag(t)) & 255u)) == 0u; }
__device__ __forceinline__ unsigned bfi(unsigned m, unsigned a, unsigned b) { return (a & m) | (b & ~m); }
__device__ __forceinline__ double rec_a(u32x4 g) { return mkd(g[0] & ~1023u, g[1]); }
__device__ __forceinline__ double rec_b(u32x4 g) { return mkd(g[2] & ~1023u, g[3]); }
__device__ __forceinline__ double rec_l(u32x4 g) { return __builtin_bit_cast(double, __builtin_amdgcn_ballot_w64((g[0] & 512u) != 0u)); }
__device__ __forceinline__ float rec_r(u32x4 g) { return __uint_as_float((unsigned)__builtin_amdgcn_ballot_w64((g[2] & 512u) != 0u)); }
__device__ __forceinline__ int edif(float a, float b) {
  int r;
  asm("v_cvt_i32_f32 %0, %1" : "=v"(r) : "v"(a - b));
  return r;
}
__device__ __forceinline__ unsigned p2hi(float a, float b) { return (unsigned)max(edif(a, b) + 1023, 0) << 20; }
__device__ __forceinline__ float bucket(float m) { return fmaf(__builtin_rintf(m * (1.f / 512.f)), 512.f, 0.f); }
__device__ __forceinline__ float rfl(float x) { return __uint_as_float(__builtin_amdgcn_readfirstlane(__float_as_uint(x))); }
__device__ __forceinline__ unsigned rdu(unsigned v, int l) { return __builtin_amdgcn_readlane(v, l); }

__device__ __forceinline__ void diag_gemv(const u32x4 (&wr)[16], const u32x4 (&xp)[2][2], const unsigned (&mask)[4], f32x4 (&acc)[2]) {
#pragma unroll
  for (int i = 0; i < 16; i++) {
#pragma unroll
    for (int s = 0; s < 2; s++) {
      const unsigned src = xp[s][i >> 3][(i >> 1) & 3];
      const unsigned dup = __builtin_amdgcn_perm(src, src, (i & 1) ? 0x03020302u : 0x01000100u);
      const u32x4 a = {dup & mask[0], dup & mask[1], dup & mask[2], dup & mask[3]};
      acc[s] = mfma16(a, wr[i], acc[s]);
    }
  }
}

constexpr int bitpos(char c) { for (int i = 0; i < 10; i++) if (PAT[i] == c) return 10 + i; return 31; }
constexpr int goff(int t) { int o = 0; for (int j = 0; j < 6; j++) o |= ((t >> j) & 1) << bitpos('0' + j); return o; }
struct Acc { double o[2]; double l; float R; };
__device__ __forceinline__ u32x4 ldnt(rsrc_t r, int voff, int so) { return __builtin_amdgcn_raw_buffer_load_b128(r, voff, so, AUX_NT); }
__device__ __forceinline__ u32x4 ldslot(rsrc_t r, int voff, int so, int i) { return ldnt(r, voff + goff(i & 3), so ^ goff(i & ~3)); }
__device__ __forceinline__ u32x4 ldunit(rsrc_t r, int voff, int so, int i) { return ldnt(r, voff + i * 1024, so); }
__device__ __forceinline__ u32x4 ldsc(rsrc_t r, int voff, int so) { return __builtin_amdgcn_raw_buffer_load_b128(r, voff, so, AUX_SC1); }
__device__ __forceinline__ void stsc(u32x4 v, rsrc_t r, int voff, int so) { __builtin_amdgcn_raw_buffer_store_b128(v, r, voff + so, 0, AUX_SC1); }
__device__ __forceinline__ float dot8(u32x4 k, u32x4 q) { return dot2(k[3], q[3], dot2(k[2], q[2], dot2(k[1], q[1], dot2(k[0], q[0], 0.f)))); }
#define TR_STEP(HALF, CTRL, BIT) _Pragma("unroll") for (int h = 0; h < (HALF); ++h) { \
    const float t1 = x[h] + dpp<CTRL>(x[h]), t2 = x[h + (HALF)] + dpp<CTRL>(x[h + (HALF)]); x[h] = (c & (BIT)) ? t2 : t1; }
template <int N> __device__ __forceinline__ float k_step(u32x4 (&ring)[16], u32x4 q, int c, rsrc_t rn, int voff, int so, bool ext, LAS char* xp) {
  float x[16];
#pragma unroll
  for (int i = 0; i < 16; i++) {
    x[i] = dot8(ring[i], q);
    asm volatile("s_waitcnt vmcnt(%1)" : "+v"(x[i]) : "n"(WTH - 1));
    if (i >= 16 - N && ext) ring[i] = *(volatile LAS u32x4*)(xp + (i - (16 - N)) * 1024);
    else ring[i] = ldslot(rn, voff, so, i);
    SB();
  }
  TR_STEP(8, XOR8, 8) TR_STEP(4, XOR7, 4) TR_STEP(2, XOR2, 2) TR_STEP(1, XOR1, 1)
  return rbf(rbf(x[0]) * SCALE);
}
template <int BC> __device__ __forceinline__ void pv(u32x4 v, unsigned pk, const unsigned (&mask)[4], f32x4& acc) {
  const unsigned d = dppu<0x150 + BC>(pk);
  acc = mfma16(u32x4{d & mask[0], d & mask[1], d & mask[2], d & mask[3]}, v, acc);
}
template <int I, int MODE> __device__ __forceinline__ void v_step(u32x4 (&ring)[16], unsigned pk, const unsigned (&mask)[4], f32x4& acc,
                                                                  rsrc_t rk, rsrc_t rv, int voff, int so) {
  pv<I>(ring[I], pk, mask, acc);
  if (MODE == 1 || (MODE == 2 && I < 8)) {
    asm volatile("s_waitcnt vmcnt(%1)" : "+v"(acc) : "n"(WTH - 1));
    ring[I] = MODE == 1 ? ldslot(rv, voff, so, I) : ldunit(I < 4 ? rk : rv, voff, so, I & 3);
  } else {
    asm volatile("" : "+v"(acc));
  }
  SB();
  if constexpr (I < 15) v_step<I + 1, MODE>(ring, pk, mask, acc, rk, rv, voff, so);
}
__device__ __forceinline__ float exp_ref(float mx, float& f) {
  const float M = __builtin_ceilf(mx * LOG2E);
  f = __builtin_amdgcn_fmed3f(fmaf(mx, LOG2E, -M), -1.f, 0.f);
  return M;
}
__device__ __forceinline__ unsigned lds_inc(LAS unsigned* p, int lane) {
  unsigned t = 0, z = 0;
  asm volatile("" : "+v"(z));
  if (lane == 0) t = __hip_atomic_fetch_add(p + z, 1u, __ATOMIC_RELAXED, __HIP_MEMORY_SCOPE_WORKGROUP);
  return t;
}
struct Take { unsigned unit, pend; bool fetch; };
__device__ __forceinline__ void lds_add(LAS unsigned* p, unsigned v) { __hip_atomic_fetch_add(p, v, __ATOMIC_RELAXED, __HIP_MEMORY_SCOPE_WORKGROUP); }
__device__ __forceinline__ Take take(LAS unsigned* ctl, unsigned kv, rsrc_t rws, int cntoff, int lane) {
  const unsigned k = __builtin_amdgcn_readfirstlane(kv);
  unsigned b, s;
  auto look = [&]() {
    const unsigned vs = ((volatile LAS unsigned*)ctl)[5], vb = ((volatile LAS unsigned*)ctl)[8 + (k >> 2)];
    s = __builtin_amdgcn_readfirstlane(vs); b = __builtin_amdgcn_readfirstlane(vb);
    return (b == ~0u) & ((s < 0x10000u) | ((s & 0xffffu) != 0u));
  };
  if (__builtin_expect(look(), 0))
    for (unsigned spins = 0; spins < SPIN_CAP * 16; spins++) {
      __builtin_amdgcn_s_sleep(1);
      if (!look()) break;
    }
  Take t;
  t.unit = min(b, (unsigned)NUNIT) + (k & 3);
  t.fetch = ((k & 3) == 0) & (b < NUNIT) & (s < 0x10000u);
  int off = OOB;
  if (t.fetch) {
    unsigned z = 0;
    asm volatile("" : "+v"(z));
    if (lane == 0) { lds_add(ctl + 5 + z, 1u); off = cntoff; }
    asm volatile("s_waitcnt lgkmcnt(0)" ::: "memory");
  }
  t.pend = __builtin_amdgcn_raw_ptr_buffer_atomic_add_i32(4, rws, off, 0, AUX_SC1);
  SB();
  return t;
}
__device__ __forceinline__ void give(LAS unsigned* ctl, const Take& t, int lane) {
  if (t.fetch && lane == 0) {
    unsigned z = 0;
    asm volatile("" : "+v"(z));
    if (t.pend < NUNIT) {
      const unsigned i = __hip_atomic_fetch_add(ctl + 4 + z, 1u, __ATOMIC_RELAXED, __HIP_MEMORY_SCOPE_WORKGROUP);
      ((volatile LAS unsigned*)ctl)[8 + i] = t.pend;
    }
    asm volatile("" ::: "memory");
    lds_add(ctl + 5 + z, t.pend >= NUNIT - 4 ? 0xffffu : ~0u);
  }
}
__device__ __forceinline__ int unit_off(unsigned t) { return (int)min(t, (unsigned)NUNIT) * 4096; }

__device__ __forceinline__ void b_units(u32x4 (&ra)[16], rsrc_t rk, rsrc_t rv, unsigned cur, u32x4 q, int lane, const unsigned (&mask)[4],
                                        LAS unsigned* ctl, rsrc_t rws, int cntoff, Acc& A) {
  const int c = lane & 15, voff = lane * 16;
  float Mr = -INFINITY;
  double s1 = 0.0;
  A.o[0] = A.o[1] = A.l = 0.0; A.R = cur < NUNIT ? -INFINITY : 0.f;
#pragma unroll 1
  while (cur < NUNIT) {
    const Take t = take(ctl, lds_inc(ctl, lane), rws, cntoff, lane);
    const int so = unit_off(t.unit);
    SB();
    float x[4];
#pragma unroll
    for (int i = 0; i < 4; i++) x[i] = dot8(ra[i], q);
    asm volatile("" : "+v"(x[0]), "+v"(x[1]), "+v"(x[2]), "+v"(x[3]));
    SB();
#pragma unroll
    for (int i = 0; i < 4; i++) {
      ra[i] = ldunit(rk, voff, so, i);
      SB();
    }
    TR_STEP(2, XOR8, 8) TR_STEP(1, XOR7, 4)
    x[0] += dpp<XOR2>(x[0]); x[0] += dpp<XOR1>(x[0]);
    const float s = rbf(rbf(x[0]) * SCALE);
    const float n = __builtin_rintf(s * LOG2E);
    float e = fexp2(__builtin_amdgcn_fmed3f(fmaf(s, LOG2E, -n), -1.f, 1.f)), d = n - Mr;
    asm volatile("" : "+v"(e));
    if (__builtin_expect(__any(d > 57.f), 0)) {
      float mx = fmaxf(n, dpp<XOR8>(n));
      mx = xrow_max(fmaxf(mx, dpp<ROR4>(mx)));
      const float Rn = rfl(fmaxf(A.R, bucket(mx)));
      const double s0 = mkd(0u, p2hi(A.R, Rn));
      A.o[0] *= s0; A.o[1] *= s0; A.l *= s0;
      A.R = Rn; Mr = mx; d = n - mx;
      s1 = mkd(0u, p2hi(mx, Rn));
    }
    const float p = __builtin_ldexpf(e, edif(d, 0.f));
    unsigned pk = cvtpk(p, p);
    asm volatile("" : "+v"(pk));
    SB();
    f32x4 acc = {0.f, 0.f, 0.f, 0.f};
    float ls = bf_lo(pk);
    pv<0>(ra[4], pk, mask, acc);  asm volatile("" : "+v"(acc)); ra[4] = ldunit(rv, voff, so, 0); ls += dpp<XOR8>(ls); asm volatile("" : "+v"(ls)); SB();
    pv<4>(ra[5], pk, mask, acc);  asm volatile("" : "+v"(acc)); ra[5] = ldunit(rv, voff, so, 1); ls += dpp<ROR4>(ls); asm volatile("" : "+v"(ls)); SB();
    pv<8>(ra[6], pk, mask, acc);  asm volatile("" : "+v"(acc)); ra[6] = ldunit(rv, voff, so, 2); SB();
    pv<12>(ra[7], pk, mask, acc); asm volatile("" : "+v"(acc)); ra[7] = ldunit(rv, voff, so, 3); SB();
    A.l = __builtin_fma((double)xrow_sum(ls), s1, A.l);
    asm volatile("" : "+v"(A.l));
    A.o[0] = __builtin_fma((double)(lane < 32 ? acc[0] : acc[2]), s1, A.o[0]);
    A.o[1] = __builtin_fma((double)(lane < 32 ? acc[1] : acc[3]), s1, A.o[1]);
    asm volatile("" : "+v"(A.o[0]), "+v"(A.o[1]));
    SB();
    cur = t.unit;
    if (cur >= NUNIT) break;
    give(ctl, t, lane);
  }
}
#undef TR_STEP
__device__ __forceinline__ int rec_col(int lane) { return 8 * (lane & 15) + 4 * ((lane >> 4) & 1) + 2 * (lane >> 5); }
struct Mrg { unsigned s0, s4, s8, ta, tb; int off; };
__device__ __forceinline__ Mrg mkmrg(LAS char* sum, int lane, int roff, const Tag& tg) {
  Mrg m = {(unsigned)(uintptr_t)sum, (unsigned)(uintptr_t)sum + (lane >> 5) * 4, (unsigned)(uintptr_t)sum + lane * 8, tg.lo & 511u, tg.rb, roff + lane * 16};
  asm volatile("" : "+v"(m.s0), "+v"(m.s4), "+v"(m.s8), "+v"(m.ta), "+v"(m.tb), "+v"(m.off));
  return m;
}
__device__ __forceinline__ void b_merge(const Mrg& m, LAS char* mrg, int w, int lane, const Acc& A, rsrc_t rws, u64 e0) {
  unsigned inc = 1;
  double a = A.o[0], b = A.o[1], l = A.l;
  if (__builtin_expect(__float_as_uint(A.R) != 0u, 0)) {
    *(volatile LAS f64x2*)(mrg + w * 1024 + lane * 16) = f64x2{a, b};
    *(volatile LAS double*)(mrg + 8192 + w * 8) = l;
    *(volatile LAS float*)(mrg + 8256 + w * 4) = A.R;
    a = b = l = 0.0;
    inc = 1u + (256u << w);
  }
  f64x2 o;
  unsigned n, lw;
  asm volatile("ds_add_f64 %3, %5\n\tds_add_f64 %3, %6 offset:512\n\ts_mov_b64 exec, 1\n\tds_add_f64 %3, %7 offset:1024\n\tds_add_u32 %3, %8 offset:1032\n\t"
               "s_mov_b64 exec, -1\n\tds_read_b32 %1, %9 offset:1032\n\tds_read_b32 %2, %4 offset:1024\n\tds_read2st64_b64 %0, %3 offset1:1\n\ts_waitcnt lgkmcnt(0)"
               : "=&v"(o), "=&v"(n), "=&v"(lw)
               : "v"(m.s8), "v"(m.s4), "v"(a), "v"(b), "v"(l), "v"(inc), "v"(m.s0)
               : "memory");
  stsc(rpack0(o, lw, lane, m.ta, m.tb), rws, n == 8u ? m.off : OOB, 0);
  SB();
  n = __builtin_amdgcn_readfirstlane(n);
  if (__builtin_expect(n > 8u && (n & 255u) == 8u, 0)) {
    double L = *(volatile LAS double*)(uintptr_t)(m.s0 + 1024);
    float R = L != 0.0 ? 0.f : -INFINITY;
#pragma unroll 1
    for (int i = 0; i < 8; i++)
      if (n >> (8 + i) & 1) {
        const f64x2 oi = *(volatile LAS f64x2*)(mrg + i * 1024 + lane * 16);
        const float Ri = *(volatile LAS float*)(mrg + 8256 + i * 4), Rn = fmaxf(R, Ri);
        const double s0 = mkd(0u, p2hi(R, Rn)), s1 = mkd(0u, p2hi(Ri, Rn));
        o[0] = __builtin_fma(oi[0], s1, o[0] * s0); o[1] = __builtin_fma(oi[1], s1, o[1] * s0);
        L = __builtin_fma(*(volatile LAS double*)(mrg + 8192 + i * 8), s1, L * s0);
        R = Rn;
      }
    stsc(rpack(o[0], o[1], L, R, mktag(e0)), rws, m.off, 0);
  }
}

__device__ __forceinline__ unsigned fz(unsigned row) { return ((row & 2) << 1) | ((row >> 1) & 2); }
__device__ __forceinline__ void dma1(rsrc_t rs, int voff, int so, unsigned ldsaddr) {
  unsigned keep;
  asm volatile("s_mov_b32 %0, m0\n\ts_mov_b32 m0, %1\n\ts_nop 0\n\tbuffer_load_dwordx4 %2, %3, %4 offen nt lds\n\ts_mov_b32 m0, %0"
               : "=&s"(keep) : "s"(ldsaddr), "v"(voff), "s"(rs), "s"(so) : "memory");
}
template <int NR>
__device__ __forceinline__ void dma_rows(rsrc_t rs, int srcoff, LAS char* img, int r0, int lane) {
#pragma unroll
  for (int u = 0; u < NR; u++) {
    const int r = r0 + u;
    unsigned keep;
    asm volatile("s_mov_b32 %0, m0\n\ts_mov_b32 m0, %1\n\ts_nop 0\n\tbuffer_load_dwordx4 %2, %3, %4 offen nt lds\n\ts_mov_b32 m0, %0"
                 : "=&s"(keep) : "s"((unsigned)(uintptr_t)img + r * 1024), "v"((lane ^ swz(r)) * 16), "s"(rs), "s"(srcoff + r * 1024) : "memory");
  }
  SB();
}

__device__ __forceinline__ void wo_frags(LAS char* img, int w, int lane, u32x4 (&wf)[4][4]) {
  const int g = lane >> 4, q = (lane & 15) >> 2, p = lane & 3;
#pragma unroll
  for (int ks = 0; ks < 4; ks++)
#pragma unroll
    for (int tl = 0; tl < 4; tl++) {
      const int ch = 8 * w + 2 * tl + (p >> 1);
      const int r0 = 32 * ks + 8 * g + q, r1 = r0 + 4;
      const u32x2 a = trread(img + r0 * 1024 + ((ch ^ swz(r0)) * 16) + 8 * (p & 1));
      const u32x2 b = trread(img + r1 * 1024 + ((ch ^ swz(r1)) * 16) + 8 * (p & 1));
      wf[tl][ks] = u32x4{a[0], a[1], b[0], b[1]};
    }
}
__device__ __forceinline__ f32x4 wo_calc(const u32x4 (&wf)[4][4], LAS char* cb, int lane) {
  const int n = lane & 15, grp = lane >> 4;
  u32x4 bf[4];
#pragma unroll
  for (int ks = 0; ks < 4; ks++) bf[ks] = *(LAS u32x4*)(cb + (n & 3) * 256 + ks * 64 + grp * 16);
  f32x4 acc[4];
#pragma unroll
  for (int tl = 0; tl < 4; tl++) acc[tl] = f32x4{0.f, 0.f, 0.f, 0.f};
#pragma unroll
  for (int ks = 0; ks < 4; ks++)
#pragma unroll
    for (int tl = 0; tl < 4; tl++) acc[tl] = mfma16(wf[tl][ks], bf[ks], acc[tl]);
  const f32x4 lo = (n & 4) ? acc[1] : acc[0], hi = (n & 4) ? acc[3] : acc[2];
  return (n & 8) ? hi : lo;
}
__device__ __forceinline__ void y_store(f32x4 y, int w, int lane, int slot, rsrc_t rws, int yoff, const Tag& tg) {
  const int off = yoff + (((lane & 3) * 8 + w) * 32 + slot) * 512 + ((lane & 12) + (lane >> 4)) * 16;
  stsc(gpair(y[0], y[1], tg), rws, off, 0);
  stsc(gpair(y[2], y[3], tg), rws, off, 256);
}
template <int K, int N> __device__ __forceinline__ unsigned bc1(const unsigned (&p)[N]) {
  unsigned v = dppu<0x150 + K>(p[0]);
  if constexpr (N > 1) {
    v = (unsigned)__builtin_amdgcn_update_dpp((int)v, (int)p[1], 0x150 + K, 0xf, 2, false);
    v = (unsigned)__builtin_amdgcn_update_dpp((int)v, (int)p[2], 0x150 + K, 0xf, 4, false);
  }
  return v;
}
template <int K, int N> __device__ __forceinline__ u32x4 bc4(const unsigned (&p)[N]) { return u32x4{bc1<K>(p), bc1<K + 1>(p), bc1<K + 2>(p), bc1<K + 3>(p)}; }
template <int N> __device__ __forceinline__ f32x4 proj(const unsigned (&pr)[N], const u32x4 (&wf)[4][4], int lane) {
  const u32x4 bf[4] = {bc4<0>(pr), bc4<4>(pr), bc4<8>(pr), bc4<12>(pr)};
  f32x4 acc[4];
#pragma unroll
  for (int tl = 0; tl < 4; tl++) acc[tl] = f32x4{0.f, 0.f, 0.f, 0.f};
#pragma unroll
  for (int ks = 0; ks < 4; ks++)
#pragma unroll
    for (int tl = 0; tl < 4; tl++) acc[tl] = mfma16(wf[tl][ks], bf[ks], acc[tl]);
  const f32x4 lo = (lane & 1) ? acc[1] : acc[0], hi = (lane & 1) ? acc[3] : acc[2];
  f32x4 r = (lane & 2) ? hi : lo;
  asm volatile("" : "+v"(r));
  return r;
}
__device__ __forceinline__ unsigned arrive(LAS unsigned* p, int lane) {
  unsigned z = 0;
  asm volatile("" : "+v"(z) :: "memory");
  if (lane == 0) lds_add(p + z, 1u);
  asm volatile("" ::: "memory");
  return *(volatile LAS unsigned*)p;
}
template <int N> __device__ __forceinline__ f32x2 rows_sum(LAS char* p) {
  f32x2 v[N];
#pragma unroll
  for (int i = 0; i < N; i++) v[i] = *(volatile LAS f32x2*)(p + i * 256);
#pragma unroll
  for (int st = 1; st < N; st *= 2)
#pragma unroll
    for (int i = 0; i + st < N; i += 2 * st) v[i] += v[i + st];
  return v[0];
}
__device__ __forceinline__ float half_sum(float x) {
  auto t = __builtin_amdgcn_permlane32_swap(__float_as_uint(x), __float_as_uint(x), false, false);
  return __uint_as_float(t[0]) + __uint_as_float(t[1]);
}

__global__ __launch_bounds__(512, 2) void k_all(const char* a0_, const char* a1_, const char* a2_, const char* a3_, const char* a4_, const char* a5_,
                                                const char* a6_, const char* a7_, const char* a8_, const char* a9_, const char* a10_, char* a11_, unsigned* a12_) {
  u32x16 ka; u32x8 kb_; u32x2 kc_;
  asm volatile("s_load_dwordx16 %0, %3, 0x0\n\ts_load_dwordx8 %1, %3, 0x40\n\ts_load_dwordx2 %2, %3, 0x60"
               : "=&s"(ka), "=&s"(kb_), "=&s"(kc_) : "s"(__builtin_amdgcn_kernarg_segment_ptr()));
  __shared__ __attribute__((aligned(16))) char smem[LDS_BYTES];
  LAS char* lds = (LAS char*)smem;
  LAS char* scr = lds + LDS_SCR;
  LAS unsigned* ctl = (LAS unsigned*)scr;
  const int tid = threadIdx.x, lane = tid & 63, w = __builtin_amdgcn_readfirstlane(tid >> 6);
  const int blk = blockIdx.x, head = blk >> 3, prod = (blk + head) & 7;
  const int sub = (0x54372106u >> (4 * prod)) & 7;
  const unsigned tst = (unsigned)__builtin_amdgcn_s_memrealtime();
  unsigned cpf = 0;
  {
    u64 pc;
    asm volatile("s_getpc_b64 %0" : "=s"(pc));
    cpf = __builtin_amdgcn_raw_buffer_load_b32(mkrs((const GAS char*)(pc & ~255ull), CPF), w == 7 ? head * 2048 + lane * 32 : OOB, 0, 0);
    SB();
  }
  {
    unsigned c = 16 + 4 * (DCH * sub + tid);
    asm volatile("" : "+v"(c));
    c = tid < 8 + DCH ? c : ~0u;
    c = tid < 8 ? 0u : c;
    ctl[tid] = tid == 4 ? DCH : c;
    *(LAS u64*)(scr + SCR_SUM + min(tid, 2 * SUM_STR / 8 - 1) * 8) = 0;
  }
  const int l16 = lane & 15, grp = lane >> 4;
  const int b4 = head & 3, cntoff = WS_CNT + head * CSTR;
  const int bm = (b4 + 1 + (sub >= 3 ? sub - 3 : sub)) & 3;
  const size_t base = (size_t)(bm * 32 + head) << 20;
  const int so = ((w & 1) << bitpos('a')) | (((w >> 1) & 1) << bitpos('b')) | ((w >> 2) << bitpos('c')) | (sub >= 3 ? 1 << bitpos('p') : 0);
  int v0 = l16 * 16 + grp * (sub < 6 ? 256 : 131072) + (sub < 6 ? so : w * 524288 + head * 256);
  unsigned boff = sub < 6 ? (unsigned)base : 0u, bsz = sub < 6 ? 1u << 20 : 4u << 20;
  int s0[WTH];
#pragma unroll
  for (int i = 0; i < WTH; i++) {
    s0[i] = sub < 6 ? goff(i) : i * 8192;
    asm volatile("" : "+s"(s0[i]));
  }
  asm volatile("" : "+v"(v0), "+s"(boff), "+s"(bsz));
  SB();
  asm volatile("s_waitcnt lgkmcnt(0)" : "+s"(ka), "+s"(kb_), "+s"(kc_));
  auto ptr = [](unsigned lo, unsigned hi) { return (GAS char*)(((u64)hi << 32) | lo); };
  GAS char *const xA = ptr(ka[0], ka[1]), *const ckv = ptr(ka[2], ka[3]), *const wqA = ptr(ka[4], ka[5]), *const wuk = ptr(ka[6], ka[7]),
           *const wuv = ptr(ka[8], ka[9]), *const woA = ptr(ka[10], ka[11]), *const xB = ptr(ka[12], ka[13]), *const kc = ptr(ka[14], ka[15]),
           *const vc = ptr(kb_[0], kb_[1]), *const wqB = ptr(kb_[2], kb_[3]), *const woB = ptr(kb_[4], kb_[5]), *const ws = ptr(kb_[6], kb_[7]),
           *const out = ptr(kc_[0], kc_[1]);
  const u64 e0 = *(const __attribute__((address_space(4))) u64*)(u64)(ws + WS_EPOCH);
  const rsrc_t r0 = mkrs((sub < 6 ? kc : sub == 6 ? wqB : wqA) + boff, bsz);
  SB();
  u32x4 ra[16];
#pragma unroll
  for (int i = 0; i < WTH; i++) ra[i] = ldnt(r0, v0, s0[i]);
  SB();
  u32x4 xp[2][2];
  if (sub >= 6) {
    const GAS char* x = sub == 6 ? xB : xA;
#pragma unroll
    for (int s = 0; s < 2; s++) {
      const GAS u32x4* xs = (const GAS u32x4*)(x + (2 * s + (l16 >> 3)) * 1024 + (64 * w + 16 * grp) * 2);
      xp[s][0] = xs[0]; xp[s][1] = xs[1];
    }
#pragma unroll
    for (int i = WTH; i < 16; i++) ra[i] = ldnt(r0, v0, i * 8192);
    asm volatile("" ::: "memory");
    SB();
  }
  const rsrc_t rws = mkrs(ws, WS_END);
  __builtin_amdgcn_raw_buffer_store_b32(tst, mkrs(ws + (8 << 20), 4096), tid == 0 ? blk * 4 : OOB, 0, AUX_SC1);
  unsigned mask[4];
#pragma unroll
  for (int v = 0; v < 4; v++) mask[v] = (((lane & 7) >> 1) == v) ? ((lane & 1) ? 0xffff0000u : 0x0000ffffu) : 0u;
  const size_t base4 = (size_t)(b4 * 32 + head) << 20;
  const rsrc_t rk4 = mkrs(kc + base4, 1u << 20), rv4 = mkrs(vc + base4, 1u << 20);
  const int voff = lane * 16;
  LAS char* qst = scr + 1024 + w * 512;
  const int qo = WS_QB + head * 2048 + (lane < 32 ? bm : b4) * 512 + (lane & 31) * 16;
  auto qwait = [&](u32x4 g, const Tag& tg) {
    if (!__all(qok(g, tg))) {
      for (unsigned spins = 0;; spins++) {
        asm volatile("" ::: "memory");
        g = ldsc(rws, qo, 0);
        if (__all(qok(g, tg)) || spins > SPIN_CAP) break;
        __builtin_amdgcn_s_sleep(2);
      }
    }
    *(LAS u32x2*)(qst + lane * 8) = u32x2{g[0], g[2]};
  };
  unsigned cur;

  if (__builtin_expect(sub < 6, 1)) {
    const rsrc_t rv = mkrs(vc + base, 1u << 20);
    u32x4 rb[16], g;
#pragma unroll
    for (int i = WTH; i < 32 + NXA + NXB; i++) {
      if (i == WTH) asm volatile("s_barrier" ::: "memory");
      vmwait<WTH - 1>();
      if (i == 32 + NXA + NXB - WTH) asm volatile("buffer_load_dwordx4 %0, %1, %2, 0 offen sc1" : "=&v"(g) : "v"(qo), "s"(rws));
      if (i < 32) {
        const u32x4 v = ldslot(r0, voff, so, i);
        if (i < 16) ra[i] = v; else rb[i - 16] = v;
      } else {
        const int j = i < 32 + NXA ? i + 16 - NXA : i + 32 - NXA - NXB;
        dma1(r0, voff + goff(j & 3), so ^ goff(j & ~3), (unsigned)(uintptr_t)lds + w * 16384 + (i - 32) * 1024);
      }
      SB();
    }
    asm volatile("s_waitcnt vmcnt(%1)" : "+v"(g) : "n"(WTH), "v"(cpf));
    qwait(g, mktag(e0));
    const u32x4 q = *(LAS u32x4*)(qst + l16 * 16);
    float sc[4] = {0.f, 0.f, 0.f, 0.f};
#pragma unroll 1
    for (int it = 0; it < 2; it++) {
      const rsrc_t rn = it ? rv : r0;
      sc[2] = sc[0]; sc[3] = sc[1];
      sc[1] = k_step<NXA>(ra, q, l16, rn, voff, so ^ (it ? goff(48) : goff(32)), it == 0, lds + w * 16384 + voff);
      sc[0] = k_step<NXB>(rb, q, l16, rn, voff, so ^ (it ? goff(32) : goff(48)), it == 0, lds + w * 16384 + NXA * 1024 + voff);
    }
    const float mx = xrow_max(row_max(fmaxf(fmaxf(sc[0], sc[1]), fmaxf(sc[2], sc[3]))));
    float f, l = 0.f;
    const float M = exp_ref(mx, f);
    unsigned pk[4];
#pragma unroll
    for (int k = 0; k < 4; k++) {
      const float p = fexp2(fmaf(sc[k] - mx, LOG2E, f));
      pk[k] = cvtpk(p, p);
      l += bf_lo(pk[k]);
    }
    l = xrow_sum(row_sum(l));
    cur = 8 * sub + w;
    f32x4 acc = {0.f, 0.f, 0.f, 0.f};
    v_step<0, 1>(ra, pk[0], mask, acc, r0, rv, voff, so ^ goff(16));
    v_step<0, 1>(rb, pk[1], mask, acc, r0, rv, voff, so);
    v_step<0, 2>(ra, pk[2], mask, acc, rk4, rv4, voff, cur * 4096);
    v_step<0, 0>(rb, pk[3], mask, acc, r0, rv, voff, 0);
    Acc A;
    A.R = rfl(bucket(M));
    const double sm = mkd(0u, p2hi(M, A.R));
    A.o[0] = (lane < 32 ? acc[0] : acc[2]) * sm; A.o[1] = (lane < 32 ? acc[1] : acc[3]) * sm;
    A.l = l * sm;
    b_merge(mkmrg(scr + SCR_SUM, lane, WS_RS + (head * 16 + sub) * 1024, mktag(e0)), scr + 8192, w, lane, A, rws, e0);
  } else {
  {
    __builtin_amdgcn_raw_buffer_store_b32(48 + 24 * DCH, rws, (sub == 6 && tid == 0) ? cntoff : OOB, 0, AUX_SC1);
    {
      const rsrc_t rk = mkrs(wuk, sub == 7 ? 4u << 20 : 0u);
#pragma unroll
      for (int i = 0; i < 16; i++)
        __builtin_amdgcn_raw_ptr_buffer_load_lds(rk, (LAS void*)(lds + w * 16384 + i * 1024), 16,
                                                 (64 * w + grp) * 8192 + head * 256 + ((l16 ^ (4 * (i & 3) + grp)) * 16), i * 4 * 8192, 0, AUX_NT);
    }
    SB();
    f32x4 acc[2] = {{0.f, 0.f, 0.f, 0.f}, {0.f, 0.f, 0.f, 0.f}};
    diag_gemv(ra, xp, mask, acc);
    LAS float* red = (LAS float*)(scr + 8192);
#pragma unroll
    for (int s = 0; s < 2; s++) *(LAS f32x4*)(red + w * 512 + (2 * s + (grp >> 1)) * 128 + 8 * l16 + 4 * (grp & 1)) = acc[s];
    asm volatile("" :: "v"(cpf));
    __builtin_amdgcn_s_waitcnt(0x0f70);
    drain();
    bar();
    if (tid < 128) {
      f32x4 a = {0.f, 0.f, 0.f, 0.f};
#pragma unroll
      for (int k = 0; k < 8; k++) a += *(LAS f32x4*)(red + k * 512 + 4 * tid);
      const unsigned p0 = cvtpk(a[0], a[1]), p1 = cvtpk(a[2], a[3]);
      const Tag tg = mktag(e0);
      stsc(u32x4{p0, tg.th, p1, tg.th}, rws, sub == 6 ? WS_QB + head * 2048 + tid * 16 : OOB, 0);
      *(LAS u32x2*)(scr + 1024 + tid * 8) = u32x2{p0, p1};
    }
  }

  if (sub == 6) {
    const int aid = ((head & 6) << 2) | ((head & 1) << 2) | (head >> 3), b = aid >> 3;
    const rsrc_t rc = mkrs(ckv + (size_t)aid * 524288, 524288);
    const int dvo = (lane >> 3) * 1024 + w * 128 + (((lane & 7) ^ fz(lane >> 3)) * 16);
    const unsigned dlo = (unsigned)(uintptr_t)lds + w * 8192;
    const int xt = (aid & 7) << 16;
#pragma unroll
    for (int j = 0; j < 16; j++) dma1(rc, dvo, (j * 8192) ^ xt, dlo + (j >> 3) * 65536 + (j & 7) * 1024);
    SB();
    const Tag tg = mktag(e0);
    bar();
    {
      u32x4 t[8];
      unsigned miss = 15;
      __builtin_amdgcn_s_sleep(90);
      for (unsigned spins = 0;; spins++) {
        asm volatile("" ::: "memory");
#pragma unroll
        for (int i = 0; i < 4; i++)
          if (miss >> i & 1) {
            t[2 * i] = ldsc(rws, WS_QABS + (b * 32 + w) * 2048 + lane * 16, i * 16384);
            t[2 * i + 1] = ldsc(rws, WS_QABS + (b * 32 + w) * 2048 + lane * 16 + 1024, i * 16384);
          }
        SB();
#pragma unroll
        for (int i = 0; i < 4; i++) if (__all(qok(t[2 * i], tg) & qok(t[2 * i + 1], tg))) miss &= ~(1u << i);
        if (!miss || spins > SPIN_CAP) break;
        __builtin_amdgcn_s_sleep(4);
      }
#pragma unroll
      for (int i = 0; i < 8; i++) {
        const int row = 8 * (i >> 1) + w;
        *(LAS u32x2*)(scr + row * 1024 + (((32 * (i & 1) + (lane >> 1)) ^ swz(row)) * 16) + (lane & 1) * 8) = u32x2{t[i][0], t[i][2]};
      }
    }
    bar();
    const int kb = w & 3, hb = w >> 2;
    u32x4 qf[16];
    {
      const int row = 16 * hb + l16;
#pragma unroll
      for (int ks = 0; ks < 16; ks++) qf[ks] = *(LAS u32x4*)(scr + row * 1024 + (((4 * ks + grp) ^ swz(row)) * 16));
    }
    LAS float* mx = (LAS float*)scr;
    LAS char* pl = scr + 1024;
    LAS float* lr = (LAS float*)(scr + 8192);
    f32x16 o0, o1;
#pragma unroll
    for (int j = 0; j < 16; j++) { o0[j] = 0.f; o1[j] = 0.f; }
    float m1 = -INFINITY, m2 = -INFINITY, l1 = 0.f;
    const int h1 = 16 * hb + l16, h2 = lane & 31, hh = lane >> 5;
    const int arow = 16 * kb + l16;
    const int tq = (lane & 15) >> 2, tp = lane & 3, tblk = (lane >> 4) & 1;
    const int qa0 = arow * 128 + ((grp ^ fz(arow)) * 16), qa1 = qa0 ^ 64;
    const int pr = 8 * hh + tq, pc = 2 * tblk + (tp >> 1);
    const int pa0 = w * 8192 + pr * 128 + ((pc ^ fz(pr)) * 16) + 8 * (tp & 1), pa1 = w * 8192 + (pr + 4) * 128 + ((pc ^ fz(pr + 4)) * 16) + 8 * (tp & 1);
#pragma unroll 1
    for (int t = 0; t < 8; t++) {
      if (t < 7) asm volatile("s_waitcnt vmcnt(8)" ::: "memory"); else asm volatile("s_waitcnt vmcnt(0)" ::: "memory");
      bar();
      LAS char* img = lds + (t & 1) * 65536;
      f32x4 s0 = {0.f, 0.f, 0.f, 0.f}, s1 = s0;
      u32x4 ka[16];
      u32x2 ta[4][4];
      const int nso = t < 6 ? ((t + 2) * 65536) ^ xt : OOB;
#pragma unroll
      for (int ks = 0; ks < 16; ks++) ka[ks] = *(LAS u32x4*)(img + ((ks & 1) ? qa1 : qa0) + (ks >> 1) * 8192);
      SB();
#pragma unroll
      for (int k2 = 0; k2 < 8; k2++) {
        s0 = mfma16(ka[2 * k2], qf[2 * k2], s0);
        s1 = mfma16(ka[2 * k2 + 1], qf[2 * k2 + 1], s1);
      }
      asm volatile("" : "+v"(s0), "+v"(s1));
      SB();
#pragma unroll
      for (int ks = 0; ks < 4; ks++) {
        ta[ks][0] = trread(img + pa0 + ks * 2048); ta[ks][1] = trread(img + pa1 + ks * 2048);
        ta[ks][2] = trread(img + (pa0 ^ 64) + ks * 2048); ta[ks][3] = trread(img + (pa1 ^ 64) + ks * 2048);
      }
      SB();
      float x[4];
#pragma unroll
      for (int j = 0; j < 4; j++) x[j] = rbf(rbf(s0[j] + s1[j]) * SCALE);
      const float lm = xrow_max(fmaxf(fmaxf(x[0], x[1]), fmaxf(x[2], x[3])));
      if (grp == 0) mx[h1 * 4 + kb] = lm;
      LAS u32x2* pw = (LAS u32x2*)(pl + h1 * 144 + (16 * kb + 4 * grp) * 2);
      u32x2 pp = {cvtpk(fexp2((x[0] - m1) * LOG2E), fexp2((x[1] - m1) * LOG2E)), cvtpk(fexp2((x[2] - m1) * LOG2E), fexp2((x[3] - m1) * LOG2E))};
      *pw = pp;
      bar();
      const f32x4 ma = *(LAS f32x4*)(mx + h1 * 4), mb = *(LAS f32x4*)(mx + h2 * 4);
      u32x4 pb[4];
#pragma unroll
      for (int ks = 0; ks < 4; ks++) pb[ks] = *(LAS u32x4*)(pl + h2 * 144 + (16 * ks + 8 * hh) * 2);
      const float n1 = fmaxf(m1, fmaxf(fmaxf(ma[0], ma[1]), fmaxf(ma[2], ma[3])));
      const float n2 = fmaxf(m2, fmaxf(fmaxf(mb[0], mb[1]), fmaxf(mb[2], mb[3])));
      float al1 = fexp2((m1 - n1) * LOG2E), al2 = fexp2((m2 - n2) * LOG2E);
      asm volatile("" : "+v"(pb[0]), "+v"(pb[1]), "+v"(pb[2]), "+v"(pb[3]));
      if (__any(n2 - m2 > 40.f)) {
        bar();
        pp = u32x2{cvtpk(fexp2((x[0] - n1) * LOG2E), fexp2((x[1] - n1) * LOG2E)), cvtpk(fexp2((x[2] - n1) * LOG2E), fexp2((x[3] - n1) * LOG2E))};
        *pw = pp;
        l1 *= al1;
#pragma unroll
        for (int j = 0; j < 16; j++) { o0[j] *= al2; o1[j] *= al2; }
        al1 = al2 = 1.f;
        bar();
#pragma unroll
        for (int ks = 0; ks < 4; ks++) pb[ks] = *(LAS u32x4*)(pl + h2 * 144 + (16 * ks + 8 * hh) * 2);
      }
      m1 = n1; m2 = n2;
#pragma unroll
      for (int ks = 0; ks < 4; ks++) {
        o0 = mfma32(u32x4{ta[ks][0][0], ta[ks][0][1], ta[ks][1][0], ta[ks][1][1]}, pb[ks], o0);
        o1 = mfma32(u32x4{ta[ks][2][0], ta[ks][2][1], ta[ks][3][0], ta[ks][3][1]}, pb[ks], o1);
        SB();
        if (t < 6) {
          dma1(rc, dvo, nso + 2 * ks * 8192, dlo + (t & 1) * 65536 + 2 * ks * 1024);
          dma1(rc, dvo, nso + (2 * ks + 1) * 8192, dlo + (t & 1) * 65536 + (2 * ks + 1) * 1024);
        }
        SB();
      }
      l1 = (l1 + ((bf_lo(pp[0]) + bf_hi(pp[0])) + (bf_lo(pp[1]) + bf_hi(pp[1])))) * al1;
#pragma unroll
      for (int j = 0; j < 16; j++) { o0[j] *= al2; o1[j] *= al2; }
    }
    bar();
    {
      float am = 0.f;
#pragma unroll
      for (int j = 0; j < 16; j++) am = fmaxf(am, fmaxf(fabsf(o0[j]), fabsf(o1[j])));
      auto sw = __builtin_amdgcn_permlane32_swap(__float_as_uint(am), __float_as_uint(am), false, false);
      am = fmaxf(__uint_as_float(sw[0]), __uint_as_float(sw[1]));
      const int e = max(__builtin_amdgcn_frexp_expf(am), -127);
      LAS char* tp = lds + h2 * 1040 + (64 * w + 4 * hh) * 2;
      auto pkn = [&](float a, float b) { return __builtin_bit_cast(unsigned, __builtin_amdgcn_cvt_pknorm_i16(__builtin_ldexpf(a, -e), __builtin_ldexpf(b, -e))); };
#pragma unroll
      for (int j4 = 0; j4 < 4; j4++) {
        *(LAS u32x2*)(tp + j4 * 16) = u32x2{pkn(o0[4 * j4], o0[4 * j4 + 1]), pkn(o0[4 * j4 + 2], o0[4 * j4 + 3])};
        *(LAS u32x2*)(tp + 64 + j4 * 16) = u32x2{pkn(o1[4 * j4], o1[4 * j4 + 1]), pkn(o1[4 * j4 + 2], o1[4 * j4 + 3])};
      }
      if (hh == 0) *(LAS unsigned char*)(scr + 9216 + h2 * 8 + w) = (unsigned char)(e + 127);
      const float ls = xrow_sum(l1);
      if (grp == 0) lr[kb * 32 + h1] = ls;
      bar();
#pragma unroll
      for (int i = 0; i < 4; i++) {
        u32x4 v = *(LAS u32x4*)(lds + (4 * w + i) * 1040 + lane * 16);
#pragma unroll
        for (int k = 0; k < 4; k++) v[k] = bfi(PMASK, ptag(tg, k), v[k]);
        stsc(v, rws, WS_PO + (aid * 32 + 4 * w) * 1024 + lane * 16, i * 1024);
      }
      if (tid < 32) {
        const u32x2 eb = *(LAS u32x2*)(scr + 9216 + tid * 8);
        const float l = (lr[tid] + lr[32 + tid]) + (lr[64 + tid] + lr[96 + tid]);
        stsc(u32x4{bfi(0xffffu, tg.lo, __float_as_uint(m2)), bfi(255u, ltag(tg), __float_as_uint(l)), eb[0], eb[1]}, rws, WS_PML + (aid * 32 + tid) * 16, 0);
      }
      if (w == 0) {
        const unsigned t = __builtin_amdgcn_readfirstlane(__builtin_amdgcn_raw_ptr_buffer_atomic_add_i32(8 + 4 * DCH, rws, lane == 0 ? cntoff : OOB, 0, AUX_SC1));
        const unsigned bs = t + 8 + 4 * (lane - 8);
        ctl[lane] = lane == 3 ? t : lane == 4 ? DCH : lane == 5 ? (t + 8 + 4 * DCH >= NUNIT ? 0x10000u : 0u) : lane < 8 ? 0u : (lane < 8 + DCH && bs < NUNIT) ? bs : ~0u;
        if (lane < 32) ctl[64 + lane] = ~0u;
      }
      *(LAS u64*)(scr + SCR_SUM + SUM_STR + min(tid, SUM_STR / 8 - 1) * 8) = 0;
      bar();
    }
    const u32x4 g = ldsc(rws, qo, 0);
    cur = __builtin_amdgcn_readfirstlane(ctl[3]) + w;
#pragma unroll
    for (int i = 0; i < 8; i++) ra[i] = ldunit(i < 4 ? rk4 : rv4, voff, unit_off(cur), i & 3);
    SB();
    qwait(g, mktag(e0));
  } else {
    const Tag tg = mktag(e0);
    bar();
    {
      u32x4 bf[4];
#pragma unroll
      for (int ks = 0; ks < 4; ks++) bf[ks] = *(LAS u32x4*)(scr + 1024 + (l16 & 3) * 256 + ks * 64 + grp * 16);
      LAS char* tt = scr + 2048 + w * 512;
#pragma unroll
      for (int rt = 0; rt < 4; rt++) {
        f32x4 acc = {0.f, 0.f, 0.f, 0.f};
#pragma unroll
        for (int ks = 0; ks < 4; ks++) acc = mfma16(*(LAS u32x4*)(lds + (64 * w + 16 * rt + l16) * 256 + (((4 * ks + grp) ^ l16) * 16)), bf[ks], acc);
        if (l16 < 4) *(LAS u32x2*)(tt + l16 * 128 + (16 * rt + 4 * grp) * 2) = u32x2{cvtpk(acc[0], acc[1]), cvtpk(acc[2], acc[3])};
      }
      asm volatile("s_waitcnt lgkmcnt(0)" ::: "memory");
      const u32x2 v = *(LAS u32x2*)(tt + lane * 8);
      stsc(u32x4{v[0], tg.th, v[1], tg.th}, rws, WS_QABS + (grp * 32 + head) * 2048 + w * 256 + l16 * 16, 0);
    }
    bar();
    u32x4 wf[4][4], wr[16];
    {
      const rsrc_t ro = mkrs(woA + (size_t)head * 131072, 131072);
      dma_rows<16>(ro, 0, lds, 16 * w, lane);
      const rsrc_t rv = mkrs(wuv, 4u << 20);
      const int voff = (64 * w + 16 * grp) * 8192 + head * 256 + l16 * 16;
#pragma unroll
      for (int i = 0; i < 16; i++) wr[i] = ldnt(rv, voff, i * 8192);
      SB();
      asm volatile("s_waitcnt vmcnt(16)" ::: "memory");
      bar();
      wo_frags(lds, w, lane, wf);
      bar();
      const rsrc_t rb = mkrs(woB, 4u << 20);
      const int bvo = (lane >> 3) * 1024 + (((lane & 7) ^ ((lane >> 5) << 1)) * 16);
#pragma unroll
      for (int i = 0; i < 16; i++)
        dma1(rb, (i & 1) ? bvo ^ 64 : bvo, (4 * w + b4) * 131072 + (head >> 2) * 128 + i * 8192, (unsigned)(uintptr_t)lds + w * 16384 + i * 1024);
      SB();
      if (tid < 8) ((LAS unsigned*)(scr + 24576))[tid] = 0u;
    }
    const int hw = 4 * w + b4, cc = head >> 2, roff = WS_RS + hw * 16384 + lane * 16;
    u32x4 g[6];
#pragma unroll
    for (int s = 0; s < 6; s++) asm volatile("" : "=v"(g[s]));
    if (w == 0) {
      for (unsigned spins = 0;; spins++) {
        asm volatile("" ::: "memory");
        const u32x4 v = ldsc(rws, WS_PML + ((lane & 31) * 32 + head) * 16, 0);
        if (__builtin_popcount((unsigned)__builtin_amdgcn_ballot_w64(mok(v, tg))) >= NREL || spins > SPIN_CAP) break;
        __builtin_amdgcn_s_sleep(16);
      }
    }
    bar();
    {
      const int b = w >> 1, c4 = tid & 127;
      const unsigned ta = (lane & 1) ? ptag(tg, 2) : ptag(tg, 0), tb = (lane & 1) ? ptag(tg, 3) : ptag(tg, 1);
      u32x4 ml;
      u32x2 po[8];
#pragma unroll
      for (int s = 0; s < 8; s++) asm volatile("" : "=v"(po[s]));
      unsigned missP = 255;
      for (unsigned spins = 0;; spins++) {
        asm volatile("" ::: "memory");
        ml = ldsc(rws, WS_PML + ((8 * b + (lane & 7)) * 32 + head) * 16, 0);
#pragma unroll
        for (int s = 0; s < 8; s++)
          if (missP >> s & 1) po[s] = __builtin_amdgcn_raw_buffer_load_b64(rws, WS_PO + (8 * b * 32 + head) * 1024 + c4 * 8, s * 32768, AUX_SC1);
        SB();
#pragma unroll
        for (int s = 0; s < 8; s++) if (__all((((po[s][0] ^ ta) | (po[s][1] ^ tb)) & PMASK) == 0u)) missP &= ~(1u << s);
        if ((!missP && __all(mok(ml, tg))) || spins > SPIN_CAP) break;
        __builtin_amdgcn_s_sleep(2);
      }
#pragma unroll
      for (int s = 0; s < 6; s++) g[s] = ldsc(rws, roff, s * 1024);
      SB();
      ml[0] &= 0xffff0000u; ml[1] &= ~255u;
#pragma unroll
      for (int s = 0; s < 8; s++) po[s] = u32x2{bfi(PMASK, 0x00020002u, po[s][0]), bfi(PMASK, 0x00020002u, po[s][1])};
      float M = __uint_as_float(ml[0]);
      M = fmaxf(M, dpp<XOR1>(M)); M = fmaxf(M, dpp<XOR2>(M)); M = fmaxf(M, dpp<XOR7>(M));
      const float f = fexp2((__uint_as_float(ml[0]) - M) * LOG2E);
      float L = f * __uint_as_float(ml[1]);
      L += dpp<XOR1>(L); L += dpp<XOR2>(L); L += dpp<XOR7>(L);
      const float gl = __builtin_ldexpf(f, (int)((((w & 1) ? ml[3] : ml[2]) >> (8 * grp)) & 255) - 142);
      const float gs[8] = {dpp<0x150>(gl), dpp<0x151>(gl), dpp<0x152>(gl), dpp<0x153>(gl), dpp<0x154>(gl), dpp<0x155>(gl), dpp<0x156>(gl), dpp<0x157>(gl)};
      f32x4 a = {0.f, 0.f, 0.f, 0.f};
#pragma unroll
      for (int s = 0; s < 8; s++)
        a += gs[s] * f32x4{(float)(short)po[s][0], (float)((int)po[s][0] >> 16), (float)(short)po[s][1], (float)((int)po[s][1] >> 16)};
      const float inv = (32768.f / 32767.f) / L;
      const u32x2 v = {cvtpk(a[0] * inv, a[1] * inv), cvtpk(a[2] * inv, a[3] * inv)};
      *(LAS u32x2*)(scr + b * 1024 + c4 * 8) = v;
    }
    bar();
    {
      u32x4 xp[2][2];
#pragma unroll
      for (int s = 0; s < 2; s++) {
        LAS u32x4* xs = (LAS u32x4*)(scr + (2 * s + (l16 >> 3)) * 1024 + (64 * w + 16 * grp) * 2);
        xp[s][0] = xs[0]; xp[s][1] = xs[1];
      }
      f32x4 acc[2] = {{0.f, 0.f, 0.f, 0.f}, {0.f, 0.f, 0.f, 0.f}};
      diag_gemv(wr, xp, mask, acc);
      LAS float* red = (LAS float*)(scr + 8192);
#pragma unroll
      for (int s = 0; s < 2; s++) *(LAS f32x4*)(red + w * 512 + (2 * s + (grp >> 1)) * 128 + 8 * l16 + 4 * (grp & 1)) = acc[s];
      bar();
      if (tid < 256) {
        float a = 0.f, b = 0.f;
#pragma unroll
        for (int k = 0; k < 8; k++) { const f32x2 v = *(LAS f32x2*)(red + k * 512 + 2 * tid); a += v[0]; b += v[1]; }
        ((LAS unsigned*)(scr + 4096))[tid] = cvtpk(a, b);
      }
      bar();
    }
    const f32x4 yA = wo_calc(wf, scr + 4096, lane);
    drain();
    y_store(yA, w, lane, head, rws, WS_Y, tg);
    {
      const int q = l16 >> 2;
      LAS char* img = lds + w * 16384 + (8 * (q >> 1) + 4 * (grp & 1) + 2 * (grp >> 1) + (q & 1)) * 128 + 8 * (lane & 3);
      const int fl = (grp & 1) | (q & 2);
#pragma unroll
      for (int ks = 0; ks < 4; ks++)
#pragma unroll
        for (int tl = 0; tl < 4; tl++) {
          const u32x2 a = trread(img + (tl ^ fl) * 32 + ks * 4096), b = trread(img + (tl ^ fl) * 32 + ks * 4096 + 2048);
          wf[tl][ks] = u32x4{a[0], a[1], b[0], b[1]};
        }
    }
    const rsrc_t rout = mkrs(out, 8192);
    LAS unsigned* cnt = (LAS unsigned*)(scr + 24576);
    LAS char* const re = scr + 8192;
    LAS char* const rA = scr + 8192 + 8192;
    LAS char* rl = scr + 8192 + 10240 + (lane & 31) * 8;
    LAS char* rw = scr + 8192 + 10240 + w * 256 + (lane & 3) * 64 + grp * 16;
    LAS char* rx = rl + (w < 3 ? 8 + w : 11) * 256;
    int oo = lane < 32 ? b4 * 2048 + 1024 + cc * 128 + lane * 4 : OOB, oe = head == 0 && lane == 0 ? WS_EPOCH : OOB;
    asm volatile("" : "+v"(rl), "+v"(rw), "+v"(rx), "+v"(oo), "+v"(oe), "+v"(cnt));
    {
      const int ofa = WS_Y + (b4 * 8 + cc) * 16384 + w * 2048 + lane * 16;
      u32x4 x0, x1;
      for (unsigned spins = 0;; spins++) {
        asm volatile("" ::: "memory");
        x0 = ldsc(rws, ofa, 0); x1 = ldsc(rws, ofa + 1024, 0);
        if (__all(gok(x0, tg) & gok(x1, tg)) || spins > SPIN_CAP) break;
        __builtin_amdgcn_s_sleep(4);
      }
      const f32x2 r = {half_sum(gval(x0[0]) + gval(x1[0])), half_sum(gval(x0[2]) + gval(x1[2]))};
      if (lane < 32) *(volatile LAS f32x2*)(rA + w * 256 + lane * 8) = r;
      const unsigned n = arrive(cnt + 1, lane);
      const f32x2 v = rows_sum<8>(rA + (lane & 31) * 8);
      __builtin_amdgcn_raw_buffer_store_b32(cvtpk(v[0], v[1]), rout, lane < 32 && n == 8u ? b4 * 2048 + cc * 128 + l16 * 8 + grp * 4 : OOB, 0, OAUX);
    }
    {
      for (unsigned spins = 0;; spins++) {
        unsigned bad = 0;
#pragma unroll
        for (int s = 0; s < 6; s++) bad |= (g[s][0] ^ tg.lo) | (g[s][2] ^ tg.rb);
        if (__all((bad & (spins > SPIN_CAP * 4 ? 0u : 511u)) == 0u)) break;
        asm volatile("" ::: "memory");
#pragma unroll
        for (int s = 0; s < 6; s++) g[s] = ldsc(rws, roff + s * 1024, 0);
        SB();
      }
      unsigned pr[3];
#pragma unroll
      for (int t = 0; t < 3; t++) {
        const u32x4 g0 = g[t], g1 = g[t + 3];
        const float R0 = rec_r(g0), R1 = rec_r(g1), Rm = fmaxf(R0, R1);
        const double s0 = mkd(0u, p2hi(R0, Rm)), s1 = mkd(0u, p2hi(R1, Rm));
        const double inv = __builtin_amdgcn_rcp(__builtin_fma(rec_l(g0), s0, rec_l(g1) * s1));
        pr[t] = cvtpk((float)(__builtin_fma(rec_a(g0), s0, rec_a(g1) * s1) * inv), (float)(__builtin_fma(rec_b(g0), s0, rec_b(g1) * s1) * inv));
      }
      *(volatile LAS f32x4*)(re + ((l16 >> 2) * 8 + w) * 256 + (lane & 3) * 64 + grp * 16) = proj(pr, wf, lane);
      const unsigned n = arrive(cnt, lane);
#pragma unroll
      for (int u = 0; u < 2; u++) {
        const int t = u ? 2 : lane >> 5;
        const f32x2 v = rows_sum<8>(re + t * 2048 + (lane & 31) * 8);
        stsc(gpair(v[0], v[1], tg), rws, n != 8u || (u && lane >= 32) ? OOB : WS_YB + ((((b4 + 1 + t) & 3) * 8 + cc) * 4 + b4) * 512 + (lane & 31) * 16, 0);
      }
    }
    const int ofb = w < 3 ? WS_YB + ((b4 * 8 + cc) * 4 + ((b4 + 1 + w) & 3)) * 512 + (lane & 31) * 16 : OOB;
    u32x4 xb, p[NSLOT];
    unsigned bad;
    for (unsigned spins = 0;; spins++) {
      asm volatile("" ::: "memory");
      xb = ldsc(rws, ofb, 0);
#pragma unroll
      for (int s = 0; s < NSLOT; s++) p[s] = ldsc(rws, roff + 8192 + s * 1024, 0);
      unsigned m = spins > SPIN_CAP * 4 ? 0u : 511u;
      asm volatile("" : "+s"(m));
      SB();
      unsigned ax = min((((xb[0] ^ tg.tl) | (xb[2] ^ tg.tl)) >> 24) | (xb[1] ^ tg.th) | (xb[3] ^ tg.th), w < 3 ? 1u : 0u), az = 0;
#pragma unroll
      for (int s = 0; s < NSLOT; s++) { ax |= p[s][0] ^ tg.lo; az |= p[s][2] ^ tg.rb; }
      bad = az & 512u;
      if (__all(((ax | az) & m) == 0u)) break;
    }
    *(volatile LAS f32x2*)rx = f32x2{gval(xb[0]), gval(xb[2])};
    double S0, S1, L;
    if (__builtin_expect(__all(bad == 0u), 1)) {
      S0 = mkd(p[0][0], p[0][1]); S1 = mkd(p[0][2], p[0][3]); L = rec_l(p[0]);
#pragma unroll
      for (int s = 1; s < NSLOT; s++) { S0 += mkd(p[s][0], p[s][1]); S1 += mkd(p[s][2], p[s][3]); L += rec_l(p[s]); }
    } else {
      float R = -INFINITY;
      S0 = S1 = L = 0.0;
#pragma unroll
      for (int s = 0; s < NSLOT; s++) {
        const float Rs = rec_l(p[s]) != 0.0 ? rec_r(p[s]) : -INFINITY, Rn = fmaxf(R, Rs);
        const double s0 = mkd(0u, p2hi(R, Rn)), s1 = mkd(0u, p2hi(Rs, Rn));
        L = __builtin_fma(rec_l(p[s]), s1, L * s0);
        S0 = __builtin_fma(rec_a(p[s]), s1, S0 * s0); S1 = __builtin_fma(rec_b(p[s]), s1, S1 * s0);
        R = Rn;
      }
    }
    {
      const double inv = __builtin_amdgcn_rcp(L);
      const unsigned pr[1] = {cvtpk((float)(S0 * inv), (float)(S1 * inv))};
      const f32x4 y = proj(pr, wf, lane);
      if ((lane & 12) == 0) *(volatile LAS f32x4*)rw = y;
      {
        const unsigned now = (unsigned)__builtin_amdgcn_s_memrealtime();
        const u32x4 t4 = __builtin_amdgcn_raw_buffer_load_b128(mkrs(ws + (8 << 20), 4096), lane * 16, 0, AUX_SC1);
        int mn = 1 << 30, mx = -(1 << 30);
        for (int i = 0; i < 4; i++) { const int d = (int)(now - t4[i]); mn = min(mn, d); mx = max(mx, d); }
        for (int o = 1; o < 64; o *= 2) { mn = min(mn, __shfl_xor(mn, o)); mx = max(mx, __shfl_xor(mx, o)); }
        int ex = 6000 - mx;
        if (PROBE == 3) asm volatile("buffer_wbl2 sc1\n\ts_waitcnt vmcnt(0)" ::: "memory");
        ex = min(max(ex, 0), 20000);
        ex = __builtin_amdgcn_readfirstlane(ex);
        while ((int)((unsigned)__builtin_amdgcn_s_memrealtime() - now) < ex) __builtin_amdgcn_s_sleep(2);
      }
      const unsigned n = arrive(cnt + 2, lane);
      const f32x2 v = rows_sum<11>(rl);
      __builtin_amdgcn_raw_buffer_store_b32(cvtpk(v[0], v[1]), rout, n == 8u ? oo : OOB, 0, OAUXB);
      __builtin_amdgcn_raw_buffer_store_b64(__builtin_bit_cast(u32x2, tg.e), rws, n == 8u ? oe : OOB, 0, AUX_SC1);
    }
    return;
  }
  }
  {
    const u32x4 q4 = *(LAS u32x4*)(qst + 256 + l16 * 16);
    const Mrg mg = mkmrg(scr + SCR_SUM + SUM_STR, lane, WS_RS + (head * 16 + 8 + sub) * 1024, mktag(e0));
    Acc A;
    b_units(ra, rk4, rv4, cur, q4, lane, mask, ctl, rws, cntoff, A);
    b_merge(mg, scr + 8192 + 8704, w, lane, A, rws, e0);
    SB();
    asm volatile("" :: "v"(ra[0]), "v"(ra[1]), "v"(ra[2]), "v"(ra[3]), "v"(ra[4]), "v"(ra[5]), "v"(ra[6]), "v"(ra[7]));
  }
}

extern "C" void kernel_launch(void* const* d_in, const int* in_sizes, int n_in, void* d_out, int out_size, void* d_ws, size_t ws_size,
                              hipStream_t stream) {
  k_all<<<256, 512, 0, stream>>>((const char*)d_in[0], (const char*)d_in[1], (const char*)d_in[2], (const char*)d_in[3], (const char*)d_in[4],
                                 (const char*)d_in[5], (const char*)d_in[6], (const char*)d_in[7], (const char*)d_in[8], (const char*)d_in[9],
                                 (const char*)d_in[10], (char*)d_ws, (unsigned*)d_out);
}
```

```cpp
#define PROBE 4
#define OAUX 16
#define TI 5
#include <hip/hip_runtime.h>
#include <stdint.h>

typedef __attribute__((ext_vector_type(4))) unsigned u32x4;
typedef __attribute__((ext_vector_type(2))) unsigned u32x2;
typedef __attribute__((ext_vector_type(8))) unsigned u32x8;
typedef __attribute__((ext_vector_type(16))) unsigned u32x16;
typedef __attribute__((ext_vector_type(4))) float f32x4;
typedef __attribute__((ext_vector_type(2))) float f32x2;
typedef __attribute__((ext_vector_type(2))) double f64x2;
typedef __attribute__((ext_vector_type(16))) float f32x16;
typedef __attribute__((ext_vector_type(8))) __bf16 bf16x8;
typedef __attribute__((ext_vector_type(2))) __bf16 bf16x2;
typedef __attribute__((ext_vector_type(4))) short s16x4;
typedef unsigned long long u64;
typedef __amdgpu_buffer_rsrc_t rsrc_t;
#define LAS __attribute__((address_space(3)))
#define GAS __attribute__((address_space(1)))

#define CPF $$$
#define PAT "5c10p2ba43"
constexpr int WTH = 5;
constexpr int DCH = 2;
constexpr int NXA = 8, NXB = 8;
constexpr int NREL = 24;

constexpr int AUX_NT = 2, AUX_SC1 = 16, OOB = 0x40000000;
constexpr float SCALE = 0.08838834764831845f;
constexpr float LOG2E = 1.4426950408889634f;
constexpr unsigned SPIN_CAP = 1u << 17;
constexpr int NSLOT = 7, NUNIT = 256;

constexpr int WS_EPOCH = 0;
constexpr int CSTR    = 4352;
constexpr int WS_CNT  = 8192;
constexpr int WS_QB   = WS_CNT + 32 * CSTR;
constexpr int WS_QABS = WS_QB + 32 * 2048;
constexpr int WS_PML  = WS_QABS + 4 * 32 * 2048;
constexpr int WS_PO   = WS_PML + 32 * 32 * 16;
constexpr int WS_RS   = WS_PO + 32 * 32 * 1024;
constexpr int WS_Y    = WS_RS + 32 * 16 * 1024;
constexpr int WS_YB   = WS_Y + 4 * 8 * 32 * 512;
constexpr int WS_END  = WS_YB + 4 * 8 * 4 * 512;

constexpr int LDS_BYTES = 163840;
constexpr int LDS_SCR = 131072;
constexpr int SCR_SUM = 5120, SUM_STR = 1040;

__device__ __forceinline__ unsigned cvtpk(float lo, float hi) {
  f32x2 v = {lo, hi};
  return __builtin_bit_cast(unsigned, __builtin_convertvector(v, bf16x2));
}
__device__ __forceinline__ float bf_lo(unsigned u) { return __uint_as_float(u << 16); }
__device__ __forceinline__ float bf_hi(unsigned u) { return __uint_as_float(u & 0xffff0000u); }
__device__ __forceinline__ float rbf(float x) { return __uint_as_float(cvtpk(x, x) << 16); }
__device__ __forceinline__ float dot2(unsigned k, unsigned q, float acc) {
  return __builtin_amdgcn_fdot2_f32_bf16(__builtin_bit_cast(bf16x2, k), __builtin_bit_cast(bf16x2, q), acc, false);
}
template <int CTRL> __device__ __forceinline__ float dpp(float x) {
  return __builtin_bit_cast(float, __builtin_amdgcn_mov_dpp(__builtin_bit_cast(int, x), CTRL, 0xf, 0xf, true));
}
template <int CTRL> __device__ __forceinline__ unsigned dppu(unsigned x) {
  return (unsigned)__builtin_amdgcn_mov_dpp((int)x, CTRL, 0xf, 0xf, true);
}
constexpr int XOR1 = 0xB1, XOR2 = 0x4E, XOR7 = 0x141, XOR8 = 0x128, ROR4 = 0x124;
__device__ __forceinline__ float xrow_max(float x) {
  auto s = __builtin_amdgcn_permlane16_swap(__float_as_uint(x), __float_as_uint(x), false, false);
  x = fmaxf(__uint_as_float(s[0]), __uint_as_float(s[1]));
  auto t = __builtin_amdgcn_permlane32_swap(__float_as_uint(x), __float_as_uint(x), false, false);
  return fmaxf(__uint_as_float(t[0]), __uint_as_float(t[1]));
}
__device__ __forceinline__ float xrow_sum(float x) {
  auto s = __builtin_amdgcn_permlane16_swap(__float_as_uint(x), __float_as_uint(x), false, false);
  x = __uint_as_float(s[0]) + __uint_as_float(s[1]);
  auto t = __builtin_amdgcn_permlane32_swap(__float_as_uint(x), __float_as_uint(x), false, false);
  return __uint_as_float(t[0]) + __uint_as_float(t[1]);
}
__device__ __forceinline__ float row_max(float x) {
  x = fmaxf(x, dpp<XOR8>(x)); x = fmaxf(x, dpp<ROR4>(x)); x = fmaxf(x, dpp<XOR2>(x)); x = fmaxf(x, dpp<XOR1>(x));
  return x;
}
__device__ __forceinline__ float row_sum(float x) {
  x += dpp<XOR8>(x); x += dpp<ROR4>(x); x += dpp<XOR2>(x); x += dpp<XOR1>(x);
  return x;
}
__device__ __forceinline__ float fexp2(float x) { return __builtin_amdgcn_exp2f(x); }
__device__ __forceinline__ f32x4 mfma16(u32x4 a, u32x4 b, f32x4 c) {
  return __builtin_amdgcn_mfma_f32_16x16x32_bf16(__builtin_bit_cast(bf16x8, a), __builtin_bit_cast(bf16x8, b), c, 0, 0, 0);
}
__device__ __forceinline__ f32x16 mfma32(u32x4 a, u32x4 b, f32x16 c) {
  return __builtin_amdgcn_mfma_f32_32x32x16_bf16(__builtin_bit_cast(bf16x8, a), __builtin_bit_cast(bf16x8, b), c, 0, 0, 0);
}
__device__ __forceinline__ rsrc_t mkrs(const GAS char* p, unsigned bytes) {
  return __builtin_amdgcn_make_buffer_rsrc((void*)p, 0, bytes, 0x00020000);
}
__device__ __forceinline__ void bar() { asm volatile("s_waitcnt lgkmcnt(0)\n\ts_barrier" ::: "memory"); }
__device__ __forceinline__ void drain() { asm volatile("s_waitcnt vmcnt(0)" ::: "memory"); }
#define SB() __builtin_amdgcn_sched_barrier(0)
template <int N> __device__ __forceinline__ void vmwait() { asm volatile("s_waitcnt vmcnt(%0)" :: "n"(N) : "memory"); }
__device__ __forceinline__ unsigned swz(unsigned row) { return ((row & 3) << 2) | ((row >> 2) & 2); }
__device__ __forceinline__ u32x2 trread(LAS char* p) {
  return __builtin_bit_cast(u32x2, __builtin_amdgcn_ds_read_tr16_b64_v4i16((LAS s16x4*)p));
}

struct Tag { unsigned tl, th, lo, rb; u64 e; };
__device__ __forceinline__ Tag mktag(u64 e0) {
  Tag t;
  asm volatile("" : "+s"(e0));
  t.e = e0 * 6364136223846793005ull + 1442695040888963407ull;
  t.th = (unsigned)(t.e >> 32);
  t.lo = (unsigned)t.e;
  t.tl = t.lo & 0xff000000u;
  t.rb = ((t.lo >> 9) & 255u) | (~t.lo & 256u);
  return t;
}
__device__ __forceinline__ unsigned gpack(float v, const Tag& t) { return ((__float_as_uint(v) + 0x80u) >> 8) | t.tl; }
__device__ __forceinline__ u32x4 gpair(float a, float b, const Tag& t) { return u32x4{gpack(a, t), t.th, gpack(b, t), t.th}; }
__device__ __forceinline__ bool gok(u32x4 g, const Tag& t) {
  return ((((g[0] ^ t.tl) | (g[2] ^ t.tl)) >> 24) | (g[1] ^ t.th) | (g[3] ^ t.th)) == 0u;
}
__device__ __forceinline__ float gval(unsigned lo) { return __uint_as_float(lo << 8); }
__device__ __forceinline__ bool qok(u32x4 g, const Tag& t) { return ((g[1] ^ t.th) | (g[3] ^ t.th)) == 0u; }
__device__ __forceinline__ double mkd(unsigned lo, unsigned hi) { return __builtin_bit_cast(double, u32x2{lo, hi}); }
__device__ __forceinline__ u32x4 rpack(double a, double b, double l, float r, const Tag& t) {
  const u32x2 ua = __builtin_bit_cast(u32x2, a), ub = __builtin_bit_cast(u32x2, b), ul = __builtin_bit_cast(u32x2, l);
  const u64 ml = ((u64)(unsigned)__builtin_amdgcn_readfirstlane(ul[1]) << 32) | (unsigned)__builtin_amdgcn_readfirstlane(ul[0]);
  const u64 mr = (unsigned)__builtin_amdgcn_readfirstlane(__float_as_uint(r));
  const unsigned ta = t.lo & 511u, tb = t.rb;
  return u32x4{(ua[0] & ~1023u) | (__builtin_amdgcn_inverse_ballot_w64(ml) ? ta | 512u : ta), ua[1],
               (ub[0] & ~1023u) | (__builtin_amdgcn_inverse_ballot_w64(mr) ? tb | 512u : tb), ub[1]};
}
__device__ __forceinline__ u32x4 rpack0(f64x2 o, unsigned lw, int lane, unsigned ta, unsigned tb) {
  const u32x4 u = __builtin_bit_cast(u32x4, o);
  return u32x4{(u[0] & ~1023u) | (ta | (__builtin_amdgcn_ubfe(lw, lane, 1) << 9)), u[1], (u[2] & ~1023u) | tb, u[3]};
}
__device__ __forceinline__ bool rok(u32x4 g, const Tag& t) { return (((g[0] ^ t.lo) | (g[2] ^ t.rb)) & 511u) == 0u; }
constexpr unsigned PMASK = 0x00030003u;
__device__ __forceinline__ unsigned ptag(const Tag& t, int k) {
  const unsigned n = k == 0 ? t.lo : k == 3 ? t.lo >> 10 : ((t.lo >> (1 + 3 * k)) & 7u) | (~t.lo & 8u);
  return (n & 3u) | ((n & 12u) << 14);
}
__device__ __forceinline__ unsigned ltag(const Tag& t) { return ((t.lo >> 16) & 127u) | (~t.lo & 128u); }
__device__ __forceinline__ bool mok(u32x4 g, const Tag& t) { return (((g[0] ^ t.lo) & 0xffffu) | ((g[1] ^ ltag(t)) & 255u)) == 0u; }
__device__ __forceinline__ unsigned bfi(unsigned m, unsigned a, unsigned b) { return (a & m) | (b & ~m); }
__device__ __forceinline__ double rec_a(u32x4 g) { return mkd(g[0] & ~1023u, g[1]); }
__device__ __forceinline__ double rec_b(u32x4 g) { return mkd(g[2] & ~1023u, g[3]); }
__device__ __forceinline__ double rec_l(u32x4 g) { return __builtin_bit_cast(double, __builtin_amdgcn_ballot_w64((g[0] & 512u) != 0u)); }
__device__ __forceinline__ float rec_r(u32x4 g) { return __uint_as_float((unsigned)__builtin_amdgcn_ballot_w64((g[2] & 512u) != 0u)); }
__device__ __forceinline__ int edif(float a, float b) {
  int r;
  asm("v_cvt_i32_f32 %0, %1" : "=v"(r) : "v"(a - b));
  return r;
}
__device__ __forceinline__ unsigned p2hi(float a, float b) { return (unsigned)max(edif(a, b) + 1023, 0) << 20; }
__device__ __forceinline__ float bucket(float m) { return fmaf(__builtin_rintf(m * (1.f / 512.f)), 512.f, 0.f); }
__device__ __forceinline__ float rfl(float x) { return __uint_as_float(__builtin_amdgcn_readfirstlane(__float_as_uint(x))); }
__device__ __forceinline__ unsigned rdu(unsigned v, int l) { return __builtin_amdgcn_readlane(v, l); }

__device__ __forceinline__ void diag_gemv(const u32x4 (&wr)[16], const u32x4 (&xp)[2][2], const unsigned (&mask)[4], f32x4 (&acc)[2]) {
#pragma unroll
  for (int i = 0; i < 16; i++) {
#pragma unroll
    for (int s = 0; s < 2; s++) {
      const unsigned src = xp[s][i >> 3][(i >> 1) & 3];
      const unsigned dup = __builtin_amdgcn_perm(src, src, (i & 1) ? 0x03020302u : 0x01000100u);
      const u32x4 a = {dup & mask[0], dup & mask[1], dup & mask[2], dup & mask[3]};
      acc[s] = mfma16(a, wr[i], acc[s]);
    }
  }
}

constexpr int bitpos(char c) { for (int i = 0; i < 10; i++) if (PAT[i] == c) return 10 + i; return 31; }
constexpr int goff(int t) { int o = 0; for (int j = 0; j < 6; j++) o |= ((t >> j) & 1) << bitpos('0' + j); return o; }
struct Acc { double o[2]; double l; float R; };
__device__ __forceinline__ u32x4 ldnt(rsrc_t r, int voff, int so) { return __builtin_amdgcn_raw_buffer_load_b128(r, voff, so, AUX_NT); }
__device__ __forceinline__ u32x4 ldslot(rsrc_t r, int voff, int so, int i) { return ldnt(r, voff + goff(i & 3), so ^ goff(i & ~3)); }
__device__ __forceinline__ u32x4 ldunit(rsrc_t r, int voff, int so, int i) { return ldnt(r, voff + i * 1024, so); }
__device__ __forceinline__ u32x4 ldsc(rsrc_t r, int voff, int so) { return __builtin_amdgcn_raw_buffer_load_b128(r, voff, so, AUX_SC1); }
__device__ __forceinline__ void stsc(u32x4 v, rsrc_t r, int voff, int so) { __builtin_amdgcn_raw_buffer_store_b128(v, r, voff + so, 0, AUX_SC1); }
__device__ __forceinline__ float dot8(u32x4 k, u32x4 q) { return dot2(k[3], q[3], dot2(k[2], q[2], dot2(k[1], q[1], dot2(k[0], q[0], 0.f)))); }
#define TR_STEP(HALF, CTRL, BIT) _Pragma("unroll") for (int h = 0; h < (HALF); ++h) { \
    const float t1 = x[h] + dpp<CTRL>(x[h]), t2 = x[h + (HALF)] + dpp<CTRL>(x[h + (HALF)]); x[h] = (c & (BIT)) ? t2 : t1; }
template <int N> __device__ __forceinline__ float k_step(u32x4 (&ring)[16], u32x4 q, int c, rsrc_t rn, int voff, int so, bool ext, LAS char* xp) {
  float x[16];
#pragma unroll
  for (int i = 0; i < 16; i++) {
    x[i] = dot8(ring[i], q);
    asm volatile("s_waitcnt vmcnt(%1)" : "+v"(x[i]) : "n"(WTH - 1));
    if (i >= 16 - N && ext) ring[i] = *(volatile LAS u32x4*)(xp + (i - (16 - N)) * 1024);
    else ring[i] = ldslot(rn, voff, so, i);
    SB();
  }
  TR_STEP(8, XOR8, 8) TR_STEP(4, XOR7, 4) TR_STEP(2, XOR2, 2) TR_STEP(1, XOR1, 1)
  return rbf(rbf(x[0]) * SCALE);
}
template <int BC> __device__ __forceinline__ void pv(u32x4 v, unsigned pk, const unsigned (&mask)[4], f32x4& acc) {
  const unsigned d = dppu<0x150 + BC>(pk);
  acc = mfma16(u32x4{d & mask[0], d & mask[1], d & mask[2], d & mask[3]}, v, acc);
}
template <int I, int MODE> __device__ __forceinline__ void v_step(u32x4 (&ring)[16], unsigned pk, const unsigned (&mask)[4], f32x4& acc,
                                                                  rsrc_t rk, rsrc_t rv, int voff, int so) {
  pv<I>(ring[I], pk, mask, acc);
  if (MODE == 1 || (MODE == 2 && I < 8)) {
    asm volatile("s_waitcnt vmcnt(%1)" : "+v"(acc) : "n"(WTH - 1));
    ring[I] = MODE == 1 ? ldslot(rv, voff, so, I) : ldunit(I < 4 ? rk : rv, voff, so, I & 3);
  } else {
    asm volatile("" : "+v"(acc));
  }
  SB();
  if constexpr (I < 15) v_step<I + 1, MODE>(ring, pk, mask, acc, rk, rv, voff, so);
}
__device__ __forceinline__ float exp_ref(float mx, float& f) {
  const float M = __builtin_ceilf(mx * LOG2E);
  f = __builtin_amdgcn_fmed3f(fmaf(mx, LOG2E, -M), -1.f, 0.f);
  return M;
}
__device__ __forceinline__ unsigned lds_inc(LAS unsigned* p, int lane) {
  unsigned t = 0, z = 0;
  asm volatile("" : "+v"(z));
  if (lane == 0) t = __hip_atomic_fetch_add(p + z, 1u, __ATOMIC_RELAXED, __HIP_MEMORY_SCOPE_WORKGROUP);
  return t;
}
struct Take { unsigned unit, pend; bool fetch; };
__device__ __forceinline__ void lds_add(LAS unsigned* p, unsigned v) { __hip_atomic_fetch_add(p, v, __ATOMIC_RELAXED, __HIP_MEMORY_SCOPE_WORKGROUP); }
__device__ __forceinline__ Take take(LAS unsigned* ctl, unsigned kv, rsrc_t rws, int cntoff, int lane) {
  const unsigned k = __builtin_amdgcn_readfirstlane(kv);
  unsigned b, s;
  auto look = [&]() {
    const unsigned vs = ((volatile LAS unsigned*)ctl)[5], vb = ((volatile LAS unsigned*)ctl)[8 + (k >> 2)];
    s = __builtin_amdgcn_readfirstlane(vs); b = __builtin_amdgcn_readfirstlane(vb);
    return (b == ~0u) & ((s < 0x10000u) | ((s & 0xffffu) != 0u));
  };
  if (__builtin_expect(look(), 0))
    for (unsigned spins = 0; spins < SPIN_CAP * 16; spins++) {
      __builtin_amdgcn_s_sleep(1);
      if (!look()) break;
    }
  Take t;
  t.unit = min(b, (unsigned)NUNIT) + (k & 3);
  t.fetch = ((k & 3) == 0) & (b < NUNIT) & (s < 0x10000u);
  int off = OOB;
  if (t.fetch) {
    unsigned z = 0;
    asm volatile("" : "+v"(z));
    if (lane == 0) { lds_add(ctl + 5 + z, 1u); off = cntoff; }
    asm volatile("s_waitcnt lgkmcnt(0)" ::: "memory");
  }
  t.pend = __builtin_amdgcn_raw_ptr_buffer_atomic_add_i32(4, rws, off, 0, AUX_SC1);
  SB();
  return t;
}
__device__ __forceinline__ void give(LAS unsigned* ctl, const Take& t, int lane) {
  if (t.fetch && lane == 0) {
    unsigned z = 0;
    asm volatile("" : "+v"(z));
    if (t.pend < NUNIT) {
      const unsigned i = __hip_atomic_fetch_add(ctl + 4 + z, 1u, __ATOMIC_RELAXED, __HIP_MEMORY_SCOPE_WORKGROUP);
      ((volatile LAS unsigned*)ctl)[8 + i] = t.pend;
    }
    asm volatile("" ::: "memory");
    lds_add(ctl + 5 + z, t.pend >= NUNIT - 4 ? 0xffffu : ~0u);
  }
}
__device__ __forceinline__ int unit_off(unsigned t) { return (int)min(t, (unsigned)NUNIT) * 4096; }

__device__ __forceinline__ void b_units(u32x4 (&ra)[16], rsrc_t rk, rsrc_t rv, unsigned cur, u32x4 q, int lane, const unsigned (&mask)[4],
                                        LAS unsigned* ctl, rsrc_t rws, int cntoff, Acc& A) {
  const int c = lane & 15, voff = lane * 16;
  float Mr = -INFINITY;
  double s1 = 0.0;
  A.o[0] = A.o[1] = A.l = 0.0; A.R = cur < NUNIT ? -INFINITY : 0.f;
#pragma unroll 1
  while (cur < NUNIT) {
    const Take t = take(ctl, lds_inc(ctl, lane), rws, cntoff, lane);
    const int so = unit_off(t.unit);
    SB();
    float x[4];
#pragma unroll
    for (int i = 0; i < 4; i++) x[i] = dot8(ra[i], q);
    asm volatile("" : "+v"(x[0]), "+v"(x[1]), "+v"(x[2]), "+v"(x[3]));
    SB();
#pragma unroll
    for (int i = 0; i < 4; i++) {
      ra[i] = ldunit(rk, voff, so, i);
      SB();
    }
    TR_STEP(2, XOR8, 8) TR_STEP(1, XOR7, 4)
    x[0] += dpp<XOR2>(x[0]); x[0] += dpp<XOR1>(x[0]);
    const float s = rbf(rbf(x[0]) * SCALE);
    const float n = __builtin_rintf(s * LOG2E);
    float e = fexp2(__builtin_amdgcn_fmed3f(fmaf(s, LOG2E, -n), -1.f, 1.f)), d = n - Mr;
    asm volatile("" : "+v"(e));
    if (__builtin_expect(__any(d > 57.f), 0)) {
      float mx = fmaxf(n, dpp<XOR8>(n));
      mx = xrow_max(fmaxf(mx, dpp<ROR4>(mx)));
      const float Rn = rfl(fmaxf(A.R, bucket(mx)));
      const double s0 = mkd(0u, p2hi(A.R, Rn));
      A.o[0] *= s0; A.o[1] *= s0; A.l *= s0;
      A.R = Rn; Mr = mx; d = n - mx;
      s1 = mkd(0u, p2hi(mx, Rn));
    }
    const float p = __builtin_ldexpf(e, edif(d, 0.f));
    unsigned pk = cvtpk(p, p);
    asm volatile("" : "+v"(pk));
    SB();
    f32x4 acc = {0.f, 0.f, 0.f, 0.f};
    float ls = bf_lo(pk);
    pv<0>(ra[4], pk, mask, acc);  asm volatile("" : "+v"(acc)); ra[4] = ldunit(rv, voff, so, 0); ls += dpp<XOR8>(ls); asm volatile("" : "+v"(ls)); SB();
    pv<4>(ra[5], pk, mask, acc);  asm volatile("" : "+v"(acc)); ra[5] = ldunit(rv, voff, so, 1); ls += dpp<ROR4>(ls); asm volatile("" : "+v"(ls)); SB();
    pv<8>(ra[6], pk, mask, acc);  asm volatile("" : "+v"(acc)); ra[6] = ldunit(rv, voff, so, 2); SB();
    pv<12>(ra[7], pk, mask, acc); asm volatile("" : "+v"(acc)); ra[7] = ldunit(rv, voff, so, 3); SB();
    A.l = __builtin_fma((double)xrow_sum(ls), s1, A.l);
    asm volatile("" : "+v"(A.l));
    A.o[0] = __builtin_fma((double)(lane < 32 ? acc[0] : acc[2]), s1, A.o[0]);
    A.o[1] = __builtin_fma((double)(lane < 32 ? acc[1] : acc[3]), s1, A.o[1]);
    asm volatile("" : "+v"(A.o[0]), "+v"(A.o[1]));
    SB();
    cur = t.unit;
    if (cur >= NUNIT) break;
    give(ctl, t, lane);
  }
}
#undef TR_STEP
__device__ __forceinline__ int rec_col(int lane) { return 8 * (lane & 15) + 4 * ((lane >> 4) & 1) + 2 * (lane >> 5); }
struct Mrg { unsigned s0, s4, s8, ta, tb; int off; };
__device__ __forceinline__ Mrg mkmrg(LAS char* sum, int lane, int roff, const Tag& tg) {
  Mrg m = {(unsigned)(uintptr_t)sum, (unsigned)(uintptr_t)sum + (lane >> 5) * 4, (unsigned)(uintptr_t)sum + lane * 8, tg.lo & 511u, tg.rb, roff + lane * 16};
  asm volatile("" : "+v"(m.s0), "+v"(m.s4), "+v"(m.s8), "+v"(m.ta), "+v"(m.tb), "+v"(m.off));
  return m;
}
__device__ __forceinline__ void b_merge(const Mrg& m, LAS char* mrg, int w, int lane, const Acc& A, rsrc_t rws, u64 e0) {
  unsigned inc = 1;
  double a = A.o[0], b = A.o[1], l = A.l;
  if (__builtin_expect(__float_as_uint(A.R) != 0u, 0)) {
    *(volatile LAS f64x2*)(mrg + w * 1024 + lane * 16) = f64x2{a, b};
    *(volatile LAS double*)(mrg + 8192 + w * 8) = l;
    *(volatile LAS float*)(mrg + 8256 + w * 4) = A.R;
    a = b = l = 0.0;
    inc = 1u + (256u << w);
  }
  f64x2 o;
  unsigned n, lw;
  asm volatile("ds_add_f64 %3, %5\n\tds_add_f64 %3, %6 offset:512\n\ts_mov_b64 exec, 1\n\tds_add_f64 %3, %7 offset:1024\n\tds_add_u32 %3, %8 offset:1032\n\t"
               "s_mov_b64 exec, -1\n\tds_read_b32 %1, %9 offset:1032\n\tds_read_b32 %2, %4 offset:1024\n\tds_read2st64_b64 %0, %3 offset1:1\n\ts_waitcnt lgkmcnt(0)"
               : "=&v"(o), "=&v"(n), "=&v"(lw)
               : "v"(m.s8), "v"(m.s4), "v"(a), "v"(b), "v"(l), "v"(inc), "v"(m.s0)
               : "memory");
  stsc(rpack0(o, lw, lane, m.ta, m.tb), rws, n == 8u ? m.off : OOB, 0);
  SB();
  n = __builtin_amdgcn_readfirstlane(n);
  if (__builtin_expect(n > 8u && (n & 255u) == 8u, 0)) {
    double L = *(volatile LAS double*)(uintptr_t)(m.s0 + 1024);
    float R = L != 0.0 ? 0.f : -INFINITY;
#pragma unroll 1
    for (int i = 0; i < 8; i++)
      if (n >> (8 + i) & 1) {
        const f64x2 oi = *(volatile LAS f64x2*)(mrg + i * 1024 + lane * 16);
        const float Ri = *(volatile LAS float*)(mrg + 8256 + i * 4), Rn = fmaxf(R, Ri);
        const double s0 = mkd(0u, p2hi(R, Rn)), s1 = mkd(0u, p2hi(Ri, Rn));
        o[0] = __builtin_fma(oi[0], s1, o[0] * s0); o[1] = __builtin_fma(oi[1], s1, o[1] * s0);
        L = __builtin_fma(*(volatile LAS double*)(mrg + 8192 + i * 8), s1, L * s0);
        R = Rn;
      }
    stsc(rpack(o[0], o[1], L, R, mktag(e0)), rws, m.off, 0);
  }
}

__device__ __forceinline__ unsigned fz(unsigned row) { return ((row & 2) << 1) | ((row >> 1) & 2); }
__device__ __forceinline__ void dma1(rsrc_t rs, int voff, int so, unsigned ldsaddr) {
  unsigned keep;
  asm volatile("s_mov_b32 %0, m0\n\ts_mov_b32 m0, %1\n\ts_nop 0\n\tbuffer_load_dwordx4 %2, %3, %4 offen nt lds\n\ts_mov_b32 m0, %0"
               : "=&s"(keep) : "s"(ldsaddr), "v"(voff), "s"(rs), "s"(so) : "memory");
}
template <int NR>
__device__ __forceinline__ void dma_rows(rsrc_t rs, int srcoff, LAS char* img, int r0, int lane) {
#pragma unroll
  for (int u = 0; u < NR; u++) {
    const int r = r0 + u;
    unsigned keep;
    asm volatile("s_mov_b32 %0, m0\n\ts_mov_b32 m0, %1\n\ts_nop 0\n\tbuffer_load_dwordx4 %2, %3, %4 offen nt lds\n\ts_mov_b32 m0, %0"
                 : "=&s"(keep) : "s"((unsigned)(uintptr_t)img + r * 1024), "v"((lane ^ swz(r)) * 16), "s"(rs), "s"(srcoff + r * 1024) : "memory");
  }
  SB();
}

__device__ __forceinline__ void wo_frags(LAS char* img, int w, int lane, u32x4 (&wf)[4][4]) {
  const int g = lane >> 4, q = (lane & 15) >> 2, p = lane & 3;
#pragma unroll
  for (int ks = 0; ks < 4; ks++)
#pragma unroll
    for (int tl = 0; tl < 4; tl++) {
      const int ch = 8 * w + 2 * tl + (p >> 1);
      const int r0 = 32 * ks + 8 * g + q, r1 = r0 + 4;
      const u32x2 a = trread(img + r0 * 1024 + ((ch ^ swz(r0)) * 16) + 8 * (p & 1));
      const u32x2 b = trread(img + r1 * 1024 + ((ch ^ swz(r1)) * 16) + 8 * (p & 1));
      wf[tl][ks] = u32x4{a[0], a[1], b[0], b[1]};
    }
}
__device__ __forceinline__ f32x4 wo_calc(const u32x4 (&wf)[4][4], LAS char* cb, int lane) {
  const int n = lane & 15, grp = lane >> 4;
  u32x4 bf[4];
#pragma unroll
  for (int ks = 0; ks < 4; ks++) bf[ks] = *(LAS u32x4*)(cb + (n & 3) * 256 + ks * 64 + grp * 16);
  f32x4 acc[4];
#pragma unroll
  for (int tl = 0; tl < 4; tl++) acc[tl] = f32x4{0.f, 0.f, 0.f, 0.f};
#pragma unroll
  for (int ks = 0; ks < 4; ks++)
#pragma unroll
    for (int tl = 0; tl < 4; tl++) acc[tl] = mfma16(wf[tl][ks], bf[ks], acc[tl]);
  const f32x4 lo = (n & 4) ? acc[1] : acc[0], hi = (n & 4) ? acc[3] : acc[2];
  return (n & 8) ? hi : lo;
}
__device__ __forceinline__ void y_store(f32x4 y, int w, int lane, int slot, rsrc_t rws, int yoff, const Tag& tg) {
  const int off = yoff + (((lane & 3) * 8 + w) * 32 + slot) * 512 + ((lane & 12) + (lane >> 4)) * 16;
  stsc(gpair(y[0], y[1], tg), rws, off, 0);
  stsc(gpair(y[2], y[3], tg), rws, off, 256);
}
template <int K, int N> __device__ __forceinline__ unsigned bc1(const unsigned (&p)[N]) {
  unsigned v = dppu<0x150 + K>(p[0]);
  if constexpr (N > 1) {
    v = (unsigned)__builtin_amdgcn_update_dpp((int)v, (int)p[1], 0x150 + K, 0xf, 2, false);
    v = (unsigned)__builtin_amdgcn_update_dpp((int)v, (int)p[2], 0x150 + K, 0xf, 4, false);
  }
  return v;
}
template <int K, int N> __device__ __forceinline__ u32x4 bc4(const unsigned (&p)[N]) { return u32x4{bc1<K>(p), bc1<K + 1>(p), bc1<K + 2>(p), bc1<K + 3>(p)}; }
template <int N> __device__ __forceinline__ f32x4 proj(const unsigned (&pr)[N], const u32x4 (&wf)[4][4], int lane) {
  const u32x4 bf[4] = {bc4<0>(pr), bc4<4>(pr), bc4<8>(pr), bc4<12>(pr)};
  f32x4 acc[4];
#pragma unroll
  for (int tl = 0; tl < 4; tl++) acc[tl] = f32x4{0.f, 0.f, 0.f, 0.f};
#pragma unroll
  for (int ks = 0; ks < 4; ks++)
#pragma unroll
    for (int tl = 0; tl < 4; tl++) acc[tl] = mfma16(wf[tl][ks], bf[ks], acc[tl]);
  const f32x4 lo = (lane & 1) ? acc[1] : acc[0], hi = (lane & 1) ? acc[3] : acc[2];
  f32x4 r = (lane & 2) ? hi : lo;
  asm volatile("" : "+v"(r));
  return r;
}
__device__ __forceinline__ unsigned arrive(LAS unsigned* p, int lane) {
  unsigned z = 0;
  asm volatile("" : "+v"(z) :: "memory");
  if (lane == 0) lds_add(p + z, 1u);
  asm volatile("" ::: "memory");
  return *(volatile LAS unsigned*)p;
}
template <int N> __device__ __forceinline__ f32x2 rows_sum(LAS char* p) {
  f32x2 v[N];
#pragma unroll
  for (int i = 0; i < N; i++) v[i] = *(volatile LAS f32x2*)(p + i * 256);
#pragma unroll
  for (int st = 1; st < N; st *= 2)
#pragma unroll
    for (int i = 0; i + st < N; i += 2 * st) v[i] += v[i + st];
  return v[0];
}
__device__ __forceinline__ float half_sum(float x) {
  auto t = __builtin_amdgcn_permlane32_swap(__float_as_uint(x), __float_as_uint(x), false, false);
  return __uint_as_float(t[0]) + __uint_as_float(t[1]);
}

__global__ __launch_bounds__(512, 2) void k_all(const char* a0_, const char* a1_, const char* a2_, const char* a3_, const char* a4_, const char* a5_,
                                                const char* a6_, const char* a7_, const char* a8_, const char* a9_, const char* a10_, char* a11_, unsigned* a12_) {
  u32x16 ka; u32x8 kb_; u32x2 kc_;
  asm volatile("s_load_dwordx16 %0, %3, 0x0\n\ts_load_dwordx8 %1, %3, 0x40\n\ts_load_dwordx2 %2, %3, 0x60"
               : "=&s"(ka), "=&s"(kb_), "=&s"(kc_) : "s"(__builtin_amdgcn_kernarg_segment_ptr()));
  __shared__ __attribute__((aligned(16))) char smem[LDS_BYTES];
  LAS char* lds = (LAS char*)smem;
  LAS char* scr = lds + LDS_SCR;
  LAS unsigned* ctl = (LAS unsigned*)scr;
  const int tid = threadIdx.x, lane = tid & 63, w = __builtin_amdgcn_readfirstlane(tid >> 6);
  const int blk = blockIdx.x, head = blk >> 3, prod = (blk + head) & 7;
  const int sub = (0x54372106u >> (4 * prod)) & 7;
  const unsigned tst = (unsigned)__builtin_amdgcn_s_memrealtime();
  unsigned cpf = 0;
  {
    u64 pc;
    asm volatile("s_getpc_b64 %0" : "=s"(pc));
    cpf = __builtin_amdgcn_raw_buffer_load_b32(mkrs((const GAS char*)(pc & ~255ull), CPF), w == 7 ? head * 2048 + lane * 32 : OOB, 0, 0);
    SB();
  }
  {
    unsigned c = 16 + 4 * (DCH * sub + tid);
    asm volatile("" : "+v"(c));
    c = tid < 8 + DCH ? c : ~0u;
    c = tid < 8 ? 0u : c;
    ctl[tid] = tid == 4 ? DCH : c;
    *(LAS u64*)(scr + SCR_SUM + min(tid, 2 * SUM_STR / 8 - 1) * 8) = 0;
  }
  const int l16 = lane & 15, grp = lane >> 4;
  const int b4 = head & 3, cntoff = WS_CNT + head * CSTR;
  const int bm = (b4 + 1 + (sub >= 3 ? sub - 3 : sub)) & 3;
  const size_t base = (size_t)(bm * 32 + head) << 20;
  const int so = ((w & 1) << bitpos('a')) | (((w >> 1) & 1) << bitpos('b')) | ((w >> 2) << bitpos('c')) | (sub >= 3 ? 1 << bitpos('p') : 0);
  int v0 = l16 * 16 + grp * (sub < 6 ? 256 : 131072) + (sub < 6 ? so : w * 524288 + head * 256);
  unsigned boff = sub < 6 ? (unsigned)base : 0u, bsz = sub < 6 ? 1u << 20 : 4u << 20;
  int s0[WTH];
#pragma unroll
  for (int i = 0; i < WTH; i++) {
    s0[i] = sub < 6 ? goff(i) : i * 8192;
    asm volatile("" : "+s"(s0[i]));
  }
  asm volatile("" : "+v"(v0), "+s"(boff), "+s"(bsz));
  SB();
  asm volatile("s_waitcnt lgkmcnt(0)" : "+s"(ka), "+s"(kb_), "+s"(kc_));
  const unsigned tk1 = (unsigned)__builtin_amdgcn_s_memrealtime();
  auto ptr = [](unsigned lo, unsigned hi) { return (GAS char*)(((u64)hi << 32) | lo); };
  GAS char *const xA = ptr(ka[0], ka[1]), *const ckv = ptr(ka[2], ka[3]), *const wqA = ptr(ka[4], ka[5]), *const wuk = ptr(ka[6], ka[7]),
           *const wuv = ptr(ka[8], ka[9]), *const woA = ptr(ka[10], ka[11]), *const xB = ptr(ka[12], ka[13]), *const kc = ptr(ka[14], ka[15]),
           *const vc = ptr(kb_[0], kb_[1]), *const wqB = ptr(kb_[2], kb_[3]), *const woB = ptr(kb_[4], kb_[5]), *const ws = ptr(kb_[6], kb_[7]),
           *const out = ptr(kc_[0], kc_[1]);
  const u64 e0 = *(const __attribute__((address_space(4))) u64*)(u64)(ws + WS_EPOCH);
  const rsrc_t r0 = mkrs((sub < 6 ? kc : sub == 6 ? wqB : wqA) + boff, bsz);
  SB();
  u32x4 ra[16];
#pragma unroll
  for (int i = 0; i < WTH; i++) ra[i] = ldnt(r0, v0, s0[i]);
  SB();
  u32x4 xp[2][2];
  if (sub >= 6) {
    const GAS char* x = sub == 6 ? xB : xA;
#pragma unroll
    for (int s = 0; s < 2; s++) {
      const GAS u32x4* xs = (const GAS u32x4*)(x + (2 * s + (l16 >> 3)) * 1024 + (64 * w + 16 * grp) * 2);
      xp[s][0] = xs[0]; xp[s][1] = xs[1];
    }
#pragma unroll
    for (int i = WTH; i < 16; i++) ra[i] = ldnt(r0, v0, i * 8192);
    asm volatile("" ::: "memory");
    SB();
  }
  const rsrc_t rws = mkrs(ws, WS_END);
  __builtin_amdgcn_raw_buffer_store_b32(tst, mkrs(ws + (8 << 20), 4096), tid == 0 ? blk * 4 : OOB, 0, AUX_SC1);
  unsigned mask[4];
#pragma unroll
  for (int v = 0; v < 4; v++) mask[v] = (((lane & 7) >> 1) == v) ? ((lane & 1) ? 0xffff0000u : 0x0000ffffu) : 0u;
  const size_t base4 = (size_t)(b4 * 32 + head) << 20;
  const rsrc_t rk4 = mkrs(kc + base4, 1u << 20), rv4 = mkrs(vc + base4, 1u << 20);
  const int voff = lane * 16;
  LAS char* qst = scr + 1024 + w * 512;
  const int qo = WS_QB + head * 2048 + (lane < 32 ? bm : b4) * 512 + (lane & 31) * 16;
  auto qwait = [&](u32x4 g, const Tag& tg) {
    if (!__all(qok(g, tg))) {
      for (unsigned spins = 0;; spins++) {
        asm volatile("" ::: "memory");
        g = ldsc(rws, qo, 0);
        if (__all(qok(g, tg)) || spins > SPIN_CAP) break;
        __builtin_amdgcn_s_sleep(2);
      }
    }
    *(LAS u32x2*)(qst + lane * 8) = u32x2{g[0], g[2]};
  };
  unsigned cur;

  if (__builtin_expect(sub < 6, 1)) {
    const rsrc_t rv = mkrs(vc + base, 1u << 20);
    u32x4 rb[16], g;
#pragma unroll
    for (int i = WTH; i < 32 + NXA + NXB; i++) {
      if (i == WTH) asm volatile("s_barrier" ::: "memory");
      vmwait<WTH - 1>();
      if (i == TI) { const unsigned tk2 = (unsigned)__builtin_amdgcn_s_memrealtime();
        __builtin_amdgcn_raw_buffer_store_b32(PROBE == 4 ? tk1 - tst : tk2 - tst, mkrs(ws + (9 << 20), 4096), tid == 0 ? (head * 6 + sub) * 4 : OOB, 0, AUX_SC1); }
      if (i == 32 + NXA + NXB - WTH) asm volatile("buffer_load_dwordx4 %0, %1, %2, 0 offen sc1" : "=&v"(g) : "v"(qo), "s"(rws));
      if (i < 32) {
        const u32x4 v = ldslot(r0, voff, so, i);
        if (i < 16) ra[i] = v; else rb[i - 16] = v;
      } else {
        const int j = i < 32 + NXA ? i + 16 - NXA : i + 32 - NXA - NXB;
        dma1(r0, voff + goff(j & 3), so ^ goff(j & ~3), (unsigned)(uintptr_t)lds + w * 16384 + (i - 32) * 1024);
      }
      SB();
    }
    asm volatile("s_waitcnt vmcnt(%1)" : "+v"(g) : "n"(WTH), "v"(cpf));
    qwait(g, mktag(e0));
    const u32x4 q = *(LAS u32x4*)(qst + l16 * 16);
    float sc[4] = {0.f, 0.f, 0.f, 0.f};
#pragma unroll 1
    for (int it = 0; it < 2; it++) {
      const rsrc_t rn = it ? rv : r0;
      sc[2] = sc[0]; sc[3] = sc[1];
      sc[1] = k_step<NXA>(ra, q, l16, rn, voff, so ^ (it ? goff(48) : goff(32)), it == 0, lds + w * 16384 + voff);
      sc[0] = k_step<NXB>(rb, q, l16, rn, voff, so ^ (it ? goff(32) : goff(48)), it == 0, lds + w * 16384 + NXA * 1024 + voff);
    }
    const float mx = xrow_max(row_max(fmaxf(fmaxf(sc[0], sc[1]), fmaxf(sc[2], sc[3]))));
    float f, l = 0.f;
    const float M = exp_ref(mx, f);
    unsigned pk[4];
#pragma unroll
    for (int k = 0; k < 4; k++) {
      const float p = fexp2(fmaf(sc[k] - mx, LOG2E, f));
      pk[k] = cvtpk(p, p);
      l += bf_lo(pk[k]);
    }
    l = xrow_sum(row_sum(l));
    cur = 8 * sub + w;
    f32x4 acc = {0.f, 0.f, 0.f, 0.f};
    v_step<0, 1>(ra, pk[0], mask, acc, r0, rv, voff, so ^ goff(16));
    v_step<0, 1>(rb, pk[1], mask, acc, r0, rv, voff, so);
    v_step<0, 2>(ra, pk[2], mask, acc, rk4, rv4, voff, cur * 4096);
    v_step<0, 0>(rb, pk[3], mask, acc, r0, rv, voff, 0);
    Acc A;
    A.R = rfl(bucket(M));
    const double sm = mkd(0u, p2hi(M, A.R));
    A.o[0] = (lane < 32 ? acc[0] : acc[2]) * sm; A.o[1] = (lane < 32 ? acc[1] : acc[3]) * sm;
    A.l = l * sm;
    b_merge(mkmrg(scr + SCR_SUM, lane, WS_RS + (head * 16 + sub) * 1024, mktag(e0)), scr + 8192, w, lane, A, rws, e0);
  } else {
  {
    __builtin_amdgcn_raw_buffer_store_b32(48 + 24 * DCH, rws, (sub == 6 && tid == 0) ? cntoff : OOB, 0, AUX_SC1);
    {
      const rsrc_t rk = mkrs(wuk, sub == 7 ? 4u << 20 : 0u);
#pragma unroll
      for (int i = 0; i < 16; i++)
        __builtin_amdgcn_raw_ptr_buffer_load_lds(rk, (LAS void*)(lds + w * 16384 + i * 1024), 16,
                                                 (64 * w + grp) * 8192 + head * 256 + ((l16 ^ (4 * (i & 3) + grp)) * 16), i * 4 * 8192, 0, AUX_NT);
    }
    SB();
    f32x4 acc[2] = {{0.f, 0.f, 0.f, 0.f}, {0.f, 0.f, 0.f, 0.f}};
    diag_gemv(ra, xp, mask, acc);
    LAS float* red = (LAS float*)(scr + 8192);
#pragma unroll
    for (int s = 0; s < 2; s++) *(LAS f32x4*)(red + w * 512 + (2 * s + (grp >> 1)) * 128 + 8 * l16 + 4 * (grp & 1)) = acc[s];
    asm volatile("" :: "v"(cpf));
    __builtin_amdgcn_s_waitcnt(0x0f70);
    drain();
    bar();
    if (tid < 128) {
      f32x4 a = {0.f, 0.f, 0.f, 0.f};
#pragma unroll
      for (int k = 0; k < 8; k++) a += *(LAS f32x4*)(red + k * 512 + 4 * tid);
      const unsigned p0 = cvtpk(a[0], a[1]), p1 = cvtpk(a[2], a[3]);
      const Tag tg = mktag(e0);
      stsc(u32x4{p0, tg.th, p1, tg.th}, rws, sub == 6 ? WS_QB + head * 2048 + tid * 16 : OOB, 0);
      *(LAS u32x2*)(scr + 1024 + tid * 8) = u32x2{p0, p1};
    }
  }

  if (sub == 6) {
    const int aid = ((head & 6) << 2) | ((head & 1) << 2) | (head >> 3), b = aid >> 3;
    const rsrc_t rc = mkrs(ckv + (size_t)aid * 524288, 524288);
    const int dvo = (lane >> 3) * 1024 + w * 128 + (((lane & 7) ^ fz(lane >> 3)) * 16);
    const unsigned dlo = (unsigned)(uintptr_t)lds + w * 8192;
    const int xt = (aid & 7) << 16;
#pragma unroll
    for (int j = 0; j < 16; j++) dma1(rc, dvo, (j * 8192) ^ xt, dlo + (j >> 3) * 65536 + (j & 7) * 1024);
    SB();
    const Tag tg = mktag(e0);
    bar();
    {
      u32x4 t[8];
      unsigned miss = 15;
      __builtin_amdgcn_s_sleep(90);
      for (unsigned spins = 0;; spins++) {
        asm volatile("" ::: "memory");
#pragma unroll
        for (int i = 0; i < 4; i++)
          if (miss >> i & 1) {
            t[2 * i] = ldsc(rws, WS_QABS + (b * 32 + w) * 2048 + lane * 16, i * 16384);
            t[2 * i + 1] = ldsc(rws, WS_QABS + (b * 32 + w) * 2048 + lane * 16 + 1024, i * 16384);
          }
        SB();
#pragma unroll
        for (int i = 0; i < 4; i++) if (__all(qok(t[2 * i], tg) & qok(t[2 * i + 1], tg))) miss &= ~(1u << i);
        if (!miss || spins > SPIN_CAP) break;
        __builtin_amdgcn_s_sleep(4);
      }
#pragma unroll
      for (int i = 0; i < 8; i++) {
        const int row = 8 * (i >> 1) + w;
        *(LAS u32x2*)(scr + row * 1024 + (((32 * (i & 1) + (lane >> 1)) ^ swz(row)) * 16) + (lane & 1) * 8) = u32x2{t[i][0], t[i][2]};
      }
    }
    bar();
    const int kb = w & 3, hb = w >> 2;
    u32x4 qf[16];
    {
      const int row = 16 * hb + l16;
#pragma unroll
      for (int ks = 0; ks < 16; ks++) qf[ks] = *(LAS u32x4*)(scr + row * 1024 + (((4 * ks + grp) ^ swz(row)) * 16));
    }
    LAS float* mx = (LAS float*)scr;
    LAS char* pl = scr + 1024;
    LAS float* lr = (LAS float*)(scr + 8192);
    f32x16 o0, o1;
#pragma unroll
    for (int j = 0; j < 16; j++) { o0[j] = 0.f; o1[j] = 0.f; }
    float m1 = -INFINITY, m2 = -INFINITY, l1 = 0.f;
    const int h1 = 16 * hb + l16, h2 = lane & 31, hh = lane >> 5;
    const int arow = 16 * kb + l16;
    const int tq = (lane & 15) >> 2, tp = lane & 3, tblk = (lane >> 4) & 1;
    const int qa0 = arow * 128 + ((grp ^ fz(arow)) * 16), qa1 = qa0 ^ 64;
    const int pr = 8 * hh + tq, pc = 2 * tblk + (tp >> 1);
    const int pa0 = w * 8192 + pr * 128 + ((pc ^ fz(pr)) * 16) + 8 * (tp & 1), pa1 = w * 8192 + (pr + 4) * 128 + ((pc ^ fz(pr + 4)) * 16) + 8 * (tp & 1);
#pragma unroll 1
    for (int t = 0; t < 8; t++) {
      if (t < 7) asm volatile("s_waitcnt vmcnt(8)" ::: "memory"); else asm volatile("s_waitcnt vmcnt(0)" ::: "memory");
      bar();
      LAS char* img = lds + (t & 1) * 65536;
      f32x4 s0 = {0.f, 0.f, 0.f, 0.f}, s1 = s0;
      u32x4 ka[16];
      u32x2 ta[4][4];
      const int nso = t < 6 ? ((t + 2) * 65536) ^ xt : OOB;
#pragma unroll
      for (int ks = 0; ks < 16; ks++) ka[ks] = *(LAS u32x4*)(img + ((ks & 1) ? qa1 : qa0) + (ks >> 1) * 8192);
      SB();
#pragma unroll
      for (int k2 = 0; k2 < 8; k2++) {
        s0 = mfma16(ka[2 * k2], qf[2 * k2], s0);
        s1 = mfma16(ka[2 * k2 + 1], qf[2 * k2 + 1], s1);
      }
      asm volatile("" : "+v"(s0), "+v"(s1));
      SB();
#pragma unroll
      for (int ks = 0; ks < 4; ks++) {
        ta[ks][0] = trread(img + pa0 + ks * 2048); ta[ks][1] = trread(img + pa1 + ks * 2048);
        ta[ks][2] = trread(img + (pa0 ^ 64) + ks * 2048); ta[ks][3] = trread(img + (pa1 ^ 64) + ks * 2048);
      }
      SB();
      float x[4];
#pragma unroll
      for (int j = 0; j < 4; j++) x[j] = rbf(rbf(s0[j] + s1[j]) * SCALE);
      const float lm = xrow_max(fmaxf(fmaxf(x[0], x[1]), fmaxf(x[2], x[3])));
      if (grp == 0) mx[h1 * 4 + kb] = lm;
      LAS u32x2* pw = (LAS u32x2*)(pl + h1 * 144 + (16 * kb + 4 * grp) * 2);
      u32x2 pp = {cvtpk(fexp2((x[0] - m1) * LOG2E), fexp2((x[1] - m1) * LOG2E)), cvtpk(fexp2((x[2] - m1) * LOG2E), fexp2((x[3] - m1) * LOG2E))};
      *pw = pp;
      bar();
      const f32x4 ma = *(LAS f32x4*)(mx + h1 * 4), mb = *(LAS f32x4*)(mx + h2 * 4);
      u32x4 pb[4];
#pragma unroll
      for (int ks = 0; ks < 4; ks++) pb[ks] = *(LAS u32x4*)(pl + h2 * 144 + (16 * ks + 8 * hh) * 2);
      const float n1 = fmaxf(m1, fmaxf(fmaxf(ma[0], ma[1]), fmaxf(ma[2], ma[3])));
      const float n2 = fmaxf(m2, fmaxf(fmaxf(mb[0], mb[1]), fmaxf(mb[2], mb[3])));
      float al1 = fexp2((m1 - n1) * LOG2E), al2 = fexp2((m2 - n2) * LOG2E);
      asm volatile("" : "+v"(pb[0]), "+v"(pb[1]), "+v"(pb[2]), "+v"(pb[3]));
      if (__any(n2 - m2 > 40.f)) {
        bar();
        pp = u32x2{cvtpk(fexp2((x[0] - n1) * LOG2E), fexp2((x[1] - n1) * LOG2E)), cvtpk(fexp2((x[2] - n1) * LOG2E), fexp2((x[3] - n1) * LOG2E))};
        *pw = pp;
        l1 *= al1;
#pragma unroll
        for (int j = 0; j < 16; j++) { o0[j] *= al2; o1[j] *= al2; }
        al1 = al2 = 1.f;
        bar();
#pragma unroll
        for (int ks = 0; ks < 4; ks++) pb[ks] = *(LAS u32x4*)(pl + h2 * 144 + (16 * ks + 8 * hh) * 2);
      }
      m1 = n1; m2 = n2;
#pragma unroll
      for (int ks = 0; ks < 4; ks++) {
        o0 = mfma32(u32x4{ta[ks][0][0], ta[ks][0][1], ta[ks][1][0], ta[ks][1][1]}, pb[ks], o0);
        o1 = mfma32(u32x4{ta[ks][2][0], ta[ks][2][1], ta[ks][3][0], ta[ks][3][1]}, pb[ks], o1);
        SB();
        if (t < 6) {
          dma1(rc, dvo, nso + 2 * ks * 8192, dlo + (t & 1) * 65536 + 2 * ks * 1024);
          dma1(rc, dvo, nso + (2 * ks + 1) * 8192, dlo + (t & 1) * 65536 + (2 * ks + 1) * 1024);
        }
        SB();
      }
      l1 = (l1 + ((bf_lo(pp[0]) + bf_hi(pp[0])) + (bf_lo(pp[1]) + bf_hi(pp[1])))) * al1;
#pragma unroll
      for (int j = 0; j < 16; j++) { o0[j] *= al2; o1[j] *= al2; }
    }
    bar();
    {
      float am = 0.f;
#pragma unroll
      for (int j = 0; j < 16; j++) am = fmaxf(am, fmaxf(fabsf(o0[j]), fabsf(o1[j])));
      auto sw = __builtin_amdgcn_permlane32_swap(__float_as_uint(am), __float_as_uint(am), false, false);
      am = fmaxf(__uint_as_float(sw[0]), __uint_as_float(sw[1]));
      const int e = max(__builtin_amdgcn_frexp_expf(am), -127);
      LAS char* tp = lds + h2 * 1040 + (64 * w + 4 * hh) * 2;
      auto pkn = [&](float a, float b) { return __builtin_bit_cast(unsigned, __builtin_amdgcn_cvt_pknorm_i16(__builtin_ldexpf(a, -e), __builtin_ldexpf(b, -e))); };
#pragma unroll
      for (int j4 = 0; j4 < 4; j4++) {
        *(LAS u32x2*)(tp + j4 * 16) = u32x2{pkn(o0[4 * j4], o0[4 * j4 + 1]), pkn(o0[4 * j4 + 2], o0[4 * j4 + 3])};
        *(LAS u32x2*)(tp + 64 + j4 * 16) = u32x2{pkn(o1[4 * j4], o1[4 * j4 + 1]), pkn(o1[4 * j4 + 2], o1[4 * j4 + 3])};
      }
      if (hh == 0) *(LAS unsigned char*)(scr + 9216 + h2 * 8 + w) = (unsigned char)(e + 127);
      const float ls = xrow_sum(l1);
      if (grp == 0) lr[kb * 32 + h1] = ls;
      bar();
#pragma unroll
      for (int i = 0; i < 4; i++) {
        u32x4 v = *(LAS u32x4*)(lds + (4 * w + i) * 1040 + lane * 16);
#pragma unroll
        for (int k = 0; k < 4; k++) v[k] = bfi(PMASK, ptag(tg, k), v[k]);
        stsc(v, rws, WS_PO + (aid * 32 + 4 * w) * 1024 + lane * 16, i * 1024);
      }
      if (tid < 32) {
        const u32x2 eb = *(LAS u32x2*)(scr + 9216 + tid * 8);
        const float l = (lr[tid] + lr[32 + tid]) + (lr[64 + tid] + lr[96 + tid]);
        stsc(u32x4{bfi(0xffffu, tg.lo, __float_as_uint(m2)), bfi(255u, ltag(tg), __float_as_uint(l)), eb[0], eb[1]}, rws, WS_PML + (aid * 32 + tid) * 16, 0);
      }
      if (w == 0) {
        const unsigned t = __builtin_amdgcn_readfirstlane(__builtin_amdgcn_raw_ptr_buffer_atomic_add_i32(8 + 4 * DCH, rws, lane == 0 ? cntoff : OOB, 0, AUX_SC1));
        const unsigned bs = t + 8 + 4 * (lane - 8);
        ctl[lane] = lane == 3 ? t : lane == 4 ? DCH : lane == 5 ? (t + 8 + 4 * DCH >= NUNIT ? 0x10000u : 0u) : lane < 8 ? 0u : (lane < 8 + DCH && bs < NUNIT) ? bs : ~0u;
        if (lane < 32) ctl[64 + lane] = ~0u;
      }
      *(LAS u64*)(scr + SCR_SUM + SUM_STR + min(tid, SUM_STR / 8 - 1) * 8) = 0;
      bar();
    }
    const u32x4 g = ldsc(rws, qo, 0);
    cur = __builtin_amdgcn_readfirstlane(ctl[3]) + w;
#pragma unroll
    for (int i = 0; i < 8; i++) ra[i] = ldunit(i < 4 ? rk4 : rv4, voff, unit_off(cur), i & 3);
    SB();
    qwait(g, mktag(e0));
  } else {
    const Tag tg = mktag(e0);
    bar();
    {
      u32x4 bf[4];
#pragma unroll
      for (int ks = 0; ks < 4; ks++) bf[ks] = *(LAS u32x4*)(scr + 1024 + (l16 & 3) * 256 + ks * 64 + grp * 16);
      LAS char* tt = scr + 2048 + w * 512;
#pragma unroll
      for (int rt = 0; rt < 4; rt++) {
        f32x4 acc = {0.f, 0.f, 0.f, 0.f};
#pragma unroll
        for (int ks = 0; ks < 4; ks++) acc = mfma16(*(LAS u32x4*)(lds + (64 * w + 16 * rt + l16) * 256 + (((4 * ks + grp) ^ l16) * 16)), bf[ks], acc);
        if (l16 < 4) *(LAS u32x2*)(tt + l16 * 128 + (16 * rt + 4 * grp) * 2) = u32x2{cvtpk(acc[0], acc[1]), cvtpk(acc[2], acc[3])};
      }
      asm volatile("s_waitcnt lgkmcnt(0)" ::: "memory");
      const u32x2 v = *(LAS u32x2*)(tt + lane * 8);
      stsc(u32x4{v[0], tg.th, v[1], tg.th}, rws, WS_QABS + (grp * 32 + head) * 2048 + w * 256 + l16 * 16, 0);
    }
    bar();
    u32x4 wf[4][4], wr[16];
    {
      const rsrc_t ro = mkrs(woA + (size_t)head * 131072, 131072);
      dma_rows<16>(ro, 0, lds, 16 * w, lane);
      const rsrc_t rv = mkrs(wuv, 4u << 20);
      const int voff = (64 * w + 16 * grp) * 8192 + head * 256 + l16 * 16;
#pragma unroll
      for (int i = 0; i < 16; i++) wr[i] = ldnt(rv, voff, i * 8192);
      SB();
      asm volatile("s_waitcnt vmcnt(16)" ::: "memory");
      bar();
      wo_frags(lds, w, lane, wf);
      bar();
      const rsrc_t rb = mkrs(woB, 4u << 20);
      const int bvo = (lane >> 3) * 1024 + (((lane & 7) ^ ((lane >> 5) << 1)) * 16);
#pragma unroll
      for (int i = 0; i < 16; i++)
        dma1(rb, (i & 1) ? bvo ^ 64 : bvo, (4 * w + b4) * 131072 + (head >> 2) * 128 + i * 8192, (unsigned)(uintptr_t)lds + w * 16384 + i * 1024);
      SB();
      if (tid < 8) ((LAS unsigned*)(scr + 24576))[tid] = 0u;
    }
    const int hw = 4 * w + b4, cc = head >> 2, roff = WS_RS + hw * 16384 + lane * 16;
    u32x4 g[6];
#pragma unroll
    for (int s = 0; s < 6; s++) asm volatile("" : "=v"(g[s]));
    if (w == 0) {
      for (unsigned spins = 0;; spins++) {
        asm volatile("" ::: "memory");
        const u32x4 v = ldsc(rws, WS_PML + ((lane & 31) * 32 + head) * 16, 0);
        if (__builtin_popcount((unsigned)__builtin_amdgcn_ballot_w64(mok(v, tg))) >= NREL || spins > SPIN_CAP) break;
        __builtin_amdgcn_s_sleep(16);
      }
    }
    bar();
    {
      const int b = w >> 1, c4 = tid & 127;
      const unsigned ta = (lane & 1) ? ptag(tg, 2) : ptag(tg, 0), tb = (lane & 1) ? ptag(tg, 3) : ptag(tg, 1);
      u32x4 ml;
      u32x2 po[8];
#pragma unroll
      for (int s = 0; s < 8; s++) asm volatile("" : "=v"(po[s]));
      unsigned missP = 255;
      for (unsigned spins = 0;; spins++) {
        asm volatile("" ::: "memory");
        ml = ldsc(rws, WS_PML + ((8 * b + (lane & 7)) * 32 + head) * 16, 0);
#pragma unroll
        for (int s = 0; s < 8; s++)
          if (missP >> s & 1) po[s] = __builtin_amdgcn_raw_buffer_load_b64(rws, WS_PO + (8 * b * 32 + head) * 1024 + c4 * 8, s * 32768, AUX_SC1);
        SB();
#pragma unroll
        for (int s = 0; s < 8; s++) if (__all((((po[s][0] ^ ta) | (po[s][1] ^ tb)) & PMASK) == 0u)) missP &= ~(1u << s);
        if ((!missP && __all(mok(ml, tg))) || spins > SPIN_CAP) break;
        __builtin_amdgcn_s_sleep(2);
      }
#pragma unroll
      for (int s = 0; s < 6; s++) g[s] = ldsc(rws, roff, s * 1024);
      SB();
      ml[0] &= 0xffff0000u; ml[1] &= ~255u;
#pragma unroll
      for (int s = 0; s < 8; s++) po[s] = u32x2{bfi(PMASK, 0x00020002u, po[s][0]), bfi(PMASK, 0x00020002u, po[s][1])};
      float M = __uint_as_float(ml[0]);
      M = fmaxf(M, dpp<XOR1>(M)); M = fmaxf(M, dpp<XOR2>(M)); M = fmaxf(M, dpp<XOR7>(M));
      const float f = fexp2((__uint_as_float(ml[0]) - M) * LOG2E);
      float L = f * __uint_as_float(ml[1]);
      L += dpp<XOR1>(L); L += dpp<XOR2>(L); L += dpp<XOR7>(L);
      const float gl = __builtin_ldexpf(f, (int)((((w & 1) ? ml[3] : ml[2]) >> (8 * grp)) & 255) - 142);
      const float gs[8] = {dpp<0x150>(gl), dpp<0x151>(gl), dpp<0x152>(gl), dpp<0x153>(gl), dpp<0x154>(gl), dpp<0x155>(gl), dpp<0x156>(gl), dpp<0x157>(gl)};
      f32x4 a = {0.f, 0.f, 0.f, 0.f};
#pragma unroll
      for (int s = 0; s < 8; s++)
        a += gs[s] * f32x4{(float)(short)po[s][0], (float)((int)po[s][0] >> 16), (float)(short)po[s][1], (float)((int)po[s][1] >> 16)};
      const float inv = (32768.f / 32767.f) / L;
      const u32x2 v = {cvtpk(a[0] * inv, a[1] * inv), cvtpk(a[2] * inv, a[3] * inv)};
      *(LAS u32x2*)(scr + b * 1024 + c4 * 8) = v;
    }
    bar();
    {
      u32x4 xp[2][2];
#pragma unroll
      for (int s = 0; s < 2; s++) {
        LAS u32x4* xs = (LAS u32x4*)(scr + (2 * s + (l16 >> 3)) * 1024 + (64 * w + 16 * grp) * 2);
        xp[s][0] = xs[0]; xp[s][1] = xs[1];
      }
      f32x4 acc[2] = {{0.f, 0.f, 0.f, 0.f}, {0.f, 0.f, 0.f, 0.f}};
      diag_gemv(wr, xp, mask, acc);
      LAS float* red = (LAS float*)(scr + 8192);
#pragma unroll
      for (int s = 0; s < 2; s++) *(LAS f32x4*)(red + w * 512 + (2 * s + (grp >> 1)) * 128 + 8 * l16 + 4 * (grp & 1)) = acc[s];
      bar();
      if (tid < 256) {
        float a = 0.f, b = 0.f;
#pragma unroll
        for (int k = 0; k < 8; k++) { const f32x2 v = *(LAS f32x2*)(red + k * 512 + 2 * tid); a += v[0]; b += v[1]; }
        ((LAS unsigned*)(scr + 4096))[tid] = cvtpk(a, b);
      }
      bar();
    }
    const f32x4 yA = wo_calc(wf, scr + 4096, lane);
    drain();
    y_store(yA, w, lane, head, rws, WS_Y, tg);
    {
      const int q = l16 >> 2;
      LAS char* img = lds + w * 16384 + (8 * (q >> 1) + 4 * (grp & 1) + 2 * (grp >> 1) + (q & 1)) * 128 + 8 * (lane & 3);
      const int fl = (grp & 1) | (q & 2);
#pragma unroll
      for (int ks = 0; ks < 4; ks++)
#pragma unroll
        for (int tl = 0; tl < 4; tl++) {
          const u32x2 a = trread(img + (tl ^ fl) * 32 + ks * 4096), b = trread(img + (tl ^ fl) * 32 + ks * 4096 + 2048);
          wf[tl][ks] = u32x4{a[0], a[1], b[0], b[1]};
        }
    }
    const rsrc_t rout = mkrs(out, 8192);
    LAS unsigned* cnt = (LAS unsigned*)(scr + 24576);
    LAS char* const re = scr + 8192;
    LAS char* const rA = scr + 8192 + 8192;
    LAS char* rl = scr + 8192 + 10240 + (lane & 31) * 8;
    LAS char* rw = scr + 8192 + 10240 + w * 256 + (lane & 3) * 64 + grp * 16;
    LAS char* rx = rl + (w < 3 ? 8 + w : 11) * 256;
    int oo = lane < 32 ? b4 * 2048 + 1024 + cc * 128 + lane * 4 : OOB, oe = head == 0 && lane == 0 ? WS_EPOCH : OOB;
    asm volatile("" : "+v"(rl), "+v"(rw), "+v"(rx), "+v"(oo), "+v"(oe), "+v"(cnt));
    {
      const int ofa = WS_Y + (b4 * 8 + cc) * 16384 + w * 2048 + lane * 16;
      u32x4 x0, x1;
      for (unsigned spins = 0;; spins++) {
        asm volatile("" ::: "memory");
        x0 = ldsc(rws, ofa, 0); x1 = ldsc(rws, ofa + 1024, 0);
        if (__all(gok(x0, tg) & gok(x1, tg)) || spins > SPIN_CAP) break;
        __builtin_amdgcn_s_sleep(4);
      }
      const f32x2 r = {half_sum(gval(x0[0]) + gval(x1[0])), half_sum(gval(x0[2]) + gval(x1[2]))};
      if (lane < 32) *(volatile LAS f32x2*)(rA + w * 256 + lane * 8) = r;
      const unsigned n = arrive(cnt + 1, lane);
      const f32x2 v = rows_sum<8>(rA + (lane & 31) * 8);
      __builtin_amdgcn_raw_buffer_store_b32(cvtpk(v[0], v[1]), rout, lane < 32 && n == 8u ? b4 * 2048 + cc * 128 + l16 * 8 + grp * 4 : OOB, 0, OAUX);
    }
    {
      for (unsigned spins = 0;; spins++) {
        unsigned bad = 0;
#pragma unroll
        for (int s = 0; s < 6; s++) bad |= (g[s][0] ^ tg.lo) | (g[s][2] ^ tg.rb);
        if (__all((bad & (spins > SPIN_CAP * 4 ? 0u : 511u)) == 0u)) break;
        asm volatile("" ::: "memory");
#pragma unroll
        for (int s = 0; s < 6; s++) g[s] = ldsc(rws, roff + s * 1024, 0);
        SB();
      }
      unsigned pr[3];
#pragma unroll
      for (int t = 0; t < 3; t++) {
        const u32x4 g0 = g[t], g1 = g[t + 3];
        const float R0 = rec_r(g0), R1 = rec_r(g1), Rm = fmaxf(R0, R1);
        const double s0 = mkd(0u, p2hi(R0, Rm)), s1 = mkd(0u, p2hi(R1, Rm));
        const double inv = __builtin_amdgcn_rcp(__builtin_fma(rec_l(g0), s0, rec_l(g1) * s1));
        pr[t] = cvtpk((float)(__builtin_fma(rec_a(g0), s0, rec_a(g1) * s1) * inv), (float)(__builtin_fma(rec_b(g0), s0, rec_b(g1) * s1) * inv));
      }
      *(volatile LAS f32x4*)(re + ((l16 >> 2) * 8 + w) * 256 + (lane & 3) * 64 + grp * 16) = proj(pr, wf, lane);
      const unsigned n = arrive(cnt, lane);
#pragma unroll
      for (int u = 0; u < 2; u++) {
        const int t = u ? 2 : lane >> 5;
        const f32x2 v = rows_sum<8>(re + t * 2048 + (lane & 31) * 8);
        stsc(gpair(v[0], v[1], tg), rws, n != 8u || (u && lane >= 32) ? OOB : WS_YB + ((((b4 + 1 + t) & 3) * 8 + cc) * 4 + b4) * 512 + (lane & 31) * 16, 0);
      }
    }
    const int ofb = w < 3 ? WS_YB + ((b4 * 8 + cc) * 4 + ((b4 + 1 + w) & 3)) * 512 + (lane & 31) * 16 : OOB;
    u32x4 xb, p[NSLOT];
    unsigned bad;
    for (unsigned spins = 0;; spins++) {
      asm volatile("" ::: "memory");
      xb = ldsc(rws, ofb, 0);
#pragma unroll
      for (int s = 0; s < NSLOT; s++) p[s] = ldsc(rws, roff + 8192 + s * 1024, 0);
      unsigned m = spins > SPIN_CAP * 4 ? 0u : 511u;
      asm volatile("" : "+s"(m));
      SB();
      unsigned ax = min((((xb[0] ^ tg.tl) | (xb[2] ^ tg.tl)) >> 24) | (xb[1] ^ tg.th) | (xb[3] ^ tg.th), w < 3 ? 1u : 0u), az = 0;
#pragma unroll
      for (int s = 0; s < NSLOT; s++) { ax |= p[s][0] ^ tg.lo; az |= p[s][2] ^ tg.rb; }
      bad = az & 512u;
      if (__all(((ax | az) & m) == 0u)) break;
    }
    *(volatile LAS f32x2*)rx = f32x2{gval(xb[0]), gval(xb[2])};
    double S0, S1, L;
    if (__builtin_expect(__all(bad == 0u), 1)) {
      S0 = mkd(p[0][0], p[0][1]); S1 = mkd(p[0][2], p[0][3]); L = rec_l(p[0]);
#pragma unroll
      for (int s = 1; s < NSLOT; s++) { S0 += mkd(p[s][0], p[s][1]); S1 += mkd(p[s][2], p[s][3]); L += rec_l(p[s]); }
    } else {
      float R = -INFINITY;
      S0 = S1 = L = 0.0;
#pragma unroll
      for (int s = 0; s < NSLOT; s++) {
        const float Rs = rec_l(p[s]) != 0.0 ? rec_r(p[s]) : -INFINITY, Rn = fmaxf(R, Rs);
        const double s0 = mkd(0u, p2hi(R, Rn)), s1 = mkd(0u, p2hi(Rs, Rn));
        L = __builtin_fma(rec_l(p[s]), s1, L * s0);
        S0 = __builtin_fma(rec_a(p[s]), s1, S0 * s0); S1 = __builtin_fma(rec_b(p[s]), s1, S1 * s0);
        R = Rn;
      }
    }
    {
      const double inv = __builtin_amdgcn_rcp(L);
      const unsigned pr[1] = {cvtpk((float)(S0 * inv), (float)(S1 * inv))};
      const f32x4 y = proj(pr, wf, lane);
      if ((lane & 12) == 0) *(volatile LAS f32x4*)rw = y;
      const unsigned n = arrive(cnt + 2, lane);
      const f32x2 v = rows_sum<11>(rl);
      __builtin_amdgcn_raw_buffer_store_b32(cvtpk(v[0], v[1]), rout, n == 8u ? oo : OOB, 0, OAUX);
      __builtin_amdgcn_raw_buffer_store_b64(__builtin_bit_cast(u32x2, tg.e), rws, n == 8u ? oe : OOB, 0, AUX_SC1);
      if (w == 0) {
        const unsigned now = (unsigned)__builtin_amdgcn_s_memrealtime();
        const u32x4 t4 = __builtin_amdgcn_raw_buffer_load_b128(mkrs(ws + (8 << 20), 4096), lane * 16, 0, AUX_SC1);
        int mn = 1 << 30, mx = -(1 << 30);
        for (int i = 0; i < 4; i++) { const int d = (int)(now - t4[i]); mn = min(mn, d); mx = max(mx, d); }
        for (int o = 1; o < 64; o *= 2) { mn = min(mn, __shfl_xor(mn, o)); mx = max(mx, __shfl_xor(mx, o)); }
        const u32x4 t5 = __builtin_amdgcn_raw_buffer_load_b128(mkrs(ws + (9 << 20), 768), lane * 16, 0, AUX_SC1);
        int sm = (int)(t5[0] + t5[1] + t5[2] + t5[3]);
        for (int o = 1; o < 64; o *= 2) sm += __shfl_xor(sm, o);
        int ex = 6000 - mx + sm * 10 / 192;
        if (PROBE == 3) asm volatile("buffer_wbl2 sc1\n\ts_waitcnt vmcnt(0)" ::: "memory");
        ex = min(max(ex, 0), 20000);
        ex = __builtin_amdgcn_readfirstlane(ex);
        while ((int)((unsigned)__builtin_amdgcn_s_memrealtime() - now) < ex) __builtin_amdgcn_s_sleep(2);
      }
    }
    return;
  }
  }
  {
    const u32x4 q4 = *(LAS u32x4*)(qst + 256 + l16 * 16);
    const Mrg mg = mkmrg(scr + SCR_SUM + SUM_STR, lane, WS_RS + (head * 16 + 8 + sub) * 1024, mktag(e0));
    Acc A;
    b_units(ra, rk4, rv4, cur, q4, lane, mask, ctl, rws, cntoff, A);
    b_merge(mg, scr + 8192 + 8704, w, lane, A, rws, e0);
    SB();
    asm volatile("" :: "v"(ra[0]), "v"(ra[1]), "v"(ra[2]), "v"(ra[3]), "v"(ra[4]), "v"(ra[5]), "v"(ra[6]), "v"(ra[7]));
  }
}

extern "C" void kernel_launch(void* const* d_in, const int* in_sizes, int n_in, void* d_out, int out_size, void* d_ws, size_t ws_size,
                              hipStream_t stream) {
  k_all<<<256, 512, 0, stream>>>((const char*)d_in[0], (const char*)d_in[1], (const char*)d_in[2], (const char*)d_in[3], (const char*)d_in[4],
                                 (const char*)d_in[5], (const char*)d_in[6], (const char*)d_in[7], (const char*)d_in[8], (const char*)d_in[9],
                                 (const char*)d_in[10], (char*)d_ws, (unsigned*)d_out);
}
```

```cpp
#define PROBE 1
#define OAUX 16
#include <hip/hip_runtime.h>
#include <stdint.h>

typedef __attribute__((ext_vector_type(4))) unsigned u32x4;
typedef __attribute__((ext_vector_type(2))) unsigned u32x2;
typedef __attribute__((ext_vector_type(8))) unsigned u32x8;
typedef __attribute__((ext_vector_type(16))) unsigned u32x16;
typedef __attribute__((ext_vector_type(4))) float f32x4;
typedef __attribute__((ext_vector_type(2))) float f32x2;
typedef __attribute__((ext_vector_type(2))) double f64x2;
typedef __attribute__((ext_vector_type(16))) float f32x16;
typedef __attribute__((ext_vector_type(8))) __bf16 bf16x8;
typedef __attribute__((ext_vector_type(2))) __bf16 bf16x2;
typedef __attribute__((ext_vector_type(4))) short s16x4;
typedef unsigned long long u64;
typedef __amdgpu_buffer_rsrc_t rsrc_t;
#define LAS __attribute__((address_space(3)))
#define GAS __attribute__((address_space(1)))

#define CPF $$$
#define PAT "5c10p2ba43"
constexpr int WTH = 5;
constexpr int DCH = 2;
constexpr int NXA = 8, NXB = 8;
constexpr int NREL = 24;

constexpr int AUX_NT = 2, AUX_SC1 = 16, OOB = 0x40000000;
constexpr float SCALE = 0.08838834764831845f;
constexpr float LOG2E = 1.4426950408889634f;
constexpr unsigned SPIN_CAP = 1u << 17;
constexpr int NSLOT = 7, NUNIT = 256;

constexpr int WS_EPOCH = 0;
constexpr int CSTR    = 4352;
constexpr int WS_CNT  = 8192;
constexpr int WS_QB   = WS_CNT + 32 * CSTR;
constexpr int WS_QABS = WS_QB + 32 * 2048;
constexpr int WS_PML  = WS_QABS + 4 * 32 * 2048;
constexpr int WS_PO   = WS_PML + 32 * 32 * 16;
constexpr int WS_RS   = WS_PO + 32 * 32 * 1024;
constexpr int WS_Y    = WS_RS + 32 * 16 * 1024;
constexpr int WS_YB   = WS_Y + 4 * 8 * 32 * 512;
constexpr int WS_END  = WS_YB + 4 * 8 * 4 * 512;

constexpr int LDS_BYTES = 163840;
constexpr int LDS_SCR = 131072;
constexpr int SCR_SUM = 5120, SUM_STR = 1040;

__device__ __forceinline__ unsigned cvtpk(float lo, float hi) {
  f32x2 v = {lo, hi};
  return __builtin_bit_cast(unsigned, __builtin_convertvector(v, bf16x2));
}
__device__ __forceinline__ float bf_lo(unsigned u) { return __uint_as_float(u << 16); }
__device__ __forceinline__ float bf_hi(unsigned u) { return __uint_as_float(u & 0xffff0000u); }
__device__ __forceinline__ float rbf(float x) { return __uint_as_float(cvtpk(x, x) << 16); }
__device__ __forceinline__ float dot2(unsigned k, unsigned q, float acc) {
  return __builtin_amdgcn_fdot2_f32_bf16(__builtin_bit_cast(bf16x2, k), __builtin_bit_cast(bf16x2, q), acc, false);
}
template <int CTRL> __device__ __forceinline__ float dpp(float x) {
  return __builtin_bit_cast(float, __builtin_amdgcn_mov_dpp(__builtin_bit_cast(int, x), CTRL, 0xf, 0xf, true));
}
template <int CTRL> __device__ __forceinline__ unsigned dppu(unsigned x) {
  return (unsigned)__builtin_amdgcn_mov_dpp((int)x, CTRL, 0xf, 0xf, true);
}
constexpr int XOR1 = 0xB1, XOR2 = 0x4E, XOR7 = 0x141, XOR8 = 0x128, ROR4 = 0x124;
__device__ __forceinline__ float xrow_max(float x) {
  auto s = __builtin_amdgcn_permlane16_swap(__float_as_uint(x), __float_as_uint(x), false, false);
  x = fmaxf(__uint_as_float(s[0]), __uint_as_float(s[1]));
  auto t = __builtin_amdgcn_permlane32_swap(__float_as_uint(x), __float_as_uint(x), false, false);
  return fmaxf(__uint_as_float(t[0]), __uint_as_float(t[1]));
}
__device__ __forceinline__ float xrow_sum(float x) {
  auto s = __builtin_amdgcn_permlane16_swap(__float_as_uint(x), __float_as_uint(x), false, false);
  x = __uint_as_float(s[0]) + __uint_as_float(s[1]);
  auto t = __builtin_amdgcn_permlane32_swap(__float_as_uint(x), __float_as_uint(x), false, false);
  return __uint_as_float(t[0]) + __uint_as_float(t[1]);
}
__device__ __forceinline__ float row_max(float x) {
  x = fmaxf(x, dpp<XOR8>(x)); x = fmaxf(x, dpp<ROR4>(x)); x = fmaxf(x, dpp<XOR2>(x)); x = fmaxf(x, dpp<XOR1>(x));
  return x;
}
__device__ __forceinline__ float row_sum(float x) {
  x += dpp<XOR8>(x); x += dpp<ROR4>(x); x += dpp<XOR2>(x); x += dpp<XOR1>(x);
  return x;
}
__device__ __forceinline__ float fexp2(float x) { return __builtin_amdgcn_exp2f(x); }
__device__ __forceinline__ f32x4 mfma16(u32x4 a, u32x4 b, f32x4 c) {
  return __builtin_amdgcn_mfma_f32_16x16x32_bf16(__builtin_bit_cast(bf16x8, a), __builtin_bit_cast(bf16x8, b), c, 0, 0, 0);
}
__device__ __forceinline__ f32x16 mfma32(u32x4 a, u32x4 b, f32x16 c) {
  return __builtin_amdgcn_mfma_f32_32x32x16_bf16(__builtin_bit_cast(bf16x8, a), __builtin_bit_cast(bf16x8, b), c, 0, 0, 0);
}
__device__ __forceinline__ rsrc_t mkrs(const GAS char* p, unsigned bytes) {
  return __builtin_amdgcn_make_buffer_rsrc((void*)p, 0, bytes, 0x00020000);
}
__device__ __forceinline__ void bar() { asm volatile("s_waitcnt lgkmcnt(0)\n\ts_barrier" ::: "memory"); }
__device__ __forceinline__ void drain() { asm volatile("s_waitcnt vmcnt(0)" ::: "memory"); }
#define SB() __builtin_amdgcn_sched_barrier(0)
template <int N> __device__ __forceinline__ void vmwait() { asm volatile("s_waitcnt vmcnt(%0)" :: "n"(N) : "memory"); }
__device__ __forceinline__ unsigned swz(unsigned row) { return ((row & 3) << 2) | ((row >> 2) & 2); }
__device__ __forceinline__ u32x2 trread(LAS char* p) {
  return __builtin_bit_cast(u32x2, __builtin_amdgcn_ds_read_tr16_b64_v4i16((LAS s16x4*)p));
}

struct Tag { unsigned tl, th, lo, rb; u64 e; };
__device__ __forceinline__ Tag mktag(u64 e0) {
  Tag t;
  asm volatile("" : "+s"(e0));
  t.e = e0 * 6364136223846793005ull + 1442695040888963407ull;
  t.th = (unsigned)(t.e >> 32);
  t.lo = (unsigned)t.e;
  t.tl = t.lo & 0xff000000u;
  t.rb = ((t.lo >> 9) & 255u) | (~t.lo & 256u);
  return t;
}
__device__ __forceinline__ unsigned gpack(float v, const Tag& t) { return ((__float_as_uint(v) + 0x80u) >> 8) | t.tl; }
__device__ __forceinline__ u32x4 gpair(float a, float b, const Tag& t) { return u32x4{gpack(a, t), t.th, gpack(b, t), t.th}; }
__device__ __forceinline__ bool gok(u32x4 g, const Tag& t) {
  return ((((g[0] ^ t.tl) | (g[2] ^ t.tl)) >> 24) | (g[1] ^ t.th) | (g[3] ^ t.th)) == 0u;
}
__device__ __forceinline__ float gval(unsigned lo) { return __uint_as_float(lo << 8); }
__device__ __forceinline__ bool qok(u32x4 g, const Tag& t) { return ((g[1] ^ t.th) | (g[3] ^ t.th)) == 0u; }
__device__ __forceinline__ double mkd(unsigned lo, unsigned hi) { return __builtin_bit_cast(double, u32x2{lo, hi}); }
__device__ __forceinline__ u32x4 rpack(double a, double b, double l, float r, const Tag& t) {
  const u32x2 ua = __builtin_bit_cast(u32x2, a), ub = __builtin_bit_cast(u32x2, b), ul = __builtin_bit_cast(u32x2, l);
  const u64 ml = ((u64)(unsigned)__builtin_amdgcn_readfirstlane(ul[1]) << 32) | (unsigned)__builtin_amdgcn_readfirstlane(ul[0]);
  const u64 mr = (unsigned)__builtin_amdgcn_readfirstlane(__float_as_uint(r));
  const unsigned ta = t.lo & 511u, tb = t.rb;
  return u32x4{(ua[0] & ~1023u) | (__builtin_amdgcn_inverse_ballot_w64(ml) ? ta | 512u : ta), ua[1],
               (ub[0] & ~1023u) | (__builtin_amdgcn_inverse_ballot_w64(mr) ? tb | 512u : tb), ub[1]};
}
__device__ __forceinline__ u32x4 rpack0(f64x2 o, unsigned lw, int lane, unsigned ta, unsigned tb) {
  const u32x4 u = __builtin_bit_cast(u32x4, o);
  return u32x4{(u[0] & ~1023u) | (ta | (__builtin_amdgcn_ubfe(lw, lane, 1) << 9)), u[1], (u[2] & ~1023u) | tb, u[3]};
}
__device__ __forceinline__ bool rok(u32x4 g, const Tag& t) { return (((g[0] ^ t.lo) | (g[2] ^ t.rb)) & 511u) == 0u; }
constexpr unsigned PMASK = 0x00030003u;
__device__ __forceinline__ unsigned ptag(const Tag& t, int k) {
  const unsigned n = k == 0 ? t.lo : k == 3 ? t.lo >> 10 : ((t.lo >> (1 + 3 * k)) & 7u) | (~t.lo & 8u);
  return (n & 3u) | ((n & 12u) << 14);
}
__device__ __forceinline__ unsigned ltag(const Tag& t) { return ((t.lo >> 16) & 127u) | (~t.lo & 128u); }
__device__ __forceinline__ bool mok(u32x4 g, const Tag& t) { return (((g[0] ^ t.lo) & 0xffffu) | ((g[1] ^ ltag(t)) & 255u)) == 0u; }
__device__ __forceinline__ unsigned bfi(unsigned m, unsigned a, unsigned b) { return (a & m) | (b & ~m); }
__device__ __forceinline__ double rec_a(u32x4 g) { return mkd(g[0] & ~1023u, g[1]); }
__device__ __forceinline__ double rec_b(u32x4 g) { return mkd(g[2] & ~1023u, g[3]); }
__device__ __forceinline__ double rec_l(u32x4 g) { return __builtin_bit_cast(double, __builtin_amdgcn_ballot_w64((g[0] & 512u) != 0u)); }
__device__ __forceinline__ float rec_r(u32x4 g) { return __uint_as_float((unsigned)__builtin_amdgcn_ballot_w64((g[2] & 512u) != 0u)); }
__device__ __forceinline__ int edif(float a, float b) {
  int r;
  asm("v_cvt_i32_f32 %0, %1" : "=v"(r) : "v"(a - b));
  return r;
}
__device__ __forceinline__ unsigned p2hi(float a, float b) { return (unsigned)max(edif(a, b) + 1023, 0) << 20; }
__device__ __forceinline__ float bucket(float m) { return fmaf(__builtin_rintf(m * (1.f / 512.f)), 512.f, 0.f); }
__device__ __forceinline__ float rfl(float x) { return __uint_as_float(__builtin_amdgcn_readfirstlane(__float_as_uint(x))); }
__device__ __forceinline__ unsigned rdu(unsigned v, int l) { return __builtin_amdgcn_readlane(v, l); }

__device__ __forceinline__ void diag_gemv(const u32x4 (&wr)[16], const u32x4 (&xp)[2][2], const unsigned (&mask)[4], f32x4 (&acc)[2]) {
#pragma unroll
  for (int i = 0; i < 16; i++) {
#pragma unroll
    for (int s = 0; s < 2; s++) {
      const unsigned src = xp[s][i >> 3][(i >> 1) & 3];
      const unsigned dup = __builtin_amdgcn_perm(src, src, (i & 1) ? 0x03020302u : 0x01000100u);
      const u32x4 a = {dup & mask[0], dup & mask[1], dup & mask[2], dup & mask[3]};
      acc[s] = mfma16(a, wr[i], acc[s]);
    }
  }
}

constexpr int bitpos(char c) { for (int i = 0; i < 10; i++) if (PAT[i] == c) return 10 + i; return 31; }
constexpr int goff(int t) { int o = 0; for (int j = 0; j < 6; j++) o |= ((t >> j) & 1) << bitpos('0' + j); return o; }
struct Acc { double o[2]; double l; float R; };
__device__ __forceinline__ u32x4 ldnt(rsrc_t r, int voff, int so) { return __builtin_amdgcn_raw_buffer_load_b128(r, voff, so, AUX_NT); }
__device__ __forceinline__ u32x4 ldslot(rsrc_t r, int voff, int so, int i) { return ldnt(r, voff + goff(i & 3), so ^ goff(i & ~3)); }
__device__ __forceinline__ u32x4 ldunit(rsrc_t r, int voff, int so, int i) { return ldnt(r, voff + i * 1024, so); }
__device__ __forceinline__ u32x4 ldsc(rsrc_t r, int voff, int so) { return __builtin_amdgcn_raw_buffer_load_b128(r, voff, so, AUX_SC1); }
__device__ __forceinline__ void stsc(u32x4 v, rsrc_t r, int voff, int so) { __builtin_amdgcn_raw_buffer_store_b128(v, r, voff + so, 0, AUX_SC1); }
__device__ __forceinline__ float dot8(u32x4 k, u32x4 q) { return dot2(k[3], q[3], dot2(k[2], q[2], dot2(k[1], q[1], dot2(k[0], q[0], 0.f)))); }
#define TR_STEP(HALF, CTRL, BIT) _Pragma("unroll") for (int h = 0; h < (HALF); ++h) { \
    const float t1 = x[h] + dpp<CTRL>(x[h]), t2 = x[h + (HALF)] + dpp<CTRL>(x[h + (HALF)]); x[h] = (c & (BIT)) ? t2 : t1; }
template <int N> __device__ __forceinline__ float k_step(u32x4 (&ring)[16], u32x4 q, int c, rsrc_t rn, int voff, int so, bool ext, LAS char* xp) {
  float x[16];
#pragma unroll
  for (int i = 0; i < 16; i++) {
    x[i] = dot8(ring[i], q);
    asm volatile("s_waitcnt vmcnt(%1)" : "+v"(x[i]) : "n"(WTH - 1));
    if (i >= 16 - N && ext) ring[i] = *(volatile LAS u32x4*)(xp + (i - (16 - N)) * 1024);
    else ring[i] = ldslot(rn, voff, so, i);
    SB();
  }
  TR_STEP(8, XOR8, 8) TR_STEP(4, XOR7, 4) TR_STEP(2, XOR2, 2) TR_STEP(1, XOR1, 1)
  return rbf(rbf(x[0]) * SCALE);
}
template <int BC> __device__ __forceinline__ void pv(u32x4 v, unsigned pk, const unsigned (&mask)[4], f32x4& acc) {
  const unsigned d = dppu<0x150 + BC>(pk);
  acc = mfma16(u32x4{d & mask[0], d & mask[1], d & mask[2], d & mask[3]}, v, acc);
}
template <int I, int MODE> __device__ __forceinline__ void v_step(u32x4 (&ring)[16], unsigned pk, const unsigned (&mask)[4], f32x4& acc,
                                                                  rsrc_t rk, rsrc_t rv, int voff, int so) {
  pv<I>(ring[I], pk, mask, acc);
  if (MODE == 1 || (MODE == 2 && I < 8)) {
    asm volatile("s_waitcnt vmcnt(%1)" : "+v"(acc) : "n"(WTH - 1));
    ring[I] = MODE == 1 ? ldslot(rv, voff, so, I) : ldunit(I < 4 ? rk : rv, voff, so, I & 3);
  } else {
    asm volatile("" : "+v"(acc));
  }
  SB();
  if constexpr (I < 15) v_step<I + 1, MODE>(ring, pk, mask, acc, rk, rv, voff, so);
}
__device__ __forceinline__ float exp_ref(float mx, float& f) {
  const float M = __builtin_ceilf(mx * LOG2E);
  f = __builtin_amdgcn_fmed3f(fmaf(mx, LOG2E, -M), -1.f, 0.f);
  return M;
}
__device__ __forceinline__ unsigned lds_inc(LAS unsigned* p, int lane) {
  unsigned t = 0, z = 0;
  asm volatile("" : "+v"(z));
  if (lane == 0) t = __hip_atomic_fetch_add(p + z, 1u, __ATOMIC_RELAXED, __HIP_MEMORY_SCOPE_WORKGROUP);
  return t;
}
struct Take { unsigned unit, pend; bool fetch; };
__device__ __forceinline__ void lds_add(LAS unsigned* p, unsigned v) { __hip_atomic_fetch_add(p, v, __ATOMIC_RELAXED, __HIP_MEMORY_SCOPE_WORKGROUP); }
__device__ __forceinline__ Take take(LAS unsigned* ctl, unsigned kv, rsrc_t rws, int cntoff, int lane) {
  const unsigned k = __builtin_amdgcn_readfirstlane(kv);
  unsigned b, s;
  auto look = [&]() {
    const unsigned vs = ((volatile LAS unsigned*)ctl)[5], vb = ((volatile LAS unsigned*)ctl)[8 + (k >> 2)];
    s = __builtin_amdgcn_readfirstlane(vs); b = __builtin_amdgcn_readfirstlane(vb);
    return (b == ~0u) & ((s < 0x10000u) | ((s & 0xffffu) != 0u));
  };
  if (__builtin_expect(look(), 0))
    for (unsigned spins = 0; spins < SPIN_CAP * 16; spins++) {
      __builtin_amdgcn_s_sleep(1);
      if (!look()) break;
    }
  Take t;
  t.unit = min(b, (unsigned)NUNIT) + (k & 3);
  t.fetch = ((k & 3) == 0) & (b < NUNIT) & (s < 0x10000u);
  int off = OOB;
  if (t.fetch) {
    unsigned z = 0;
    asm volatile("" : "+v"(z));
    if (lane == 0) { lds_add(ctl + 5 + z, 1u); off = cntoff; }
    asm volatile("s_waitcnt lgkmcnt(0)" ::: "memory");
  }
  t.pend = __builtin_amdgcn_raw_ptr_buffer_atomic_add_i32(4, rws, off, 0, AUX_SC1);
  SB();
  return t;
}
__device__ __forceinline__ void give(LAS unsigned* ctl, const Take& t, int lane) {
  if (t.fetch && lane == 0) {
    unsigned z = 0;
    asm volatile("" : "+v"(z));
    if (t.pend < NUNIT) {
      const unsigned i = __hip_atomic_fetch_add(ctl + 4 + z, 1u, __ATOMIC_RELAXED, __HIP_MEMORY_SCOPE_WORKGROUP);
      ((volatile LAS unsigned*)ctl)[8 + i] = t.pend;
    }
    asm volatile("" ::: "memory");
    lds_add(ctl + 5 + z, t.pend >= NUNIT - 4 ? 0xffffu : ~0u);
  }
}
__device__ __forceinline__ int unit_off(unsigned t) { return (int)min(t, (unsigned)NUNIT) * 4096; }

__device__ __forceinline__ void b_units(u32x4 (&ra)[16], rsrc_t rk, rsrc_t rv, unsigned cur, u32x4 q, int lane, const unsigned (&mask)[4],
                                        LAS unsigned* ctl, rsrc_t rws, int cntoff, Acc& A) {
  const int c = lane & 15, voff = lane * 16;
  float Mr = -INFINITY;
  double s1 = 0.0;
  A.o[0] = A.o[1] = A.l = 0.0; A.R = cur < NUNIT ? -INFINITY : 0.f;
#pragma unroll 1
  while (cur < NUNIT) {
    const Take t = take(ctl, lds_inc(ctl, lane), rws, cntoff, lane);
    const int so = unit_off(t.unit);
    SB();
    float x[4];
#pragma unroll
    for (int i = 0; i < 4; i++) x[i] = dot8(ra[i], q);
    asm volatile("" : "+v"(x[0]), "+v"(x[1]), "+v"(x[2]), "+v"(x[3]));
    SB();
#pragma unroll
    for (int i = 0; i < 4; i++) {
      ra[i] = ldunit(rk, voff, so, i);
      SB();
    }
    TR_STEP(2, XOR8, 8) TR_STEP(1, XOR7, 4)
    x[0] += dpp<XOR2>(x[0]); x[0] += dpp<XOR1>(x[0]);
    const float s = rbf(rbf(x[0]) * SCALE);
    const float n = __builtin_rintf(s * LOG2E);
    float e = fexp2(__builtin_amdgcn_fmed3f(fmaf(s, LOG2E, -n), -1.f, 1.f)), d = n - Mr;
    asm volatile("" : "+v"(e));
    if (__builtin_expect(__any(d > 57.f), 0)) {
      float mx = fmaxf(n, dpp<XOR8>(n));
      mx = xrow_max(fmaxf(mx, dpp<ROR4>(mx)));
      const float Rn = rfl(fmaxf(A.R, bucket(mx)));
      const double s0 = mkd(0u, p2hi(A.R, Rn));
      A.o[0] *= s0; A.o[1] *= s0; A.l *= s0;
      A.R = Rn; Mr = mx; d = n - mx;
      s1 = mkd(0u, p2hi(mx, Rn));
    }
    const float p = __builtin_ldexpf(e, edif(d, 0.f));
    unsigned pk = cvtpk(p, p);
    asm volatile("" : "+v"(pk));
    SB();
    f32x4 acc = {0.f, 0.f, 0.f, 0.f};
    float ls = bf_lo(pk);
    pv<0>(ra[4], pk, mask, acc);  asm volatile("" : "+v"(acc)); ra[4] = ldunit(rv, voff, so, 0); ls += dpp<XOR8>(ls); asm volatile("" : "+v"(ls)); SB();
    pv<4>(ra[5], pk, mask, acc);  asm volatile("" : "+v"(acc)); ra[5] = ldunit(rv, voff, so, 1); ls += dpp<ROR4>(ls); asm volatile("" : "+v"(ls)); SB();
    pv<8>(ra[6], pk, mask, acc);  asm volatile("" : "+v"(acc)); ra[6] = ldunit(rv, voff, so, 2); SB();
    pv<12>(ra[7], pk, mask, acc); asm volatile("" : "+v"(acc)); ra[7] = ldunit(rv, voff, so, 3); SB();
    A.l = __builtin_fma((double)xrow_sum(ls), s1, A.l);
    asm volatile("" : "+v"(A.l));
    A.o[0] = __builtin_fma((double)(lane < 32 ? acc[0] : acc[2]), s1, A.o[0]);
    A.o[1] = __builtin_fma((double)(lane < 32 ? acc[1] : acc[3]), s1, A.o[1]);
    asm volatile("" : "+v"(A.o[0]), "+v"(A.o[1]));
    SB();
    cur = t.unit;
    if (cur >= NUNIT) break;
    give(ctl, t, lane);
  }
}
#undef TR_STEP
__device__ __forceinline__ int rec_col(int lane) { return 8 * (lane & 15) + 4 * ((lane >> 4) & 1) + 2 * (lane >> 5); }
struct Mrg { unsigned s0, s4, s8, ta, tb; int off; };
__device__ __forceinline__ Mrg mkmrg(LAS char* sum, int lane, int roff, const Tag& tg) {
  Mrg m = {(unsigned)(uintptr_t)sum, (unsigned)(uintptr_t)sum + (lane >> 5) * 4, (unsigned)(uintptr_t)sum + lane * 8, tg.lo & 511u, tg.rb, roff + lane * 16};
  asm volatile("" : "+v"(m.s0), "+v"(m.s4), "+v"(m.s8), "+v"(m.ta), "+v"(m.tb), "+v"(m.off));
  return m;
}
__device__ __forceinline__ void b_merge(const Mrg& m, LAS char* mrg, int w, int lane, const Acc& A, rsrc_t rws, u64 e0) {
  unsigned inc = 1;
  double a = A.o[0], b = A.o[1], l = A.l;
  if (__builtin_expect(__float_as_uint(A.R) != 0u, 0)) {
    *(volatile LAS f64x2*)(mrg + w * 1024 + lane * 16) = f64x2{a, b};
    *(volatile LAS double*)(mrg + 8192 + w * 8) = l;
    *(volatile LAS float*)(mrg + 8256 + w * 4) = A.R;
    a = b = l = 0.0;
    inc = 1u + (256u << w);
  }
  f64x2 o;
  unsigned n, lw;
  asm volatile("ds_add_f64 %3, %5\n\tds_add_f64 %3, %6 offset:512\n\ts_mov_b64 exec, 1\n\tds_add_f64 %3, %7 offset:1024\n\tds_add_u32 %3, %8 offset:1032\n\t"
               "s_mov_b64 exec, -1\n\tds_read_b32 %1, %9 offset:1032\n\tds_read_b32 %2, %4 offset:1024\n\tds_read2st64_b64 %0, %3 offset1:1\n\ts_waitcnt lgkmcnt(0)"
               : "=&v"(o), "=&v"(n), "=&v"(lw)
               : "v"(m.s8), "v"(m.s4), "v"(a), "v"(b), "v"(l), "v"(inc), "v"(m.s0)
               : "memory");
  stsc(rpack0(o, lw, lane, m.ta, m.tb), rws, n == 8u ? m.off : OOB, 0);
  SB();
  n = __builtin_amdgcn_readfirstlane(n);
  if (__builtin_expect(n > 8u && (n & 255u) == 8u, 0)) {
    double L = *(volatile LAS double*)(uintptr_t)(m.s0 + 1024);
    float R = L != 0.0 ? 0.f : -INFINITY;
#pragma unroll 1
    for (int i = 0; i < 8; i++)
      if (n >> (8 + i) & 1) {
        const f64x2 oi = *(volatile LAS f64x2*)(mrg + i * 1024 + lane * 16);
        const float Ri = *(volatile LAS float*)(mrg + 8256 + i * 4), Rn = fmaxf(R, Ri);
        const double s0 = mkd(0u, p2hi(R, Rn)), s1 = mkd(0u, p2hi(Ri, Rn));
        o[0] = __builtin_fma(oi[0], s1, o[0] * s0); o[1] = __builtin_fma(oi[1], s1, o[1] * s0);
        L = __builtin_fma(*(volatile LAS double*)(mrg + 8192 + i * 8), s1, L * s0);
        R = Rn;
      }
    stsc(rpack(o[0], o[1], L, R, mktag(e0)), rws, m.off, 0);
  }
}

__device__ __forceinline__ unsigned fz(unsigned row) { return ((row & 2) << 1) | ((row >> 1) & 2); }
__device__ __forceinline__ void dma1(rsrc_t rs, int voff, int so, unsigned ldsaddr) {
  unsigned keep;
  asm volatile("s_mov_b32 %0, m0\n\ts_mov_b32 m0, %1\n\ts_nop 0\n\tbuffer_load_dwordx4 %2, %3, %4 offen nt lds\n\ts_mov_b32 m0, %0"
               : "=&s"(keep) : "s"(ldsaddr), "v"(voff), "s"(rs), "s"(so) : "memory");
}
template <int NR>
__device__ __forceinline__ void dma_rows(rsrc_t rs, int srcoff, LAS char* img, int r0, int lane) {
#pragma unroll
  for (int u = 0; u < NR; u++) {
    const int r = r0 + u;
    unsigned keep;
    asm volatile("s_mov_b32 %0, m0\n\ts_mov_b32 m0, %1\n\ts_nop 0\n\tbuffer_load_dwordx4 %2, %3, %4 offen nt lds\n\ts_mov_b32 m0, %0"
                 : "=&s"(keep) : "s"((unsigned)(uintptr_t)img + r * 1024), "v"((lane ^ swz(r)) * 16), "s"(rs), "s"(srcoff + r * 1024) : "memory");
  }
  SB();
}

__device__ __forceinline__ void wo_frags(LAS char* img, int w, int lane, u32x4 (&wf)[4][4]) {
  const int g = lane >> 4, q = (lane & 15) >> 2, p = lane & 3;
#pragma unroll
  for (int ks = 0; ks < 4; ks++)
#pragma unroll
    for (int tl = 0; tl < 4; tl++) {
      const int ch = 8 * w + 2 * tl + (p >> 1);
      const int r0 = 32 * ks + 8 * g + q, r1 = r0 + 4;
      const u32x2 a = trread(img + r0 * 1024 + ((ch ^ swz(r0)) * 16) + 8 * (p & 1));
      const u32x2 b = trread(img + r1 * 1024 + ((ch ^ swz(r1)) * 16) + 8 * (p & 1));
      wf[tl][ks] = u32x4{a[0], a[1], b[0], b[1]};
    }
}
__device__ __forceinline__ f32x4 wo_calc(const u32x4 (&wf)[4][4], LAS char* cb, int lane) {
  const int n = lane & 15, grp = lane >> 4;
  u32x4 bf[4];
#pragma unroll
  for (int ks = 0; ks < 4; ks++) bf[ks] = *(LAS u32x4*)(cb + (n & 3) * 256 + ks * 64 + grp * 16);
  f32x4 acc[4];
#pragma unroll
  for (int tl = 0; tl < 4; tl++) acc[tl] = f32x4{0.f, 0.f, 0.f, 0.f};
#pragma unroll
  for (int ks = 0; ks < 4; ks++)
#pragma unroll
    for (int tl = 0; tl < 4; tl++) acc[tl] = mfma16(wf[tl][ks], bf[ks], acc[tl]);
  const f32x4 lo = (n & 4) ? acc[1] : acc[0], hi = (n & 4) ? acc[3] : acc[2];
  return (n & 8) ? hi : lo;
}
__device__ __forceinline__ void y_store(f32x4 y, int w, int lane, int slot, rsrc_t rws, int yoff, const Tag& tg) {
  const int off = yoff + (((lane & 3) * 8 + w) * 32 + slot) * 512 + ((lane & 12) + (lane >> 4)) * 16;
  stsc(gpair(y[0], y[1], tg), rws, off, 0);
  stsc(gpair(y[2], y[3], tg), rws, off, 256);
}
template <int K, int N> __device__ __forceinline__ unsigned bc1(const unsigned (&p)[N]) {
  unsigned v = dppu<0x150 + K>(p[0]);
  if constexpr (N > 1) {
    v = (unsigned)__builtin_amdgcn_update_dpp((int)v, (int)p[1], 0x150 + K, 0xf, 2, false);
    v = (unsigned)__builtin_amdgcn_update_dpp((int)v, (int)p[2], 0x150 + K, 0xf, 4, false);
  }
  return v;
}
template <int K, int N> __device__ __forceinline__ u32x4 bc4(const unsigned (&p)[N]) { return u32x4{bc1<K>(p), bc1<K + 1>(p), bc1<K + 2>(p), bc1<K + 3>(p)}; }
template <int N> __device__ __forceinline__ f32x4 proj(const unsigned (&pr)[N], const u32x4 (&wf)[4][4], int lane) {
  const u32x4 bf[4] = {bc4<0>(pr), bc4<4>(pr), bc4<8>(pr), bc4<12>(pr)};
  f32x4 acc[4];
#pragma unroll
  for (int tl = 0; tl < 4; tl++) acc[tl] = f32x4{0.f, 0.f, 0.f, 0.f};
#pragma unroll
  for (int ks = 0; ks < 4; ks++)
#pragma unroll
    for (int tl = 0; tl < 4; tl++) acc[tl] = mfma16(wf[tl][ks], bf[ks], acc[tl]);
  const f32x4 lo = (lane & 1) ? acc[1] : acc[0], hi = (lane & 1) ? acc[3] : acc[2];
  f32x4 r = (lane & 2) ? hi : lo;
  asm volatile("" : "+v"(r));
  return r;
}
__device__ __forceinline__ unsigned arrive(LAS unsigned* p, int lane) {
  unsigned z = 0;
  asm volatile("" : "+v"(z) :: "memory");
  if (lane == 0) lds_add(p + z, 1u);
  asm volatile("" ::: "memory");
  return *(volatile LAS unsigned*)p;
}
template <int N> __device__ __forceinline__ f32x2 rows_sum(LAS char* p) {
  f32x2 v[N];
#pragma unroll
  for (int i = 0; i < N; i++) v[i] = *(volatile LAS f32x2*)(p + i * 256);
#pragma unroll
  for (int st = 1; st < N; st *= 2)
#pragma unroll
    for (int i = 0; i + st < N; i += 2 * st) v[i] += v[i + st];
  return v[0];
}
__device__ __forceinline__ float half_sum(float x) {
  auto t = __builtin_amdgcn_permlane32_swap(__float_as_uint(x), __float_as_uint(x), false, false);
  return __uint_as_float(t[0]) + __uint_as_float(t[1]);
}

__global__ __launch_bounds__(512, 2) void k_all(const char* a0_, const char* a1_, const char* a2_, const char* a3_, const char* a4_, const char* a5_,
                                                const char* a6_, const char* a7_, const char* a8_, const char* a9_, const char* a10_, char* a11_, unsigned* a12_) {
  u32x16 ka; u32x8 kb_; u32x2 kc_;
  asm volatile("s_load_dwordx16 %0, %3, 0x0\n\ts_load_dwordx8 %1, %3, 0x40\n\ts_load_dwordx2 %2, %3, 0x60"
               : "=&s"(ka), "=&s"(kb_), "=&s"(kc_) : "s"(__builtin_amdgcn_kernarg_segment_ptr()));
  __shared__ __attribute__((aligned(16))) char smem[LDS_BYTES];
  LAS char* lds = (LAS char*)smem;
  LAS char* scr = lds + LDS_SCR;
  LAS unsigned* ctl = (LAS unsigned*)scr;
  const int tid = threadIdx.x, lane = tid & 63, w = __builtin_amdgcn_readfirstlane(tid >> 6);
  const int blk = blockIdx.x, head = blk >> 3, prod = (blk + head) & 7;
  const int sub = (0x54372106u >> (4 * prod)) & 7;
  const unsigned tst = (unsigned)__builtin_amdgcn_s_memrealtime();
  unsigned cpf = 0;
  {
    u64 pc;
    asm volatile("s_getpc_b64 %0" : "=s"(pc));
    cpf = __builtin_amdgcn_raw_buffer_load_b32(mkrs((const GAS char*)(pc & ~255ull), CPF), w == 7 ? head * 2048 + lane * 32 : OOB, 0, 0);
    SB();
  }
  {
    unsigned c = 16 + 4 * (DCH * sub + tid);
    asm volatile("" : "+v"(c));
    c = tid < 8 + DCH ? c : ~0u;
    c = tid < 8 ? 0u : c;
    ctl[tid] = tid == 4 ? DCH : c;
    *(LAS u64*)(scr + SCR_SUM + min(tid, 2 * SUM_STR / 8 - 1) * 8) = 0;
  }
  const int l16 = lane & 15, grp = lane >> 4;
  const int b4 = head & 3, cntoff = WS_CNT + head * CSTR;
  const int bm = (b4 + 1 + (sub >= 3 ? sub - 3 : sub)) & 3;
  const size_t base = (size_t)(bm * 32 + head) << 20;
  const int so = ((w & 1) << bitpos('a')) | (((w >> 1) & 1) << bitpos('b')) | ((w >> 2) << bitpos('c')) | (sub >= 3 ? 1 << bitpos('p') : 0);
  int v0 = l16 * 16 + grp * (sub < 6 ? 256 : 131072) + (sub < 6 ? so : w * 524288 + head * 256);
  unsigned boff = sub < 6 ? (unsigned)base : 0u, bsz = sub < 6 ? 1u << 20 : 4u << 20;
  int s0[WTH];
#pragma unroll
  for (int i = 0; i < WTH; i++) {
    s0[i] = sub < 6 ? goff(i) : i * 8192;
    asm volatile("" : "+s"(s0[i]));
  }
  asm volatile("" : "+v"(v0), "+s"(boff), "+s"(bsz));
  SB();
  asm volatile("s_waitcnt lgkmcnt(0)" : "+s"(ka), "+s"(kb_), "+s"(kc_));
  auto ptr = [](unsigned lo, unsigned hi) { return (GAS char*)(((u64)hi << 32) | lo); };
  GAS char *const xA = ptr(ka[0], ka[1]), *const ckv = ptr(ka[2], ka[3]), *const wqA = ptr(ka[4], ka[5]), *const wuk = ptr(ka[6], ka[7]),
           *const wuv = ptr(ka[8], ka[9]), *const woA = ptr(ka[10], ka[11]), *const xB = ptr(ka[12], ka[13]), *const kc = ptr(ka[14], ka[15]),
           *const vc = ptr(kb_[0], kb_[1]), *const wqB = ptr(kb_[2], kb_[3]), *const woB = ptr(kb_[4], kb_[5]), *const ws = ptr(kb_[6], kb_[7]),
           *const out = ptr(kc_[0], kc_[1]);
  const u64 e0 = *(const __attribute__((address_space(4))) u64*)(u64)(ws + WS_EPOCH);
  const rsrc_t r0 = mkrs((sub < 6 ? kc : sub == 6 ? wqB : wqA) + boff, bsz);
  SB();
  u32x4 ra[16];
#pragma unroll
  for (int i = 0; i < WTH; i++) ra[i] = ldnt(r0, v0, s0[i]);
  SB();
  u32x4 xp[2][2];
  if (sub >= 6) {
    const GAS char* x = sub == 6 ? xB : xA;
#pragma unroll
    for (int s = 0; s < 2; s++) {
      const GAS u32x4* xs = (const GAS u32x4*)(x + (2 * s + (l16 >> 3)) * 1024 + (64 * w + 16 * grp) * 2);
      xp[s][0] = xs[0]; xp[s][1] = xs[1];
    }
#pragma unroll
    for (int i = WTH; i < 16; i++) ra[i] = ldnt(r0, v0, i * 8192);
    asm volatile("" ::: "memory");
    SB();
  }
  const rsrc_t rws = mkrs(ws, WS_END);
  __builtin_amdgcn_raw_buffer_store_b32(tst, mkrs(ws + (8 << 20), 4096), tid == 0 ? blk * 4 : OOB, 0, AUX_SC1);
  unsigned mask[4];
#pragma unroll
  for (int v = 0; v < 4; v++) mask[v] = (((lane & 7) >> 1) == v) ? ((lane & 1) ? 0xffff0000u : 0x0000ffffu) : 0u;
  const size_t base4 = (size_t)(b4 * 32 + head) << 20;
  const rsrc_t rk4 = mkrs(kc + base4, 1u << 20), rv4 = mkrs(vc + base4, 1u << 20);
  const int voff = lane * 16;
  LAS char* qst = scr + 1024 + w * 512;
  const int qo = WS_QB + head * 2048 + (lane < 32 ? bm : b4) * 512 + (lane & 31) * 16;
  auto qwait = [&](u32x4 g, const Tag& tg) {
    if (!__all(qok(g, tg))) {
      for (unsigned spins = 0;; spins++) {
        asm volatile("" ::: "memory");
        g = ldsc(rws, qo, 0);
        if (__all(qok(g, tg)) || spins > SPIN_CAP) break;
        __builtin_amdgcn_s_sleep(2);
      }
    }
    *(LAS u32x2*)(qst + lane * 8) = u32x2{g[0], g[2]};
  };
  unsigned cur;

  if (__builtin_expect(sub < 6, 1)) {
    const rsrc_t rv = mkrs(vc + base, 1u << 20);
    u32x4 rb[16], g;
#pragma unroll
    for (int i = WTH; i < 32 + NXA + NXB; i++) {
      if (i == WTH) asm volatile("s_barrier" ::: "memory");
      vmwait<WTH - 1>();
      if (i == 32 + NXA + NXB - WTH) asm volatile("buffer_load_dwordx4 %0, %1, %2, 0 offen sc1" : "=&v"(g) : "v"(qo), "s"(rws));
      if (i < 32) {
        const u32x4 v = ldslot(r0, voff, so, i);
        if (i < 16) ra[i] = v; else rb[i - 16] = v;
      } else {
        const int j = i < 32 + NXA ? i + 16 - NXA : i + 32 - NXA - NXB;
        dma1(r0, voff + goff(j & 3), so ^ goff(j & ~3), (unsigned)(uintptr_t)lds + w * 16384 + (i - 32) * 1024);
      }
      SB();
    }
    asm volatile("s_waitcnt vmcnt(%1)" : "+v"(g) : "n"(WTH), "v"(cpf));
    qwait(g, mktag(e0));
    const u32x4 q = *(LAS u32x4*)(qst + l16 * 16);
    float sc[4] = {0.f, 0.f, 0.f, 0.f};
#pragma unroll 1
    for (int it = 0; it < 2; it++) {
      const rsrc_t rn = it ? rv : r0;
      sc[2] = sc[0]; sc[3] = sc[1];
      sc[1] = k_step<NXA>(ra, q, l16, rn, voff, so ^ (it ? goff(48) : goff(32)), it == 0, lds + w * 16384 + voff);
      sc[0] = k_step<NXB>(rb, q, l16, rn, voff, so ^ (it ? goff(32) : goff(48)), it == 0, lds + w * 16384 + NXA * 1024 + voff);
    }
    const float mx = xrow_max(row_max(fmaxf(fmaxf(sc[0], sc[1]), fmaxf(sc[2], sc[3]))));
    float f, l = 0.f;
    const float M = exp_ref(mx, f);
    unsigned pk[4];
#pragma unroll
    for (int k = 0; k < 4; k++) {
      const float p = fexp2(fmaf(sc[k] - mx, LOG2E, f));
      pk[k] = cvtpk(p, p);
      l += bf_lo(pk[k]);
    }
    l = xrow_sum(row_sum(l));
    cur = 8 * sub + w;
    f32x4 acc = {0.f, 0.f, 0.f, 0.f};
    v_step<0, 1>(ra, pk[0], mask, acc, r0, rv, voff, so ^ goff(16));
    v_step<0, 1>(rb, pk[1], mask, acc, r0, rv, voff, so);
    v_step<0, 2>(ra, pk[2], mask, acc, rk4, rv4, voff, cur * 4096);
    v_step<0, 0>(rb, pk[3], mask, acc, r0, rv, voff, 0);
    Acc A;
    A.R = rfl(bucket(M));
    const double sm = mkd(0u, p2hi(M, A.R));
    A.o[0] = (lane < 32 ? acc[0] : acc[2]) * sm; A.o[1] = (lane < 32 ? acc[1] : acc[3]) * sm;
    A.l = l * sm;
    b_merge(mkmrg(scr + SCR_SUM, lane, WS_RS + (head * 16 + sub) * 1024, mktag(e0)), scr + 8192, w, lane, A, rws, e0);
  } else {
  {
    __builtin_amdgcn_raw_buffer_store_b32(48 + 24 * DCH, rws, (sub == 6 && tid == 0) ? cntoff : OOB, 0, AUX_SC1);
    {
      const rsrc_t rk = mkrs(wuk, sub == 7 ? 4u << 20 : 0u);
#pragma unroll
      for (int i = 0; i < 16; i++)
        __builtin_amdgcn_raw_ptr_buffer_load_lds(rk, (LAS void*)(lds + w * 16384 + i * 1024), 16,
                                                 (64 * w + grp) * 8192 + head * 256 + ((l16 ^ (4 * (i & 3) + grp)) * 16), i * 4 * 8192, 0, AUX_NT);
    }
    SB();
    f32x4 acc[2] = {{0.f, 0.f, 0.f, 0.f}, {0.f, 0.f, 0.f, 0.f}};
    diag_gemv(ra, xp, mask, acc);
    LAS float* red = (LAS float*)(scr + 8192);
#pragma unroll
    for (int s = 0; s < 2; s++) *(LAS f32x4*)(red + w * 512 + (2 * s + (grp >> 1)) * 128 + 8 * l16 + 4 * (grp & 1)) = acc[s];
    asm volatile("" :: "v"(cpf));
    __builtin_amdgcn_s_waitcnt(0x0f70);
    drain();
    bar();
    if (tid < 128) {
      f32x4 a = {0.f, 0.f, 0.f, 0.f};
#pragma unroll
      for (int k = 0; k < 8; k++) a += *(LAS f32x4*)(red + k * 512 + 4 * tid);
      const unsigned p0 = cvtpk(a[0], a[1]), p1 = cvtpk(a[2], a[3]);
      const Tag tg = mktag(e0);
      stsc(u32x4{p0, tg.th, p1, tg.th}, rws, sub == 6 ? WS_QB + head * 2048 + tid * 16 : OOB, 0);
      *(LAS u32x2*)(scr + 1024 + tid * 8) = u32x2{p0, p1};
    }
  }

  if (sub == 6) {
    const int aid = ((head & 6) << 2) | ((head & 1) << 2) | (head >> 3), b = aid >> 3;
    const rsrc_t rc = mkrs(ckv + (size_t)aid * 524288, 524288);
    const int dvo = (lane >> 3) * 1024 + w * 128 + (((lane & 7) ^ fz(lane >> 3)) * 16);
    const unsigned dlo = (unsigned)(uintptr_t)lds + w * 8192;
    const int xt = (aid & 7) << 16;
#pragma unroll
    for (int j = 0; j < 16; j++) dma1(rc, dvo, (j * 8192) ^ xt, dlo + (j >> 3) * 65536 + (j & 7) * 1024);
    SB();
    const Tag tg = mktag(e0);
    bar();
    {
      u32x4 t[8];
      unsigned miss = 15;
      __builtin_amdgcn_s_sleep(90);
      for (unsigned spins = 0;; spins++) {
        asm volatile("" ::: "memory");
#pragma unroll
        for (int i = 0; i < 4; i++)
          if (miss >> i & 1) {
            t[2 * i] = ldsc(rws, WS_QABS + (b * 32 + w) * 2048 + lane * 16, i * 16384);
            t[2 * i + 1] = ldsc(rws, WS_QABS + (b * 32 + w) * 2048 + lane * 16 + 1024, i * 16384);
          }
        SB();
#pragma unroll
        for (int i = 0; i < 4; i++) if (__all(qok(t[2 * i], tg) & qok(t[2 * i + 1], tg))) miss &= ~(1u << i);
        if (!miss || spins > SPIN_CAP) break;
        __builtin_amdgcn_s_sleep(4);
      }
#pragma unroll
      for (int i = 0; i < 8; i++) {
        const int row = 8 * (i >> 1) + w;
        *(LAS u32x2*)(scr + row * 1024 + (((32 * (i & 1) + (lane >> 1)) ^ swz(row)) * 16) + (lane & 1) * 8) = u32x2{t[i][0], t[i][2]};
      }
    }
    bar();
    const int kb = w & 3, hb = w >> 2;
    u32x4 qf[16];
    {
      const int row = 16 * hb + l16;
#pragma unroll
      for (int ks = 0; ks < 16; ks++) qf[ks] = *(LAS u32x4*)(scr + row * 1024 + (((4 * ks + grp) ^ swz(row)) * 16));
    }
    LAS float* mx = (LAS float*)scr;
    LAS char* pl = scr + 1024;
    LAS float* lr = (LAS float*)(scr + 8192);
    f32x16 o0, o1;
#pragma unroll
    for (int j = 0; j < 16; j++) { o0[j] = 0.f; o1[j] = 0.f; }
    float m1 = -INFINITY, m2 = -INFINITY, l1 = 0.f;
    const int h1 = 16 * hb + l16, h2 = lane & 31, hh = lane >> 5;
    const int arow = 16 * kb + l16;
    const int tq = (lane & 15) >> 2, tp = lane & 3, tblk = (lane >> 4) & 1;
    const int qa0 = arow * 128 + ((grp ^ fz(arow)) * 16), qa1 = qa0 ^ 64;
    const int pr = 8 * hh + tq, pc = 2 * tblk + (tp >> 1);
    const int pa0 = w * 8192 + pr * 128 + ((pc ^ fz(pr)) * 16) + 8 * (tp & 1), pa1 = w * 8192 + (pr + 4) * 128 + ((pc ^ fz(pr + 4)) * 16) + 8 * (tp & 1);
#pragma unroll 1
    for (int t = 0; t < 8; t++) {
      if (t < 7) asm volatile("s_waitcnt vmcnt(8)" ::: "memory"); else asm volatile("s_waitcnt vmcnt(0)" ::: "memory");
      bar();
      LAS char* img = lds + (t & 1) * 65536;
      f32x4 s0 = {0.f, 0.f, 0.f, 0.f}, s1 = s0;
      u32x4 ka[16];
      u32x2 ta[4][4];
      const int nso = t < 6 ? ((t + 2) * 65536) ^ xt : OOB;
#pragma unroll
      for (int ks = 0; ks < 16; ks++) ka[ks] = *(LAS u32x4*)(img + ((ks & 1) ? qa1 : qa0) + (ks >> 1) * 8192);
      SB();
#pragma unroll
      for (int k2 = 0; k2 < 8; k2++) {
        s0 = mfma16(ka[2 * k2], qf[2 * k2], s0);
        s1 = mfma16(ka[2 * k2 + 1], qf[2 * k2 + 1], s1);
      }
      asm volatile("" : "+v"(s0), "+v"(s1));
      SB();
#pragma unroll
      for (int ks = 0; ks < 4; ks++) {
        ta[ks][0] = trread(img + pa0 + ks * 2048); ta[ks][1] = trread(img + pa1 + ks * 2048);
        ta[ks][2] = trread(img + (pa0 ^ 64) + ks * 2048); ta[ks][3] = trread(img + (pa1 ^ 64) + ks * 2048);
      }
      SB();
      float x[4];
#pragma unroll
      for (int j = 0; j < 4; j++) x[j] = rbf(rbf(s0[j] + s1[j]) * SCALE);
      const float lm = xrow_max(fmaxf(fmaxf(x[0], x[1]), fmaxf(x[2], x[3])));
      if (grp == 0) mx[h1 * 4 + kb] = lm;
      LAS u32x2* pw = (LAS u32x2*)(pl + h1 * 144 + (16 * kb + 4 * grp) * 2);
      u32x2 pp = {cvtpk(fexp2((x[0] - m1) * LOG2E), fexp2((x[1] - m1) * LOG2E)), cvtpk(fexp2((x[2] - m1) * LOG2E), fexp2((x[3] - m1) * LOG2E))};
      *pw = pp;
      bar();
      const f32x4 ma = *(LAS f32x4*)(mx + h1 * 4), mb = *(LAS f32x4*)(mx + h2 * 4);
      u32x4 pb[4];
#pragma unroll
      for (int ks = 0; ks < 4; ks++) pb[ks] = *(LAS u32x4*)(pl + h2 * 144 + (16 * ks + 8 * hh) * 2);
      const float n1 = fmaxf(m1, fmaxf(fmaxf(ma[0], ma[1]), fmaxf(ma[2], ma[3])));
      const float n2 = fmaxf(m2, fmaxf(fmaxf(mb[0], mb[1]), fmaxf(mb[2], mb[3])));
      float al1 = fexp2((m1 - n1) * LOG2E), al2 = fexp2((m2 - n2) * LOG2E);
      asm volatile("" : "+v"(pb[0]), "+v"(pb[1]), "+v"(pb[2]), "+v"(pb[3]));
      if (__any(n2 - m2 > 40.f)) {
        bar();
        pp = u32x2{cvtpk(fexp2((x[0] - n1) * LOG2E), fexp2((x[1] - n1) * LOG2E)), cvtpk(fexp2((x[2] - n1) * LOG2E), fexp2((x[3] - n1) * LOG2E))};
        *pw = pp;
        l1 *= al1;
#pragma unroll
        for (int j = 0; j < 16; j++) { o0[j] *= al2; o1[j] *= al2; }
        al1 = al2 = 1.f;
        bar();
#pragma unroll
        for (int ks = 0; ks < 4; ks++) pb[ks] = *(LAS u32x4*)(pl + h2 * 144 + (16 * ks + 8 * hh) * 2);
      }
      m1 = n1; m2 = n2;
#pragma unroll
      for (int ks = 0; ks < 4; ks++) {
        o0 = mfma32(u32x4{ta[ks][0][0], ta[ks][0][1], ta[ks][1][0], ta[ks][1][1]}, pb[ks], o0);
        o1 = mfma32(u32x4{ta[ks][2][0], ta[ks][2][1], ta[ks][3][0], ta[ks][3][1]}, pb[ks], o1);
        SB();
        if (t < 6) {
          dma1(rc, dvo, nso + 2 * ks * 8192, dlo + (t & 1) * 65536 + 2 * ks * 1024);
          dma1(rc, dvo, nso + (2 * ks + 1) * 8192, dlo + (t & 1) * 65536 + (2 * ks + 1) * 1024);
        }
        SB();
      }
      l1 = (l1 + ((bf_lo(pp[0]) + bf_hi(pp[0])) + (bf_lo(pp[1]) + bf_hi(pp[1])))) * al1;
#pragma unroll
      for (int j = 0; j < 16; j++) { o0[j] *= al2; o1[j] *= al2; }
    }
    bar();
    {
      float am = 0.f;
#pragma unroll
      for (int j = 0; j < 16; j++) am = fmaxf(am, fmaxf(fabsf(o0[j]), fabsf(o1[j])));
      auto sw = __builtin_amdgcn_permlane32_swap(__float_as_uint(am), __float_as_uint(am), false, false);
      am = fmaxf(__uint_as_float(sw[0]), __uint_as_float(sw[1]));
      const int e = max(__builtin_amdgcn_frexp_expf(am), -127);
      LAS char* tp = lds + h2 * 1040 + (64 * w + 4 * hh) * 2;
      auto pkn = [&](float a, float b) { return __builtin_bit_cast(unsigned, __builtin_amdgcn_cvt_pknorm_i16(__builtin_ldexpf(a, -e), __builtin_ldexpf(b, -e))); };
#pragma unroll
      for (int j4 = 0; j4 < 4; j4++) {
        *(LAS u32x2*)(tp + j4 * 16) = u32x2{pkn(o0[4 * j4], o0[4 * j4 + 1]), pkn(o0[4 * j4 + 2], o0[4 * j4 + 3])};
        *(LAS u32x2*)(tp + 64 + j4 * 16) = u32x2{pkn(o1[4 * j4], o1[4 * j4 + 1]), pkn(o1[4 * j4 + 2], o1[4 * j4 + 3])};
      }
      if (hh == 0) *(LAS unsigned char*)(scr + 9216 + h2 * 8 + w) = (unsigned char)(e + 127);
      const float ls = xrow_sum(l1);
      if (grp == 0) lr[kb * 32 + h1] = ls;
      bar();
#pragma unroll
      for (int i = 0; i < 4; i++) {
        u32x4 v = *(LAS u32x4*)(lds + (4 * w + i) * 1040 + lane * 16);
#pragma unroll
        for (int k = 0; k < 4; k++) v[k] = bfi(PMASK, ptag(tg, k), v[k]);
        stsc(v, rws, WS_PO + (aid * 32 + 4 * w) * 1024 + lane * 16, i * 1024);
      }
      if (tid < 32) {
        const u32x2 eb = *(LAS u32x2*)(scr + 9216 + tid * 8);
        const float l = (lr[tid] + lr[32 + tid]) + (lr[64 + tid] + lr[96 + tid]);
        stsc(u32x4{bfi(0xffffu, tg.lo, __float_as_uint(m2)), bfi(255u, ltag(tg), __float_as_uint(l)), eb[0], eb[1]}, rws, WS_PML + (aid * 32 + tid) * 16, 0);
      }
      if (w == 0) {
        const unsigned t = __builtin_amdgcn_readfirstlane(__builtin_amdgcn_raw_ptr_buffer_atomic_add_i32(8 + 4 * DCH, rws, lane == 0 ? cntoff : OOB, 0, AUX_SC1));
        const unsigned bs = t + 8 + 4 * (lane - 8);
        ctl[lane] = lane == 3 ? t : lane == 4 ? DCH : lane == 5 ? (t + 8 + 4 * DCH >= NUNIT ? 0x10000u : 0u) : lane < 8 ? 0u : (lane < 8 + DCH && bs < NUNIT) ? bs : ~0u;
        if (lane < 32) ctl[64 + lane] = ~0u;
      }
      *(LAS u64*)(scr + SCR_SUM + SUM_STR + min(tid, SUM_STR / 8 - 1) * 8) = 0;
      bar();
    }
    const u32x4 g = ldsc(rws, qo, 0);
    cur = __builtin_amdgcn_readfirstlane(ctl[3]) + w;
#pragma unroll
    for (int i = 0; i < 8; i++) ra[i] = ldunit(i < 4 ? rk4 : rv4, voff, unit_off(cur), i & 3);
    SB();
    qwait(g, mktag(e0));
  } else {
    const Tag tg = mktag(e0);
    bar();
    {
      u32x4 bf[4];
#pragma unroll
      for (int ks = 0; ks < 4; ks++) bf[ks] = *(LAS u32x4*)(scr + 1024 + (l16 & 3) * 256 + ks * 64 + grp * 16);
      LAS char* tt = scr + 2048 + w * 512;
#pragma unroll
      for (int rt = 0; rt < 4; rt++) {
        f32x4 acc = {0.f, 0.f, 0.f, 0.f};
#pragma unroll
        for (int ks = 0; ks < 4; ks++) acc = mfma16(*(LAS u32x4*)(lds + (64 * w + 16 * rt + l16) * 256 + (((4 * ks + grp) ^ l16) * 16)), bf[ks], acc);
        if (l16 < 4) *(LAS u32x2*)(tt + l16 * 128 + (16 * rt + 4 * grp) * 2) = u32x2{cvtpk(acc[0], acc[1]), cvtpk(acc[2], acc[3])};
      }
      asm volatile("s_waitcnt lgkmcnt(0)" ::: "memory");
      const u32x2 v = *(LAS u32x2*)(tt + lane * 8);
      stsc(u32x4{v[0], tg.th, v[1], tg.th}, rws, WS_QABS + (grp * 32 + head) * 2048 + w * 256 + l16 * 16, 0);
    }
    bar();
    u32x4 wf[4][4], wr[16];
    {
      const rsrc_t ro = mkrs(woA + (size_t)head * 131072, 131072);
      dma_rows<16>(ro, 0, lds, 16 * w, lane);
      const rsrc_t rv = mkrs(wuv, 4u << 20);
      const int voff = (64 * w + 16 * grp) * 8192 + head * 256 + l16 * 16;
#pragma unroll
      for (int i = 0; i < 16; i++) wr[i] = ldnt(rv, voff, i * 8192);
      SB();
      asm volatile("s_waitcnt vmcnt(16)" ::: "memory");
      bar();
      wo_frags(lds, w, lane, wf);
      bar();
      const rsrc_t rb = mkrs(woB, 4u << 20);
      const int bvo = (lane >> 3) * 1024 + (((lane & 7) ^ ((lane >> 5) << 1)) * 16);
#pragma unroll
      for (int i = 0; i < 16; i++)
        dma1(rb, (i & 1) ? bvo ^ 64 : bvo, (4 * w + b4) * 131072 + (head >> 2) * 128 + i * 8192, (unsigned)(uintptr_t)lds + w * 16384 + i * 1024);
      SB();
      if (tid < 8) ((LAS unsigned*)(scr + 24576))[tid] = 0u;
    }
    const int hw = 4 * w + b4, cc = head >> 2, roff = WS_RS + hw * 16384 + lane * 16;
    u32x4 g[6];
#pragma unroll
    for (int s = 0; s < 6; s++) asm volatile("" : "=v"(g[s]));
    if (w == 0) {
      for (unsigned spins = 0;; spins++) {
        asm volatile("" ::: "memory");
        const u32x4 v = ldsc(rws, WS_PML + ((lane & 31) * 32 + head) * 16, 0);
        if (__builtin_popcount((unsigned)__builtin_amdgcn_ballot_w64(mok(v, tg))) >= NREL || spins > SPIN_CAP) break;
        __builtin_amdgcn_s_sleep(16);
      }
    }
    bar();
    {
      const int b = w >> 1, c4 = tid & 127;
      const unsigned ta = (lane & 1) ? ptag(tg, 2) : ptag(tg, 0), tb = (lane & 1) ? ptag(tg, 3) : ptag(tg, 1);
      u32x4 ml;
      u32x2 po[8];
#pragma unroll
      for (int s = 0; s < 8; s++) asm volatile("" : "=v"(po[s]));
      unsigned missP = 255;
      for (unsigned spins = 0;; spins++) {
        asm volatile("" ::: "memory");
        ml = ldsc(rws, WS_PML + ((8 * b + (lane & 7)) * 32 + head) * 16, 0);
#pragma unroll
        for (int s = 0; s < 8; s++)
          if (missP >> s & 1) po[s] = __builtin_amdgcn_raw_buffer_load_b64(rws, WS_PO + (8 * b * 32 + head) * 1024 + c4 * 8, s * 32768, AUX_SC1);
        SB();
#pragma unroll
        for (int s = 0; s < 8; s++) if (__all((((po[s][0] ^ ta) | (po[s][1] ^ tb)) & PMASK) == 0u)) missP &= ~(1u << s);
        if ((!missP && __all(mok(ml, tg))) || spins > SPIN_CAP) break;
        __builtin_amdgcn_s_sleep(2);
      }
#pragma unroll
      for (int s = 0; s < 6; s++) g[s] = ldsc(rws, roff, s * 1024);
      SB();
      ml[0] &= 0xffff0000u; ml[1] &= ~255u;
#pragma unroll
      for (int s = 0; s < 8; s++) po[s] = u32x2{bfi(PMASK, 0x00020002u, po[s][0]), bfi(PMASK, 0x00020002u, po[s][1])};
      float M = __uint_as_float(ml[0]);
      M = fmaxf(M, dpp<XOR1>(M)); M = fmaxf(M, dpp<XOR2>(M)); M = fmaxf(M, dpp<XOR7>(M));
      const float f = fexp2((__uint_as_float(ml[0]) - M) * LOG2E);
      float L = f * __uint_as_float(ml[1]);
      L += dpp<XOR1>(L); L += dpp<XOR2>(L); L += dpp<XOR7>(L);
      const float gl = __builtin_ldexpf(f, (int)((((w & 1) ? ml[3] : ml[2]) >> (8 * grp)) & 255) - 142);
      const float gs[8] = {dpp<0x150>(gl), dpp<0x151>(gl), dpp<0x152>(gl), dpp<0x153>(gl), dpp<0x154>(gl), dpp<0x155>(gl), dpp<0x156>(gl), dpp<0x157>(gl)};
      f32x4 a = {0.f, 0.f, 0.f, 0.f};
#pragma unroll
      for (int s = 0; s < 8; s++)
        a += gs[s] * f32x4{(float)(short)po[s][0], (float)((int)po[s][0] >> 16), (float)(short)po[s][1], (float)((int)po[s][1] >> 16)};
      const float inv = (32768.f / 32767.f) / L;
      const u32x2 v = {cvtpk(a[0] * inv, a[1] * inv), cvtpk(a[2] * inv, a[3] * inv)};
      *(LAS u32x2*)(scr + b * 1024 + c4 * 8) = v;
    }
    bar();
    {
      u32x4 xp[2][2];
#pragma unroll
      for (int s = 0; s < 2; s++) {
        LAS u32x4* xs = (LAS u32x4*)(scr + (2 * s + (l16 >> 3)) * 1024 + (64 * w + 16 * grp) * 2);
        xp[s][0] = xs[0]; xp[s][1] = xs[1];
      }
      f32x4 acc[2] = {{0.f, 0.f, 0.f, 0.f}, {0.f, 0.f, 0.f, 0.f}};
      diag_gemv(wr, xp, mask, acc);
      LAS float* red = (LAS float*)(scr + 8192);
#pragma unroll
      for (int s = 0; s < 2; s++) *(LAS f32x4*)(red + w * 512 + (2 * s + (grp >> 1)) * 128 + 8 * l16 + 4 * (grp & 1)) = acc[s];
      bar();
      if (tid < 256) {
        float a = 0.f, b = 0.f;
#pragma unroll
        for (int k = 0; k < 8; k++) { const f32x2 v = *(LAS f32x2*)(red + k * 512 + 2 * tid); a += v[0]; b += v[1]; }
        ((LAS unsigned*)(scr + 4096))[tid] = cvtpk(a, b);
      }
      bar();
    }
    const f32x4 yA = wo_calc(wf, scr + 4096, lane);
    drain();
    y_store(yA, w, lane, head, rws, WS_Y, tg);
    {
      const int q = l16 >> 2;
      LAS char* img = lds + w * 16384 + (8 * (q >> 1) + 4 * (grp & 1) + 2 * (grp >> 1) + (q & 1)) * 128 + 8 * (lane & 3);
      const int fl = (grp & 1) | (q & 2);
#pragma unroll
      for (int ks = 0; ks < 4; ks++)
#pragma unroll
        for (int tl = 0; tl < 4; tl++) {
          const u32x2 a = trread(img + (tl ^ fl) * 32 + ks * 4096), b = trread(img + (tl ^ fl) * 32 + ks * 4096 + 2048);
          wf[tl][ks] = u32x4{a[0], a[1], b[0], b[1]};
        }
    }
    const rsrc_t rout = mkrs(out, 8192);
    LAS unsigned* cnt = (LAS unsigned*)(scr + 24576);
    LAS char* const re = scr + 8192;
    LAS char* const rA = scr + 8192 + 8192;
    LAS char* rl = scr + 8192 + 10240 + (lane & 31) * 8;
    LAS char* rw = scr + 8192 + 10240 + w * 256 + (lane & 3) * 64 + grp * 16;
    LAS char* rx = rl + (w < 3 ? 8 + w : 11) * 256;
    int oo = lane < 32 ? b4 * 2048 + 1024 + cc * 128 + lane * 4 : OOB, oe = head == 0 && lane == 0 ? WS_EPOCH : OOB;
    asm volatile("" : "+v"(rl), "+v"(rw), "+v"(rx), "+v"(oo), "+v"(oe), "+v"(cnt));
    {
      const int ofa = WS_Y + (b4 * 8 + cc) * 16384 + w * 2048 + lane * 16;
      u32x4 x0, x1;
      for (unsigned spins = 0;; spins++) {
        asm volatile("" ::: "memory");
        x0 = ldsc(rws, ofa, 0); x1 = ldsc(rws, ofa + 1024, 0);
        if (__all(gok(x0, tg) & gok(x1, tg)) || spins > SPIN_CAP) break;
        __builtin_amdgcn_s_sleep(4);
      }
      const f32x2 r = {half_sum(gval(x0[0]) + gval(x1[0])), half_sum(gval(x0[2]) + gval(x1[2]))};
      if (lane < 32) *(volatile LAS f32x2*)(rA + w * 256 + lane * 8) = r;
      const unsigned n = arrive(cnt + 1, lane);
      const f32x2 v = rows_sum<8>(rA + (lane & 31) * 8);
      __builtin_amdgcn_raw_buffer_store_b32(cvtpk(v[0], v[1]), rout, lane < 32 && n == 8u ? b4 * 2048 + cc * 128 + l16 * 8 + grp * 4 : OOB, 0, OAUX);
    }
    {
      for (unsigned spins = 0;; spins++) {
        unsigned bad = 0;
#pragma unroll
        for (int s = 0; s < 6; s++) bad |= (g[s][0] ^ tg.lo) | (g[s][2] ^ tg.rb);
        if (__all((bad & (spins > SPIN_CAP * 4 ? 0u : 511u)) == 0u)) break;
        asm volatile("" ::: "memory");
#pragma unroll
        for (int s = 0; s < 6; s++) g[s] = ldsc(rws, roff + s * 1024, 0);
        SB();
      }
      unsigned pr[3];
#pragma unroll
      for (int t = 0; t < 3; t++) {
        const u32x4 g0 = g[t], g1 = g[t + 3];
        const float R0 = rec_r(g0), R1 = rec_r(g1), Rm = fmaxf(R0, R1);
        const double s0 = mkd(0u, p2hi(R0, Rm)), s1 = mkd(0u, p2hi(R1, Rm));
        const double inv = __builtin_amdgcn_rcp(__builtin_fma(rec_l(g0), s0, rec_l(g1) * s1));
        pr[t] = cvtpk((float)(__builtin_fma(rec_a(g0), s0, rec_a(g1) * s1) * inv), (float)(__builtin_fma(rec_b(g0), s0, rec_b(g1) * s1) * inv));
      }
      *(volatile LAS f32x4*)(re + ((l16 >> 2) * 8 + w) * 256 + (lane & 3) * 64 + grp * 16) = proj(pr, wf, lane);
      const unsigned n = arrive(cnt, lane);
#pragma unroll
      for (int u = 0; u < 2; u++) {
        const int t = u ? 2 : lane >> 5;
        const f32x2 v = rows_sum<8>(re + t * 2048 + (lane & 31) * 8);
        stsc(gpair(v[0], v[1], tg), rws, n != 8u || (u && lane >= 32) ? OOB : WS_YB + ((((b4 + 1 + t) & 3) * 8 + cc) * 4 + b4) * 512 + (lane & 31) * 16, 0);
      }
    }
    const int ofb = w < 3 ? WS_YB + ((b4 * 8 + cc) * 4 + ((b4 + 1 + w) & 3)) * 512 + (lane & 31) * 16 : OOB;
    u32x4 xb, p[NSLOT];
    unsigned bad;
    for (unsigned spins = 0;; spins++) {
      asm volatile("" ::: "memory");
      xb = ldsc(rws, ofb, 0);
#pragma unroll
      for (int s = 0; s < NSLOT; s++) p[s] = ldsc(rws, roff + 8192 + s * 1024, 0);
      unsigned m = spins > SPIN_CAP * 4 ? 0u : 511u;
      asm volatile("" : "+s"(m));
      SB();
      unsigned ax = min((((xb[0] ^ tg.tl) | (xb[2] ^ tg.tl)) >> 24) | (xb[1] ^ tg.th) | (xb[3] ^ tg.th), w < 3 ? 1u : 0u), az = 0;
#pragma unroll
      for (int s = 0; s < NSLOT; s++) { ax |= p[s][0] ^ tg.lo; az |= p[s][2] ^ tg.rb; }
      bad = az & 512u;
      if (__all(((ax | az) & m) == 0u)) break;
    }
    *(volatile LAS f32x2*)rx = f32x2{gval(xb[0]), gval(xb[2])};
    double S0, S1, L;
    if (__builtin_expect(__all(bad == 0u), 1)) {
      S0 = mkd(p[0][0], p[0][1]); S1 = mkd(p[0][2], p[0][3]); L = rec_l(p[0]);
#pragma unroll
      for (int s = 1; s < NSLOT; s++) { S0 += mkd(p[s][0], p[s][1]); S1 += mkd(p[s][2], p[s][3]); L += rec_l(p[s]); }
    } else {
      float R = -INFINITY;
      S0 = S1 = L = 0.0;
#pragma unroll
      for (int s = 0; s < NSLOT; s++) {
        const float Rs = rec_l(p[s]) != 0.0 ? rec_r(p[s]) : -INFINITY, Rn = fmaxf(R, Rs);
        const double s0 = mkd(0u, p2hi(R, Rn)), s1 = mkd(0u, p2hi(Rs, Rn));
        L = __builtin_fma(rec_l(p[s]), s1, L * s0);
        S0 = __builtin_fma(rec_a(p[s]), s1, S0 * s0); S1 = __builtin_fma(rec_b(p[s]), s1, S1 * s0);
        R = Rn;
      }
    }
    {
      const double inv = __builtin_amdgcn_rcp(L);
      const unsigned pr[1] = {cvtpk((float)(S0 * inv), (float)(S1 * inv))};
      const f32x4 y = proj(pr, wf, lane);
      if ((lane & 12) == 0) *(volatile LAS f32x4*)rw = y;
      const unsigned n = arrive(cnt + 2, lane);
      const f32x2 v = rows_sum<11>(rl);
      __builtin_amdgcn_raw_buffer_store_b32(cvtpk(v[0], v[1]), rout, n == 8u ? oo : OOB, 0, OAUX);
      __builtin_amdgcn_raw_buffer_store_b64(__builtin_bit_cast(u32x2, tg.e), rws, n == 8u ? oe : OOB, 0, AUX_SC1);
      if (w == 0) {
        const unsigned now = (unsigned)__builtin_amdgcn_s_memrealtime();
        const u32x4 t4 = __builtin_amdgcn_raw_buffer_load_b128(mkrs(ws + (8 << 20), 4096), lane * 16, 0, AUX_SC1);
        int mn = 1 << 30, mx = -(1 << 30);
        for (int i = 0; i < 4; i++) { const int d = (int)(now - t4[i]); mn = min(mn, d); mx = max(mx, d); }
        for (int o = 1; o < 64; o *= 2) { mn = min(mn, __shfl_xor(mn, o)); mx = max(mx, __shfl_xor(mx, o)); }
        int ex = 6000 - mx;
        if (PROBE == 3) asm volatile("buffer_wbl2 sc1\n\ts_waitcnt vmcnt(0)" ::: "memory");
        if (PROBE == 6) asm volatile("buffer_inv sc1\n\ts_waitcnt vmcnt(0)" ::: "memory");
        if (PROBE == 7) asm volatile("buffer_wbl2 sc0 sc1\n\ts_waitcnt vmcnt(0)\n\tbuffer_inv sc0 sc1\n\ts_waitcnt vmcnt(0)" ::: "memory");
        ex = min(max(ex, 0), 20000);
        ex = __builtin_amdgcn_readfirstlane(ex);
        while ((int)((unsigned)__builtin_amdgcn_s_memrealtime() - now) < ex) __builtin_amdgcn_s_sleep(2);
      }
    }
    return;
  }
  }
  {
    const u32x4 q4 = *(LAS u32x4*)(qst + 256 + l16 * 16);
    const Mrg mg = mkmrg(scr + SCR_SUM + SUM_STR, lane, WS_RS + (head * 16 + 8 + sub) * 1024, mktag(e0));
    Acc A;
    b_units(ra, rk4, rv4, cur, q4, lane, mask, ctl, rws, cntoff, A);
    b_merge(mg, scr + 8192 + 8704, w, lane, A, rws, e0);
    SB();
    asm volatile("" :: "v"(ra[0]), "v"(ra[1]), "v"(ra[2]), "v"(ra[3]), "v"(ra[4]), "v"(ra[5]), "v"(ra[6]), "v"(ra[7]));
  }
}

extern "C" void kernel_launch(void* const* d_in, const int* in_sizes, int n_in, void* d_out, int out_size, void* d_ws, size_t ws_size,
                              hipStream_t stream) {
  k_all<<<256, 512, 0, stream>>>((const char*)d_in[0], (const char*)d_in[1], (const char*)d_in[2], (const char*)d_in[3], (const char*)d_in[4],
                                 (const char*)d_in[5], (const char*)d_in[6], (const char*)d_in[7], (const char*)d_in[8], (const char*)d_in[9],
                                 (const char*)d_in[10], (char*)d_ws, (unsigned*)d_out);
}
```

```cpp
#include <hip/hip_runtime.h>
#include <stdint.h>

typedef __attribute__((ext_vector_type(4))) unsigned u32x4;
typedef __attribute__((ext_vector_type(2))) unsigned u32x2;
typedef __attribute__((ext_vector_type(8))) unsigned u32x8;
typedef __attribute__((ext_vector_type(16))) unsigned u32x16;
typedef __attribute__((ext_vector_type(4))) float f32x4;
typedef __attribute__((ext_vector_type(2))) float f32x2;
typedef __attribute__((ext_vector_type(2))) double f64x2;
typedef __attribute__((ext_vector_type(16))) float f32x16;
typedef __attribute__((ext_vector_type(8))) __bf16 bf16x8;
typedef __attribute__((ext_vector_type(2))) __bf16 bf16x2;
typedef __attribute__((ext_vector_type(4))) short s16x4;
typedef unsigned long long u64;
typedef __amdgpu_buffer_rsrc_t rsrc_t;
#define LAS __attribute__((address_space(3)))
#define GAS __attribute__((address_space(1)))

#define CPF 34944
#define PAT "5c10p2ba43"
constexpr int WTH = 5;
constexpr int DCH = 2;
constexpr int NXA = 8, NXB = 8;
constexpr int NREL = 24;

constexpr int AUX_NT = 2, AUX_SC1 = 16, OOB = 0x40000000;
constexpr float SCALE = 0.08838834764831845f;
constexpr float LOG2E = 1.4426950408889634f;
constexpr unsigned SPIN_CAP = 1u << 17;
constexpr int NSLOT = 7, NUNIT = 256;

constexpr int WS_EPOCH = 0;
constexpr int CSTR    = 4352;
constexpr int WS_CNT  = 8192;
constexpr int WS_QB   = WS_CNT + 32 * CSTR;
constexpr int WS_QABS = WS_QB + 32 * 2048;
constexpr int WS_PML  = WS_QABS + 4 * 32 * 2048;
constexpr int WS_PO   = WS_PML + 32 * 32 * 16;
constexpr int WS_RS   = WS_PO + 32 * 32 * 1024;
constexpr int WS_Y    = WS_RS + 32 * 16 * 1024;
constexpr int WS_YB   = WS_Y + 4 * 8 * 32 * 512;
constexpr int WS_END  = WS_YB + 4 * 8 * 4 * 512;

constexpr int LDS_BYTES = 163840;
constexpr int LDS_SCR = 131072;
constexpr int SCR_SUM = 5120, SUM_STR = 1040;

__device__ __forceinline__ unsigned cvtpk(float lo, float hi) {
  f32x2 v = {lo, hi};
  return __builtin_bit_cast(unsigned, __builtin_convertvector(v, bf16x2));
}
__device__ __forceinline__ float bf_lo(unsigned u) { return __uint_as_float(u << 16); }
__device__ __forceinline__ float bf_hi(unsigned u) { return __uint_as_float(u & 0xffff0000u); }
__device__ __forceinline__ float rbf(float x) { return __uint_as_float(cvtpk(x, x) << 16); }
__device__ __forceinline__ float dot2(unsigned k, unsigned q, float acc) {
  return __builtin_amdgcn_fdot2_f32_bf16(__builtin_bit_cast(bf16x2, k), __builtin_bit_cast(bf16x2, q), acc, false);
}
template <int CTRL> __device__ __forceinline__ float dpp(float x) {
  return __builtin_bit_cast(float, __builtin_amdgcn_mov_dpp(__builtin_bit_cast(int, x), CTRL, 0xf, 0xf, true));
}
template <int CTRL> __device__ __forceinline__ unsigned dppu(unsigned x) {
  return (unsigned)__builtin_amdgcn_mov_dpp((int)x, CTRL, 0xf, 0xf, true);
}
constexpr int XOR1 = 0xB1, XOR2 = 0x4E, XOR7 = 0x141, XOR8 = 0x128, ROR4 = 0x124;
__device__ __forceinline__ float xrow_max(float x) {
  auto s = __builtin_amdgcn_permlane16_swap(__float_as_uint(x), __float_as_uint(x), false, false);
  x = fmaxf(__uint_as_float(s[0]), __uint_as_float(s[1]));
  auto t = __builtin_amdgcn_permlane32_swap(__float_as_uint(x), __float_as_uint(x), false, false);
  return fmaxf(__uint_as_float(t[0]), __uint_as_float(t[1]));
}
__device__ __forceinline__ float xrow_sum(float x) {
  auto s = __builtin_amdgcn_permlane16_swap(__float_as_uint(x), __float_as_uint(x), false, false);
  x = __uint_as_float(s[0]) + __uint_as_float(s[1]);
  auto t = __builtin_amdgcn_permlane32_swap(__float_as_uint(x), __float_as_uint(x), false, false);
  return __uint_as_float(t[0]) + __uint_as_float(t[1]);
}
__device__ __forceinline__ float row_max(float x) {
  x = fmaxf(x, dpp<XOR8>(x)); x = fmaxf(x, dpp<ROR4>(x)); x = fmaxf(x, dpp<XOR2>(x)); x = fmaxf(x, dpp<XOR1>(x));
  return x;
}
__device__ __forceinline__ float row_sum(float x) {
  x += dpp<XOR8>(x); x += dpp<ROR4>(x); x += dpp<XOR2>(x); x += dpp<XOR1>(x);
  return x;
}
__device__ __forceinline__ float fexp2(float x) { return __builtin_amdgcn_exp2f(x); }
__device__ __forceinline__ f32x4 mfma16(u32x4 a, u32x4 b, f32x4 c) {
  return __builtin_amdgcn_mfma_f32_16x16x32_bf16(__builtin_bit_cast(bf16x8, a), __builtin_bit_cast(bf16x8, b), c, 0, 0, 0);
}
__device__ __forceinline__ f32x16 mfma32(u32x4 a, u32x4 b, f32x16 c) {
  return __builtin_amdgcn_mfma_f32_32x32x16_bf16(__builtin_bit_cast(bf16x8, a), __builtin_bit_cast(bf16x8, b), c, 0, 0, 0);
}
__device__ __forceinline__ rsrc_t mkrs(const GAS char* p, unsigned bytes) {
  return __builtin_amdgcn_make_buffer_rsrc((void*)p, 0, bytes, 0x00020000);
}
__device__ __forceinline__ void bar() { asm volatile("s_waitcnt lgkmcnt(0)\n\ts_barrier" ::: "memory"); }
__device__ __forceinline__ void drain() { asm volatile("s_waitcnt vmcnt(0)" ::: "memory"); }
#define SB() __builtin_amdgcn_sched_barrier(0)
template <int N> __device__ __forceinline__ void vmwait() { asm volatile("s_waitcnt vmcnt(%0)" :: "n"(N) : "memory"); }
__device__ __forceinline__ unsigned swz(unsigned row) { return ((row & 3) << 2) | ((row >> 2) & 2); }
__device__ __forceinline__ u32x2 trread(LAS char* p) {
  return __builtin_bit_cast(u32x2, __builtin_amdgcn_ds_read_tr16_b64_v4i16((LAS s16x4*)p));
}

struct Tag { unsigned tl, th, lo, rb; u64 e; };
__device__ __forceinline__ Tag mktag(u64 e0) {
  Tag t;
  asm volatile("" : "+s"(e0));
  t.e = e0 * 6364136223846793005ull + 1442695040888963407ull;
  t.th = (unsigned)(t.e >> 32);
  t.lo = (unsigned)t.e;
  t.tl = t.lo & 0xff000000u;
  t.rb = ((t.lo >> 9) & 255u) | (~t.lo & 256u);
  return t;
}
__device__ __forceinline__ unsigned gpack(float v, const Tag& t) { return ((__float_as_uint(v) + 0x80u) >> 8) | t.tl; }
__device__ __forceinline__ u32x4 gpair(float a, float b, const Tag& t) { return u32x4{gpack(a, t), t.th, gpack(b, t), t.th}; }
__device__ __forceinline__ bool gok(u32x4 g, const Tag& t) {
  return ((((g[0] ^ t.tl) | (g[2] ^ t.tl)) >> 24) | (g[1] ^ t.th) | (g[3] ^ t.th)) == 0u;
}
__device__ __forceinline__ float gval(unsigned lo) { return __uint_as_float(lo << 8); }
__device__ __forceinline__ bool qok(u32x4 g, const Tag& t) { return ((g[1] ^ t.th) | (g[3] ^ t.th)) == 0u; }
__device__ __forceinline__ double mkd(unsigned lo, unsigned hi) { return __builtin_bit_cast(double, u32x2{lo, hi}); }
__device__ __forceinline__ u32x4 rpack(double a, double b, double l, float r, const Tag& t) {
  const u32x2 ua = __builtin_bit_cast(u32x2, a), ub = __builtin_bit_cast(u32x2, b), ul = __builtin_bit_cast(u32x2, l);
  const u64 ml = ((u64)(unsigned)__builtin_amdgcn_readfirstlane(ul[1]) << 32) | (unsigned)__builtin_amdgcn_readfirstlane(ul[0]);
  const u64 mr = (unsigned)__builtin_amdgcn_readfirstlane(__float_as_uint(r));
  const unsigned ta = t.lo & 511u, tb = t.rb;
  return u32x4{(ua[0] & ~1023u) | (__builtin_amdgcn_inverse_ballot_w64(ml) ? ta | 512u : ta), ua[1],
               (ub[0] & ~1023u) | (__builtin_amdgcn_inverse_ballot_w64(mr) ? tb | 512u : tb), ub[1]};
}
__device__ __forceinline__ u32x4 rpack0(f64x2 o, unsigned lw, int lane, unsigned ta, unsigned tb) {
  const u32x4 u = __builtin_bit_cast(u32x4, o);
  return u32x4{(u[0] & ~1023u) | (ta | (__builtin_amdgcn_ubfe(lw, lane, 1) << 9)), u[1], (u[2] & ~1023u) | tb, u[3]};
}
__device__ __forceinline__ bool rok(u32x4 g, const Tag& t) { return (((g[0] ^ t.lo) | (g[2] ^ t.rb)) & 511u) == 0u; }
constexpr unsigned PMASK = 0x00030003u;
__device__ __forceinline__ unsigned ptag(const Tag& t, int k) {
  const unsigned n = k == 0 ? t.lo : k == 3 ? t.lo >> 10 : ((t.lo >> (1 + 3 * k)) & 7u) | (~t.lo & 8u);
  return (n & 3u) | ((n & 12u) << 14);
}
__device__ __forceinline__ unsigned ltag(const Tag& t) { return ((t.lo >> 16) & 127u) | (~t.lo & 128u); }
__device__ __forceinline__ bool mok(u32x4 g, const Tag& t) { return (((g[0] ^ t.lo) & 0xffffu) | ((g[1] ^ ltag(t)) & 255u)) == 0u; }
__device__ __forceinline__ unsigned bfi(unsigned m, unsigned a, unsigned b) { return (a & m) | (b & ~m); }
__device__ __forceinline__ double rec_a(u32x4 g) { return mkd(g[0] & ~1023u, g[1]); }
__device__ __forceinline__ double rec_b(u32x4 g) { return mkd(g[2] & ~1023u, g[3]); }
__device__ __forceinline__ double rec_l(u32x4 g) { return __builtin_bit_cast(double, __builtin_amdgcn_ballot_w64((g[0] & 512u) != 0u)); }
__device__ __forceinline__ float rec_r(u32x4 g) { return __uint_as_float((unsigned)__builtin_amdgcn_ballot_w64((g[2] & 512u) != 0u)); }
__device__ __forceinline__ int edif(float a, float b) {
  int r;
  asm("v_cvt_i32_f32 %0, %1" : "=v"(r) : "v"(a - b));
  return r;
}
__device__ __forceinline__ unsigned p2hi(float a, float b) { return (unsigned)max(edif(a, b) + 1023, 0) << 20; }
__device__ __forceinline__ float bucket(float m) { return fmaf(__builtin_rintf(m * (1.f / 512.f)), 512.f, 0.f); }
__device__ __forceinline__ float rfl(float x) { return __uint_as_float(__builtin_amdgcn_readfirstlane(__float_as_uint(x))); }
__device__ __forceinline__ unsigned rdu(unsigned v, int l) { return __builtin_amdgcn_readlane(v, l); }

__device__ __forceinline__ void diag_gemv(const u32x4 (&wr)[16], const u32x4 (&xp)[2][2], const unsigned (&mask)[4], f32x4 (&acc)[2]) {
#pragma unroll
  for (int i = 0; i < 16; i++) {
#pragma unroll
    for (int s = 0; s < 2; s++) {
      const unsigned src = xp[s][i >> 3][(i >> 1) & 3];
      const unsigned dup = __builtin_amdgcn_perm(src, src, (i & 1) ? 0x03020302u : 0x01000100u);
      const u32x4 a = {dup & mask[0], dup & mask[1], dup & mask[2], dup & mask[3]};
      acc[s] = mfma16(a, wr[i], acc[s]);
    }
  }
}

constexpr int bitpos(char c) { for (int i = 0; i < 10; i++) if (PAT[i] == c) return 10 + i; return 31; }
constexpr int goff(int t) { int o = 0; for (int j = 0; j < 6; j++) o |= ((t >> j) & 1) << bitpos('0' + j); return o; }
struct Acc { double o[2]; double l; float R; };
__device__ __forceinline__ u32x4 ldnt(rsrc_t r, int voff, int so) { return __builtin_amdgcn_raw_buffer_load_b128(r, voff, so, AUX_NT); }
__device__ __forceinline__ u32x4 ldslot(rsrc_t r, int voff, int so, int i) { return ldnt(r, voff + goff(i & 3), so ^ goff(i & ~3)); }
__device__ __forceinline__ u32x4 ldunit(rsrc_t r, int voff, int so, int i) { return ldnt(r, voff + i * 1024, so); }
__device__ __forceinline__ u32x4 ldsc(rsrc_t r, int voff, int so) { return __builtin_amdgcn_raw_buffer_load_b128(r, voff, so, AUX_SC1); }
__device__ __forceinline__ void stsc(u32x4 v, rsrc_t r, int voff, int so) { __builtin_amdgcn_raw_buffer_store_b128(v, r, voff + so, 0, AUX_SC1); }
__device__ __forceinline__ float dot8(u32x4 k, u32x4 q) { return dot2(k[3], q[3], dot2(k[2], q[2], dot2(k[1], q[1], dot2(k[0], q[0], 0.f)))); }
#define TR_STEP(HALF, CTRL, BIT) _Pragma("unroll") for (int h = 0; h < (HALF); ++h) { \
    const float t1 = x[h] + dpp<CTRL>(x[h]), t2 = x[h + (HALF)] + dpp<CTRL>(x[h + (HALF)]); x[h] = (c & (BIT)) ? t2 : t1; }
template <int N> __device__ __forceinline__ float k_step(u32x4 (&ring)[16], u32x4 q, int c, rsrc_t rn, int voff, int so, bool ext, LAS char* xp) {
  float x[16];
#pragma unroll
  for (int i = 0; i < 16; i++) {
    x[i] = dot8(ring[i], q);
    asm volatile("s_waitcnt vmcnt(%1)" : "+v"(x[i]) : "n"(WTH - 1));
    if (i >= 16 - N && ext) ring[i] = *(volatile LAS u32x4*)(xp + (i - (16 - N)) * 1024);
    else ring[i] = ldslot(rn, voff, so, i);
    SB();
  }
  TR_STEP(8, XOR8, 8) TR_STEP(4, XOR7, 4) TR_STEP(2, XOR2, 2) TR_STEP(1, XOR1, 1)
  return rbf(rbf(x[0]) * SCALE);
}
template <int BC> __device__ __forceinline__ void pv(u32x4 v, unsigned pk, const unsigned (&mask)[4], f32x4& acc) {
  const unsigned d = dppu<0x150 + BC>(pk);
  acc = mfma16(u32x4{d & mask[0], d & mask[1], d & mask[2], d & mask[3]}, v, acc);
}
template <int I, int MODE> __device__ __forceinline__ void v_step(u32x4 (&ring)[16], unsigned pk, const unsigned (&mask)[4], f32x4& acc,
                                                                  rsrc_t rk, rsrc_t rv, int voff, int so) {
  pv<I>(ring[I], pk, mask, acc);
  if (MODE == 1 || (MODE == 2 && I < 8)) {
    asm volatile("s_waitcnt vmcnt(%1)" : "+v"(acc) : "n"(WTH - 1));
    ring[I] = MODE == 1 ? ldslot(rv, voff, so, I) : ldunit(I < 4 ? rk : rv, voff, so, I & 3);
  } else {
    asm volatile("" : "+v"(acc));
  }
  SB();
  if constexpr (I < 15) v_step<I + 1, MODE>(ring, pk, mask, acc, rk, rv, voff, so);
}
__device__ __forceinline__ float exp_ref(float mx, float& f) {
  const float M = __builtin_ceilf(mx * LOG2E);
  f = __builtin_amdgcn_fmed3f(fmaf(mx, LOG2E, -M), -1.f, 0.f);
  return M;
}
__device__ __forceinline__ unsigned lds_inc(LAS unsigned* p, int lane) {
  unsigned t = 0, z = 0;
  asm volatile("" : "+v"(z));
  if (lane == 0) t = __hip_atomic_fetch_add(p + z, 1u, __ATOMIC_RELAXED, __HIP_MEMORY_SCOPE_WORKGROUP);
  return t;
}
struct Take { unsigned unit, pend; bool fetch; };
__device__ __forceinline__ void lds_add(LAS unsigned* p, unsigned v) { __hip_atomic_fetch_add(p, v, __ATOMIC_RELAXED, __HIP_MEMORY_SCOPE_WORKGROUP); }
__device__ __forceinline__ Take take(LAS unsigned* ctl, unsigned kv, rsrc_t rws, int cntoff, int lane) {
  const unsigned k = __builtin_amdgcn_readfirstlane(kv);
  unsigned b, s;
  auto look = [&]() {
    const unsigned vs = ((volatile LAS unsigned*)ctl)[5], vb = ((volatile LAS unsigned*)ctl)[8 + (k >> 2)];
    s = __builtin_amdgcn_readfirstlane(vs); b = __builtin_amdgcn_readfirstlane(vb);
    return (b == ~0u) & ((s < 0x10000u) | ((s & 0xffffu) != 0u));
  };
  if (__builtin_expect(look(), 0))
    for (unsigned spins = 0; spins < SPIN_CAP * 16; spins++) {
      __builtin_amdgcn_s_sleep(1);
      if (!look()) break;
    }
  Take t;
  t.unit = min(b, (unsigned)NUNIT) + (k & 3);
  t.fetch = ((k & 3) == 0) & (b < NUNIT) & (s < 0x10000u);
  int off = OOB;
  if (t.fetch) {
    unsigned z = 0;
    asm volatile("" : "+v"(z));
    if (lane == 0) { lds_add(ctl + 5 + z, 1u); off = cntoff; }
    asm volatile("s_waitcnt lgkmcnt(0)" ::: "memory");
  }
  t.pend = __builtin_amdgcn_raw_ptr_buffer_atomic_add_i32(4, rws, off, 0, AUX_SC1);
  SB();
  return t;
}
__device__ __forceinline__ void give(LAS unsigned* ctl, const Take& t, int lane) {
  if (t.fetch && lane == 0) {
    unsigned z = 0;
    asm volatile("" : "+v"(z));
    if (t.pend < NUNIT) {
      const unsigned i = __hip_atomic_fetch_add(ctl + 4 + z, 1u, __ATOMIC_RELAXED, __HIP_MEMORY_SCOPE_WORKGROUP);
      ((volatile LAS unsigned*)ctl)[8 + i] = t.pend;
    }
    asm volatile("" ::: "memory");
    lds_add(ctl + 5 + z, t.pend >= NUNIT - 4 ? 0xffffu : ~0u);
  }
}
__device__ __forceinline__ int unit_off(unsigned t) { return (int)min(t, (unsigned)NUNIT) * 4096; }

__device__ __forceinline__ void b_units(u32x4 (&ra)[16], rsrc_t rk, rsrc_t rv, unsigned cur, u32x4 q, int lane, const unsigned (&mask)[4],
                                        LAS unsigned* ctl, rsrc_t rws, int cntoff, Acc& A) {
  const int c = lane & 15, voff = lane * 16;
  float Mr = -INFINITY;
  double s1 = 0.0;
  A.o[0] = A.o[1] = A.l = 0.0; A.R = cur < NUNIT ? -INFINITY : 0.f;
#pragma unroll 1
  while (cur < NUNIT) {
    const Take t = take(ctl, lds_inc(ctl, lane), rws, cntoff, lane);
    const int so = unit_off(t.unit);
    SB();
    float x[4];
#pragma unroll
    for (int i = 0; i < 4; i++) x[i] = dot8(ra[i], q);
    asm volatile("" : "+v"(x[0]), "+v"(x[1]), "+v"(x[2]), "+v"(x[3]));
    SB();
#pragma unroll
    for (int i = 0; i < 4; i++) {
      ra[i] = ldunit(rk, voff, so, i);
      SB();
    }
    TR_STEP(2, XOR8, 8) TR_STEP(1, XOR7, 4)
    x[0] += dpp<XOR2>(x[0]); x[0] += dpp<XOR1>(x[0]);
    const float s = rbf(rbf(x[0]) * SCALE);
    const float n = __builtin_rintf(s * LOG2E);
    float e = fexp2(__builtin_amdgcn_fmed3f(fmaf(s, LOG2E, -n), -1.f, 1.f)), d = n - Mr;
    asm volatile("" : "+v"(e));
    if (__builtin_expect(__any(d > 57.f), 0)) {
      float mx = fmaxf(n, dpp<XOR8>(n));
      mx = xrow_max(fmaxf(mx, dpp<ROR4>(mx)));
      const float Rn = rfl(fmaxf(A.R, bucket(mx)));
      const double s0 = mkd(0u, p2hi(A.R, Rn));
      A.o[0] *= s0; A.o[1] *= s0; A.l *= s0;
      A.R = Rn; Mr = mx; d = n - mx;
      s1 = mkd(0u, p2hi(mx, Rn));
    }
    const float p = __builtin_ldexpf(e, edif(d, 0.f));
    unsigned pk = cvtpk(p, p);
    asm volatile("" : "+v"(pk));
    SB();
    f32x4 acc = {0.f, 0.f, 0.f, 0.f};
    float ls = bf_lo(pk);
    pv<0>(ra[4], pk, mask, acc);  asm volatile("" : "+v"(acc)); ra[4] = ldunit(rv, voff, so, 0); ls += dpp<XOR8>(ls); asm volatile("" : "+v"(ls)); SB();
    pv<4>(ra[5], pk, mask, acc);  asm volatile("" : "+v"(acc)); ra[5] = ldunit(rv, voff, so, 1); ls += dpp<ROR4>(ls); asm volatile("" : "+v"(ls)); SB();
    pv<8>(ra[6], pk, mask, acc);  asm volatile("" : "+v"(acc)); ra[6] = ldunit(rv, voff, so, 2); SB();
    pv<12>(ra[7], pk, mask, acc); asm volatile("" : "+v"(acc)); ra[7] = ldunit(rv, voff, so, 3); SB();
    A.l = __builtin_fma((double)xrow_sum(ls), s1, A.l);
    asm volatile("" : "+v"(A.l));
    A.o[0] = __builtin_fma((double)(lane < 32 ? acc[0] : acc[2]), s1, A.o[0]);
    A.o[1] = __builtin_fma((double)(lane < 32 ? acc[1] : acc[3]), s1, A.o[1]);
    asm volatile("" : "+v"(A.o[0]), "+v"(A.o[1]));
    SB();
    cur = t.unit;
    if (cur >= NUNIT) break;
    give(ctl, t, lane);
  }
}
#undef TR_STEP
__device__ __forceinline__ int rec_col(int lane) { return 8 * (lane & 15) + 4 * ((lane >> 4) & 1) + 2 * (lane >> 5); }
struct Mrg { unsigned s0, s4, s8, ta, tb; int off; };
__device__ __forceinline__ Mrg mkmrg(LAS char* sum, int lane, int roff, const Tag& tg) {
  Mrg m = {(unsigned)(uintptr_t)sum, (unsigned)(uintptr_t)sum + (lane >> 5) * 4, (unsigned)(uintptr_t)sum + lane * 8, tg.lo & 511u, tg.rb, roff + lane * 16};
  asm volatile("" : "+v"(m.s0), "+v"(m.s4), "+v"(m.s8), "+v"(m.ta), "+v"(m.tb), "+v"(m.off));
  return m;
}
__device__ __forceinline__ void b_merge(const Mrg& m, LAS char* mrg, int w, int lane, const Acc& A, rsrc_t rws, u64 e0) {
  unsigned inc = 1;
  double a = A.o[0], b = A.o[1], l = A.l;
  if (__builtin_expect(__float_as_uint(A.R) != 0u, 0)) {
    *(volatile LAS f64x2*)(mrg + w * 1024 + lane * 16) = f64x2{a, b};
    *(volatile LAS double*)(mrg + 8192 + w * 8) = l;
    *(volatile LAS float*)(mrg + 8256 + w * 4) = A.R;
    a = b = l = 0.0;
    inc = 1u + (256u << w);
  }
  f64x2 o;
  unsigned n, lw;
  asm volatile("ds_add_f64 %3, %5\n\tds_add_f64 %3, %6 offset:512\n\ts_mov_b64 exec, 1\n\tds_add_f64 %3, %7 offset:1024\n\tds_add_u32 %3, %8 offset:1032\n\t"
               "s_mov_b64 exec, -1\n\tds_read_b32 %1, %9 offset:1032\n\tds_read_b32 %2, %4 offset:1024\n\tds_read2st64_b64 %0, %3 offset1:1\n\ts_waitcnt lgkmcnt(0)"
               : "=&v"(o), "=&v"(n), "=&v"(lw)
               : "v"(m.s8), "v"(m.s4), "v"(a), "v"(b), "v"(l), "v"(inc), "v"(m.s0)
               : "memory");
  stsc(rpack0(o, lw, lane, m.ta, m.tb), rws, n == 8u ? m.off : OOB, 0);
  SB();
  n = __builtin_amdgcn_readfirstlane(n);
  if (__builtin_expect(n > 8u && (n & 255u) == 8u, 0)) {
    double L = *(volatile LAS double*)(uintptr_t)(m.s0 + 1024);
    float R = L != 0.0 ? 0.f : -INFINITY;
#pragma unroll 1
    for (int i = 0; i < 8; i++)
      if (n >> (8 + i) & 1) {
        const f64x2 oi = *(volatile LAS f64x2*)(mrg + i * 1024 + lane * 16);
        const float Ri = *(volatile LAS float*)(mrg + 8256 + i * 4), Rn = fmaxf(R, Ri);
        const double s0 = mkd(0u, p2hi(R, Rn)), s1 = mkd(0u, p2hi(Ri, Rn));
        o[0] = __builtin_fma(oi[0], s1, o[0] * s0); o[1] = __builtin_fma(oi[1], s1, o[1] * s0);
        L = __builtin_fma(*(volatile LAS double*)(mrg + 8192 + i * 8), s1, L * s0);
        R = Rn;
      }
    stsc(rpack(o[0], o[1], L, R, mktag(e0)), rws, m.off, 0);
  }
}

__device__ __forceinline__ unsigned fz(unsigned row) { return ((row & 2) << 1) | ((row >> 1) & 2); }
__device__ __forceinline__ void dma1(rsrc_t rs, int voff, int so, unsigned ldsaddr) {
  unsigned keep;
  asm volatile("s_mov_b32 %0, m0\n\ts_mov_b32 m0, %1\n\ts_nop 0\n\tbuffer_load_dwordx4 %2, %3, %4 offen nt lds\n\ts_mov_b32 m0, %0"
               : "=&s"(keep) : "s"(ldsaddr), "v"(voff), "s"(rs), "s"(so) : "memory");
}
template <int NR>
__device__ __forceinline__ void dma_rows(rsrc_t rs, int srcoff, LAS char* img, int r0, int lane) {
#pragma unroll
  for (int u = 0; u < NR; u++) {
    const int r = r0 + u;
    unsigned keep;
    asm volatile("s_mov_b32 %0, m0\n\ts_mov_b32 m0, %1\n\ts_nop 0\n\tbuffer_load_dwordx4 %2, %3, %4 offen nt lds\n\ts_mov_b32 m0, %0"
                 : "=&s"(keep) : "s"((unsigned)(uintptr_t)img + r * 1024), "v"((lane ^ swz(r)) * 16), "s"(rs), "s"(srcoff + r * 1024) : "memory");
  }
  SB();
}

__device__ __forceinline__ void wo_frags(LAS char* img, int w, int lane, u32x4 (&wf)[4][4]) {
  const int g = lane >> 4, q = (lane & 15) >> 2, p = lane & 3;
#pragma unroll
  for (int ks = 0; ks < 4; ks++)
#pragma unroll
    for (int tl = 0; tl < 4; tl++) {
      const int ch = 8 * w + 2 * tl + (p >> 1);
      const int r0 = 32 * ks + 8 * g + q, r1 = r0 + 4;
      const u32x2 a = trread(img + r0 * 1024 + ((ch ^ swz(r0)) * 16) + 8 * (p & 1));
      const u32x2 b = trread(img + r1 * 1024 + ((ch ^ swz(r1)) * 16) + 8 * (p & 1));
      wf[tl][ks] = u32x4{a[0], a[1], b[0], b[1]};
    }
}
__device__ __forceinline__ f32x4 wo_calc(const u32x4 (&wf)[4][4], LAS char* cb, int lane) {
  const int n = lane & 15, grp = lane >> 4;
  u32x4 bf[4];
#pragma unroll
  for (int ks = 0; ks < 4; ks++) bf[ks] = *(LAS u32x4*)(cb + (n & 3) * 256 + ks * 64 + grp * 16);
  f32x4 acc[4];
#pragma unroll
  for (int tl = 0; tl < 4; tl++) acc[tl] = f32x4{0.f, 0.f, 0.f, 0.f};
#pragma unroll
  for (int ks = 0; ks < 4; ks++)
#pragma unroll
    for (int tl = 0; tl < 4; tl++) acc[tl] = mfma16(wf[tl][ks], bf[ks], acc[tl]);
  const f32x4 lo = (n & 4) ? acc[1] : acc[0], hi = (n & 4) ? acc[3] : acc[2];
  return (n & 8) ? hi : lo;
}
__device__ __forceinline__ void y_store(f32x4 y, int w, int lane, int slot, rsrc_t rws, int yoff, const Tag& tg) {
  const int off = yoff + (((lane & 3) * 8 + w) * 32 + slot) * 512 + ((lane & 12) + (lane >> 4)) * 16;
  stsc(gpair(y[0], y[1], tg), rws, off, 0);
  stsc(gpair(y[2], y[3], tg), rws, off, 256);
}
template <int K, int N> __device__ __forceinline__ unsigned bc1(const unsigned (&p)[N]) {
  unsigned v = dppu<0x150 + K>(p[0]);
  if constexpr (N > 1) {
    v = (unsigned)__builtin_amdgcn_update_dpp((int)v, (int)p[1], 0x150 + K, 0xf, 2, false);
    v = (unsigned)__builtin_amdgcn_update_dpp((int)v, (int)p[2], 0x150 + K, 0xf, 4, false);
  }
  return v;
}
template <int K, int N> __device__ __forceinline__ u32x4 bc4(const unsigned (&p)[N]) { return u32x4{bc1<K>(p), bc1<K + 1>(p), bc1<K + 2>(p), bc1<K + 3>(p)}; }
template <int N> __device__ __forceinline__ f32x4 proj(const unsigned (&pr)[N], const u32x4 (&wf)[4][4], int lane) {
  const u32x4 bf[4] = {bc4<0>(pr), bc4<4>(pr), bc4<8>(pr), bc4<12>(pr)};
  f32x4 acc[4];
#pragma unroll
  for (int tl = 0; tl < 4; tl++) acc[tl] = f32x4{0.f, 0.f, 0.f, 0.f};
#pragma unroll
  for (int ks = 0; ks < 4; ks++)
#pragma unroll
    for (int tl = 0; tl < 4; tl++) acc[tl] = mfma16(wf[tl][ks], bf[ks], acc[tl]);
  const f32x4 lo = (lane & 1) ? acc[1] : acc[0], hi = (lane & 1) ? acc[3] : acc[2];
  f32x4 r = (lane & 2) ? hi : lo;
  asm volatile("" : "+v"(r));
  return r;
}
__device__ __forceinline__ unsigned arrive(LAS unsigned* p, int lane) {
  unsigned z = 0;
  asm volatile("" : "+v"(z) :: "memory");
  if (lane == 0) lds_add(p + z, 1u);
  asm volatile("" ::: "memory");
  return *(volatile LAS unsigned*)p;
}
template <int N> __device__ __forceinline__ f32x2 rows_sum(LAS char* p) {
  f32x2 v[N];
#pragma unroll
  for (int i = 0; i < N; i++) v[i] = *(volatile LAS f32x2*)(p + i * 256);
#pragma unroll
  for (int st = 1; st < N; st *= 2)
#pragma unroll
    for (int i = 0; i + st < N; i += 2 * st) v[i] += v[i + st];
  return v[0];
}
__device__ __forceinline__ float half_sum(float x) {
  auto t = __builtin_amdgcn_permlane32_swap(__float_as_uint(x), __float_as_uint(x), false, false);
  return __uint_as_float(t[0]) + __uint_as_float(t[1]);
}

__global__ __launch_bounds__(512, 2) void k_all(const char* a0_, const char* a1_, const char* a2_, const char* a3_, const char* a4_, const char* a5_,
                                                const char* a6_, const char* a7_, const char* a8_, const char* a9_, const char* a10_, char* a11_, unsigned* a12_) {
  u32x16 ka; u32x8 kb_; u32x2 kc_;
  asm volatile("s_load_dwordx16 %0, %3, 0x0\n\ts_load_dwordx8 %1, %3, 0x40\n\ts_load_dwordx2 %2, %3, 0x60"
               : "=&s"(ka), "=&s"(kb_), "=&s"(kc_) : "s"(__builtin_amdgcn_kernarg_segment_ptr()));
  __shared__ __attribute__((aligned(16))) char smem[LDS_BYTES];
  LAS char* lds = (LAS char*)smem;
  LAS char* scr = lds + LDS_SCR;
  LAS unsigned* ctl = (LAS unsigned*)scr;
  const int tid = threadIdx.x, lane = tid & 63, w = __builtin_amdgcn_readfirstlane(tid >> 6);
  const int blk = blockIdx.x, head = blk >> 3, prod = (blk + head) & 7;
  const int sub = (0x54372106u >> (4 * prod)) & 7;
  unsigned cpf = 0;
  {
    u64 pc;
    asm volatile("s_getpc_b64 %0" : "=s"(pc));
    cpf = __builtin_amdgcn_raw_buffer_load_b32(mkrs((const GAS char*)(pc & ~255ull), CPF), w == 7 ? head * 2048 + lane * 32 : OOB, 0, 0);
    SB();
  }
  {
    unsigned c = 16 + 4 * (DCH * sub + tid);
    asm volatile("" : "+v"(c));
    c = tid < 8 + DCH ? c : ~0u;
    c = tid < 8 ? 0u : c;
    ctl[tid] = tid == 4 ? DCH : c;
    *(LAS u64*)(scr + SCR_SUM + min(tid, 2 * SUM_STR / 8 - 1) * 8) = 0;
  }
  const int l16 = lane & 15, grp = lane >> 4;
  const int b4 = head & 3, cntoff = WS_CNT + head * CSTR;
  const int bm = (b4 + 1 + (sub >= 3 ? sub - 3 : sub)) & 3;
  const size_t base = (size_t)(bm * 32 + head) << 20;
  const int so = ((w & 1) << bitpos('a')) | (((w >> 1) & 1) << bitpos('b')) | ((w >> 2) << bitpos('c')) | (sub >= 3 ? 1 << bitpos('p') : 0);
  int v0 = l16 * 16 + grp * (sub < 6 ? 256 : 131072) + (sub < 6 ? so : w * 524288 + head * 256);
  unsigned boff = sub < 6 ? (unsigned)base : 0u, bsz = sub < 6 ? 1u << 20 : 4u << 20;
  int s0[WTH];
#pragma unroll
  for (int i = 0; i < WTH; i++) {
    s0[i] = sub < 6 ? goff(i) : i * 8192;
    asm volatile("" : "+s"(s0[i]));
  }
  asm volatile("" : "+v"(v0), "+s"(boff), "+s"(bsz));
  SB();
  asm volatile("s_waitcnt lgkmcnt(0)" : "+s"(ka), "+s"(kb_), "+s"(kc_));
  auto ptr = [](unsigned lo, unsigned hi) { return (GAS char*)(((u64)hi << 32) | lo); };
  GAS char *const xA = ptr(ka[0], ka[1]), *const ckv = ptr(ka[2], ka[3]), *const wqA = ptr(ka[4], ka[5]), *const wuk = ptr(ka[6], ka[7]),
           *const wuv = ptr(ka[8], ka[9]), *const woA = ptr(ka[10], ka[11]), *const xB = ptr(ka[12], ka[13]), *const kc = ptr(ka[14], ka[15]),
           *const vc = ptr(kb_[0], kb_[1]), *const wqB = ptr(kb_[2], kb_[3]), *const woB = ptr(kb_[4], kb_[5]), *const ws = ptr(kb_[6], kb_[7]),
           *const out = ptr(kc_[0], kc_[1]);
  const u64 e0 = *(const __attribute__((address_space(4))) u64*)(u64)(ws + WS_EPOCH);
  const rsrc_t r0 = mkrs((sub < 6 ? kc : sub == 6 ? wqB : wqA) + boff, bsz);
  SB();
  u32x4 ra[16];
#pragma unroll
  for (int i = 0; i < WTH; i++) ra[i] = ldnt(r0, v0, s0[i]);
  SB();
  u32x4 xp[2][2];
  if (sub >= 6) {
    const GAS char* x = sub == 6 ? xB : xA;
#pragma unroll
    for (int s = 0; s < 2; s++) {
      const GAS u32x4* xs = (const GAS u32x4*)(x + (2 * s + (l16 >> 3)) * 1024 + (64 * w + 16 * grp) * 2);
      xp[s][0] = xs[0]; xp[s][1] = xs[1];
    }
#pragma unroll
    for (int i = WTH; i < 16; i++) ra[i] = ldnt(r0, v0, i * 8192);
    asm volatile("" ::: "memory");
    SB();
  }
  const rsrc_t rws = mkrs(ws, WS_END);
  unsigned mask[4];
#pragma unroll
  for (int v = 0; v < 4; v++) mask[v] = (((lane & 7) >> 1) == v) ? ((lane & 1) ? 0xffff0000u : 0x0000ffffu) : 0u;
  const size_t base4 = (size_t)(b4 * 32 + head) << 20;
  const rsrc_t rk4 = mkrs(kc + base4, 1u << 20), rv4 = mkrs(vc + base4, 1u << 20);
  const int voff = lane * 16;
  LAS char* qst = scr + 1024 + w * 512;
  const int qo = WS_QB + head * 2048 + (lane < 32 ? bm : b4) * 512 + (lane & 31) * 16;
  auto qwait = [&](u32x4 g, const Tag& tg) {
    if (!__all(qok(g, tg))) {
      for (unsigned spins = 0;; spins++) {
        asm volatile("" ::: "memory");
        g = ldsc(rws, qo, 0);
        if (__all(qok(g, tg)) || spins > SPIN_CAP) break;
        __builtin_amdgcn_s_sleep(2);
      }
    }
    *(LAS u32x2*)(qst + lane * 8) = u32x2{g[0], g[2]};
  };
  unsigned cur;

  if (__builtin_expect(sub < 6, 1)) {
    const rsrc_t rv = mkrs(vc + base, 1u << 20);
    u32x4 rb[16], g;
#pragma unroll
    for (int i = WTH; i < 32 + NXA + NXB; i++) {
      if (i == WTH) asm volatile("s_barrier" ::: "memory");
      vmwait<WTH - 1>();
      if (i == 32 + NXA + NXB - WTH) asm volatile("buffer_load_dwordx4 %0, %1, %2, 0 offen sc1" : "=&v"(g) : "v"(qo), "s"(rws));
      if (i < 32) {
        const u32x4 v = ldslot(r0, voff, so, i);
        if (i < 16) ra[i] = v; else rb[i - 16] = v;
      } else {
        const int j = i < 32 + NXA ? i + 16 - NXA : i + 32 - NXA - NXB;
        dma1(r0, voff + goff(j & 3), so ^ goff(j & ~3), (unsigned)(uintptr_t)lds + w * 16384 + (i - 32) * 1024);
      }
      SB();
    }
    asm volatile("s_waitcnt vmcnt(%1)" : "+v"(g) : "n"(WTH), "v"(cpf));
    qwait(g, mktag(e0));
    const u32x4 q = *(LAS u32x4*)(qst + l16 * 16);
    float sc[4] = {0.f, 0.f, 0.f, 0.f};
#pragma unroll 1
    for (int it = 0; it < 2; it++) {
      const rsrc_t rn = it ? rv : r0;
      sc[2] = sc[0]; sc[3] = sc[1];
      sc[1] = k_step<NXA>(ra, q, l16, rn, voff, so ^ (it ? goff(48) : goff(32)), it == 0, lds + w * 16384 + voff);
      sc[0] = k_step<NXB>(rb, q, l16, rn, voff, so ^ (it ? goff(32) : goff(48)), it == 0, lds + w * 16384 + NXA * 1024 + voff);
    }
    const float mx = xrow_max(row_max(fmaxf(fmaxf(sc[0], sc[1]), fmaxf(sc[2], sc[3]))));
    float f, l = 0.f;
    const float M = exp_ref(mx, f);
    unsigned pk[4];
#pragma unroll
    for (int k = 0; k < 4; k++) {
      const float p = fexp2(fmaf(sc[k] - mx, LOG2E, f));
      pk[k] = cvtpk(p, p);
      l += bf_lo(pk[k]);
    }
    l = xrow_sum(row_sum(l));
    cur = 8 * sub + w;
    f32x4 acc = {0.f, 0.f, 0.f, 0.f};
    v_step<0, 1>(ra, pk[0], mask, acc, r0, rv, voff, so ^ goff(16));
    v_step<0, 1>(rb, pk[1], mask, acc, r0, rv, voff, so);
    v_step<0, 2>(ra, pk[2], mask, acc, rk4, rv4, voff, cur * 4096);
    v_step<0, 0>(rb, pk[3], mask, acc, r0, rv, voff, 0);
    Acc A;
    A.R = rfl(bucket(M));
    const double sm = mkd(0u, p2hi(M, A.R));
    A.o[0] = (lane < 32 ? acc[0] : acc[2]) * sm; A.o[1] = (lane < 32 ? acc[1] : acc[3]) * sm;
    A.l = l * sm;
    b_merge(mkmrg(scr + SCR_SUM, lane, WS_RS + (head * 16 + sub) * 1024, mktag(e0)), scr + 8192, w, lane, A, rws, e0);
  } else {
  {
    __builtin_amdgcn_raw_buffer_store_b32(48 + 24 * DCH, rws, (sub == 6 && tid == 0) ? cntoff : OOB, 0, AUX_SC1);
    {
      const rsrc_t rk = mkrs(wuk, sub == 7 ? 4u << 20 : 0u);
#pragma unroll
      for (int i = 0; i < 16; i++)
        __builtin_amdgcn_raw_ptr_buffer_load_lds(rk, (LAS void*)(lds + w * 16384 + i * 1024), 16,
                                                 (64 * w + grp) * 8192 + head * 256 + ((l16 ^ (4 * (i & 3) + grp)) * 16), i * 4 * 8192, 0, AUX_NT);
    }
    SB();
    f32x4 acc[2] = {{0.f, 0.f, 0.f, 0.f}, {0.f, 0.f, 0.f, 0.f}};
    diag_gemv(ra, xp, mask, acc);
    LAS float* red = (LAS float*)(scr + 8192);
#pragma unroll
    for (int s = 0; s < 2; s++) *(LAS f32x4*)(red + w * 512 + (2 * s + (grp >> 1)) * 128 + 8 * l16 + 4 * (grp & 1)) = acc[s];
    asm volatile("" :: "v"(cpf));
    __builtin_amdgcn_s_waitcnt(0x0f70);
    drain();
    bar();
    if (tid < 128) {
      f32x4 a = {0.f, 0.f, 0.f, 0.f};
#pragma unroll
      for (int k = 0; k < 8; k++) a += *(LAS f32x4*)(red + k * 512 + 4 * tid);
      const unsigned p0 = cvtpk(a[0], a[1]), p1 = cvtpk(a[2], a[3]);
      const Tag tg = mktag(e0);
      stsc(u32x4{p0, tg.th, p1, tg.th}, rws, sub == 6 ? WS_QB + head * 2048 + tid * 16 : OOB, 0);
      *(LAS u32x2*)(scr + 1024 + tid * 8) = u32x2{p0, p1};
    }
  }

  if (sub == 6) {
    const int aid = ((head & 6) << 2) | ((head & 1) << 2) | (head >> 3), b = aid >> 3;
    const rsrc_t rc = mkrs(ckv + (size_t)aid * 524288, 524288);
    const int dvo = (lane >> 3) * 1024 + w * 128 + (((lane & 7) ^ fz(lane >> 3)) * 16);
    const unsigned dlo = (unsigned)(uintptr_t)lds + w * 8192;
    const int xt = (aid & 7) << 16;
#pragma unroll
    for (int j = 0; j < 16; j++) dma1(rc, dvo, (j * 8192) ^ xt, dlo + (j >> 3) * 65536 + (j & 7) * 1024);
    SB();
    const Tag tg = mktag(e0);
    bar();
    {
      u32x4 t[8];
      unsigned miss = 15;
      __builtin_amdgcn_s_sleep(90);
      for (unsigned spins = 0;; spins++) {
        asm volatile("" ::: "memory");
#pragma unroll
        for (int i = 0; i < 4; i++)
          if (miss >> i & 1) {
            t[2 * i] = ldsc(rws, WS_QABS + (b * 32 + w) * 2048 + lane * 16, i * 16384);
            t[2 * i + 1] = ldsc(rws, WS_QABS + (b * 32 + w) * 2048 + lane * 16 + 1024, i * 16384);
          }
        SB();
#pragma unroll
        for (int i = 0; i < 4; i++) if (__all(qok(t[2 * i], tg) & qok(t[2 * i + 1], tg))) miss &= ~(1u << i);
        if (!miss || spins > SPIN_CAP) break;
        __builtin_amdgcn_s_sleep(4);
      }
#pragma unroll
      for (int i = 0; i < 8; i++) {
        const int row = 8 * (i >> 1) + w;
        *(LAS u32x2*)(scr + row * 1024 + (((32 * (i & 1) + (lane >> 1)) ^ swz(row)) * 16) + (lane & 1) * 8) = u32x2{t[i][0], t[i][2]};
      }
    }
    bar();
    const int kb = w & 3, hb = w >> 2;
    u32x4 qf[16];
    {
      const int row = 16 * hb + l16;
#pragma unroll
      for (int ks = 0; ks < 16; ks++) qf[ks] = *(LAS u32x4*)(scr + row * 1024 + (((4 * ks + grp) ^ swz(row)) * 16));
    }
    LAS float* mx = (LAS float*)scr;
    LAS char* pl = scr + 1024;
    LAS float* lr = (LAS float*)(scr + 8192);
    f32x16 o0, o1;
#pragma unroll
    for (int j = 0; j < 16; j++) { o0[j] = 0.f; o1[j] = 0.f; }
    float m1 = -INFINITY, m2 = -INFINITY, l1 = 0.f;
    const int h1 = 16 * hb + l16, h2 = lane & 31, hh = lane >> 5;
    const int arow = 16 * kb + l16;
    const int tq = (lane & 15) >> 2, tp = lane & 3, tblk = (lane >> 4) & 1;
    const int qa0 = arow * 128 + ((grp ^ fz(arow)) * 16), qa1 = qa0 ^ 64;
    const int pr = 8 * hh + tq, pc = 2 * tblk + (tp >> 1);
    const int pa0 = w * 8192 + pr * 128 + ((pc ^ fz(pr)) * 16) + 8 * (tp & 1), pa1 = w * 8192 + (pr + 4) * 128 + ((pc ^ fz(pr + 4)) * 16) + 8 * (tp & 1);
#pragma unroll 1
    for (int t = 0; t < 8; t++) {
      if (t < 7) asm volatile("s_waitcnt vmcnt(8)" ::: "memory"); else asm volatile("s_waitcnt vmcnt(0)" ::: "memory");
      bar();
      LAS char* img = lds + (t & 1) * 65536;
      f32x4 s0 = {0.f, 0.f, 0.f, 0.f}, s1 = s0;
      u32x4 ka[16];
      u32x2 ta[4][4];
      const int nso = t < 6 ? ((t + 2) * 65536) ^ xt : OOB;
#pragma unroll
      for (int ks = 0; ks < 16; ks++) ka[ks] = *(LAS u32x4*)(img + ((ks & 1) ? qa1 : qa0) + (ks >> 1) * 8192);
      SB();
#pragma unroll
      for (int k2 = 0; k2 < 8; k2++) {
        s0 = mfma16(ka[2 * k2], qf[2 * k2], s0);
        s1 = mfma16(ka[2 * k2 + 1], qf[2 * k2 + 1], s1);
      }
      asm volatile("" : "+v"(s0), "+v"(s1));
      SB();
#pragma unroll
      for (int ks = 0; ks < 4; ks++) {
        ta[ks][0] = trread(img + pa0 + ks * 2048); ta[ks][1] = trread(img + pa1 + ks * 2048);
        ta[ks][2] = trread(img + (pa0 ^ 64) + ks * 2048); ta[ks][3] = trread(img + (pa1 ^ 64) + ks * 2048);
      }
      SB();
      float x[4];
#pragma unroll
      for (int j = 0; j < 4; j++) x[j] = rbf(rbf(s0[j] + s1[j]) * SCALE);
      const float lm = xrow_max(fmaxf(fmaxf(x[0], x[1]), fmaxf(x[2], x[3])));
      if (grp == 0) mx[h1 * 4 + kb] = lm;
      LAS u32x2* pw = (LAS u32x2*)(pl + h1 * 144 + (16 * kb + 4 * grp) * 2);
      u32x2 pp = {cvtpk(fexp2((x[0] - m1) * LOG2E), fexp2((x[1] - m1) * LOG2E)), cvtpk(fexp2((x[2] - m1) * LOG2E), fexp2((x[3] - m1) * LOG2E))};
      *pw = pp;
      bar();
      const f32x4 ma = *(LAS f32x4*)(mx + h1 * 4), mb = *(LAS f32x4*)(mx + h2 * 4);
      u32x4 pb[4];
#pragma unroll
      for (int ks = 0; ks < 4; ks++) pb[ks] = *(LAS u32x4*)(pl + h2 * 144 + (16 * ks + 8 * hh) * 2);
      const float n1 = fmaxf(m1, fmaxf(fmaxf(ma[0], ma[1]), fmaxf(ma[2], ma[3])));
      const float n2 = fmaxf(m2, fmaxf(fmaxf(mb[0], mb[1]), fmaxf(mb[2], mb[3])));
      float al1 = fexp2((m1 - n1) * LOG2E), al2 = fexp2((m2 - n2) * LOG2E);
      asm volatile("" : "+v"(pb[0]), "+v"(pb[1]), "+v"(pb[2]), "+v"(pb[3]));
      if (__any(n2 - m2 > 40.f)) {
        bar();
        pp = u32x2{cvtpk(fexp2((x[0] - n1) * LOG2E), fexp2((x[1] - n1) * LOG2E)), cvtpk(fexp2((x[2] - n1) * LOG2E), fexp2((x[3] - n1) * LOG2E))};
        *pw = pp;
        l1 *= al1;
#pragma unroll
        for (int j = 0; j < 16; j++) { o0[j] *= al2; o1[j] *= al2; }
        al1 = al2 = 1.f;
        bar();
#pragma unroll
        for (int ks = 0; ks < 4; ks++) pb[ks] = *(LAS u32x4*)(pl + h2 * 144 + (16 * ks + 8 * hh) * 2);
      }
      m1 = n1; m2 = n2;
#pragma unroll
      for (int ks = 0; ks < 4; ks++) {
        o0 = mfma32(u32x4{ta[ks][0][0], ta[ks][0][1], ta[ks][1][0], ta[ks][1][1]}, pb[ks], o0);
        o1 = mfma32(u32x4{ta[ks][2][0], ta[ks][2][1], ta[ks][3][0], ta[ks][3][1]}, pb[ks], o1);
        SB();
        if (t < 6) {
          dma1(rc, dvo, nso + 2 * ks * 8192, dlo + (t & 1) * 65536 + 2 * ks * 1024);
          dma1(rc, dvo, nso + (2 * ks + 1) * 8192, dlo + (t & 1) * 65536 + (2 * ks + 1) * 1024);
        }
        SB();
      }
      l1 = (l1 + ((bf_lo(pp[0]) + bf_hi(pp[0])) + (bf_lo(pp[1]) + bf_hi(pp[1])))) * al1;
#pragma unroll
      for (int j = 0; j < 16; j++) { o0[j] *= al2; o1[j] *= al2; }
    }
    bar();
    {
      float am = 0.f;
#pragma unroll
      for (int j = 0; j < 16; j++) am = fmaxf(am, fmaxf(fabsf(o0[j]), fabsf(o1[j])));
      auto sw = __builtin_amdgcn_permlane32_swap(__float_as_uint(am), __float_as_uint(am), false, false);
      am = fmaxf(__uint_as_float(sw[0]), __uint_as_float(sw[1]));
      const int e = max(__builtin_amdgcn_frexp_expf(am), -127);
      LAS char* tp = lds + h2 * 1040 + (64 * w + 4 * hh) * 2;
      auto pkn = [&](float a, float b) { return __builtin_bit_cast(unsigned, __builtin_amdgcn_cvt_pknorm_i16(__builtin_ldexpf(a, -e), __builtin_ldexpf(b, -e))); };
#pragma unroll
      for (int j4 = 0; j4 < 4; j4++) {
        *(LAS u32x2*)(tp + j4 * 16) = u32x2{pkn(o0[4 * j4], o0[4 * j4 + 1]), pkn(o0[4 * j4 + 2], o0[4 * j4 + 3])};
        *(LAS u32x2*)(tp + 64 + j4 * 16) = u32x2{pkn(o1[4 * j4], o1[4 * j4 + 1]), pkn(o1[4 * j4 + 2], o1[4 * j4 + 3])};
      }
      if (hh == 0) *(LAS unsigned char*)(scr + 9216 + h2 * 8 + w) = (unsigned char)(e + 127);
      const float ls = xrow_sum(l1);
      if (grp == 0) lr[kb * 32 + h1] = ls;
      bar();
#pragma unroll
      for (int i = 0; i < 4; i++) {
        u32x4 v = *(LAS u32x4*)(lds + (4 * w + i) * 1040 + lane * 16);
#pragma unroll
        for (int k = 0; k < 4; k++) v[k] = bfi(PMASK, ptag(tg, k), v[k]);
        stsc(v, rws, WS_PO + (aid * 32 + 4 * w) * 1024 + lane * 16, i * 1024);
      }
      if (tid < 32) {
        const u32x2 eb = *(LAS u32x2*)(scr + 9216 + tid * 8);
        const float l = (lr[tid] + lr[32 + tid]) + (lr[64 + tid] + lr[96 + tid]);
        stsc(u32x4{bfi(0xffffu, tg.lo, __float_as_uint(m2)), bfi(255u, ltag(tg), __float_as_uint(l)), eb[0], eb[1]}, rws, WS_PML + (aid * 32 + tid) * 16, 0);
      }
      if (w == 0) {
        const unsigned t = __builtin_amdgcn_readfirstlane(__builtin_amdgcn_raw_ptr_buffer_atomic_add_i32(8 + 4 * DCH, rws, lane == 0 ? cntoff : OOB, 0, AUX_SC1));
        const unsigned bs = t + 8 + 4 * (lane - 8);
        ctl[lane] = lane == 3 ? t : lane == 4 ? DCH : lane == 5 ? (t + 8 + 4 * DCH >= NUNIT ? 0x10000u : 0u) : lane < 8 ? 0u : (lane < 8 + DCH && bs < NUNIT) ? bs : ~0u;
        if (lane < 32) ctl[64 + lane] = ~0u;
      }
      *(LAS u64*)(scr + SCR_SUM + SUM_STR + min(tid, SUM_STR / 8 - 1) * 8) = 0;
      bar();
    }
    const u32x4 g = ldsc(rws, qo, 0);
    cur = __builtin_amdgcn_readfirstlane(ctl[3]) + w;
#pragma unroll
    for (int i = 0; i < 8; i++) ra[i] = ldunit(i < 4 ? rk4 : rv4, voff, unit_off(cur), i & 3);
    SB();
    qwait(g, mktag(e0));
  } else {
    const Tag tg = mktag(e0);
    bar();
    {
      u32x4 bf[4];
#pragma unroll
      for (int ks = 0; ks < 4; ks++) bf[ks] = *(LAS u32x4*)(scr + 1024 + (l16 & 3) * 256 + ks * 64 + grp * 16);
      LAS char* tt = scr + 2048 + w * 512;
#pragma unroll
      for (int rt = 0; rt < 4; rt++) {
        f32x4 acc = {0.f, 0.f, 0.f, 0.f};
#pragma unroll
        for (int ks = 0; ks < 4; ks++) acc = mfma16(*(LAS u32x4*)(lds + (64 * w + 16 * rt + l16) * 256 + (((4 * ks + grp) ^ l16) * 16)), bf[ks], acc);
        if (l16 < 4) *(LAS u32x2*)(tt + l16 * 128 + (16 * rt + 4 * grp) * 2) = u32x2{cvtpk(acc[0], acc[1]), cvtpk(acc[2], acc[3])};
      }
      asm volatile("s_waitcnt lgkmcnt(0)" ::: "memory");
      const u32x2 v = *(LAS u32x2*)(tt + lane * 8);
      stsc(u32x4{v[0], tg.th, v[1], tg.th}, rws, WS_QABS + (grp * 32 + head) * 2048 + w * 256 + l16 * 16, 0);
    }
    bar();
    u32x4 wf[4][4], wr[16];
    {
      const rsrc_t ro = mkrs(woA + (size_t)head * 131072, 131072);
      dma_rows<16>(ro, 0, lds, 16 * w, lane);
      const rsrc_t rv = mkrs(wuv, 4u << 20);
      const int voff = (64 * w + 16 * grp) * 8192 + head * 256 + l16 * 16;
#pragma unroll
      for (int i = 0; i < 16; i++) wr[i] = ldnt(rv, voff, i * 8192);
      SB();
      asm volatile("s_waitcnt vmcnt(16)" ::: "memory");
      bar();
      wo_frags(lds, w, lane, wf);
      bar();
      const rsrc_t rb = mkrs(woB, 4u << 20);
      const int bvo = (lane >> 3) * 1024 + (((lane & 7) ^ ((lane >> 5) << 1)) * 16);
#pragma unroll
      for (int i = 0; i < 16; i++)
        dma1(rb, (i & 1) ? bvo ^ 64 : bvo, (4 * w + b4) * 131072 + (head >> 2) * 128 + i * 8192, (unsigned)(uintptr_t)lds + w * 16384 + i * 1024);
      SB();
      if (tid < 8) ((LAS unsigned*)(scr + 24576))[tid] = 0u;
    }
    const int hw = 4 * w + b4, cc = head >> 2, roff = WS_RS + hw * 16384 + lane * 16;
    u32x4 g[6];
#pragma unroll
    for (int s = 0; s < 6; s++) asm volatile("" : "=v"(g[s]));
    if (w == 0) {
      for (unsigned spins = 0;; spins++) {
        asm volatile("" ::: "memory");
        const u32x4 v = ldsc(rws, WS_PML + ((lane & 31) * 32 + head) * 16, 0);
        if (__builtin_popcount((unsigned)__builtin_amdgcn_ballot_w64(mok(v, tg))) >= NREL || spins > SPIN_CAP) break;
        __builtin_amdgcn_s_sleep(16);
      }
    }
    bar();
    {
      const int b = w >> 1, c4 = tid & 127;
      const unsigned ta = (lane & 1) ? ptag(tg, 2) : ptag(tg, 0), tb = (lane & 1) ? ptag(tg, 3) : ptag(tg, 1);
      u32x4 ml;
      u32x2 po[8];
#pragma unroll
      for (int s = 0; s < 8; s++) asm volatile("" : "=v"(po[s]));
      unsigned missP = 255;
      for (unsigned spins = 0;; spins++) {
        asm volatile("" ::: "memory");
        ml = ldsc(rws, WS_PML + ((8 * b + (lane & 7)) * 32 + head) * 16, 0);
#pragma unroll
        for (int s = 0; s < 8; s++)
          if (missP >> s & 1) po[s] = __builtin_amdgcn_raw_buffer_load_b64(rws, WS_PO + (8 * b * 32 + head) * 1024 + c4 * 8, s * 32768, AUX_SC1);
        SB();
#pragma unroll
        for (int s = 0; s < 8; s++) if (__all((((po[s][0] ^ ta) | (po[s][1] ^ tb)) & PMASK) == 0u)) missP &= ~(1u << s);
        if ((!missP && __all(mok(ml, tg))) || spins > SPIN_CAP) break;
        __builtin_amdgcn_s_sleep(2);
      }
#pragma unroll
      for (int s = 0; s < 6; s++) g[s] = ldsc(rws, roff, s * 1024);
      SB();
      ml[0] &= 0xffff0000u; ml[1] &= ~255u;
#pragma unroll
      for (int s = 0; s < 8; s++) po[s] = u32x2{bfi(PMASK, 0x00020002u, po[s][0]), bfi(PMASK, 0x00020002u, po[s][1])};
      float M = __uint_as_float(ml[0]);
      M = fmaxf(M, dpp<XOR1>(M)); M = fmaxf(M, dpp<XOR2>(M)); M = fmaxf(M, dpp<XOR7>(M));
      const float f = fexp2((__uint_as_float(ml[0]) - M) * LOG2E);
      float L = f * __uint_as_float(ml[1]);
      L += dpp<XOR1>(L); L += dpp<XOR2>(L); L += dpp<XOR7>(L);
      const float gl = __builtin_ldexpf(f, (int)((((w & 1) ? ml[3] : ml[2]) >> (8 * grp)) & 255) - 142);
      const float gs[8] = {dpp<0x150>(gl), dpp<0x151>(gl), dpp<0x152>(gl), dpp<0x153>(gl), dpp<0x154>(gl), dpp<0x155>(gl), dpp<0x156>(gl), dpp<0x157>(gl)};
      f32x4 a = {0.f, 0.f, 0.f, 0.f};
#pragma unroll
      for (int s = 0; s < 8; s++)
        a += gs[s] * f32x4{(float)(short)po[s][0], (float)((int)po[s][0] >> 16), (float)(short)po[s][1], (float)((int)po[s][1] >> 16)};
      const float inv = (32768.f / 32767.f) / L;
      const u32x2 v = {cvtpk(a[0] * inv, a[1] * inv), cvtpk(a[2] * inv, a[3] * inv)};
      *(LAS u32x2*)(scr + b * 1024 + c4 * 8) = v;
    }
    bar();
    {
      u32x4 xp[2][2];
#pragma unroll
      for (int s = 0; s < 2; s++) {
        LAS u32x4* xs = (LAS u32x4*)(scr + (2 * s + (l16 >> 3)) * 1024 + (64 * w + 16 * grp) * 2);
        xp[s][0] = xs[0]; xp[s][1] = xs[1];
      }
      f32x4 acc[2] = {{0.f, 0.f, 0.f, 0.f}, {0.f, 0.f, 0.f, 0.f}};
      diag_gemv(wr, xp, mask, acc);
      LAS float* red = (LAS float*)(scr + 8192);
#pragma unroll
      for (int s = 0; s < 2; s++) *(LAS f32x4*)(red + w * 512 + (2 * s + (grp >> 1)) * 128 + 8 * l16 + 4 * (grp & 1)) = acc[s];
      bar();
      if (tid < 256) {
        float a = 0.f, b = 0.f;
#pragma unroll
        for (int k = 0; k < 8; k++) { const f32x2 v = *(LAS f32x2*)(red + k * 512 + 2 * tid); a += v[0]; b += v[1]; }
        ((LAS unsigned*)(scr + 4096))[tid] = cvtpk(a, b);
      }
      bar();
    }
    const f32x4 yA = wo_calc(wf, scr + 4096, lane);
    drain();
    y_store(yA, w, lane, head, rws, WS_Y, tg);
    {
      const int q = l16 >> 2;
      LAS char* img = lds + w * 16384 + (8 * (q >> 1) + 4 * (grp & 1) + 2 * (grp >> 1) + (q & 1)) * 128 + 8 * (lane & 3);
      const int fl = (grp & 1) | (q & 2);
#pragma unroll
      for (int ks = 0; ks < 4; ks++)
#pragma unroll
        for (int tl = 0; tl < 4; tl++) {
          const u32x2 a = trread(img + (tl ^ fl) * 32 + ks * 4096), b = trread(img + (tl ^ fl) * 32 + ks * 4096 + 2048);
          wf[tl][ks] = u32x4{a[0], a[1], b[0], b[1]};
        }
    }
    const rsrc_t rout = mkrs(out, 8192);
    LAS unsigned* cnt = (LAS unsigned*)(scr + 24576);
    LAS char* const re = scr + 8192;
    LAS char* const rA = scr + 8192 + 8192;
    LAS char* rl = scr + 8192 + 10240 + (lane & 31) * 8;
    LAS char* rw = scr + 8192 + 10240 + w * 256 + (lane & 3) * 64 + grp * 16;
    LAS char* rx = rl + (w < 3 ? 8 + w : 11) * 256;
    int oo = lane < 32 ? b4 * 2048 + 1024 + cc * 128 + lane * 4 : OOB, oe = head == 0 && lane == 0 ? WS_EPOCH : OOB;
    asm volatile("" : "+v"(rl), "+v"(rw), "+v"(rx), "+v"(oo), "+v"(oe), "+v"(cnt));
    {
      const int ofa = WS_Y + (b4 * 8 + cc) * 16384 + w * 2048 + lane * 16;
      u32x4 x0, x1;
      for (unsigned spins = 0;; spins++) {
        asm volatile("" ::: "memory");
        x0 = ldsc(rws, ofa, 0); x1 = ldsc(rws, ofa + 1024, 0);
        if (__all(gok(x0, tg) & gok(x1, tg)) || spins > SPIN_CAP) break;
        __builtin_amdgcn_s_sleep(4);
      }
      const f32x2 r = {half_sum(gval(x0[0]) + gval(x1[0])), half_sum(gval(x0[2]) + gval(x1[2]))};
      if (lane < 32) *(volatile LAS f32x2*)(rA + w * 256 + lane * 8) = r;
      const unsigned n = arrive(cnt + 1, lane);
      const f32x2 v = rows_sum<8>(rA + (lane & 31) * 8);
      __builtin_amdgcn_raw_buffer_store_b32(cvtpk(v[0], v[1]), rout, lane < 32 && n == 8u ? b4 * 2048 + cc * 128 + l16 * 8 + grp * 4 : OOB, 0, AUX_SC1);
    }
    {
      for (unsigned spins = 0;; spins++) {
        unsigned bad = 0;
#pragma unroll
        for (int s = 0; s < 6; s++) bad |= (g[s][0] ^ tg.lo) | (g[s][2] ^ tg.rb);
        if (__all((bad & (spins > SPIN_CAP * 4 ? 0u : 511u)) == 0u)) break;
        asm volatile("" ::: "memory");
#pragma unroll
        for (int s = 0; s < 6; s++) g[s] = ldsc(rws, roff + s * 1024, 0);
        SB();
      }
      unsigned pr[3];
#pragma unroll
      for (int t = 0; t < 3; t++) {
        const u32x4 g0 = g[t], g1 = g[t + 3];
        const float R0 = rec_r(g0), R1 = rec_r(g1), Rm = fmaxf(R0, R1);
        const double s0 = mkd(0u, p2hi(R0, Rm)), s1 = mkd(0u, p2hi(R1, Rm));
        const double inv = __builtin_amdgcn_rcp(__builtin_fma(rec_l(g0), s0, rec_l(g1) * s1));
        pr[t] = cvtpk((float)(__builtin_fma(rec_a(g0), s0, rec_a(g1) * s1) * inv), (float)(__builtin_fma(rec_b(g0), s0, rec_b(g1) * s1) * inv));
      }
      *(volatile LAS f32x4*)(re + ((l16 >> 2) * 8 + w) * 256 + (lane & 3) * 64 + grp * 16) = proj(pr, wf, lane);
      const unsigned n = arrive(cnt, lane);
#pragma unroll
      for (int u = 0; u < 2; u++) {
        const int t = u ? 2 : lane >> 5;
        const f32x2 v = rows_sum<8>(re + t * 2048 + (lane & 31) * 8);
        stsc(gpair(v[0], v[1], tg), rws, n != 8u || (u && lane >= 32) ? OOB : WS_YB + ((((b4 + 1 + t) & 3) * 8 + cc) * 4 + b4) * 512 + (lane & 31) * 16, 0);
      }
    }
    const int ofb = w < 3 ? WS_YB + ((b4 * 8 + cc) * 4 + ((b4 + 1 + w) & 3)) * 512 + (lane & 31) * 16 : OOB;
    u32x4 xb, p[NSLOT];
    unsigned bad;
    for (unsigned spins = 0;; spins++) {
      asm volatile("" ::: "memory");
      xb = ldsc(rws, ofb, 0);
#pragma unroll
      for (int s = 0; s < NSLOT; s++) p[s] = ldsc(rws, roff + 8192 + s * 1024, 0);
      unsigned m = spins > SPIN_CAP * 4 ? 0u : 511u;
      asm volatile("" : "+s"(m));
      SB();
      unsigned ax = min((((xb[0] ^ tg.tl) | (xb[2] ^ tg.tl)) >> 24) | (xb[1] ^ tg.th) | (xb[3] ^ tg.th), w < 3 ? 1u : 0u), az = 0;
#pragma unroll
      for (int s = 0; s < NSLOT; s++) { ax |= p[s][0] ^ tg.lo; az |= p[s][2] ^ tg.rb; }
      bad = az & 512u;
      if (__all(((ax | az) & m) == 0u)) break;
    }
    *(volatile LAS f32x2*)rx = f32x2{gval(xb[0]), gval(xb[2])};
    double S0, S1, L;
    if (__builtin_expect(__all(bad == 0u), 1)) {
      S0 = mkd(p[0][0], p[0][1]); S1 = mkd(p[0][2], p[0][3]); L = rec_l(p[0]);
#pragma unroll
      for (int s = 1; s < NSLOT; s++) { S0 += mkd(p[s][0], p[s][1]); S1 += mkd(p[s][2], p[s][3]); L += rec_l(p[s]); }
    } else {
      float R = -INFINITY;
      S0 = S1 = L = 0.0;
#pragma unroll
      for (int s = 0; s < NSLOT; s++) {
        const float Rs = rec_l(p[s]) != 0.0 ? rec_r(p[s]) : -INFINITY, Rn = fmaxf(R, Rs);
        const double s0 = mkd(0u, p2hi(R, Rn)), s1 = mkd(0u, p2hi(Rs, Rn));
        L = __builtin_fma(rec_l(p[s]), s1, L * s0);
        S0 = __builtin_fma(rec_a(p[s]), s1, S0 * s0); S1 = __builtin_fma(rec_b(p[s]), s1, S1 * s0);
        R = Rn;
      }
    }
    {
      const double inv = __builtin_amdgcn_rcp(L);
      const unsigned pr[1] = {cvtpk((float)(S0 * inv), (float)(S1 * inv))};
      const f32x4 y = proj(pr, wf, lane);
      if ((lane & 12) == 0) *(volatile LAS f32x4*)rw = y;
      const unsigned n = arrive(cnt + 2, lane);
      const f32x2 v = rows_sum<11>(rl);
      __builtin_amdgcn_raw_buffer_store_b32(cvtpk(v[0], v[1]), rout, n == 8u ? oo : OOB, 0, AUX_SC1);
      __builtin_amdgcn_raw_buffer_store_b64(__builtin_bit_cast(u32x2, tg.e), rws, n == 8u ? oe : OOB, 0, AUX_SC1);
    }
    return;
  }
  }
  {
    const u32x4 q4 = *(LAS u32x4*)(qst + 256 + l16 * 16);
    const Mrg mg = mkmrg(scr + SCR_SUM + SUM_STR, lane, WS_RS + (head * 16 + 8 + sub) * 1024, mktag(e0));
    Acc A;
    b_units(ra, rk4, rv4, cur, q4, lane, mask, ctl, rws, cntoff, A);
    b_merge(mg, scr + 8192 + 8704, w, lane, A, rws, e0);
    SB();
    asm volatile("" :: "v"(ra[0]), "v"(ra[1]), "v"(ra[2]), "v"(ra[3]), "v"(ra[4]), "v"(ra[5]), "v"(ra[6]), "v"(ra[7]));
  }
}

extern "C" void kernel_launch(void* const* d_in, const int* in_sizes, int n_in, void* d_out, int out_size, void* d_ws, size_t ws_size,
                              hipStream_t stream) {
  k_all<<<256, 512, 0, stream>>>((const char*)d_in[0], (const char*)d_in[1], (const char*)d_in[2], (const char*)d_in[3], (const char*)d_in[4],
                                 (const char*)d_in[5], (const char*)d_in[6], (const char*)d_in[7], (const char*)d_in[8], (const char*)d_in[9],
                                 (const char*)d_in[10], (char*)d_ws, (unsigned*)d_out);
}
```

```cpp
#include <hip/hip_runtime.h>
#include <stdint.h>

typedef __attribute__((ext_vector_type(4))) unsigned u32x4;
typedef __attribute__((ext_vector_type(2))) unsigned u32x2;
typedef __attribute__((ext_vector_type(8))) unsigned u32x8;
typedef __attribute__((ext_vector_type(16))) unsigned u32x16;
typedef __attribute__((ext_vector_type(4))) float f32x4;
typedef __attribute__((ext_vector_type(2))) float f32x2;
typedef __attribute__((ext_vector_type(2))) double f64x2;
typedef __attribute__((ext_vector_type(16))) float f32x16;
typedef __attribute__((ext_vector_type(8))) __bf16 bf16x8;
typedef __attribute__((ext_vector_type(2))) __bf16 bf16x2;
typedef __attribute__((ext_vector_type(4))) short s16x4;
typedef unsigned long long u64;
typedef __amdgpu_buffer_rsrc_t rsrc_t;
#define LAS __attribute__((address_space(3)))
#define GAS __attribute__((address_space(1)))

#define CPF 35584
#define PAT "bp3x01yac524"
#define UPAT "x4630ghy2715"
#define CPAT "bacnt02vl1mu"
constexpr int WTH = 4;
constexpr int CH = 4, DCH = 2;
constexpr int NXA = 8, NXB = 8;
constexpr int NREL = 24;

constexpr int AUX_NT = 2, AUX_SC1 = 16, OOB = 0x40000000;
constexpr float SCALE = 0.08838834764831845f;
constexpr float LOG2E = 1.4426950408889634f;
constexpr unsigned SPIN_CAP = 1u << 17;
constexpr int NSLOT = 7, NUNIT = 256;

constexpr int WS_EPOCH = 0;
constexpr int CSTR    = 4352;
constexpr int WS_CNT  = 8192;
constexpr int WS_QB   = WS_CNT + 32 * CSTR;
constexpr int WS_QABS = WS_QB + 32 * 2048;
constexpr int WS_PML  = WS_QABS + 4 * 32 * 2048;
constexpr int WS_PO   = WS_PML + 32 * 32 * 16;
constexpr int WS_RS   = WS_PO + 32 * 32 * 1024;
constexpr int WS_Y    = WS_RS + 32 * 16 * 1024;
constexpr int WS_YB   = WS_Y + 4 * 8 * 32 * 512;
constexpr int WS_END  = WS_YB + 4 * 8 * 4 * 512;

constexpr int LDS_BYTES = 163840;
constexpr int LDS_SCR = 131072;
constexpr int SCR_SUM = 5120, SUM_STR = 1040;

__device__ __forceinline__ unsigned cvtpk(float lo, float hi) {
  f32x2 v = {lo, hi};
  return __builtin_bit_cast(unsigned, __builtin_convertvector(v, bf16x2));
}
__device__ __forceinline__ float bf_lo(unsigned u) { return __uint_as_float(u << 16); }
__device__ __forceinline__ float bf_hi(unsigned u) { return __uint_as_float(u & 0xffff0000u); }
__device__ __forceinline__ float rbf(float x) { return __uint_as_float(cvtpk(x, x) << 16); }
__device__ __forceinline__ float dot2(unsigned k, unsigned q, float acc) {
  return __builtin_amdgcn_fdot2_f32_bf16(__builtin_bit_cast(bf16x2, k), __builtin_bit_cast(bf16x2, q), acc, false);
}
template <int CTRL> __device__ __forceinline__ float dpp(float x) {
  return __builtin_bit_cast(float, __builtin_amdgcn_mov_dpp(__builtin_bit_cast(int, x), CTRL, 0xf, 0xf, true));
}
template <int CTRL> __device__ __forceinline__ unsigned dppu(unsigned x) {
  return (unsigned)__builtin_amdgcn_mov_dpp((int)x, CTRL, 0xf, 0xf, true);
}
constexpr int XOR1 = 0xB1, XOR2 = 0x4E, XOR7 = 0x141, XOR8 = 0x128, ROR4 = 0x124;
__device__ __forceinline__ float xrow_max(float x) {
  auto s = __builtin_amdgcn_permlane16_swap(__float_as_uint(x), __float_as_uint(x), false, false);
  x = fmaxf(__uint_as_float(s[0]), __uint_as_float(s[1]));
  auto t = __builtin_amdgcn_permlane32_swap(__float_as_uint(x), __float_as_uint(x), false, false);
  return fmaxf(__uint_as_float(t[0]), __uint_as_float(t[1]));
}
__device__ __forceinline__ float xrow_sum(float x) {
  auto s = __builtin_amdgcn_permlane16_swap(__float_as_uint(x), __float_as_uint(x), false, false);
  x = __uint_as_float(s[0]) + __uint_as_float(s[1]);
  auto t = __builtin_amdgcn_permlane32_swap(__float_as_uint(x), __float_as_uint(x), false, false);
  return __uint_as_float(t[0]) + __uint_as_float(t[1]);
}
__device__ __forceinline__ float row_max(float x) {
  x = fmaxf(x, dpp<XOR8>(x)); x = fmaxf(x, dpp<ROR4>(x)); x = fmaxf(x, dpp<XOR2>(x)); x = fmaxf(x, dpp<XOR1>(x));
  return x;
}
__device__ __forceinline__ float row_sum(float x) {
  x += dpp<XOR8>(x); x += dpp<ROR4>(x); x += dpp<XOR2>(x); x += dpp<XOR1>(x);
  return x;
}
__device__ __forceinline__ float fexp2(float x) { return __builtin_amdgcn_exp2f(x); }
__device__ __forceinline__ f32x4 mfma16(u32x4 a, u32x4 b, f32x4 c) {
  return __builtin_amdgcn_mfma_f32_16x16x32_bf16(__builtin_bit_cast(bf16x8, a), __builtin_bit_cast(bf16x8, b), c, 0, 0, 0);
}
__device__ __forceinline__ f32x16 mfma32(u32x4 a, u32x4 b, f32x16 c) {
  return __builtin_amdgcn_mfma_f32_32x32x16_bf16(__builtin_bit_cast(bf16x8, a), __builtin_bit_cast(bf16x8, b), c, 0, 0, 0);
}
__device__ __forceinline__ rsrc_t mkrs(const GAS char* p, unsigned bytes) {
  return __builtin_amdgcn_make_buffer_rsrc((void*)p, 0, bytes, 0x00020000);
}
__device__ __forceinline__ void bar() { asm volatile("s_waitcnt lgkmcnt(0)\n\ts_barrier" ::: "memory"); }
__device__ __forceinline__ void drain() { asm volatile("s_waitcnt vmcnt(0)" ::: "memory"); }
#define SB() __builtin_amdgcn_sched_barrier(0)
template <int N> __device__ __forceinline__ void vmwait() { asm volatile("s_waitcnt vmcnt(%0)" :: "n"(N) : "memory"); }
__device__ __forceinline__ unsigned swz(unsigned row) { return ((row & 3) << 2) | ((row >> 2) & 2); }
__device__ __forceinline__ u32x2 trread(LAS char* p) {
  return __builtin_bit_cast(u32x2, __builtin_amdgcn_ds_read_tr16_b64_v4i16((LAS s16x4*)p));
}

struct Tag { unsigned tl, th, lo, rb; u64 e; };
__device__ __forceinline__ Tag mktag(u64 e0) {
  Tag t;
  asm volatile("" : "+s"(e0));
  t.e = e0 * 6364136223846793005ull + 1442695040888963407ull;
  t.th = (unsigned)(t.e >> 32);
  t.lo = (unsigned)t.e;
  t.tl = t.lo & 0xff000000u;
  t.rb = ((t.lo >> 9) & 255u) | (~t.lo & 256u);
  return t;
}
__device__ __forceinline__ unsigned gpack(float v, const Tag& t) { return ((__float_as_uint(v) + 0x80u) >> 8) | t.tl; }
__device__ __forceinline__ u32x4 gpair(float a, float b, const Tag& t) { return u32x4{gpack(a, t), t.th, gpack(b, t), t.th}; }
__device__ __forceinline__ bool gok(u32x4 g, const Tag& t) {
  return ((((g[0] ^ t.tl) | (g[2] ^ t.tl)) >> 24) | (g[1] ^ t.th) | (g[3] ^ t.th)) == 0u;
}
__device__ __forceinline__ float gval(unsigned lo) { return __uint_as_float(lo << 8); }
__device__ __forceinline__ bool qok(u32x4 g, const Tag& t) { return ((g[1] ^ t.th) | (g[3] ^ t.th)) == 0u; }
__device__ __forceinline__ double mkd(unsigned lo, unsigned hi) { return __builtin_bit_cast(double, u32x2{lo, hi}); }
__device__ __forceinline__ u32x4 rpack(double a, double b, double l, float r, const Tag& t) {
  const u32x2 ua = __builtin_bit_cast(u32x2, a), ub = __builtin_bit_cast(u32x2, b), ul = __builtin_bit_cast(u32x2, l);
  const u64 ml = ((u64)(unsigned)__builtin_amdgcn_readfirstlane(ul[1]) << 32) | (unsigned)__builtin_amdgcn_readfirstlane(ul[0]);
  const u64 mr = (unsigned)__builtin_amdgcn_readfirstlane(__float_as_uint(r));
  const unsigned ta = t.lo & 511u, tb = t.rb;
  return u32x4{(ua[0] & ~1023u) | (__builtin_amdgcn_inverse_ballot_w64(ml) ? ta | 512u : ta), ua[1],
               (ub[0] & ~1023u) | (__builtin_amdgcn_inverse_ballot_w64(mr) ? tb | 512u : tb), ub[1]};
}
__device__ __forceinline__ u32x4 rpack0(f64x2 o, unsigned lw, int lane, unsigned ta, unsigned tb) {
  const u32x4 u = __builtin_bit_cast(u32x4, o);
  return u32x4{(u[0] & ~1023u) | (ta | (__builtin_amdgcn_ubfe(lw, lane, 1) << 9)), u[1], (u[2] & ~1023u) | tb, u[3]};
}
__device__ __forceinline__ bool rok(u32x4 g, const Tag& t) { return (((g[0] ^ t.lo) | (g[2] ^ t.rb)) & 511u) == 0u; }
constexpr unsigned PMASK = 0x00030003u;
__device__ __forceinline__ unsigned ptag(const Tag& t, int k) {
  const unsigned n = k == 0 ? t.lo : k == 3 ? t.lo >> 10 : ((t.lo >> (1 + 3 * k)) & 7u) | (~t.lo & 8u);
  return (n & 3u) | ((n & 12u) << 14);
}
__device__ __forceinline__ unsigned ltag(const Tag& t) { return ((t.lo >> 16) & 127u) | (~t.lo & 128u); }
__device__ __forceinline__ bool mok(u32x4 g, const Tag& t) { return (((g[0] ^ t.lo) & 0xffffu) | ((g[1] ^ ltag(t)) & 255u)) == 0u; }
__device__ __forceinline__ unsigned bfi(unsigned m, unsigned a, unsigned b) { return (a & m) | (b & ~m); }
__device__ __forceinline__ double rec_a(u32x4 g) { return mkd(g[0] & ~1023u, g[1]); }
__device__ __forceinline__ double rec_b(u32x4 g) { return mkd(g[2] & ~1023u, g[3]); }
__device__ __forceinline__ double rec_l(u32x4 g) { return __builtin_bit_cast(double, __builtin_amdgcn_ballot_w64((g[0] & 512u) != 0u)); }
__device__ __forceinline__ float rec_r(u32x4 g) { return __uint_as_float((unsigned)__builtin_amdgcn_ballot_w64((g[2] & 512u) != 0u)); }
__device__ __forceinline__ int edif(float a, float b) {
  int r;
  asm("v_cvt_i32_f32 %0, %1" : "=v"(r) : "v"(a - b));
  return r;
}
__device__ __forceinline__ unsigned p2hi(float a, float b) { return (unsigned)max(edif(a, b) + 1023, 0) << 20; }
__device__ __forceinline__ float bucket(float m) { return fmaf(__builtin_rintf(m * (1.f / 512.f)), 512.f, 0.f); }
__device__ __forceinline__ float rfl(float x) { return __uint_as_float(__builtin_amdgcn_readfirstlane(__float_as_uint(x))); }
__device__ __forceinline__ unsigned rdu(unsigned v, int l) { return __builtin_amdgcn_readlane(v, l); }

__device__ __forceinline__ void diag_gemv(const u32x4 (&wr)[16], const u32x4 (&xp)[2][2], const unsigned (&mask)[4], f32x4 (&acc)[2]) {
#pragma unroll
  for (int i = 0; i < 16; i++) {
#pragma unroll
    for (int s = 0; s < 2; s++) {
      const unsigned src = xp[s][i >> 3][(i >> 1) & 3];
      const unsigned dup = __builtin_amdgcn_perm(src, src, (i & 1) ? 0x03020302u : 0x01000100u);
      const u32x4 a = {dup & mask[0], dup & mask[1], dup & mask[2], dup & mask[3]};
      acc[s] = mfma16(a, wr[i], acc[s]);
    }
  }
}

constexpr int bitpos(char c) { for (int i = 0; i < 12; i++) if (PAT[i] == c) return 8 + i; return 31; }
constexpr int goff(int t) { int o = 0; for (int j = 0; j < 6; j++) o |= ((t >> j) & 1) << bitpos('0' + j); return o; }
struct Acc { double o[2]; double l; float R; };
__device__ __forceinline__ u32x4 ldnt(rsrc_t r, int voff, int so) { return __builtin_amdgcn_raw_buffer_load_b128(r, voff, so, AUX_NT); }
__device__ __forceinline__ u32x4 ldslot(rsrc_t r, int voff, int so, int i) { return ldnt(r, voff + goff(i & 3), so ^ goff(i & ~3)); }
constexpr int upos(char c) { for (int i = 0; i < 12; i++) if (UPAT[i] == c) return 8 + i; return 31; }
__device__ __forceinline__ u32x4 ldunit(rsrc_t r, int vu, int so, int i) { return ldnt(r, vu + ((i & 1) << upos('g')) + ((i >> 1) << upos('h')), so); }
__device__ __forceinline__ u32x4 ldsc(rsrc_t r, int voff, int so) { return __builtin_amdgcn_raw_buffer_load_b128(r, voff, so, AUX_SC1); }
__device__ __forceinline__ void stsc(u32x4 v, rsrc_t r, int voff, int so) { __builtin_amdgcn_raw_buffer_store_b128(v, r, voff + so, 0, AUX_SC1); }
__device__ __forceinline__ float dot8(u32x4 k, u32x4 q) { return dot2(k[3], q[3], dot2(k[2], q[2], dot2(k[1], q[1], dot2(k[0], q[0], 0.f)))); }
#define TR_STEP(HALF, CTRL, BIT) _Pragma("unroll") for (int h = 0; h < (HALF); ++h) { \
    const float t1 = x[h] + dpp<CTRL>(x[h]), t2 = x[h + (HALF)] + dpp<CTRL>(x[h + (HALF)]); x[h] = (c & (BIT)) ? t2 : t1; }
template <int N> __device__ __forceinline__ float k_step(u32x4 (&ring)[16], u32x4 q, int c, rsrc_t rn, int voff, int so, bool ext, LAS char* xp) {
  float x[16];
#pragma unroll
  for (int i = 0; i < 16; i++) {
    x[i] = dot8(ring[i], q);
    asm volatile("s_waitcnt vmcnt(%1)" : "+v"(x[i]) : "n"(WTH - 1));
    if (i >= 16 - N && ext) ring[i] = *(volatile LAS u32x4*)(xp + (i - (16 - N)) * 1024);
    else ring[i] = ldslot(rn, voff, so, i);
    SB();
  }
  TR_STEP(8, XOR8, 8) TR_STEP(4, XOR7, 4) TR_STEP(2, XOR2, 2) TR_STEP(1, XOR1, 1)
  return rbf(rbf(x[0]) * SCALE);
}
template <int BC> __device__ __forceinline__ void pv(u32x4 v, unsigned pk, const unsigned (&mask)[4], f32x4& acc) {
  const unsigned d = dppu<0x150 + BC>(pk);
  acc = mfma16(u32x4{d & mask[0], d & mask[1], d & mask[2], d & mask[3]}, v, acc);
}
template <int I, int MODE> __device__ __forceinline__ void v_step(u32x4 (&ring)[16], unsigned pk, const unsigned (&mask)[4], f32x4& acc,
                                                                  rsrc_t rk, rsrc_t rv, int voff, int so) {
  pv<I>(ring[I], pk, mask, acc);
  if (MODE == 1 || (MODE == 2 && I < 8)) {
    asm volatile("s_waitcnt vmcnt(%1)" : "+v"(acc) : "n"(WTH - 1));
    ring[I] = MODE == 1 ? ldslot(rv, voff, so, I) : ldunit(I < 4 ? rk : rv, voff, so, I & 3);
  } else {
    asm volatile("" : "+v"(acc));
  }
  SB();
  if constexpr (I < 15) v_step<I + 1, MODE>(ring, pk, mask, acc, rk, rv, voff, so);
}
__device__ __forceinline__ float exp_ref(float mx, float& f) {
  const float M = __builtin_ceilf(mx * LOG2E);
  f = __builtin_amdgcn_fmed3f(fmaf(mx, LOG2E, -M), -1.f, 0.f);
  return M;
}
__device__ __forceinline__ unsigned lds_inc(LAS unsigned* p, int lane) {
  unsigned t = 0, z = 0;
  asm volatile("" : "+v"(z));
  if (lane == 0) t = __hip_atomic_fetch_add(p + z, 1u, __ATOMIC_RELAXED, __HIP_MEMORY_SCOPE_WORKGROUP);
  return t;
}
struct Take { unsigned unit, pend; bool fetch; };
__device__ __forceinline__ void lds_add(LAS unsigned* p, unsigned v) { __hip_atomic_fetch_add(p, v, __ATOMIC_RELAXED, __HIP_MEMORY_SCOPE_WORKGROUP); }
__device__ __forceinline__ Take take(LAS unsigned* ctl, unsigned kv, rsrc_t rws, int cntoff, int lane) {
  const unsigned k = __builtin_amdgcn_readfirstlane(kv);
  unsigned b, s;
  auto look = [&]() {
    const unsigned vs = ((volatile LAS unsigned*)ctl)[5], vb = ((volatile LAS unsigned*)ctl)[8 + (k >> 2)];
    s = __builtin_amdgcn_readfirstlane(vs); b = __builtin_amdgcn_readfirstlane(vb);
    return (b == ~0u) & ((s < 0x10000u) | ((s & 0xffffu) != 0u));
  };
  if (__builtin_expect(look(), 0))
    for (unsigned spins = 0; spins < SPIN_CAP * 16; spins++) {
      __builtin_amdgcn_s_sleep(1);
      if (!look()) break;
    }
  Take t;
  t.unit = min(b, (unsigned)NUNIT) + (k & (CH - 1));
  t.fetch = ((k & (CH - 1)) == 0) & (b < NUNIT) & (s < 0x10000u);
  int off = OOB;
  if (t.fetch) {
    unsigned z = 0;
    asm volatile("" : "+v"(z));
    if (lane == 0) { lds_add(ctl + 5 + z, 1u); off = cntoff; }
    asm volatile("s_waitcnt lgkmcnt(0)" ::: "memory");
  }
  t.pend = __builtin_amdgcn_raw_ptr_buffer_atomic_add_i32(CH, rws, off, 0, AUX_SC1);
  SB();
  return t;
}
__device__ __forceinline__ void give(LAS unsigned* ctl, const Take& t, int lane) {
  if (t.fetch && lane == 0) {
    unsigned z = 0;
    asm volatile("" : "+v"(z));
    if (t.pend < NUNIT) {
      const unsigned i = __hip_atomic_fetch_add(ctl + 4 + z, 1u, __ATOMIC_RELAXED, __HIP_MEMORY_SCOPE_WORKGROUP);
      ((volatile LAS unsigned*)ctl)[8 + i] = t.pend;
    }
    asm volatile("" ::: "memory");
    lds_add(ctl + 5 + z, t.pend >= NUNIT - CH ? 0xffffu : ~0u);
  }
}
__device__ __forceinline__ int unit_off(unsigned t, unsigned ub) {
  const int a = __builtin_amdgcn_readlane(ub, t & 15), b = __builtin_amdgcn_readlane(ub, 16 + ((t >> 4) & 15));
  return t < NUNIT ? a | b : 1 << 20;
}

__device__ __forceinline__ void b_units(u32x4 (&ra)[16], rsrc_t rk, rsrc_t rv, unsigned cur, u32x4 q, int lane, const unsigned (&mask)[4],
                                        LAS unsigned* ctl, rsrc_t rws, int cntoff, Acc& A, int voff, unsigned ub) {
  const int c = lane & 15;
  float Mr = -INFINITY;
  double s1 = 0.0;
  A.o[0] = A.o[1] = A.l = 0.0; A.R = cur < NUNIT ? -INFINITY : 0.f;
#pragma unroll 1
  while (cur < NUNIT) {
    const Take t = take(ctl, lds_inc(ctl, lane), rws, cntoff, lane);
    const int so = unit_off(t.unit, ub);
    SB();
    float x[4];
    auto next = [&](int j) { ra[j] = ldunit(j < 4 ? rk : rv, voff, so, j & 3); };
#pragma unroll
    for (int i = 0; i < 4; i++) x[i] = dot8(ra[i], q);
    asm volatile("" : "+v"(x[0]), "+v"(x[1]), "+v"(x[2]), "+v"(x[3]));
    SB();
#pragma unroll
    for (int i = 0; i < 4; i++) {
      next(i);
      SB();
    }
    TR_STEP(2, XOR8, 8) TR_STEP(1, XOR7, 4)
    x[0] += dpp<XOR2>(x[0]); x[0] += dpp<XOR1>(x[0]);
    const float s = rbf(rbf(x[0]) * SCALE);
    const float n = __builtin_rintf(s * LOG2E);
    float e = fexp2(__builtin_amdgcn_fmed3f(fmaf(s, LOG2E, -n), -1.f, 1.f)), d = n - Mr;
    asm volatile("" : "+v"(e));
    if (__builtin_expect(__any(d > 57.f), 0)) {
      float mx = fmaxf(n, dpp<XOR8>(n));
      mx = xrow_max(fmaxf(mx, dpp<ROR4>(mx)));
      const float Rn = rfl(fmaxf(A.R, bucket(mx)));
      const double s0 = mkd(0u, p2hi(A.R, Rn));
      A.o[0] *= s0; A.o[1] *= s0; A.l *= s0;
      A.R = Rn; Mr = mx; d = n - mx;
      s1 = mkd(0u, p2hi(mx, Rn));
    }
    const float p = __builtin_ldexpf(e, edif(d, 0.f));
    unsigned pk = cvtpk(p, p);
    asm volatile("" : "+v"(pk));
    SB();
    f32x4 acc = {0.f, 0.f, 0.f, 0.f};
    float ls = bf_lo(pk);
    pv<0>(ra[4], pk, mask, acc);  asm volatile("" : "+v"(acc)); next(4); ls += dpp<XOR8>(ls); asm volatile("" : "+v"(ls)); SB();
    pv<4>(ra[5], pk, mask, acc);  asm volatile("" : "+v"(acc)); next(5); ls += dpp<ROR4>(ls); asm volatile("" : "+v"(ls)); SB();
    pv<8>(ra[6], pk, mask, acc);  asm volatile("" : "+v"(acc)); next(6); SB();
    pv<12>(ra[7], pk, mask, acc); asm volatile("" : "+v"(acc)); next(7); SB();
    A.l = __builtin_fma((double)xrow_sum(ls), s1, A.l);
    asm volatile("" : "+v"(A.l));
    A.o[0] = __builtin_fma((double)(lane < 32 ? acc[0] : acc[2]), s1, A.o[0]);
    A.o[1] = __builtin_fma((double)(lane < 32 ? acc[1] : acc[3]), s1, A.o[1]);
    asm volatile("" : "+v"(A.o[0]), "+v"(A.o[1]));
    SB();
    cur = t.unit;
    if (cur >= NUNIT) break;
    give(ctl, t, lane);
  }
}
#undef TR_STEP
__device__ __forceinline__ int rec_col(int lane) { return 8 * (lane & 15) + 4 * ((lane >> 4) & 1) + 2 * (lane >> 5); }
struct Mrg { unsigned s0, s4, s8, ta, tb; int off; };
__device__ __forceinline__ Mrg mkmrg(LAS char* sum, int lane, int roff, const Tag& tg) {
  Mrg m = {(unsigned)(uintptr_t)sum, (unsigned)(uintptr_t)sum + (lane >> 5) * 4, (unsigned)(uintptr_t)sum + lane * 8, tg.lo & 511u, tg.rb, roff + lane * 16};
  asm volatile("" : "+v"(m.s0), "+v"(m.s4), "+v"(m.s8), "+v"(m.ta), "+v"(m.tb), "+v"(m.off));
  return m;
}
__device__ __forceinline__ void b_merge(const Mrg& m, LAS char* mrg, int w, int lane, const Acc& A, rsrc_t rws, u64 e0) {
  unsigned inc = 1;
  double a = A.o[0], b = A.o[1], l = A.l;
  if (__builtin_expect(__float_as_uint(A.R) != 0u, 0)) {
    *(volatile LAS f64x2*)(mrg + w * 1024 + lane * 16) = f64x2{a, b};
    *(volatile LAS double*)(mrg + 8192 + w * 8) = l;
    *(volatile LAS float*)(mrg + 8256 + w * 4) = A.R;
    a = b = l = 0.0;
    inc = 1u + (256u << w);
  }
  f64x2 o;
  unsigned n, lw;
  asm volatile("ds_add_f64 %3, %5\n\tds_add_f64 %3, %6 offset:512\n\ts_mov_b64 exec, 1\n\tds_add_f64 %3, %7 offset:1024\n\tds_add_u32 %3, %8 offset:1032\n\t"
               "s_mov_b64 exec, -1\n\tds_read_b32 %1, %9 offset:1032\n\tds_read_b32 %2, %4 offset:1024\n\tds_read2st64_b64 %0, %3 offset1:1\n\ts_waitcnt lgkmcnt(0)"
               : "=&v"(o), "=&v"(n), "=&v"(lw)
               : "v"(m.s8), "v"(m.s4), "v"(a), "v"(b), "v"(l), "v"(inc), "v"(m.s0)
               : "memory");
  stsc(rpack0(o, lw, lane, m.ta, m.tb), rws, n == 8u ? m.off : OOB, 0);
  SB();
  n = __builtin_amdgcn_readfirstlane(n);
  if (__builtin_expect(n > 8u && (n & 255u) == 8u, 0)) {
    double L = *(volatile LAS double*)(uintptr_t)(m.s0 + 1024);
    float R = L != 0.0 ? 0.f : -INFINITY;
#pragma unroll 1
    for (int i = 0; i < 8; i++)
      if (n >> (8 + i) & 1) {
        const f64x2 oi = *(volatile LAS f64x2*)(mrg + i * 1024 + lane * 16);
        const float Ri = *(volatile LAS float*)(mrg + 8256 + i * 4), Rn = fmaxf(R, Ri);
        const double s0 = mkd(0u, p2hi(R, Rn)), s1 = mkd(0u, p2hi(Ri, Rn));
        o[0] = __builtin_fma(oi[0], s1, o[0] * s0); o[1] = __builtin_fma(oi[1], s1, o[1] * s0);
        L = __builtin_fma(*(volatile LAS double*)(mrg + 8192 + i * 8), s1, L * s0);
        R = Rn;
      }
    stsc(rpack(o[0], o[1], L, R, mktag(e0)), rws, m.off, 0);
  }
}

constexpr int cpos(char c) { for (int i = 0; i < 12; i++) if (CPAT[i] == c) return 7 + i; return 31; }
constexpr int clds(char c) {
  int r = 10;
  for (int i = 3; i < 12 && CPAT[i] != c; i++) r += CPAT[i] < 'l';
  return cpos(c) < 10 ? cpos(c) + 6 : r;
}
template <typename T> __device__ __forceinline__ constexpr T spread(T v, int b0, int b1, int b2) { return ((v & 1) << b0) | (((v >> 1) & 1) << b1) | (((v >> 2) & 1) << b2); }
__device__ __forceinline__ unsigned fz(unsigned row) { return ((row & 2) << 1) | ((row >> 1) & 2); }
__device__ __forceinline__ void dma1(rsrc_t rs, int voff, int so, unsigned ldsaddr) {
  unsigned keep;
  asm volatile("s_mov_b32 %0, m0\n\ts_mov_b32 m0, %1\n\ts_nop 0\n\tbuffer_load_dwordx4 %2, %3, %4 offen nt lds\n\ts_mov_b32 m0, %0"
               : "=&s"(keep) : "s"(ldsaddr), "v"(voff), "s"(rs), "s"(so) : "memory");
}
__device__ __forceinline__ void dma_tile(rsrc_t rs, int voff, int so, unsigned ldsaddr, int j) {
  dma1(rs, voff, so + spread(j, cpos('0'), cpos('1'), cpos('2')), ldsaddr + spread(j, clds('0'), clds('1'), clds('2')));
}
template <int NR>
__device__ __forceinline__ void dma_rows(rsrc_t rs, int srcoff, LAS char* img, int r0, int lane) {
#pragma unroll
  for (int u = 0; u < NR; u++) {
    const int r = r0 + u;
    unsigned keep;
    asm volatile("s_mov_b32 %0, m0\n\ts_mov_b32 m0, %1\n\ts_nop 0\n\tbuffer_load_dwordx4 %2, %3, %4 offen nt lds\n\ts_mov_b32 m0, %0"
                 : "=&s"(keep) : "s"((unsigned)(uintptr_t)img + r * 1024), "v"((lane ^ swz(r)) * 16), "s"(rs), "s"(srcoff + r * 1024) : "memory");
  }
  SB();
}

__device__ __forceinline__ void wo_frags(LAS char* img, int w, int lane, u32x4 (&wf)[4][4]) {
  const int g = lane >> 4, q = (lane & 15) >> 2, p = lane & 3;
#pragma unroll
  for (int ks = 0; ks < 4; ks++)
#pragma unroll
    for (int tl = 0; tl < 4; tl++) {
      const int ch = 8 * w + 2 * tl + (p >> 1);
      const int r0 = 32 * ks + 8 * g + q, r1 = r0 + 4;
      const u32x2 a = trread(img + r0 * 1024 + ((ch ^ swz(r0)) * 16) + 8 * (p & 1));
      const u32x2 b = trread(img + r1 * 1024 + ((ch ^ swz(r1)) * 16) + 8 * (p & 1));
      wf[tl][ks] = u32x4{a[0], a[1], b[0], b[1]};
    }
}
__device__ __forceinline__ f32x4 wo_calc(const u32x4 (&wf)[4][4], LAS char* cb, int lane) {
  const int n = lane & 15, grp = lane >> 4;
  u32x4 bf[4];
#pragma unroll
  for (int ks = 0; ks < 4; ks++) bf[ks] = *(LAS u32x4*)(cb + (n & 3) * 256 + ks * 64 + grp * 16);
  f32x4 acc[4];
#pragma unroll
  for (int tl = 0; tl < 4; tl++) acc[tl] = f32x4{0.f, 0.f, 0.f, 0.f};
#pragma unroll
  for (int ks = 0; ks < 4; ks++)
#pragma unroll
    for (int tl = 0; tl < 4; tl++) acc[tl] = mfma16(wf[tl][ks], bf[ks], acc[tl]);
  const f32x4 lo = (n & 4) ? acc[1] : acc[0], hi = (n & 4) ? acc[3] : acc[2];
  return (n & 8) ? hi : lo;
}
__device__ __forceinline__ void y_store(f32x4 y, int w, int lane, int slot, rsrc_t rws, int yoff, const Tag& tg) {
  const int off = yoff + (((lane & 3) * 8 + w) * 32 + slot) * 512 + ((lane & 12) + (lane >> 4)) * 16;
  stsc(gpair(y[0], y[1], tg), rws, off, 0);
  stsc(gpair(y[2], y[3], tg), rws, off, 256);
}
template <int K, int N> __device__ __forceinline__ unsigned bc1(const unsigned (&p)[N]) {
  unsigned v = dppu<0x150 + K>(p[0]);
  if constexpr (N > 1) {
    v = (unsigned)__builtin_amdgcn_update_dpp((int)v, (int)p[1], 0x150 + K, 0xf, 2, false);
    v = (unsigned)__builtin_amdgcn_update_dpp((int)v, (int)p[2], 0x150 + K, 0xf, 4, false);
  }
  return v;
}
template <int K, int N> __device__ __forceinline__ u32x4 bc4(const unsigned (&p)[N]) { return u32x4{bc1<K>(p), bc1<K + 1>(p), bc1<K + 2>(p), bc1<K + 3>(p)}; }
template <int N> __device__ __forceinline__ f32x4 proj(const unsigned (&pr)[N], const u32x4 (&wf)[4][4], int lane) {
  const u32x4 bf[4] = {bc4<0>(pr), bc4<4>(pr), bc4<8>(pr), bc4<12>(pr)};
  f32x4 acc[4];
#pragma unroll
  for (int tl = 0; tl < 4; tl++) acc[tl] = f32x4{0.f, 0.f, 0.f, 0.f};
#pragma unroll
  for (int ks = 0; ks < 4; ks++)
#pragma unroll
    for (int tl = 0; tl < 4; tl++) acc[tl] = mfma16(wf[tl][ks], bf[ks], acc[tl]);
  const f32x4 lo = (lane & 1) ? acc[1] : acc[0], hi = (lane & 1) ? acc[3] : acc[2];
  f32x4 r = (lane & 2) ? hi : lo;
  asm volatile("" : "+v"(r));
  return r;
}
__device__ __forceinline__ unsigned arrive(LAS unsigned* p, int lane) {
  unsigned z = 0;
  asm volatile("" : "+v"(z) :: "memory");
  if (lane == 0) lds_add(p + z, 1u);
  asm volatile("" ::: "memory");
  return *(volatile LAS unsigned*)p;
}
template <int N> __device__ __forceinline__ f32x2 rows_sum(LAS char* p) {
  f32x2 v[N];
#pragma unroll
  for (int i = 0; i < N; i++) v[i] = *(volatile LAS f32x2*)(p + i * 256);
#pragma unroll
  for (int st = 1; st < N; st *= 2)
#pragma unroll
    for (int i = 0; i + st < N; i += 2 * st) v[i] += v[i + st];
  return v[0];
}
__device__ __forceinline__ float half_sum(float x) {
  auto t = __builtin_amdgcn_permlane32_swap(__float_as_uint(x), __float_as_uint(x), false, false);
  return __uint_as_float(t[0]) + __uint_as_float(t[1]);
}

__global__ __launch_bounds__(512, 2) void k_all(const char* a0_, const char* a1_, const char* a2_, const char* a3_, const char* a4_, const char* a5_,
                                                const char* a6_, const char* a7_, const char* a8_, const char* a9_, const char* a10_, char* a11_, unsigned* a12_) {
  u32x16 ka; u32x8 kb_; u32x2 kc_;
  asm volatile("s_load_dwordx16 %0, %3, 0x0\n\ts_load_dwordx8 %1, %3, 0x40\n\ts_load_dwordx2 %2, %3, 0x60"
               : "=&s"(ka), "=&s"(kb_), "=&s"(kc_) : "s"(__builtin_amdgcn_kernarg_segment_ptr()));
  __shared__ __attribute__((aligned(16))) char smem[LDS_BYTES];
  LAS char* lds = (LAS char*)smem;
  LAS char* scr = lds + LDS_SCR;
  LAS unsigned* ctl = (LAS unsigned*)scr;
  const int tid = threadIdx.x, lane = tid & 63, w = __builtin_amdgcn_readfirstlane(tid >> 6);
  const int blk = blockIdx.x, head = blk >> 3, prod = (blk + head) & 7;
  const int sub = (0x54372106u >> (4 * prod)) & 7;
  unsigned cpf = 0;
  {
    u64 pc;
    asm volatile("s_getpc_b64 %0" : "=s"(pc));
    cpf = __builtin_amdgcn_raw_buffer_load_b32(mkrs((const GAS char*)(pc & ~255ull), CPF), w == 7 ? head * 2048 + lane * 32 : OOB, 0, 0);
    SB();
  }
  {
    unsigned c = 48 - 8 * CH + CH * (DCH * sub + tid);
    asm volatile("" : "+v"(c));
    c = tid < 8 + DCH ? c : ~0u;
    c = tid < 8 ? 0u : c;
    ctl[tid] = tid == 4 ? DCH : c;
    *(LAS u64*)(scr + SCR_SUM + min(tid, 2 * SUM_STR / 8 - 1) * 8) = 0;
  }
  const int l16 = lane & 15, grp = lane >> 4;
  const int b4 = head & 3, cntoff = WS_CNT + head * CSTR;
  const int bm = (b4 + 1 + (sub >= 3 ? sub - 3 : sub)) & 3;
  const size_t base = (size_t)(bm * 32 + head) << 20;
  const int so = ((w & 1) << bitpos('a')) | (((w >> 1) & 1) << bitpos('b')) | ((w >> 2) << bitpos('c')) | (sub >= 3 ? 1 << bitpos('p') : 0);
  const int vg = l16 * 16 + ((grp & 1) << bitpos('x')) + ((grp >> 1) << bitpos('y'));
  int v0 = sub < 6 ? vg + so : l16 * 16 + grp * 131072 + w * 524288 + head * 256;
  unsigned boff = sub < 6 ? (unsigned)base : 0u, bsz = sub < 6 ? 1u << 20 : 4u << 20;
  int s0[WTH];
#pragma unroll
  for (int i = 0; i < WTH; i++) {
    s0[i] = sub < 6 ? goff(i) : i * 8192;
    asm volatile("" : "+s"(s0[i]));
  }
  asm volatile("" : "+v"(v0), "+s"(boff), "+s"(bsz));
  SB();
  asm volatile("s_waitcnt lgkmcnt(0)" : "+s"(ka), "+s"(kb_), "+s"(kc_));
  auto ptr = [](unsigned lo, unsigned hi) { return (GAS char*)(((u64)hi << 32) | lo); };
  GAS char *const xA = ptr(ka[0], ka[1]), *const ckv = ptr(ka[2], ka[3]), *const wqA = ptr(ka[4], ka[5]), *const wuk = ptr(ka[6], ka[7]),
           *const wuv = ptr(ka[8], ka[9]), *const woA = ptr(ka[10], ka[11]), *const xB = ptr(ka[12], ka[13]), *const kc = ptr(ka[14], ka[15]),
           *const vc = ptr(kb_[0], kb_[1]), *const wqB = ptr(kb_[2], kb_[3]), *const woB = ptr(kb_[4], kb_[5]), *const ws = ptr(kb_[6], kb_[7]),
           *const out = ptr(kc_[0], kc_[1]);
  const u64 e0 = *(const __attribute__((address_space(4))) u64*)(u64)(ws + WS_EPOCH);
  const rsrc_t r0 = mkrs((sub < 6 ? kc : sub == 6 ? wqB : wqA) + boff, bsz);
  SB();
  u32x4 ra[16];
#pragma unroll
  for (int i = 0; i < WTH; i++) ra[i] = ldnt(r0, v0, s0[i]);
  SB();
  u32x4 xp[2][2];
  if (sub >= 6) {
    const GAS char* x = sub == 6 ? xB : xA;
#pragma unroll
    for (int s = 0; s < 2; s++) {
      const GAS u32x4* xs = (const GAS u32x4*)(x + (2 * s + (l16 >> 3)) * 1024 + (64 * w + 16 * grp) * 2);
      xp[s][0] = xs[0]; xp[s][1] = xs[1];
    }
#pragma unroll
    for (int i = WTH; i < 16; i++) ra[i] = ldnt(r0, v0, i * 8192);
    asm volatile("" ::: "memory");
    SB();
  }
  const rsrc_t rws = mkrs(ws, WS_END);
  unsigned mask[4];
#pragma unroll
  for (int v = 0; v < 4; v++) mask[v] = (((lane & 7) >> 1) == v) ? ((lane & 1) ? 0xffff0000u : 0x0000ffffu) : 0u;
  const size_t base4 = (size_t)(b4 * 32 + head) << 20;
  const rsrc_t rk4 = mkrs(kc + base4, 1u << 20), rv4 = mkrs(vc + base4, 1u << 20);
  const int voff = lane * 16, vu = l16 * 16 + ((grp & 1) << upos('x')) + ((grp >> 1) << upos('y'));
  unsigned ub = 0;
#pragma unroll
  for (int i = 0; i < 4; i++) ub |= ((lane >> i) & 1) << ((lane & 16) ? upos('4' + i) : upos('0' + i));
  LAS char* qst = scr + 1024 + w * 512;
  const int qo = WS_QB + head * 2048 + (lane < 32 ? bm : b4) * 512 + (lane & 31) * 16;
  auto qwait = [&](u32x4 g, const Tag& tg) {
    if (!__all(qok(g, tg))) {
      for (unsigned spins = 0;; spins++) {
        asm volatile("" ::: "memory");
        g = ldsc(rws, qo, 0);
        if (__all(qok(g, tg)) || spins > SPIN_CAP) break;
        __builtin_amdgcn_s_sleep(2);
      }
    }
    *(LAS u32x2*)(qst + lane * 8) = u32x2{g[0], g[2]};
  };
  unsigned cur;

  if (__builtin_expect(sub < 6, 1)) {
    const rsrc_t rv = mkrs(vc + base, 1u << 20);
    u32x4 rb[16], g;
#pragma unroll
    for (int i = WTH; i < 32 + NXA + NXB; i++) {
      if (i == WTH) asm volatile("s_barrier" ::: "memory");
      vmwait<WTH - 1>();
      if (i == 32 + NXA + NXB - WTH) asm volatile("buffer_load_dwordx4 %0, %1, %2, 0 offen sc1" : "=&v"(g) : "v"(qo), "s"(rws));
      if (i < 32) {
        const u32x4 v = ldslot(r0, vg, so, i);
        if (i < 16) ra[i] = v; else rb[i - 16] = v;
      } else {
        const int j = i < 32 + NXA ? i + 16 - NXA : i + 32 - NXA - NXB;
        dma1(r0, vg + goff(j & 3), so ^ goff(j & ~3), (unsigned)(uintptr_t)lds + w * 16384 + (i - 32) * 1024);
      }
      SB();
    }
    asm volatile("s_waitcnt vmcnt(%1)" : "+v"(g) : "n"(WTH), "v"(cpf));
    qwait(g, mktag(e0));
    const u32x4 q = *(LAS u32x4*)(qst + l16 * 16);
    float sc[4] = {0.f, 0.f, 0.f, 0.f};
#pragma unroll 1
    for (int it = 0; it < 2; it++) {
      const rsrc_t rn = it ? rv : r0;
      sc[2] = sc[0]; sc[3] = sc[1];
      sc[1] = k_step<NXA>(ra, q, l16, rn, vg, so ^ (it ? goff(48) : goff(32)), it == 0, lds + w * 16384 + voff);
      sc[0] = k_step<NXB>(rb, q, l16, rn, vg, so ^ (it ? goff(32) : goff(48)), it == 0, lds + w * 16384 + NXA * 1024 + voff);
    }
    const float mx = xrow_max(row_max(fmaxf(fmaxf(sc[0], sc[1]), fmaxf(sc[2], sc[3]))));
    float f, l = 0.f;
    const float M = exp_ref(mx, f);
    unsigned pk[4];
#pragma unroll
    for (int k = 0; k < 4; k++) {
      const float p = fexp2(fmaf(sc[k] - mx, LOG2E, f));
      pk[k] = cvtpk(p, p);
      l += bf_lo(pk[k]);
    }
    l = xrow_sum(row_sum(l));
    cur = 8 * sub + w;
    f32x4 acc = {0.f, 0.f, 0.f, 0.f};
    v_step<0, 1>(ra, pk[0], mask, acc, r0, rv, vg, so ^ goff(16));
    v_step<0, 1>(rb, pk[1], mask, acc, r0, rv, vg, so);
    v_step<0, 2>(ra, pk[2], mask, acc, rk4, rv4, vu, unit_off(cur, ub));
    v_step<0, 0>(rb, pk[3], mask, acc, r0, rv, voff, 0);
    Acc A;
    A.R = rfl(bucket(M));
    const double sm = mkd(0u, p2hi(M, A.R));
    A.o[0] = (lane < 32 ? acc[0] : acc[2]) * sm; A.o[1] = (lane < 32 ? acc[1] : acc[3]) * sm;
    A.l = l * sm;
    b_merge(mkmrg(scr + SCR_SUM, lane, WS_RS + (head * 16 + sub) * 1024, mktag(e0)), scr + 8192, w, lane, A, rws, e0);
  } else {
  {
    __builtin_amdgcn_raw_buffer_store_b32(48 + 6 * CH * DCH, rws, (sub == 6 && tid == 0) ? cntoff : OOB, 0, AUX_SC1);
    {
      const rsrc_t rk = mkrs(wuk, sub == 7 ? 4u << 20 : 0u);
#pragma unroll
      for (int i = 0; i < 16; i++)
        __builtin_amdgcn_raw_ptr_buffer_load_lds(rk, (LAS void*)(lds + w * 16384 + i * 1024), 16,
                                                 (64 * w + grp) * 8192 + head * 256 + ((l16 ^ (4 * (i & 3) + grp)) * 16), i * 4 * 8192, 0, AUX_NT);
    }
    SB();
    f32x4 acc[2] = {{0.f, 0.f, 0.f, 0.f}, {0.f, 0.f, 0.f, 0.f}};
    diag_gemv(ra, xp, mask, acc);
    LAS float* red = (LAS float*)(scr + 8192);
#pragma unroll
    for (int s = 0; s < 2; s++) *(LAS f32x4*)(red + w * 512 + (2 * s + (grp >> 1)) * 128 + 8 * l16 + 4 * (grp & 1)) = acc[s];
    asm volatile("" :: "v"(cpf));
    __builtin_amdgcn_s_waitcnt(0x0f70);
    drain();
    bar();
    if (tid < 128) {
      f32x4 a = {0.f, 0.f, 0.f, 0.f};
#pragma unroll
      for (int k = 0; k < 8; k++) a += *(LAS f32x4*)(red + k * 512 + 4 * tid);
      const unsigned p0 = cvtpk(a[0], a[1]), p1 = cvtpk(a[2], a[3]);
      const Tag tg = mktag(e0);
      stsc(u32x4{p0, tg.th, p1, tg.th}, rws, sub == 6 ? WS_QB + head * 2048 + tid * 16 : OOB, 0);
      *(LAS u32x2*)(scr + 1024 + tid * 8) = u32x2{p0, p1};
    }
  }

  if (sub == 6) {
    const int aid = ((head & 6) << 2) | ((head & 1) << 2) | (head >> 3), b = aid >> 3;
    const rsrc_t rc = mkrs(ckv + (size_t)aid * 524288, 524288);
    const int dvo = spread(lane >> 3, cpos('l'), cpos('m'), cpos('n')) + spread(w, cpos('a'), cpos('b'), cpos('c')) + (((lane & 7) ^ fz(lane >> 3)) * 16);
    const unsigned dlo = (unsigned)(uintptr_t)lds + spread(w, clds('a'), clds('b'), clds('c'));
    auto toff = [&](int t) { return spread(t ^ aid, cpos('t'), cpos('u'), cpos('v')); };
#pragma unroll
    for (int j = 0; j < 16; j++) dma_tile(rc, dvo, toff(j >> 3), dlo + (j >> 3) * 65536, j & 7);
    SB();
    const Tag tg = mktag(e0);
    bar();
    {
      u32x4 t[8];
      unsigned miss = 15;
      __builtin_amdgcn_s_sleep(90);
      for (unsigned spins = 0;; spins++) {
        asm volatile("" ::: "memory");
#pragma unroll
        for (int i = 0; i < 4; i++)
          if (miss >> i & 1) {
            t[2 * i] = ldsc(rws, WS_QABS + (b * 32 + w) * 2048 + lane * 16, i * 16384);
            t[2 * i + 1] = ldsc(rws, WS_QABS + (b * 32 + w) * 2048 + lane * 16 + 1024, i * 16384);
          }
        SB();
#pragma unroll
        for (int i = 0; i < 4; i++) if (__all(qok(t[2 * i], tg) & qok(t[2 * i + 1], tg))) miss &= ~(1u << i);
        if (!miss || spins > SPIN_CAP) break;
        __builtin_amdgcn_s_sleep(4);
      }
#pragma unroll
      for (int i = 0; i < 8; i++) {
        const int row = 8 * (i >> 1) + w;
        *(LAS u32x2*)(scr + row * 1024 + (((32 * (i & 1) + (lane >> 1)) ^ swz(row)) * 16) + (lane & 1) * 8) = u32x2{t[i][0], t[i][2]};
      }
    }
    bar();
    const int kb = w & 3, hb = w >> 2;
    u32x4 qf[16];
    {
      const int row = 16 * hb + l16;
#pragma unroll
      for (int ks = 0; ks < 16; ks++) qf[ks] = *(LAS u32x4*)(scr + row * 1024 + (((4 * ks + grp) ^ swz(row)) * 16));
    }
    LAS float* mx = (LAS float*)scr;
    LAS char* pl = scr + 1024;
    LAS float* lr = (LAS float*)(scr + 8192);
    f32x16 o0, o1;
#pragma unroll
    for (int j = 0; j < 16; j++) { o0[j] = 0.f; o1[j] = 0.f; }
    float m1 = -INFINITY, m2 = -INFINITY, l1 = 0.f;
    const int h1 = 16 * hb + l16, h2 = lane & 31, hh = lane >> 5;
    const int arow = 16 * kb + l16;
    const int tq = (lane & 15) >> 2, tp = lane & 3, tblk = (lane >> 4) & 1;
    const int qa0 = arow * 128 + ((grp ^ fz(arow)) * 16), qa1 = qa0 ^ 64;
    const int pr = 8 * hh + tq, pc = 2 * tblk + (tp >> 1);
    const int pa0 = w * 8192 + pr * 128 + ((pc ^ fz(pr)) * 16) + 8 * (tp & 1), pa1 = w * 8192 + (pr + 4) * 128 + ((pc ^ fz(pr + 4)) * 16) + 8 * (tp & 1);
#pragma unroll 1
    for (int t = 0; t < 8; t++) {
      if (t < 7) asm volatile("s_waitcnt vmcnt(8)" ::: "memory"); else asm volatile("s_waitcnt vmcnt(0)" ::: "memory");
      bar();
      LAS char* img = lds + (t & 1) * 65536;
      f32x4 s0 = {0.f, 0.f, 0.f, 0.f}, s1 = s0;
      u32x4 ka[16];
      u32x2 ta[4][4];
      const int nso = toff(t + 2);
#pragma unroll
      for (int ks = 0; ks < 16; ks++) ka[ks] = *(LAS u32x4*)(img + ((ks & 1) ? qa1 : qa0) + (ks >> 1) * 8192);
      SB();
#pragma unroll
      for (int k2 = 0; k2 < 8; k2++) {
        s0 = mfma16(ka[2 * k2], qf[2 * k2], s0);
        s1 = mfma16(ka[2 * k2 + 1], qf[2 * k2 + 1], s1);
      }
      asm volatile("" : "+v"(s0), "+v"(s1));
      SB();
#pragma unroll
      for (int ks = 0; ks < 4; ks++) {
        ta[ks][0] = trread(img + pa0 + ks * 2048); ta[ks][1] = trread(img + pa1 + ks * 2048);
        ta[ks][2] = trread(img + (pa0 ^ 64) + ks * 2048); ta[ks][3] = trread(img + (pa1 ^ 64) + ks * 2048);
      }
      SB();
      float x[4];
#pragma unroll
      for (int j = 0; j < 4; j++) x[j] = rbf(rbf(s0[j] + s1[j]) * SCALE);
      const float lm = xrow_max(fmaxf(fmaxf(x[0], x[1]), fmaxf(x[2], x[3])));
      if (grp == 0) mx[h1 * 4 + kb] = lm;
      LAS u32x2* pw = (LAS u32x2*)(pl + h1 * 144 + (16 * kb + 4 * grp) * 2);
      u32x2 pp = {cvtpk(fexp2((x[0] - m1) * LOG2E), fexp2((x[1] - m1) * LOG2E)), cvtpk(fexp2((x[2] - m1) * LOG2E), fexp2((x[3] - m1) * LOG2E))};
      *pw = pp;
      bar();
      const f32x4 ma = *(LAS f32x4*)(mx + h1 * 4), mb = *(LAS f32x4*)(mx + h2 * 4);
      u32x4 pb[4];
#pragma unroll
      for (int ks = 0; ks < 4; ks++) pb[ks] = *(LAS u32x4*)(pl + h2 * 144 + (16 * ks + 8 * hh) * 2);
      const float n1 = fmaxf(m1, fmaxf(fmaxf(ma[0], ma[1]), fmaxf(ma[2], ma[3])));
      const float n2 = fmaxf(m2, fmaxf(fmaxf(mb[0], mb[1]), fmaxf(mb[2], mb[3])));
      float al1 = fexp2((m1 - n1) * LOG2E), al2 = fexp2((m2 - n2) * LOG2E);
      asm volatile("" : "+v"(pb[0]), "+v"(pb[1]), "+v"(pb[2]), "+v"(pb[3]));
      if (__any(n2 - m2 > 40.f)) {
        bar();
        pp = u32x2{cvtpk(fexp2((x[0] - n1) * LOG2E), fexp2((x[1] - n1) * LOG2E)), cvtpk(fexp2((x[2] - n1) * LOG2E), fexp2((x[3] - n1) * LOG2E))};
        *pw = pp;
        l1 *= al1;
#pragma unroll
        for (int j = 0; j < 16; j++) { o0[j] *= al2; o1[j] *= al2; }
        al1 = al2 = 1.f;
        bar();
#pragma unroll
        for (int ks = 0; ks < 4; ks++) pb[ks] = *(LAS u32x4*)(pl + h2 * 144 + (16 * ks + 8 * hh) * 2);
      }
      m1 = n1; m2 = n2;
#pragma unroll
      for (int ks = 0; ks < 4; ks++) {
        o0 = mfma32(u32x4{ta[ks][0][0], ta[ks][0][1], ta[ks][1][0], ta[ks][1][1]}, pb[ks], o0);
        o1 = mfma32(u32x4{ta[ks][2][0], ta[ks][2][1], ta[ks][3][0], ta[ks][3][1]}, pb[ks], o1);
        SB();
        if (t < 6) {
          dma_tile(rc, dvo, nso, dlo + (t & 1) * 65536, 2 * ks);
          dma_tile(rc, dvo, nso, dlo + (t & 1) * 65536, 2 * ks + 1);
        }
        SB();
      }
      l1 = (l1 + ((bf_lo(pp[0]) + bf_hi(pp[0])) + (bf_lo(pp[1]) + bf_hi(pp[1])))) * al1;
#pragma unroll
      for (int j = 0; j < 16; j++) { o0[j] *= al2; o1[j] *= al2; }
    }
    bar();
    {
      float am = 0.f;
#pragma unroll
      for (int j = 0; j < 16; j++) am = fmaxf(am, fmaxf(fabsf(o0[j]), fabsf(o1[j])));
      auto sw = __builtin_amdgcn_permlane32_swap(__float_as_uint(am), __float_as_uint(am), false, false);
      am = fmaxf(__uint_as_float(sw[0]), __uint_as_float(sw[1]));
      const int e = max(__builtin_amdgcn_frexp_expf(am), -127);
      LAS char* tp = lds + h2 * 1040 + (64 * w + 4 * hh) * 2;
      auto pkn = [&](float a, float b) { return __builtin_bit_cast(unsigned, __builtin_amdgcn_cvt_pknorm_i16(__builtin_ldexpf(a, -e), __builtin_ldexpf(b, -e))); };
#pragma unroll
      for (int j4 = 0; j4 < 4; j4++) {
        *(LAS u32x2*)(tp + j4 * 16) = u32x2{pkn(o0[4 * j4], o0[4 * j4 + 1]), pkn(o0[4 * j4 + 2], o0[4 * j4 + 3])};
        *(LAS u32x2*)(tp + 64 + j4 * 16) = u32x2{pkn(o1[4 * j4], o1[4 * j4 + 1]), pkn(o1[4 * j4 + 2], o1[4 * j4 + 3])};
      }
      if (hh == 0) *(LAS unsigned char*)(scr + 9216 + h2 * 8 + w) = (unsigned char)(e + 127);
      const float ls = xrow_sum(l1);
      if (grp == 0) lr[kb * 32 + h1] = ls;
      bar();
#pragma unroll
      for (int i = 0; i < 4; i++) {
        u32x4 v = *(LAS u32x4*)(lds + (4 * w + i) * 1040 + lane * 16);
#pragma unroll
        for (int k = 0; k < 4; k++) v[k] = bfi(PMASK, ptag(tg, k), v[k]);
        stsc(v, rws, WS_PO + (aid * 32 + 4 * w) * 1024 + lane * 16, i * 1024);
      }
      if (tid < 32) {
        const u32x2 eb = *(LAS u32x2*)(scr + 9216 + tid * 8);
        const float l = (lr[tid] + lr[32 + tid]) + (lr[64 + tid] + lr[96 + tid]);
        stsc(u32x4{bfi(0xffffu, tg.lo, __float_as_uint(m2)), bfi(255u, ltag(tg), __float_as_uint(l)), eb[0], eb[1]}, rws, WS_PML + (aid * 32 + tid) * 16, 0);
      }
      if (w == 0) {
        const unsigned t = __builtin_amdgcn_readfirstlane(__builtin_amdgcn_raw_ptr_buffer_atomic_add_i32(8 + CH * DCH, rws, lane == 0 ? cntoff : OOB, 0, AUX_SC1));
        const unsigned bs = t + 8 + CH * (lane - 8);
        ctl[lane] = lane == 3 ? t : lane == 4 ? DCH : lane == 5 ? (t + 8 + CH * DCH >= NUNIT ? 0x10000u : 0u) : lane < 8 ? 0u : (lane < 8 + DCH && bs < NUNIT) ? bs : ~0u;
      } else if (w < 4) ctl[tid] = ~0u;
      *(LAS u64*)(scr + SCR_SUM + SUM_STR + min(tid, SUM_STR / 8 - 1) * 8) = 0;
      bar();
    }
    const u32x4 g = ldsc(rws, qo, 0);
    cur = __builtin_amdgcn_readfirstlane(ctl[3]) + w;
#pragma unroll
    for (int i = 0; i < 8; i++) ra[i] = ldunit(i < 4 ? rk4 : rv4, vu, unit_off(cur, ub), i & 3);
    SB();
    qwait(g, mktag(e0));
  } else {
    const Tag tg = mktag(e0);
    bar();
    {
      u32x4 bf[4];
#pragma unroll
      for (int ks = 0; ks < 4; ks++) bf[ks] = *(LAS u32x4*)(scr + 1024 + (l16 & 3) * 256 + ks * 64 + grp * 16);
      LAS char* tt = scr + 2048 + w * 512;
#pragma unroll
      for (int rt = 0; rt < 4; rt++) {
        f32x4 acc = {0.f, 0.f, 0.f, 0.f};
#pragma unroll
        for (int ks = 0; ks < 4; ks++) acc = mfma16(*(LAS u32x4*)(lds + (64 * w + 16 * rt + l16) * 256 + (((4 * ks + grp) ^ l16) * 16)), bf[ks], acc);
        if (l16 < 4) *(LAS u32x2*)(tt + l16 * 128 + (16 * rt + 4 * grp) * 2) = u32x2{cvtpk(acc[0], acc[1]), cvtpk(acc[2], acc[3])};
      }
      asm volatile("s_waitcnt lgkmcnt(0)" ::: "memory");
      const u32x2 v = *(LAS u32x2*)(tt + lane * 8);
      stsc(u32x4{v[0], tg.th, v[1], tg.th}, rws, WS_QABS + (grp * 32 + head) * 2048 + w * 256 + l16 * 16, 0);
    }
    bar();
    u32x4 wf[4][4], wr[16];
    {
      const rsrc_t ro = mkrs(woA + (size_t)head * 131072, 131072);
      dma_rows<16>(ro, 0, lds, 16 * w, lane);
      const rsrc_t rv = mkrs(wuv, 4u << 20);
      const int voff = (64 * w + 16 * grp) * 8192 + head * 256 + l16 * 16;
#pragma unroll
      for (int i = 0; i < 16; i++) wr[i] = ldnt(rv, voff, i * 8192);
      SB();
      asm volatile("s_waitcnt vmcnt(16)" ::: "memory");
      bar();
      wo_frags(lds, w, lane, wf);
      bar();
      const rsrc_t rb = mkrs(woB, 4u << 20);
      const int bvo = (lane >> 3) * 1024 + (((lane & 7) ^ ((lane >> 5) << 1)) * 16);
#pragma unroll
      for (int i = 0; i < 16; i++)
        dma1(rb, (i & 1) ? bvo ^ 64 : bvo, (4 * w + b4) * 131072 + (head >> 2) * 128 + i * 8192, (unsigned)(uintptr_t)lds + w * 16384 + i * 1024);
      SB();
      if (tid < 8) ((LAS unsigned*)(scr + 24576))[tid] = 0u;
    }
    const int hw = 4 * w + b4, cc = head >> 2, roff = WS_RS + hw * 16384 + lane * 16;
    u32x4 g[6];
#pragma unroll
    for (int s = 0; s < 6; s++) asm volatile("" : "=v"(g[s]));
    if (w == 0) {
      for (unsigned spins = 0;; spins++) {
        asm volatile("" ::: "memory");
        const u32x4 v = ldsc(rws, WS_PML + ((lane & 31) * 32 + head) * 16, 0);
        if (__builtin_popcount((unsigned)__builtin_amdgcn_ballot_w64(mok(v, tg))) >= NREL || spins > SPIN_CAP) break;
        __builtin_amdgcn_s_sleep(16);
      }
    }
    bar();
    {
      const int b = w >> 1, c4 = tid & 127;
      const unsigned ta = (lane & 1) ? ptag(tg, 2) : ptag(tg, 0), tb = (lane & 1) ? ptag(tg, 3) : ptag(tg, 1);
      u32x4 ml;
      u32x2 po[8];
#pragma unroll
      for (int s = 0; s < 8; s++) asm volatile("" : "=v"(po[s]));
      unsigned missP = 255;
      for (unsigned spins = 0;; spins++) {
        asm volatile("" ::: "memory");
        ml = ldsc(rws, WS_PML + ((8 * b + (lane & 7)) * 32 + head) * 16, 0);
#pragma unroll
        for (int s = 0; s < 8; s++)
          if (missP >> s & 1) po[s] = __builtin_amdgcn_raw_buffer_load_b64(rws, WS_PO + (8 * b * 32 + head) * 1024 + c4 * 8, s * 32768, AUX_SC1);
        SB();
#pragma unroll
        for (int s = 0; s < 8; s++) if (__all((((po[s][0] ^ ta) | (po[s][1] ^ tb)) & PMASK) == 0u)) missP &= ~(1u << s);
        if ((!missP && __all(mok(ml, tg))) || spins > SPIN_CAP) break;
        __builtin_amdgcn_s_sleep(2);
      }
#pragma unroll
      for (int s = 0; s < 6; s++) g[s] = ldsc(rws, roff, s * 1024);
      SB();
      ml[0] &= 0xffff0000u; ml[1] &= ~255u;
#pragma unroll
      for (int s = 0; s < 8; s++) po[s] = u32x2{bfi(PMASK, 0x00020002u, po[s][0]), bfi(PMASK, 0x00020002u, po[s][1])};
      float M = __uint_as_float(ml[0]);
      M = fmaxf(M, dpp<XOR1>(M)); M = fmaxf(M, dpp<XOR2>(M)); M = fmaxf(M, dpp<XOR7>(M));
      const float f = fexp2((__uint_as_float(ml[0]) - M) * LOG2E);
      float L = f * __uint_as_float(ml[1]);
      L += dpp<XOR1>(L); L += dpp<XOR2>(L); L += dpp<XOR7>(L);
      const float gl = __builtin_ldexpf(f, (int)((((w & 1) ? ml[3] : ml[2]) >> (8 * grp)) & 255) - 142);
      const float gs[8] = {dpp<0x150>(gl), dpp<0x151>(gl), dpp<0x152>(gl), dpp<0x153>(gl), dpp<0x154>(gl), dpp<0x155>(gl), dpp<0x156>(gl), dpp<0x157>(gl)};
      f32x4 a = {0.f, 0.f, 0.f, 0.f};
#pragma unroll
      for (int s = 0; s < 8; s++)
        a += gs[s] * f32x4{(float)(short)po[s][0], (float)((int)po[s][0] >> 16), (float)(short)po[s][1], (float)((int)po[s][1] >> 16)};
      const float inv = (32768.f / 32767.f) / L;
      const u32x2 v = {cvtpk(a[0] * inv, a[1] * inv), cvtpk(a[2] * inv, a[3] * inv)};
      *(LAS u32x2*)(scr + b * 1024 + c4 * 8) = v;
    }
    bar();
    {
      u32x4 xp[2][2];
#pragma unroll
      for (int s = 0; s < 2; s++) {
        LAS u32x4* xs = (LAS u32x4*)(scr + (2 * s + (l16 >> 3)) * 1024 + (64 * w + 16 * grp) * 2);
        xp[s][0] = xs[0]; xp[s][1] = xs[1];
      }
      f32x4 acc[2] = {{0.f, 0.f, 0.f, 0.f}, {0.f, 0.f, 0.f, 0.f}};
      diag_gemv(wr, xp, mask, acc);
      LAS float* red = (LAS float*)(scr + 8192);
#pragma unroll
      for (int s = 0; s < 2; s++) *(LAS f32x4*)(red + w * 512 + (2 * s + (grp >> 1)) * 128 + 8 * l16 + 4 * (grp & 1)) = acc[s];
      bar();
      if (tid < 256) {
        float a = 0.f, b = 0.f;
#pragma unroll
        for (int k = 0; k < 8; k++) { const f32x2 v = *(LAS f32x2*)(red + k * 512 + 2 * tid); a += v[0]; b += v[1]; }
        ((LAS unsigned*)(scr + 4096))[tid] = cvtpk(a, b);
      }
      bar();
    }
    const f32x4 yA = wo_calc(wf, scr + 4096, lane);
    drain();
    y_store(yA, w, lane, head, rws, WS_Y, tg);
    {
      const int q = l16 >> 2;
      LAS char* img = lds + w * 16384 + (8 * (q >> 1) + 4 * (grp & 1) + 2 * (grp >> 1) + (q & 1)) * 128 + 8 * (lane & 3);
      const int fl = (grp & 1) | (q & 2);
#pragma unroll
      for (int ks = 0; ks < 4; ks++)
#pragma unroll
        for (int tl = 0; tl < 4; tl++) {
          const u32x2 a = trread(img + (tl ^ fl) * 32 + ks * 4096), b = trread(img + (tl ^ fl) * 32 + ks * 4096 + 2048);
          wf[tl][ks] = u32x4{a[0], a[1], b[0], b[1]};
        }
    }
    const rsrc_t rout = mkrs(out, 8192);
    LAS unsigned* cnt = (LAS unsigned*)(scr + 24576);
    LAS char* const re = scr + 8192;
    LAS char* const rA = scr + 8192 + 8192;
    LAS char* rl = scr + 8192 + 10240 + (lane & 31) * 8;
    LAS char* rw = scr + 8192 + 10240 + w * 256 + (lane & 3) * 64 + grp * 16;
    LAS char* rx = rl + (w < 3 ? 8 + w : 11) * 256;
    int oo = lane < 32 ? b4 * 2048 + 1024 + cc * 128 + lane * 4 : OOB, oe = head == 0 && lane == 0 ? WS_EPOCH : OOB;
    asm volatile("" : "+v"(rl), "+v"(rw), "+v"(rx), "+v"(oo), "+v"(oe), "+v"(cnt));
    {
      const int ofa = WS_Y + (b4 * 8 + cc) * 16384 + w * 2048 + lane * 16;
      u32x4 x0, x1;
      for (unsigned spins = 0;; spins++) {
        asm volatile("" ::: "memory");
        x0 = ldsc(rws, ofa, 0); x1 = ldsc(rws, ofa + 1024, 0);
        if (__all(gok(x0, tg) & gok(x1, tg)) || spins > SPIN_CAP) break;
        __builtin_amdgcn_s_sleep(4);
      }
      const f32x2 r = {half_sum(gval(x0[0]) + gval(x1[0])), half_sum(gval(x0[2]) + gval(x1[2]))};
      if (lane < 32) *(volatile LAS f32x2*)(rA + w * 256 + lane * 8) = r;
      const unsigned n = arrive(cnt + 1, lane);
      const f32x2 v = rows_sum<8>(rA + (lane & 31) * 8);
      __builtin_amdgcn_raw_buffer_store_b32(cvtpk(v[0], v[1]), rout, lane < 32 && n == 8u ? b4 * 2048 + cc * 128 + l16 * 8 + grp * 4 : OOB, 0, AUX_SC1);
    }
    {
      for (unsigned spins = 0;; spins++) {
        unsigned bad = 0;
#pragma unroll
        for (int s = 0; s < 6; s++) bad |= (g[s][0] ^ tg.lo) | (g[s][2] ^ tg.rb);
        if (__all((bad & (spins > SPIN_CAP * 4 ? 0u : 511u)) == 0u)) break;
        asm volatile("" ::: "memory");
#pragma unroll
        for (int s = 0; s < 6; s++) g[s] = ldsc(rws, roff + s * 1024, 0);
        SB();
      }
      unsigned pr[3];
#pragma unroll
      for (int t = 0; t < 3; t++) {
        const u32x4 g0 = g[t], g1 = g[t + 3];
        const float R0 = rec_r(g0), R1 = rec_r(g1), Rm = fmaxf(R0, R1);
        const double s0 = mkd(0u, p2hi(R0, Rm)), s1 = mkd(0u, p2hi(R1, Rm));
        const double inv = __builtin_amdgcn_rcp(__builtin_fma(rec_l(g0), s0, rec_l(g1) * s1));
        pr[t] = cvtpk((float)(__builtin_fma(rec_a(g0), s0, rec_a(g1) * s1) * inv), (float)(__builtin_fma(rec_b(g0), s0, rec_b(g1) * s1) * inv));
      }
      *(volatile LAS f32x4*)(re + ((l16 >> 2) * 8 + w) * 256 + (lane & 3) * 64 + grp * 16) = proj(pr, wf, lane);
      const unsigned n = arrive(cnt, lane);
#pragma unroll
      for (int u = 0; u < 2; u++) {
        const int t = u ? 2 : lane >> 5;
        const f32x2 v = rows_sum<8>(re + t * 2048 + (lane & 31) * 8);
        stsc(gpair(v[0], v[1], tg), rws, n != 8u || (u && lane >= 32) ? OOB : WS_YB + ((((b4 + 1 + t) & 3) * 8 + cc) * 4 + b4) * 512 + (lane & 31) * 16, 0);
      }
    }
    const int ofb = w < 3 ? WS_YB + ((b4 * 8 + cc) * 4 + ((b4 + 1 + w) & 3)) * 512 + (lane & 31) * 16 : OOB;
    u32x4 xb, p[NSLOT];
    unsigned bad;
    for (unsigned spins = 0;; spins++) {
      asm volatile("" ::: "memory");
      xb = ldsc(rws, ofb, 0);
#pragma unroll
      for (int s = 0; s < NSLOT; s++) p[s] = ldsc(rws, roff + 8192 + s * 1024, 0);
      unsigned m = spins > SPIN_CAP * 4 ? 0u : 511u;
      asm volatile("" : "+s"(m));
      SB();
      unsigned ax = min((((xb[0] ^ tg.tl) | (xb[2] ^ tg.tl)) >> 24) | (xb[1] ^ tg.th) | (xb[3] ^ tg.th), w < 3 ? 1u : 0u), az = 0;
#pragma unroll
      for (int s = 0; s < NSLOT; s++) { ax |= p[s][0] ^ tg.lo; az |= p[s][2] ^ tg.rb; }
      bad = az & 512u;
      if (__all(((ax | az) & m) == 0u)) break;
    }
    *(volatile LAS f32x2*)rx = f32x2{gval(xb[0]), gval(xb[2])};
    double S0, S1, L;
    if (__builtin_expect(__all(bad == 0u), 1)) {
      S0 = mkd(p[0][0], p[0][1]); S1 = mkd(p[0][2], p[0][3]); L = rec_l(p[0]);
#pragma unroll
      for (int s = 1; s < NSLOT; s++) { S0 += mkd(p[s][0], p[s][1]); S1 += mkd(p[s][2], p[s][3]); L += rec_l(p[s]); }
    } else {
      float R = -INFINITY;
      S0 = S1 = L = 0.0;
#pragma unroll
      for (int s = 0; s < NSLOT; s++) {
        const float Rs = rec_l(p[s]) != 0.0 ? rec_r(p[s]) : -INFINITY, Rn = fmaxf(R, Rs);
        const double s0 = mkd(0u, p2hi(R, Rn)), s1 = mkd(0u, p2hi(Rs, Rn));
        L = __builtin_fma(rec_l(p[s]), s1, L * s0);
        S0 = __builtin_fma(rec_a(p[s]), s1, S0 * s0); S1 = __builtin_fma(rec_b(p[s]), s1, S1 * s0);
        R = Rn;
      }
    }
    {
      const double inv = __builtin_amdgcn_rcp(L);
      const unsigned pr[1] = {cvtpk((float)(S0 * inv), (float)(S1 * inv))};
      const f32x4 y = proj(pr, wf, lane);
      if ((lane & 12) == 0) *(volatile LAS f32x4*)rw = y;
      const unsigned n = arrive(cnt + 2, lane);
      const f32x2 v = rows_sum<11>(rl);
      __builtin_amdgcn_raw_buffer_store_b32(cvtpk(v[0], v[1]), rout, n == 8u ? oo : OOB, 0, AUX_SC1);
      __builtin_amdgcn_raw_buffer_store_b64(__builtin_bit_cast(u32x2, tg.e), rws, n == 8u ? oe : OOB, 0, AUX_SC1);
    }
    return;
  }
  }
  {
    const u32x4 q4 = *(LAS u32x4*)(qst + 256 + l16 * 16);
    const Mrg mg = mkmrg(scr + SCR_SUM + SUM_STR, lane, WS_RS + (head * 16 + 8 + sub) * 1024, mktag(e0));
    Acc A;
    b_units(ra, rk4, rv4, cur, q4, lane, mask, ctl, rws, cntoff, A, vu, ub);
    b_merge(mg, scr + 8192 + 8704, w, lane, A, rws, e0);
    SB();
    asm volatile("" :: "v"(ra[0]), "v"(ra[1]), "v"(ra[2]), "v"(ra[3]), "v"(ra[4]), "v"(ra[5]), "v"(ra[6]), "v"(ra[7]));
  }
}

extern "C" void kernel_launch(void* const* d_in, const int* in_sizes, int n_in, void* d_out, int out_size, void* d_ws, size_t ws_size,
                              hipStream_t stream) {
  k_all<<<256, 512, 0, stream>>>((const char*)d_in[0], (const char*)d_in[1], (const char*)d_in[2], (const char*)d_in[3], (const char*)d_in[4],
                                 (const char*)d_in[5], (const char*)d_in[6], (const char*)d_in[7], (const char*)d_in[8], (const char*)d_in[9],
                                 (const char*)d_in[10], (char*)d_ws, (unsigned*)d_out);
}
```

```cpp
#include <hip/hip_runtime.h>
#include <stdint.h>

typedef __attribute__((ext_vector_type(4))) unsigned u32x4;
typedef __attribute__((ext_vector_type(2))) unsigned u32x2;
typedef __attribute__((ext_vector_type(8))) unsigned u32x8;
typedef __attribute__((ext_vector_type(16))) unsigned u32x16;
typedef __attribute__((ext_vector_type(4))) float f32x4;
typedef __attribute__((ext_vector_type(2))) float f32x2;
typedef __attribute__((ext_vector_type(2))) double f64x2;
typedef __attribute__((ext_vector_type(16))) float f32x16;
typedef __attribute__((ext_vector_type(8))) __bf16 bf16x8;
typedef __attribute__((ext_vector_type(2))) __bf16 bf16x2;
typedef __attribute__((ext_vector_type(4))) short s16x4;
typedef unsigned long long u64;
typedef __amdgpu_buffer_rsrc_t rsrc_t;
#define LAS __attribute__((address_space(3)))
#define GAS __attribute__((address_space(1)))

#define CPF 35584
#define PAT "ac3x01ybp524"
#define UPAT "x4630ghy2715"
#define CPAT "bacnt02vl1mu"
constexpr int WTH = 4, N0 = 6;
constexpr int CH = 4, DCH = 2;
constexpr int NXA = 8, NXB = 8;
constexpr int NREL = 24;

constexpr int AUX_NT = 2, AUX_SC1 = 16, OOB = 0x40000000;
constexpr float SCALE = 0.08838834764831845f;
constexpr float LOG2E = 1.4426950408889634f;
constexpr unsigned SPIN_CAP = 1u << 17;
constexpr int NSLOT = 7, NUNIT = 256;

constexpr int WS_EPOCH = 0;
constexpr int CSTR    = 4352;
constexpr int WS_CNT  = 8192;
constexpr int WS_QB   = WS_CNT + 32 * CSTR;
constexpr int WS_QABS = WS_QB + 32 * 2048;
constexpr int WS_PML  = WS_QABS + 4 * 32 * 2048;
constexpr int WS_PO   = WS_PML + 32 * 32 * 16;
constexpr int WS_RS   = WS_PO + 32 * 32 * 1024;
constexpr int WS_Y    = WS_RS + 32 * 16 * 1024;
constexpr int WS_YB   = WS_Y + 4 * 8 * 32 * 512;
constexpr int WS_END  = WS_YB + 4 * 8 * 4 * 512;

constexpr int LDS_BYTES = 163840;
constexpr int LDS_SCR = 131072;
constexpr int SCR_SUM = 5120, SUM_STR = 1040;

__device__ __forceinline__ unsigned cvtpk(float lo, float hi) {
  f32x2 v = {lo, hi};
  return __builtin_bit_cast(unsigned, __builtin_convertvector(v, bf16x2));
}
__device__ __forceinline__ float bf_lo(unsigned u) { return __uint_as_float(u << 16); }
__device__ __forceinline__ float bf_hi(unsigned u) { return __uint_as_float(u & 0xffff0000u); }
__device__ __forceinline__ float rbf(float x) { return __uint_as_float(cvtpk(x, x) << 16); }
__device__ __forceinline__ float dot2(unsigned k, unsigned q, float acc) {
  return __builtin_amdgcn_fdot2_f32_bf16(__builtin_bit_cast(bf16x2, k), __builtin_bit_cast(bf16x2, q), acc, false);
}
template <int CTRL> __device__ __forceinline__ float dpp(float x) {
  return __builtin_bit_cast(float, __builtin_amdgcn_mov_dpp(__builtin_bit_cast(int, x), CTRL, 0xf, 0xf, true));
}
template <int CTRL> __device__ __forceinline__ unsigned dppu(unsigned x) {
  return (unsigned)__builtin_amdgcn_mov_dpp((int)x, CTRL, 0xf, 0xf, true);
}
constexpr int XOR1 = 0xB1, XOR2 = 0x4E, XOR7 = 0x141, XOR8 = 0x128, ROR4 = 0x124;
__device__ __forceinline__ float xrow_max(float x) {
  auto s = __builtin_amdgcn_permlane16_swap(__float_as_uint(x), __float_as_uint(x), false, false);
  x = fmaxf(__uint_as_float(s[0]), __uint_as_float(s[1]));
  auto t = __builtin_amdgcn_permlane32_swap(__float_as_uint(x), __float_as_uint(x), false, false);
  return fmaxf(__uint_as_float(t[0]), __uint_as_float(t[1]));
}
__device__ __forceinline__ float xrow_sum(float x) {
  auto s = __builtin_amdgcn_permlane16_swap(__float_as_uint(x), __float_as_uint(x), false, false);
  x = __uint_as_float(s[0]) + __uint_as_float(s[1]);
  auto t = __builtin_amdgcn_permlane32_swap(__float_as_uint(x), __float_as_uint(x), false, false);
  return __uint_as_float(t[0]) + __uint_as_float(t[1]);
}
__device__ __forceinline__ float row_max(float x) {
  x = fmaxf(x, dpp<XOR8>(x)); x = fmaxf(x, dpp<ROR4>(x)); x = fmaxf(x, dpp<XOR2>(x)); x = fmaxf(x, dpp<XOR1>(x));
  return x;
}
__device__ __forceinline__ float row_sum(float x) {
  x += dpp<XOR8>(x); x += dpp<ROR4>(x); x += dpp<XOR2>(x); x += dpp<XOR1>(x);
  return x;
}
__device__ __forceinline__ float fexp2(float x) { return __builtin_amdgcn_exp2f(x); }
__device__ __forceinline__ f32x4 mfma16(u32x4 a, u32x4 b, f32x4 c) {
  return __builtin_amdgcn_mfma_f32_16x16x32_bf16(__builtin_bit_cast(bf16x8, a), __builtin_bit_cast(bf16x8, b), c, 0, 0, 0);
}
__device__ __forceinline__ f32x16 mfma32(u32x4 a, u32x4 b, f32x16 c) {
  return __builtin_amdgcn_mfma_f32_32x32x16_bf16(__builtin_bit_cast(bf16x8, a), __builtin_bit_cast(bf16x8, b), c, 0, 0, 0);
}
__device__ __forceinline__ rsrc_t mkrs(const GAS char* p, unsigned bytes) {
  return __builtin_amdgcn_make_buffer_rsrc((void*)p, 0, bytes, 0x00020000);
}
__device__ __forceinline__ void bar() { asm volatile("s_waitcnt lgkmcnt(0)\n\ts_barrier" ::: "memory"); }
__device__ __forceinline__ void drain() { asm volatile("s_waitcnt vmcnt(0)" ::: "memory"); }
#define SB() __builtin_amdgcn_sched_barrier(0)
template <int N> __device__ __forceinline__ void vmwait() { asm volatile("s_waitcnt vmcnt(%0)" :: "n"(N) : "memory"); }
__device__ __forceinline__ unsigned swz(unsigned row) { return ((row & 3) << 2) | ((row >> 2) & 2); }
__device__ __forceinline__ u32x2 trread(LAS char* p) {
  return __builtin_bit_cast(u32x2, __builtin_amdgcn_ds_read_tr16_b64_v4i16((LAS s16x4*)p));
}

struct Tag { unsigned tl, th, lo, rb; u64 e; };
__device__ __forceinline__ Tag mktag(u64 e0) {
  Tag t;
  asm volatile("" : "+s"(e0));
  t.e = e0 * 6364136223846793005ull + 1442695040888963407ull;
  t.th = (unsigned)(t.e >> 32);
  t.lo = (unsigned)t.e;
  t.tl = t.lo & 0xff000000u;
  t.rb = ((t.lo >> 9) & 255u) | (~t.lo & 256u);
  return t;
}
__device__ __forceinline__ unsigned gpack(float v, const Tag& t) { return ((__float_as_uint(v) + 0x80u) >> 8) | t.tl; }
__device__ __forceinline__ u32x4 gpair(float a, float b, const Tag& t) { return u32x4{gpack(a, t), t.th, gpack(b, t), t.th}; }
__device__ __forceinline__ bool gok(u32x4 g, const Tag& t) {
  return ((((g[0] ^ t.tl) | (g[2] ^ t.tl)) >> 24) | (g[1] ^ t.th) | (g[3] ^ t.th)) == 0u;
}
__device__ __forceinline__ float gval(unsigned lo) { return __uint_as_float(lo << 8); }
__device__ __forceinline__ bool qok(u32x4 g, const Tag& t) { return ((g[1] ^ t.th) | (g[3] ^ t.th)) == 0u; }
__device__ __forceinline__ double mkd(unsigned lo, unsigned hi) { return __builtin_bit_cast(double, u32x2{lo, hi}); }
__device__ __forceinline__ u32x4 rpack(double a, double b, double l, float r, const Tag& t) {
  const u32x2 ua = __builtin_bit_cast(u32x2, a), ub = __builtin_bit_cast(u32x2, b), ul = __builtin_bit_cast(u32x2, l);
  const u64 ml = ((u64)(unsigned)__builtin_amdgcn_readfirstlane(ul[1]) << 32) | (unsigned)__builtin_amdgcn_readfirstlane(ul[0]);
  const u64 mr = (unsigned)__builtin_amdgcn_readfirstlane(__float_as_uint(r));
  const unsigned ta = t.lo & 511u, tb = t.rb;
  return u32x4{(ua[0] & ~1023u) | (__builtin_amdgcn_inverse_ballot_w64(ml) ? ta | 512u : ta), ua[1],
               (ub[0] & ~1023u) | (__builtin_amdgcn_inverse_ballot_w64(mr) ? tb | 512u : tb), ub[1]};
}
__device__ __forceinline__ u32x4 rpack0(f64x2 o, unsigned lw, int lane, unsigned ta, unsigned tb) {
  const u32x4 u = __builtin_bit_cast(u32x4, o);
  return u32x4{(u[0] & ~1023u) | (ta | (__builtin_amdgcn_ubfe(lw, lane, 1) << 9)), u[1], (u[2] & ~1023u) | tb, u[3]};
}
__device__ __forceinline__ bool rok(u32x4 g, const Tag& t) { return (((g[0] ^ t.lo) | (g[2] ^ t.rb)) & 511u) == 0u; }
constexpr unsigned PMASK = 0x00030003u;
__device__ __forceinline__ unsigned ptag(const Tag& t, int k) {
  const unsigned n = k == 0 ? t.lo : k == 3 ? t.lo >> 10 : ((t.lo >> (1 + 3 * k)) & 7u) | (~t.lo & 8u);
  return (n & 3u) | ((n & 12u) << 14);
}
__device__ __forceinline__ unsigned ltag(const Tag& t) { return ((t.lo >> 16) & 127u) | (~t.lo & 128u); }
__device__ __forceinline__ bool mok(u32x4 g, const Tag& t) { return (((g[0] ^ t.lo) & 0xffffu) | ((g[1] ^ ltag(t)) & 255u)) == 0u; }
__device__ __forceinline__ unsigned bfi(unsigned m, unsigned a, unsigned b) { return (a & m) | (b & ~m); }
__device__ __forceinline__ double rec_a(u32x4 g) { return mkd(g[0] & ~1023u, g[1]); }
__device__ __forceinline__ double rec_b(u32x4 g) { return mkd(g[2] & ~1023u, g[3]); }
__device__ __forceinline__ double rec_l(u32x4 g) { return __builtin_bit_cast(double, __builtin_amdgcn_ballot_w64((g[0] & 512u) != 0u)); }
__device__ __forceinline__ float rec_r(u32x4 g) { return __uint_as_float((unsigned)__builtin_amdgcn_ballot_w64((g[2] & 512u) != 0u)); }
__device__ __forceinline__ int edif(float a, float b) {
  int r;
  asm("v_cvt_i32_f32 %0, %1" : "=v"(r) : "v"(a - b));
  return r;
}
__device__ __forceinline__ unsigned p2hi(float a, float b) { return (unsigned)max(edif(a, b) + 1023, 0) << 20; }
__device__ __forceinline__ float bucket(float m) { return fmaf(__builtin_rintf(m * (1.f / 512.f)), 512.f, 0.f); }
__device__ __forceinline__ float rfl(float x) { return __uint_as_float(__builtin_amdgcn_readfirstlane(__float_as_uint(x))); }
__device__ __forceinline__ unsigned rdu(unsigned v, int l) { return __builtin_amdgcn_readlane(v, l); }

__device__ __forceinline__ void diag_gemv(const u32x4 (&wr)[16], const u32x4 (&xp)[2][2], const unsigned (&mask)[4], f32x4 (&acc)[2]) {
#pragma unroll
  for (int i = 0; i < 16; i++) {
#pragma unroll
    for (int s = 0; s < 2; s++) {
      const unsigned src = xp[s][i >> 3][(i >> 1) & 3];
      const unsigned dup = __builtin_amdgcn_perm(src, src, (i & 1) ? 0x03020302u : 0x01000100u);
      const u32x4 a = {dup & mask[0], dup & mask[1], dup & mask[2], dup & mask[3]};
      acc[s] = mfma16(a, wr[i], acc[s]);
    }
  }
}

constexpr int bitpos(char c) { for (int i = 0; i < 12; i++) if (PAT[i] == c) return 8 + i; return 31; }
constexpr int goff(int t) { int o = 0; for (int j = 0; j < 6; j++) o |= ((t >> j) & 1) << bitpos('0' + j); return o; }
struct Acc { double o[2]; double l; float R; };
__device__ __forceinline__ u32x4 ldnt(rsrc_t r, int voff, int so) { return __builtin_amdgcn_raw_buffer_load_b128(r, voff, so, AUX_NT); }
__device__ __forceinline__ u32x4 ldslot(rsrc_t r, int voff, int so, int i) { return ldnt(r, voff + goff(i & 3), so ^ goff(i & ~3)); }
constexpr int upos(char c) { for (int i = 0; i < 12; i++) if (UPAT[i] == c) return 8 + i; return 31; }
__device__ __forceinline__ u32x4 ldunit(rsrc_t r, int vu, int so, int i) { return ldnt(r, vu + ((i & 1) << upos('g')) + ((i >> 1) << upos('h')), so); }
__device__ __forceinline__ u32x4 ldsc(rsrc_t r, int voff, int so) { return __builtin_amdgcn_raw_buffer_load_b128(r, voff, so, AUX_SC1); }
__device__ __forceinline__ void stsc(u32x4 v, rsrc_t r, int voff, int so) { __builtin_amdgcn_raw_buffer_store_b128(v, r, voff + so, 0, AUX_SC1); }
__device__ __forceinline__ float dot8(u32x4 k, u32x4 q) { return dot2(k[3], q[3], dot2(k[2], q[2], dot2(k[1], q[1], dot2(k[0], q[0], 0.f)))); }
#define TR_STEP(HALF, CTRL, BIT) _Pragma("unroll") for (int h = 0; h < (HALF); ++h) { \
    const float t1 = x[h] + dpp<CTRL>(x[h]), t2 = x[h + (HALF)] + dpp<CTRL>(x[h + (HALF)]); x[h] = (c & (BIT)) ? t2 : t1; }
template <int N> __device__ __forceinline__ float k_step(u32x4 (&ring)[16], u32x4 q, int c, rsrc_t rn, int voff, int so, bool ext, LAS char* xp) {
  float x[16];
#pragma unroll
  for (int i = 0; i < 16; i++) {
    x[i] = dot8(ring[i], q);
    asm volatile("s_waitcnt vmcnt(%1)" : "+v"(x[i]) : "n"(WTH - 1));
    if (i >= 16 - N && ext) ring[i] = *(volatile LAS u32x4*)(xp + (i - (16 - N)) * 1024);
    else ring[i] = ldslot(rn, voff, so, i);
    SB();
  }
  TR_STEP(8, XOR8, 8) TR_STEP(4, XOR7, 4) TR_STEP(2, XOR2, 2) TR_STEP(1, XOR1, 1)
  return rbf(rbf(x[0]) * SCALE);
}
template <int BC> __device__ __forceinline__ void pv(u32x4 v, unsigned pk, const unsigned (&mask)[4], f32x4& acc) {
  const unsigned d = dppu<0x150 + BC>(pk);
  acc = mfma16(u32x4{d & mask[0], d & mask[1], d & mask[2], d & mask[3]}, v, acc);
}
template <int I, int MODE> __device__ __forceinline__ void v_step(u32x4 (&ring)[16], unsigned pk, const unsigned (&mask)[4], f32x4& acc,
                                                                  rsrc_t rk, rsrc_t rv, int voff, int so) {
  pv<I>(ring[I], pk, mask, acc);
  if (MODE == 1 || (MODE == 2 && I < 8)) {
    asm volatile("s_waitcnt vmcnt(%1)" : "+v"(acc) : "n"(WTH - 1));
    ring[I] = MODE == 1 ? ldslot(rv, voff, so, I) : ldunit(I < 4 ? rk : rv, voff, so, I & 3);
  } else {
    asm volatile("" : "+v"(acc));
  }
  SB();
  if constexpr (I < 15) v_step<I + 1, MODE>(ring, pk, mask, acc, rk, rv, voff, so);
}
__device__ __forceinline__ float exp_ref(float mx, float& f) {
  const float M = __builtin_ceilf(mx * LOG2E);
  f = __builtin_amdgcn_fmed3f(fmaf(mx, LOG2E, -M), -1.f, 0.f);
  return M;
}
__device__ __forceinline__ unsigned lds_inc(LAS unsigned* p, int lane) {
  unsigned t = 0, z = 0;
  asm volatile("" : "+v"(z));
  if (lane == 0) t = __hip_atomic_fetch_add(p + z, 1u, __ATOMIC_RELAXED, __HIP_MEMORY_SCOPE_WORKGROUP);
  return t;
}
struct Take { unsigned unit, pend; bool fetch; };
__device__ __forceinline__ void lds_add(LAS unsigned* p, unsigned v) { __hip_atomic_fetch_add(p, v, __ATOMIC_RELAXED, __HIP_MEMORY_SCOPE_WORKGROUP); }
__device__ __forceinline__ Take take(LAS unsigned* ctl, unsigned kv, rsrc_t rws, int cntoff, int lane) {
  const unsigned k = __builtin_amdgcn_readfirstlane(kv);
  unsigned b, s;
  auto look = [&]() {
    const unsigned vs = ((volatile LAS unsigned*)ctl)[5], vb = ((volatile LAS unsigned*)ctl)[8 + (k >> 2)];
    s = __builtin_amdgcn_readfirstlane(vs); b = __builtin_amdgcn_readfirstlane(vb);
    return (b == ~0u) & ((s < 0x10000u) | ((s & 0xffffu) != 0u));
  };
  if (__builtin_expect(look(), 0))
    for (unsigned spins = 0; spins < SPIN_CAP * 16; spins++) {
      __builtin_amdgcn_s_sleep(1);
      if (!look()) break;
    }
  Take t;
  t.unit = min(b, (unsigned)NUNIT) + (k & (CH - 1));
  t.fetch = ((k & (CH - 1)) == 0) & (b < NUNIT) & (s < 0x10000u);
  int off = OOB;
  if (t.fetch) {
    unsigned z = 0;
    asm volatile("" : "+v"(z));
    if (lane == 0) { lds_add(ctl + 5 + z, 1u); off = cntoff; }
    asm volatile("s_waitcnt lgkmcnt(0)" ::: "memory");
  }
  t.pend = __builtin_amdgcn_raw_ptr_buffer_atomic_add_i32(CH, rws, off, 0, AUX_SC1);
  SB();
  return t;
}
__device__ __forceinline__ void give(LAS unsigned* ctl, const Take& t, int lane) {
  if (t.fetch && lane == 0) {
    unsigned z = 0;
    asm volatile("" : "+v"(z));
    if (t.pend < NUNIT) {
      const unsigned i = __hip_atomic_fetch_add(ctl + 4 + z, 1u, __ATOMIC_RELAXED, __HIP_MEMORY_SCOPE_WORKGROUP);
      ((volatile LAS unsigned*)ctl)[8 + i] = t.pend;
    }
    asm volatile("" ::: "memory");
    lds_add(ctl + 5 + z, t.pend >= NUNIT - CH ? 0xffffu : ~0u);
  }
}
__device__ __forceinline__ int unit_off(unsigned t, unsigned ub) {
  const int a = __builtin_amdgcn_readlane(ub, t & 15), b = __builtin_amdgcn_readlane(ub, 16 + ((t >> 4) & 15));
  return t < NUNIT ? a | b : 1 << 20;
}

__device__ __forceinline__ void b_units(u32x4 (&ra)[16], rsrc_t rk, rsrc_t rv, unsigned cur, u32x4 q, int lane, const unsigned (&mask)[4],
                                        LAS unsigned* ctl, rsrc_t rws, int cntoff, Acc& A, int voff, unsigned ub) {
  const int c = lane & 15;
  float Mr = -INFINITY;
  double s1 = 0.0;
  A.o[0] = A.o[1] = A.l = 0.0; A.R = cur < NUNIT ? -INFINITY : 0.f;
#pragma unroll 1
  while (cur < NUNIT) {
    const Take t = take(ctl, lds_inc(ctl, lane), rws, cntoff, lane);
    const int so = unit_off(t.unit, ub);
    SB();
    float x[4];
    auto next = [&](int j) { ra[j] = ldunit(j < 4 ? rk : rv, voff, so, j & 3); };
#pragma unroll
    for (int i = 0; i < 4; i++) x[i] = dot8(ra[i], q);
    asm volatile("" : "+v"(x[0]), "+v"(x[1]), "+v"(x[2]), "+v"(x[3]));
    SB();
#pragma unroll
    for (int i = 0; i < 4; i++) {
      next(i);
      SB();
    }
    TR_STEP(2, XOR8, 8) TR_STEP(1, XOR7, 4)
    x[0] += dpp<XOR2>(x[0]); x[0] += dpp<XOR1>(x[0]);
    const float s = rbf(rbf(x[0]) * SCALE);
    const float n = __builtin_rintf(s * LOG2E);
    float e = fexp2(__builtin_amdgcn_fmed3f(fmaf(s, LOG2E, -n), -1.f, 1.f)), d = n - Mr;
    asm volatile("" : "+v"(e));
    if (__builtin_expect(__any(d > 57.f), 0)) {
      float mx = fmaxf(n, dpp<XOR8>(n));
      mx = xrow_max(fmaxf(mx, dpp<ROR4>(mx)));
      const float Rn = rfl(fmaxf(A.R, bucket(mx)));
      const double s0 = mkd(0u, p2hi(A.R, Rn));
      A.o[0] *= s0; A.o[1] *= s0; A.l *= s0;
      A.R = Rn; Mr = mx; d = n - mx;
      s1 = mkd(0u, p2hi(mx, Rn));
    }
    const float p = __builtin_ldexpf(e, edif(d, 0.f));
    unsigned pk = cvtpk(p, p);
    asm volatile("" : "+v"(pk));
    SB();
    f32x4 acc = {0.f, 0.f, 0.f, 0.f};
    float ls = bf_lo(pk);
    pv<0>(ra[4], pk, mask, acc);  asm volatile("" : "+v"(acc)); next(4); ls += dpp<XOR8>(ls); asm volatile("" : "+v"(ls)); SB();
    pv<4>(ra[5], pk, mask, acc);  asm volatile("" : "+v"(acc)); next(5); ls += dpp<ROR4>(ls); asm volatile("" : "+v"(ls)); SB();
    pv<8>(ra[6], pk, mask, acc);  asm volatile("" : "+v"(acc)); next(6); SB();
    pv<12>(ra[7], pk, mask, acc); asm volatile("" : "+v"(acc)); next(7); SB();
    A.l = __builtin_fma((double)xrow_sum(ls), s1, A.l);
    asm volatile("" : "+v"(A.l));
    A.o[0] = __builtin_fma((double)(lane < 32 ? acc[0] : acc[2]), s1, A.o[0]);
    A.o[1] = __builtin_fma((double)(lane < 32 ? acc[1] : acc[3]), s1, A.o[1]);
    asm volatile("" : "+v"(A.o[0]), "+v"(A.o[1]));
    SB();
    cur = t.unit;
    if (cur >= NUNIT) break;
    give(ctl, t, lane);
  }
}
#undef TR_STEP
__device__ __forceinline__ int rec_col(int lane) { return 8 * (lane & 15) + 4 * ((lane >> 4) & 1) + 2 * (lane >> 5); }
struct Mrg { unsigned s0, s4, s8, ta, tb; int off; };
__device__ __forceinline__ Mrg mkmrg(LAS char* sum, int lane, int roff, const Tag& tg) {
  Mrg m = {(unsigned)(uintptr_t)sum, (unsigned)(uintptr_t)sum + (lane >> 5) * 4, (unsigned)(uintptr_t)sum + lane * 8, tg.lo & 511u, tg.rb, roff + lane * 16};
  asm volatile("" : "+v"(m.s0), "+v"(m.s4), "+v"(m.s8), "+v"(m.ta), "+v"(m.tb), "+v"(m.off));
  return m;
}
__device__ __forceinline__ void b_merge(const Mrg& m, LAS char* mrg, int w, int lane, const Acc& A, rsrc_t rws, u64 e0) {
  unsigned inc = 1;
  double a = A.o[0], b = A.o[1], l = A.l;
  if (__builtin_expect(__float_as_uint(A.R) != 0u, 0)) {
    *(volatile LAS f64x2*)(mrg + w * 1024 + lane * 16) = f64x2{a, b};
    *(volatile LAS double*)(mrg + 8192 + w * 8) = l;
    *(volatile LAS float*)(mrg + 8256 + w * 4) = A.R;
    a = b = l = 0.0;
    inc = 1u + (256u << w);
  }
  f64x2 o;
  unsigned n, lw;
  asm volatile("ds_add_f64 %3, %5\n\tds_add_f64 %3, %6 offset:512\n\ts_mov_b64 exec, 1\n\tds_add_f64 %3, %7 offset:1024\n\tds_add_u32 %3, %8 offset:1032\n\t"
               "s_mov_b64 exec, -1\n\tds_read_b32 %1, %9 offset:1032\n\tds_read_b32 %2, %4 offset:1024\n\tds_read2st64_b64 %0, %3 offset1:1\n\ts_waitcnt lgkmcnt(0)"
               : "=&v"(o), "=&v"(n), "=&v"(lw)
               : "v"(m.s8), "v"(m.s4), "v"(a), "v"(b), "v"(l), "v"(inc), "v"(m.s0)
               : "memory");
  stsc(rpack0(o, lw, lane, m.ta, m.tb), rws, n == 8u ? m.off : OOB, 0);
  SB();
  n = __builtin_amdgcn_readfirstlane(n);
  if (__builtin_expect(n > 8u && (n & 255u) == 8u, 0)) {
    double L = *(volatile LAS double*)(uintptr_t)(m.s0 + 1024);
    float R = L != 0.0 ? 0.f : -INFINITY;
#pragma unroll 1
    for (int i = 0; i < 8; i++)
      if (n >> (8 + i) & 1) {
        const f64x2 oi = *(volatile LAS f64x2*)(mrg + i * 1024 + lane * 16);
        const float Ri = *(volatile LAS float*)(mrg + 8256 + i * 4), Rn = fmaxf(R, Ri);
        const double s0 = mkd(0u, p2hi(R, Rn)), s1 = mkd(0u, p2hi(Ri, Rn));
        o[0] = __builtin_fma(oi[0], s1, o[0] * s0); o[1] = __builtin_fma(oi[1], s1, o[1] * s0);
        L = __builtin_fma(*(volatile LAS double*)(mrg + 8192 + i * 8), s1, L * s0);
        R = Rn;
      }
    stsc(rpack(o[0], o[1], L, R, mktag(e0)), rws, m.off, 0);
  }
}

constexpr int cpos(char c) { for (int i = 0; i < 12; i++) if (CPAT[i] == c) return 7 + i; return 31; }
constexpr int clds(char c) {
  int r = 10;
  for (int i = 3; i < 12 && CPAT[i] != c; i++) r += CPAT[i] < 'l';
  return cpos(c) < 10 ? cpos(c) + 6 : r;
}
template <typename T> __device__ __forceinline__ constexpr T spread(T v, int b0, int b1, int b2) { return ((v & 1) << b0) | (((v >> 1) & 1) << b1) | (((v >> 2) & 1) << b2); }
__device__ __forceinline__ unsigned fz(unsigned row) { return ((row & 2) << 1) | ((row >> 1) & 2); }
__device__ __forceinline__ void dma1(rsrc_t rs, int voff, int so, unsigned ldsaddr) {
  unsigned keep;
  asm volatile("s_mov_b32 %0, m0\n\ts_mov_b32 m0, %1\n\ts_nop 0\n\tbuffer_load_dwordx4 %2, %3, %4 offen nt lds\n\ts_mov_b32 m0, %0"
               : "=&s"(keep) : "s"(ldsaddr), "v"(voff), "s"(rs), "s"(so) : "memory");
}
__device__ __forceinline__ void dma_tile(rsrc_t rs, int voff, int so, unsigned ldsaddr, int j) {
  dma1(rs, voff, so + spread(j, cpos('0'), cpos('1'), cpos('2')), ldsaddr + spread(j, clds('0'), clds('1'), clds('2')));
}
template <int NR>
__device__ __forceinline__ void dma_rows(rsrc_t rs, int srcoff, LAS char* img, int r0, int lane) {
#pragma unroll
  for (int u = 0; u < NR; u++) {
    const int r = r0 + u;
    unsigned keep;
    asm volatile("s_mov_b32 %0, m0\n\ts_mov_b32 m0, %1\n\ts_nop 0\n\tbuffer_load_dwordx4 %2, %3, %4 offen nt lds\n\ts_mov_b32 m0, %0"
                 : "=&s"(keep) : "s"((unsigned)(uintptr_t)img + r * 1024), "v"((lane ^ swz(r)) * 16), "s"(rs), "s"(srcoff + r * 1024) : "memory");
  }
  SB();
}

__device__ __forceinline__ void wo_frags(LAS char* img, int w, int lane, u32x4 (&wf)[4][4]) {
  const int g = lane >> 4, q = (lane & 15) >> 2, p = lane & 3;
#pragma unroll
  for (int ks = 0; ks < 4; ks++)
#pragma unroll
    for (int tl = 0; tl < 4; tl++) {
      const int ch = 8 * w + 2 * tl + (p >> 1);
      const int r0 = 32 * ks + 8 * g + q, r1 = r0 + 4;
      const u32x2 a = trread(img + r0 * 1024 + ((ch ^ swz(r0)) * 16) + 8 * (p & 1));
      const u32x2 b = trread(img + r1 * 1024 + ((ch ^ swz(r1)) * 16) + 8 * (p & 1));
      wf[tl][ks] = u32x4{a[0], a[1], b[0], b[1]};
    }
}
__device__ __forceinline__ f32x4 wo_calc(const u32x4 (&wf)[4][4], LAS char* cb, int lane) {
  const int n = lane & 15, grp = lane >> 4;
  u32x4 bf[4];
#pragma unroll
  for (int ks = 0; ks < 4; ks++) bf[ks] = *(LAS u32x4*)(cb + (n & 3) * 256 + ks * 64 + grp * 16);
  f32x4 acc[4];
#pragma unroll
  for (int tl = 0; tl < 4; tl++) acc[tl] = f32x4{0.f, 0.f, 0.f, 0.f};
#pragma unroll
  for (int ks = 0; ks < 4; ks++)
#pragma unroll
    for (int tl = 0; tl < 4; tl++) acc[tl] = mfma16(wf[tl][ks], bf[ks], acc[tl]);
  const f32x4 lo = (n & 4) ? acc[1] : acc[0], hi = (n & 4) ? acc[3] : acc[2];
  return (n & 8) ? hi : lo;
}
__device__ __forceinline__ void y_store(f32x4 y, int w, int lane, int slot, rsrc_t rws, int yoff, const Tag& tg) {
  const int off = yoff + (((lane & 3) * 8 + w) * 32 + slot) * 512 + ((lane & 12) + (lane >> 4)) * 16;
  stsc(gpair(y[0], y[1], tg), rws, off, 0);
  stsc(gpair(y[2], y[3], tg), rws, off, 256);
}
template <int K, int N> __device__ __forceinline__ unsigned bc1(const unsigned (&p)[N]) {
  unsigned v = dppu<0x150 + K>(p[0]);
  if constexpr (N > 1) {
    v = (unsigned)__builtin_amdgcn_update_dpp((int)v, (int)p[1], 0x150 + K, 0xf, 2, false);
    v = (unsigned)__builtin_amdgcn_update_dpp((int)v, (int)p[2], 0x150 + K, 0xf, 4, false);
  }
  return v;
}
template <int K, int N> __device__ __forceinline__ u32x4 bc4(const unsigned (&p)[N]) { return u32x4{bc1<K>(p), bc1<K + 1>(p), bc1<K + 2>(p), bc1<K + 3>(p)}; }
template <int N> __device__ __forceinline__ f32x4 proj(const unsigned (&pr)[N], const u32x4 (&wf)[4][4], int lane) {
  const u32x4 bf[4] = {bc4<0>(pr), bc4<4>(pr), bc4<8>(pr), bc4<12>(pr)};
  f32x4 acc[4];
#pragma unroll
  for (int tl = 0; tl < 4; tl++) acc[tl] = f32x4{0.f, 0.f, 0.f, 0.f};
#pragma unroll
  for (int ks = 0; ks < 4; ks++)
#pragma unroll
    for (int tl = 0; tl < 4; tl++) acc[tl] = mfma16(wf[tl][ks], bf[ks], acc[tl]);
  const f32x4 lo = (lane & 1) ? acc[1] : acc[0], hi = (lane & 1) ? acc[3] : acc[2];
  f32x4 r = (lane & 2) ? hi : lo;
  asm volatile("" : "+v"(r));
  return r;
}
__device__ __forceinline__ unsigned arrive(LAS unsigned* p, int lane) {
  unsigned z = 0;
  asm volatile("" : "+v"(z) :: "memory");
  if (lane == 0) lds_add(p + z, 1u);
  asm volatile("" ::: "memory");
  return *(volatile LAS unsigned*)p;
}
template <int N> __device__ __forceinline__ f32x2 rows_sum(LAS char* p) {
  f32x2 v[N];
#pragma unroll
  for (int i = 0; i < N; i++) v[i] = *(volatile LAS f32x2*)(p + i * 256);
#pragma unroll
  for (int st = 1; st < N; st *= 2)
#pragma unroll
    for (int i = 0; i + st < N; i += 2 * st) v[i] += v[i + st];
  return v[0];
}
__device__ __forceinline__ float half_sum(float x) {
  auto t = __builtin_amdgcn_permlane32_swap(__float_as_uint(x), __float_as_uint(x), false, false);
  return __uint_as_float(t[0]) + __uint_as_float(t[1]);
}

__global__ __launch_bounds__(512, 2) void k_all(const char* a0_, const char* a1_, const char* a2_, const char* a3_, const char* a4_, const char* a5_,
                                                const char* a6_, const char* a7_, const char* a8_, const char* a9_, const char* a10_, char* a11_, unsigned* a12_) {
  u32x16 ka; u32x8 kb_; u32x2 kc_;
  asm volatile("s_load_dwordx16 %0, %3, 0x0\n\ts_load_dwordx8 %1, %3, 0x40\n\ts_load_dwordx2 %2, %3, 0x60"
               : "=&s"(ka), "=&s"(kb_), "=&s"(kc_) : "s"(__builtin_amdgcn_kernarg_segment_ptr()));
  __shared__ __attribute__((aligned(16))) char smem[LDS_BYTES];
  LAS char* lds = (LAS char*)smem;
  LAS char* scr = lds + LDS_SCR;
  LAS unsigned* ctl = (LAS unsigned*)scr;
  const int tid = threadIdx.x, lane = tid & 63, w = __builtin_amdgcn_readfirstlane(tid >> 6);
  const int blk = blockIdx.x, head = blk >> 3, prod = (blk + head) & 7;
  const int sub = (0x54372106u >> (4 * prod)) & 7;
  unsigned cpf = 0;
  {
    u64 pc;
    asm volatile("s_getpc_b64 %0" : "=s"(pc));
    cpf = __builtin_amdgcn_raw_buffer_load_b32(mkrs((const GAS char*)(pc & ~255ull), CPF), w == 7 ? head * 2048 + lane * 32 : OOB, 0, 0);
    SB();
  }
  {
    unsigned c = 48 - 8 * CH + CH * (DCH * sub + tid);
    asm volatile("" : "+v"(c));
    c = tid < 8 + DCH ? c : ~0u;
    c = tid < 8 ? 0u : c;
    ctl[tid] = tid == 4 ? DCH : c;
    *(LAS u64*)(scr + SCR_SUM + min(tid, 2 * SUM_STR / 8 - 1) * 8) = 0;
  }
  const int l16 = lane & 15, grp = lane >> 4;
  const int b4 = head & 3, cntoff = WS_CNT + head * CSTR;
  const int bm = (b4 + 1 + (sub >= 3 ? sub - 3 : sub)) & 3;
  const size_t base = (size_t)(bm * 32 + head) << 20;
  const int so = ((w & 1) << bitpos('a')) | (((w >> 1) & 1) << bitpos('b')) | ((w >> 2) << bitpos('c')) | (sub >= 3 ? 1 << bitpos('p') : 0);
  const int vg = l16 * 16 + ((grp & 1) << bitpos('x')) + ((grp >> 1) << bitpos('y'));
  int v0 = sub < 6 ? vg + so : l16 * 16 + grp * 131072 + w * 524288 + head * 256;
  unsigned boff = sub < 6 ? (unsigned)base : 0u, bsz = sub < 6 ? 1u << 20 : 4u << 20;
  int s0[WTH];
#pragma unroll
  for (int i = 0; i < WTH; i++) {
    s0[i] = sub < 6 ? goff(i) : i * 8192;
    asm volatile("" : "+s"(s0[i]));
  }
  asm volatile("" : "+v"(v0), "+s"(boff), "+s"(bsz));
  SB();
  asm volatile("s_waitcnt lgkmcnt(0)" : "+s"(ka), "+s"(kb_), "+s"(kc_));
  auto ptr = [](unsigned lo, unsigned hi) { return (GAS char*)(((u64)hi << 32) | lo); };
  GAS char *const xA = ptr(ka[0], ka[1]), *const ckv = ptr(ka[2], ka[3]), *const wqA = ptr(ka[4], ka[5]), *const wuk = ptr(ka[6], ka[7]),
           *const wuv = ptr(ka[8], ka[9]), *const woA = ptr(ka[10], ka[11]), *const xB = ptr(ka[12], ka[13]), *const kc = ptr(ka[14], ka[15]),
           *const vc = ptr(kb_[0], kb_[1]), *const wqB = ptr(kb_[2], kb_[3]), *const woB = ptr(kb_[4], kb_[5]), *const ws = ptr(kb_[6], kb_[7]),
           *const out = ptr(kc_[0], kc_[1]);
  const u64 e0 = *(const __attribute__((address_space(4))) u64*)(u64)(ws + WS_EPOCH);
  const rsrc_t r0 = mkrs((sub < 6 ? kc : sub == 6 ? wqB : wqA) + boff, bsz);
  SB();
  u32x4 ra[16];
#pragma unroll
  for (int i = 0; i < WTH; i++) ra[i] = ldnt(r0, v0, s0[i]);
  SB();
  u32x4 xp[2][2];
  if (sub >= 6) {
    const GAS char* x = sub == 6 ? xB : xA;
#pragma unroll
    for (int s = 0; s < 2; s++) {
      const GAS u32x4* xs = (const GAS u32x4*)(x + (2 * s + (l16 >> 3)) * 1024 + (64 * w + 16 * grp) * 2);
      xp[s][0] = xs[0]; xp[s][1] = xs[1];
    }
#pragma unroll
    for (int i = WTH; i < 16; i++) ra[i] = ldnt(r0, v0, i * 8192);
    asm volatile("" ::: "memory");
    SB();
  }
  const rsrc_t rws = mkrs(ws, WS_END);
  unsigned mask[4];
#pragma unroll
  for (int v = 0; v < 4; v++) mask[v] = (((lane & 7) >> 1) == v) ? ((lane & 1) ? 0xffff0000u : 0x0000ffffu) : 0u;
  const size_t base4 = (size_t)(b4 * 32 + head) << 20;
  const rsrc_t rk4 = mkrs(kc + base4, 1u << 20), rv4 = mkrs(vc + base4, 1u << 20);
  const int voff = lane * 16, vu = l16 * 16 + ((grp & 1) << upos('x')) + ((grp >> 1) << upos('y'));
  unsigned ub = 0;
#pragma unroll
  for (int i = 0; i < 4; i++) ub |= ((lane >> i) & 1) << ((lane & 16) ? upos('4' + i) : upos('0' + i));
  LAS char* qst = scr + 1024 + w * 512;
  const int qo = WS_QB + head * 2048 + (lane < 32 ? bm : b4) * 512 + (lane & 31) * 16;
  auto qwait = [&](u32x4 g, const Tag& tg) {
    if (!__all(qok(g, tg))) {
      for (unsigned spins = 0;; spins++) {
        asm volatile("" ::: "memory");
        g = ldsc(rws, qo, 0);
        if (__all(qok(g, tg)) || spins > SPIN_CAP) break;
        __builtin_amdgcn_s_sleep(2);
      }
    }
    *(LAS u32x2*)(qst + lane * 8) = u32x2{g[0], g[2]};
  };
  unsigned cur;

  if (__builtin_expect(sub < 6, 1)) {
    const rsrc_t rv = mkrs(vc + base, 1u << 20);
    u32x4 rb[16], g;
#pragma unroll
    for (int i = WTH; i < 32 + NXA + NXB; i++) {
      if (i == N0) asm volatile("s_barrier" ::: "memory");
      if (i >= N0) vmwait<WTH - 1>();
      if (i == 32 + NXA + NXB - WTH) asm volatile("buffer_load_dwordx4 %0, %1, %2, 0 offen sc1" : "=&v"(g) : "v"(qo), "s"(rws));
      if (i < 32) {
        const u32x4 v = ldslot(r0, vg, so, i);
        if (i < 16) ra[i] = v; else rb[i - 16] = v;
      } else {
        const int j = i < 32 + NXA ? i + 16 - NXA : i + 32 - NXA - NXB;
        dma1(r0, vg + goff(j & 3), so ^ goff(j & ~3), (unsigned)(uintptr_t)lds + w * 16384 + (i - 32) * 1024);
      }
      SB();
    }
    asm volatile("s_waitcnt vmcnt(%1)" : "+v"(g) : "n"(WTH), "v"(cpf));
    qwait(g, mktag(e0));
    const u32x4 q = *(LAS u32x4*)(qst + l16 * 16);
    float sc[4] = {0.f, 0.f, 0.f, 0.f};
#pragma unroll 1
    for (int it = 0; it < 2; it++) {
      const rsrc_t rn = it ? rv : r0;
      sc[2] = sc[0]; sc[3] = sc[1];
      sc[1] = k_step<NXA>(ra, q, l16, rn, vg, so ^ (it ? goff(48) : goff(32)), it == 0, lds + w * 16384 + voff);
      sc[0] = k_step<NXB>(rb, q, l16, rn, vg, so ^ (it ? goff(32) : goff(48)), it == 0, lds + w * 16384 + NXA * 1024 + voff);
    }
    const float mx = xrow_max(row_max(fmaxf(fmaxf(sc[0], sc[1]), fmaxf(sc[2], sc[3]))));
    float f, l = 0.f;
    const float M = exp_ref(mx, f);
    unsigned pk[4];
#pragma unroll
    for (int k = 0; k < 4; k++) {
      const float p = fexp2(fmaf(sc[k] - mx, LOG2E, f));
      pk[k] = cvtpk(p, p);
      l += bf_lo(pk[k]);
    }
    l = xrow_sum(row_sum(l));
    cur = 8 * sub + w;
    f32x4 acc = {0.f, 0.f, 0.f, 0.f};
    v_step<0, 1>(ra, pk[0], mask, acc, r0, rv, vg, so ^ goff(16));
    v_step<0, 1>(rb, pk[1], mask, acc, r0, rv, vg, so);
    v_step<0, 2>(ra, pk[2], mask, acc, rk4, rv4, vu, unit_off(cur, ub));
    v_step<0, 0>(rb, pk[3], mask, acc, r0, rv, voff, 0);
    Acc A;
    A.R = rfl(bucket(M));
    const double sm = mkd(0u, p2hi(M, A.R));
    A.o[0] = (lane < 32 ? acc[0] : acc[2]) * sm; A.o[1] = (lane < 32 ? acc[1] : acc[3]) * sm;
    A.l = l * sm;
    b_merge(mkmrg(scr + SCR_SUM, lane, WS_RS + (head * 16 + sub) * 1024, mktag(e0)), scr + 8192, w, lane, A, rws, e0);
  } else {
  {
    __builtin_amdgcn_raw_buffer_store_b32(48 + 6 * CH * DCH, rws, (sub == 6 && tid == 0) ? cntoff : OOB, 0, AUX_SC1);
    {
      const rsrc_t rk = mkrs(wuk, sub == 7 ? 4u << 20 : 0u);
#pragma unroll
      for (int i = 0; i < 16; i++)
        __builtin_amdgcn_raw_ptr_buffer_load_lds(rk, (LAS void*)(lds + w * 16384 + i * 1024), 16,
                                                 (64 * w + grp) * 8192 + head * 256 + ((l16 ^ (4 * (i & 3) + grp)) * 16), i * 4 * 8192, 0, AUX_NT);
    }
    SB();
    f32x4 acc[2] = {{0.f, 0.f, 0.f, 0.f}, {0.f, 0.f, 0.f, 0.f}};
    diag_gemv(ra, xp, mask, acc);
    LAS float* red = (LAS float*)(scr + 8192);
#pragma unroll
    for (int s = 0; s < 2; s++) *(LAS f32x4*)(red + w * 512 + (2 * s + (grp >> 1)) * 128 + 8 * l16 + 4 * (grp & 1)) = acc[s];
    asm volatile("" :: "v"(cpf));
    __builtin_amdgcn_s_waitcnt(0x0f70);
    drain();
    bar();
    if (tid < 128) {
      f32x4 a = {0.f, 0.f, 0.f, 0.f};
#pragma unroll
      for (int k = 0; k < 8; k++) a += *(LAS f32x4*)(red + k * 512 + 4 * tid);
      const unsigned p0 = cvtpk(a[0], a[1]), p1 = cvtpk(a[2], a[3]);
      const Tag tg = mktag(e0);
      stsc(u32x4{p0, tg.th, p1, tg.th}, rws, sub == 6 ? WS_QB + head * 2048 + tid * 16 : OOB, 0);
      *(LAS u32x2*)(scr + 1024 + tid * 8) = u32x2{p0, p1};
    }
  }

  if (sub == 6) {
    const int aid = ((head & 6) << 2) | ((head & 1) << 2) | (head >> 3), b = aid >> 3;
    const rsrc_t rc = mkrs(ckv + (size_t)aid * 524288, 524288);
    const int dvo = spread(lane >> 3, cpos('l'), cpos('m'), cpos('n')) + spread(w, cpos('a'), cpos('b'), cpos('c')) + (((lane & 7) ^ fz(lane >> 3)) * 16);
    const unsigned dlo = (unsigned)(uintptr_t)lds + spread(w, clds('a'), clds('b'), clds('c'));
    auto toff = [&](int t) { return spread(t ^ aid, cpos('t'), cpos('u'), cpos('v')); };
#pragma unroll
    for (int j = 0; j < 16; j++) dma_tile(rc, dvo, toff(j >> 3), dlo + (j >> 3) * 65536, j & 7);
    SB();
    const Tag tg = mktag(e0);
    bar();
    {
      u32x4 t[8];
      unsigned miss = 15;
      __builtin_amdgcn_s_sleep(90);
      for (unsigned spins = 0;; spins++) {
        asm volatile("" ::: "memory");
#pragma unroll
        for (int i = 0; i < 4; i++)
          if (miss >> i & 1) {
            t[2 * i] = ldsc(rws, WS_QABS + (b * 32 + w) * 2048 + lane * 16, i * 16384);
            t[2 * i + 1] = ldsc(rws, WS_QABS + (b * 32 + w) * 2048 + lane * 16 + 1024, i * 16384);
          }
        SB();
#pragma unroll
        for (int i = 0; i < 4; i++) if (__all(qok(t[2 * i], tg) & qok(t[2 * i + 1], tg))) miss &= ~(1u << i);
        if (!miss || spins > SPIN_CAP) break;
        __builtin_amdgcn_s_sleep(4);
      }
#pragma unroll
      for (int i = 0; i < 8; i++) {
        const int row = 8 * (i >> 1) + w;
        *(LAS u32x2*)(scr + row * 1024 + (((32 * (i & 1) + (lane >> 1)) ^ swz(row)) * 16) + (lane & 1) * 8) = u32x2{t[i][0], t[i][2]};
      }
    }
    bar();
    const int kb = w & 3, hb = w >> 2;
    u32x4 qf[16];
    {
      const int row = 16 * hb + l16;
#pragma unroll
      for (int ks = 0; ks < 16; ks++) qf[ks] = *(LAS u32x4*)(scr + row * 1024 + (((4 * ks + grp) ^ swz(row)) * 16));
    }
    LAS float* mx = (LAS float*)scr;
    LAS char* pl = scr + 1024;
    LAS float* lr = (LAS float*)(scr + 8192);
    f32x16 o0, o1;
#pragma unroll
    for (int j = 0; j < 16; j++) { o0[j] = 0.f; o1[j] = 0.f; }
    float m1 = -INFINITY, m2 = -INFINITY, l1 = 0.f;
    const int h1 = 16 * hb + l16, h2 = lane & 31, hh = lane >> 5;
    const int arow = 16 * kb + l16;
    const int tq = (lane & 15) >> 2, tp = lane & 3, tblk = (lane >> 4) & 1;
    const int qa0 = arow * 128 + ((grp ^ fz(arow)) * 16), qa1 = qa0 ^ 64;
    const int pr = 8 * hh + tq, pc = 2 * tblk + (tp >> 1);
    const int pa0 = w * 8192 + pr * 128 + ((pc ^ fz(pr)) * 16) + 8 * (tp & 1), pa1 = w * 8192 + (pr + 4) * 128 + ((pc ^ fz(pr + 4)) * 16) + 8 * (tp & 1);
#pragma unroll 1
    for (int t = 0; t < 8; t++) {
      if (t < 7) asm volatile("s_waitcnt vmcnt(8)" ::: "memory"); else asm volatile("s_waitcnt vmcnt(0)" ::: "memory");
      bar();
      LAS char* img = lds + (t & 1) * 65536;
      f32x4 s0 = {0.f, 0.f, 0.f, 0.f}, s1 = s0;
      u32x4 ka[16];
      u32x2 ta[4][4];
      const int nso = toff(t + 2);
#pragma unroll
      for (int ks = 0; ks < 16; ks++) ka[ks] = *(LAS u32x4*)(img + ((ks & 1) ? qa1 : qa0) + (ks >> 1) * 8192);
      SB();
#pragma unroll
      for (int k2 = 0; k2 < 8; k2++) {
        s0 = mfma16(ka[2 * k2], qf[2 * k2], s0);
        s1 = mfma16(ka[2 * k2 + 1], qf[2 * k2 + 1], s1);
      }
      asm volatile("" : "+v"(s0), "+v"(s1));
      SB();
#pragma unroll
      for (int ks = 0; ks < 4; ks++) {
        ta[ks][0] = trread(img + pa0 + ks * 2048); ta[ks][1] = trread(img + pa1 + ks * 2048);
        ta[ks][2] = trread(img + (pa0 ^ 64) + ks * 2048); ta[ks][3] = trread(img + (pa1 ^ 64) + ks * 2048);
      }
      SB();
      float x[4];
#pragma unroll
      for (int j = 0; j < 4; j++) x[j] = rbf(rbf(s0[j] + s1[j]) * SCALE);
      const float lm = xrow_max(fmaxf(fmaxf(x[0], x[1]), fmaxf(x[2], x[3])));
      if (grp == 0) mx[h1 * 4 + kb] = lm;
      LAS u32x2* pw = (LAS u32x2*)(pl + h1 * 144 + (16 * kb + 4 * grp) * 2);
      u32x2 pp = {cvtpk(fexp2((x[0] - m1) * LOG2E), fexp2((x[1] - m1) * LOG2E)), cvtpk(fexp2((x[2] - m1) * LOG2E), fexp2((x[3] - m1) * LOG2E))};
      *pw = pp;
      bar();
      const f32x4 ma = *(LAS f32x4*)(mx + h1 * 4), mb = *(LAS f32x4*)(mx + h2 * 4);
      u32x4 pb[4];
#pragma unroll
      for (int ks = 0; ks < 4; ks++) pb[ks] = *(LAS u32x4*)(pl + h2 * 144 + (16 * ks + 8 * hh) * 2);
      const float n1 = fmaxf(m1, fmaxf(fmaxf(ma[0], ma[1]), fmaxf(ma[2], ma[3])));
      const float n2 = fmaxf(m2, fmaxf(fmaxf(mb[0], mb[1]), fmaxf(mb[2], mb[3])));
      float al1 = fexp2((m1 - n1) * LOG2E), al2 = fexp2((m2 - n2) * LOG2E);
      asm volatile("" : "+v"(pb[0]), "+v"(pb[1]), "+v"(pb[2]), "+v"(pb[3]));
      if (__any(n2 - m2 > 40.f)) {
        bar();
        pp = u32x2{cvtpk(fexp2((x[0] - n1) * LOG2E), fexp2((x[1] - n1) * LOG2E)), cvtpk(fexp2((x[2] - n1) * LOG2E), fexp2((x[3] - n1) * LOG2E))};
        *pw = pp;
        l1 *= al1;
#pragma unroll
        for (int j = 0; j < 16; j++) { o0[j] *= al2; o1[j] *= al2; }
        al1 = al2 = 1.f;
        bar();
#pragma unroll
        for (int ks = 0; ks < 4; ks++) pb[ks] = *(LAS u32x4*)(pl + h2 * 144 + (16 * ks + 8 * hh) * 2);
      }
      m1 = n1; m2 = n2;
#pragma unroll
      for (int ks = 0; ks < 4; ks++) {
        o0 = mfma32(u32x4{ta[ks][0][0], ta[ks][0][1], ta[ks][1][0], ta[ks][1][1]}, pb[ks], o0);
        o1 = mfma32(u32x4{ta[ks][2][0], ta[ks][2][1], ta[ks][3][0], ta[ks][3][1]}, pb[ks], o1);
        SB();
        if (t < 6) {
          dma_tile(rc, dvo, nso, dlo + (t & 1) * 65536, 2 * ks);
          dma_tile(rc, dvo, nso, dlo + (t & 1) * 65536, 2 * ks + 1);
        }
        SB();
      }
      l1 = (l1 + ((bf_lo(pp[0]) + bf_hi(pp[0])) + (bf_lo(pp[1]) + bf_hi(pp[1])))) * al1;
#pragma unroll
      for (int j = 0; j < 16; j++) { o0[j] *= al2; o1[j] *= al2; }
    }
    bar();
    {
      float am = 0.f;
#pragma unroll
      for (int j = 0; j < 16; j++) am = fmaxf(am, fmaxf(fabsf(o0[j]), fabsf(o1[j])));
      auto sw = __builtin_amdgcn_permlane32_swap(__float_as_uint(am), __float_as_uint(am), false, false);
      am = fmaxf(__uint_as_float(sw[0]), __uint_as_float(sw[1]));
      const int e = max(__builtin_amdgcn_frexp_expf(am), -127);
      LAS char* tp = lds + h2 * 1040 + (64 * w + 4 * hh) * 2;
      auto pkn = [&](float a, float b) { return __builtin_bit_cast(unsigned, __builtin_amdgcn_cvt_pknorm_i16(__builtin_ldexpf(a, -e), __builtin_ldexpf(b, -e))); };
#pragma unroll
      for (int j4 = 0; j4 < 4; j4++) {
        *(LAS u32x2*)(tp + j4 * 16) = u32x2{pkn(o0[4 * j4], o0[4 * j4 + 1]), pkn(o0[4 * j4 + 2], o0[4 * j4 + 3])};
        *(LAS u32x2*)(tp + 64 + j4 * 16) = u32x2{pkn(o1[4 * j4], o1[4 * j4 + 1]), pkn(o1[4 * j4 + 2], o1[4 * j4 + 3])};
      }
      if (hh == 0) *(LAS unsigned char*)(scr + 9216 + h2 * 8 + w) = (unsigned char)(e + 127);
      const float ls = xrow_sum(l1);
      if (grp == 0) lr[kb * 32 + h1] = ls;
      bar();
#pragma unroll
      for (int i = 0; i < 4; i++) {
        u32x4 v = *(LAS u32x4*)(lds + (4 * w + i) * 1040 + lane * 16);
#pragma unroll
        for (int k = 0; k < 4; k++) v[k] = bfi(PMASK, ptag(tg, k), v[k]);
        stsc(v, rws, WS_PO + (aid * 32 + 4 * w) * 1024 + lane * 16, i * 1024);
      }
      if (tid < 32) {
        const u32x2 eb = *(LAS u32x2*)(scr + 9216 + tid * 8);
        const float l = (lr[tid] + lr[32 + tid]) + (lr[64 + tid] + lr[96 + tid]);
        stsc(u32x4{bfi(0xffffu, tg.lo, __float_as_uint(m2)), bfi(255u, ltag(tg), __float_as_uint(l)), eb[0], eb[1]}, rws, WS_PML + (aid * 32 + tid) * 16, 0);
      }
      if (w == 0) {
        const unsigned t = __builtin_amdgcn_readfirstlane(__builtin_amdgcn_raw_ptr_buffer_atomic_add_i32(8 + CH * DCH, rws, lane == 0 ? cntoff : OOB, 0, AUX_SC1));
        const unsigned bs = t + 8 + CH * (lane - 8);
        ctl[lane] = lane == 3 ? t : lane == 4 ? DCH : lane == 5 ? (t + 8 + CH * DCH >= NUNIT ? 0x10000u : 0u) : lane < 8 ? 0u : (lane < 8 + DCH && bs < NUNIT) ? bs : ~0u;
      } else if (w < 4) ctl[tid] = ~0u;
      *(LAS u64*)(scr + SCR_SUM + SUM_STR + min(tid, SUM_STR / 8 - 1) * 8) = 0;
      bar();
    }
    const u32x4 g = ldsc(rws, qo, 0);
    cur = __builtin_amdgcn_readfirstlane(ctl[3]) + w;
#pragma unroll
    for (int i = 0; i < 8; i++) ra[i] = ldunit(i < 4 ? rk4 : rv4, vu, unit_off(cur, ub), i & 3);
    SB();
    qwait(g, mktag(e0));
  } else {
    const Tag tg = mktag(e0);
    bar();
    {
      u32x4 bf[4];
#pragma unroll
      for (int ks = 0; ks < 4; ks++) bf[ks] = *(LAS u32x4*)(scr + 1024 + (l16 & 3) * 256 + ks * 64 + grp * 16);
      LAS char* tt = scr + 2048 + w * 512;
#pragma unroll
      for (int rt = 0; rt < 4; rt++) {
        f32x4 acc = {0.f, 0.f, 0.f, 0.f};
#pragma unroll
        for (int ks = 0; ks < 4; ks++) acc = mfma16(*(LAS u32x4*)(lds + (64 * w + 16 * rt + l16) * 256 + (((4 * ks + grp) ^ l16) * 16)), bf[ks], acc);
        if (l16 < 4) *(LAS u32x2*)(tt + l16 * 128 + (16 * rt + 4 * grp) * 2) = u32x2{cvtpk(acc[0], acc[1]), cvtpk(acc[2], acc[3])};
      }
      asm volatile("s_waitcnt lgkmcnt(0)" ::: "memory");
      const u32x2 v = *(LAS u32x2*)(tt + lane * 8);
      stsc(u32x4{v[0], tg.th, v[1], tg.th}, rws, WS_QABS + (grp * 32 + head) * 2048 + w * 256 + l16 * 16, 0);
    }
    bar();
    u32x4 wf[4][4], wr[16];
    {
      const rsrc_t ro = mkrs(woA + (size_t)head * 131072, 131072);
      dma_rows<16>(ro, 0, lds, 16 * w, lane);
      const rsrc_t rv = mkrs(wuv, 4u << 20);
      const int voff = (64 * w + 16 * grp) * 8192 + head * 256 + l16 * 16;
#pragma unroll
      for (int i = 0; i < 16; i++) wr[i] = ldnt(rv, voff, i * 8192);
      SB();
      asm volatile("s_waitcnt vmcnt(16)" ::: "memory");
      bar();
      wo_frags(lds, w, lane, wf);
      bar();
      const rsrc_t rb = mkrs(woB, 4u << 20);
      const int bvo = (lane >> 3) * 1024 + (((lane & 7) ^ ((lane >> 5) << 1)) * 16);
#pragma unroll
      for (int i = 0; i < 16; i++)
        dma1(rb, (i & 1) ? bvo ^ 64 : bvo, (4 * w + b4) * 131072 + (head >> 2) * 128 + i * 8192, (unsigned)(uintptr_t)lds + w * 16384 + i * 1024);
      SB();
      if (tid < 8) ((LAS unsigned*)(scr + 24576))[tid] = 0u;
    }
    const int hw = 4 * w + b4, cc = head >> 2, roff = WS_RS + hw * 16384 + lane * 16;
    if (w == 0) {
      for (unsigned spins = 0;; spins++) {
        asm volatile("" ::: "memory");
        const u32x4 v = ldsc(rws, WS_PML + ((lane & 31) * 32 + head) * 16, 0);
        if (__builtin_popcount((unsigned)__builtin_amdgcn_ballot_w64(mok(v, tg))) >= NREL || spins > SPIN_CAP) break;
        __builtin_amdgcn_s_sleep(16);
      }
    }
    bar();
    {
      const int b = w >> 1, c4 = tid & 127;
      const unsigned ta = (lane & 1) ? ptag(tg, 2) : ptag(tg, 0), tb = (lane & 1) ? ptag(tg, 3) : ptag(tg, 1);
      u32x4 ml;
      u32x2 po[8];
#pragma unroll
      for (int s = 0; s < 8; s++) asm volatile("" : "=v"(po[s]));
      unsigned missP = 255;
      for (unsigned spins = 0;; spins++) {
        asm volatile("" ::: "memory");
        ml = ldsc(rws, WS_PML + ((8 * b + (lane & 7)) * 32 + head) * 16, 0);
#pragma unroll
        for (int s = 0; s < 8; s++)
          if (missP >> s & 1) po[s] = __builtin_amdgcn_raw_buffer_load_b64(rws, WS_PO + (8 * b * 32 + head) * 1024 + c4 * 8, s * 32768, AUX_SC1);
        SB();
#pragma unroll
        for (int s = 0; s < 8; s++) if (__all((((po[s][0] ^ ta) | (po[s][1] ^ tb)) & PMASK) == 0u)) missP &= ~(1u << s);
        if ((!missP && __all(mok(ml, tg))) || spins > SPIN_CAP) break;
        __builtin_amdgcn_s_sleep(2);
      }
      ml[0] &= 0xffff0000u; ml[1] &= ~255u;
#pragma unroll
      for (int s = 0; s < 8; s++) po[s] = u32x2{bfi(PMASK, 0x00020002u, po[s][0]), bfi(PMASK, 0x00020002u, po[s][1])};
      float M = __uint_as_float(ml[0]);
      M = fmaxf(M, dpp<XOR1>(M)); M = fmaxf(M, dpp<XOR2>(M)); M = fmaxf(M, dpp<XOR7>(M));
      const float f = fexp2((__uint_as_float(ml[0]) - M) * LOG2E);
      float L = f * __uint_as_float(ml[1]);
      L += dpp<XOR1>(L); L += dpp<XOR2>(L); L += dpp<XOR7>(L);
      const float gl = __builtin_ldexpf(f, (int)((((w & 1) ? ml[3] : ml[2]) >> (8 * grp)) & 255) - 142);
      const float gs[8] = {dpp<0x150>(gl), dpp<0x151>(gl), dpp<0x152>(gl), dpp<0x153>(gl), dpp<0x154>(gl), dpp<0x155>(gl), dpp<0x156>(gl), dpp<0x157>(gl)};
      f32x4 a = {0.f, 0.f, 0.f, 0.f};
#pragma unroll
      for (int s = 0; s < 8; s++)
        a += gs[s] * f32x4{(float)(short)po[s][0], (float)((int)po[s][0] >> 16), (float)(short)po[s][1], (float)((int)po[s][1] >> 16)};
      const float inv = (32768.f / 32767.f) / L;
      const u32x2 v = {cvtpk(a[0] * inv, a[1] * inv), cvtpk(a[2] * inv, a[3] * inv)};
      *(LAS u32x2*)(scr + b * 1024 + c4 * 8) = v;
    }
    bar();
    {
      u32x4 xp[2][2];
#pragma unroll
      for (int s = 0; s < 2; s++) {
        LAS u32x4* xs = (LAS u32x4*)(scr + (2 * s + (l16 >> 3)) * 1024 + (64 * w + 16 * grp) * 2);
        xp[s][0] = xs[0]; xp[s][1] = xs[1];
      }
      f32x4 acc[2] = {{0.f, 0.f, 0.f, 0.f}, {0.f, 0.f, 0.f, 0.f}};
      diag_gemv(wr, xp, mask, acc);
      LAS float* red = (LAS float*)(scr + 8192);
#pragma unroll
      for (int s = 0; s < 2; s++) *(LAS f32x4*)(red + w * 512 + (2 * s + (grp >> 1)) * 128 + 8 * l16 + 4 * (grp & 1)) = acc[s];
      bar();
      if (tid < 256) {
        float a = 0.f, b = 0.f;
#pragma unroll
        for (int k = 0; k < 8; k++) { const f32x2 v = *(LAS f32x2*)(red + k * 512 + 2 * tid); a += v[0]; b += v[1]; }
        ((LAS unsigned*)(scr + 4096))[tid] = cvtpk(a, b);
      }
      bar();
    }
    const f32x4 yA = wo_calc(wf, scr + 4096, lane);
    drain();
    y_store(yA, w, lane, head, rws, WS_Y, tg);
    const int ofa = WS_Y + (b4 * 8 + cc) * 16384 + w * 2048 + lane * 16;
    u32x4 g[6], x0, x1;
    auto look = [&]() {
      asm volatile("" ::: "memory");
#pragma unroll
      for (int s = 0; s < 6; s++) g[s] = ldsc(rws, roff + s * 1024, 0);
      x0 = ldsc(rws, ofa, 0); x1 = ldsc(rws, ofa + 1024, 0);
      SB();
    };
    look();
    {
      const int q = l16 >> 2;
      LAS char* img = lds + w * 16384 + (8 * (q >> 1) + 4 * (grp & 1) + 2 * (grp >> 1) + (q & 1)) * 128 + 8 * (lane & 3);
      const int fl = (grp & 1) | (q & 2);
#pragma unroll
      for (int ks = 0; ks < 4; ks++)
#pragma unroll
        for (int tl = 0; tl < 4; tl++) {
          const u32x2 a = trread(img + (tl ^ fl) * 32 + ks * 4096), b = trread(img + (tl ^ fl) * 32 + ks * 4096 + 2048);
          wf[tl][ks] = u32x4{a[0], a[1], b[0], b[1]};
        }
    }
    const rsrc_t rout = mkrs(out, 8192);
    LAS unsigned* cnt = (LAS unsigned*)(scr + 24576);
    LAS char* const re = scr + 8192;
    LAS char* const rA = scr + 8192 + 8192;
    LAS char* rl = scr + 8192 + 10240 + (lane & 31) * 8;
    LAS char* rw = scr + 8192 + 10240 + w * 256 + (lane & 3) * 64 + grp * 16;
    LAS char* rx = rl + (w < 3 ? 8 + w : 11) * 256;
    int oo = lane < 32 ? b4 * 2048 + 1024 + cc * 128 + lane * 4 : OOB, oe = head == 0 && lane == 0 ? WS_EPOCH : OOB;
    asm volatile("" : "+v"(rl), "+v"(rw), "+v"(rx), "+v"(oo), "+v"(oe), "+v"(cnt));
    {
      for (unsigned spins = 0;; spins++) {
        unsigned bad = 0;
#pragma unroll
        for (int s = 0; s < 6; s++) bad |= (g[s][0] ^ tg.lo) | (g[s][2] ^ tg.rb);
        if (__all((bad & (spins > SPIN_CAP * 4 ? 0u : 511u)) == 0u)) break;
        look();
      }
      unsigned pr[3];
#pragma unroll
      for (int t = 0; t < 3; t++) {
        const u32x4 g0 = g[t], g1 = g[t + 3];
        const float R0 = rec_r(g0), R1 = rec_r(g1), Rm = fmaxf(R0, R1);
        const double s0 = mkd(0u, p2hi(R0, Rm)), s1 = mkd(0u, p2hi(R1, Rm));
        const double inv = __builtin_amdgcn_rcp(__builtin_fma(rec_l(g0), s0, rec_l(g1) * s1));
        pr[t] = cvtpk((float)(__builtin_fma(rec_a(g0), s0, rec_a(g1) * s1) * inv), (float)(__builtin_fma(rec_b(g0), s0, rec_b(g1) * s1) * inv));
      }
      *(volatile LAS f32x4*)(re + ((l16 >> 2) * 8 + w) * 256 + (lane & 3) * 64 + grp * 16) = proj(pr, wf, lane);
      const unsigned n = arrive(cnt, lane);
#pragma unroll
      for (int u = 0; u < 2; u++) {
        const int t = u ? 2 : lane >> 5;
        const f32x2 v = rows_sum<8>(re + t * 2048 + (lane & 31) * 8);
        stsc(gpair(v[0], v[1], tg), rws, n != 8u || (u && lane >= 32) ? OOB : WS_YB + ((((b4 + 1 + t) & 3) * 8 + cc) * 4 + b4) * 512 + (lane & 31) * 16, 0);
      }
    }
    {
      for (unsigned spins = 0; !__all(gok(x0, tg) & gok(x1, tg)) && spins <= SPIN_CAP; spins++) {
        asm volatile("" ::: "memory");
        x0 = ldsc(rws, ofa, 0); x1 = ldsc(rws, ofa + 1024, 0);
      }
      const f32x2 r = {half_sum(gval(x0[0]) + gval(x1[0])), half_sum(gval(x0[2]) + gval(x1[2]))};
      if (lane < 32) *(volatile LAS f32x2*)(rA + w * 256 + lane * 8) = r;
      const unsigned n = arrive(cnt + 1, lane);
      const f32x2 v = rows_sum<8>(rA + (lane & 31) * 8);
      __builtin_amdgcn_raw_buffer_store_b32(cvtpk(v[0], v[1]), rout, lane < 32 && n == 8u ? b4 * 2048 + cc * 128 + l16 * 8 + grp * 4 : OOB, 0, AUX_SC1);
    }
    const int ofb = w < 3 ? WS_YB + ((b4 * 8 + cc) * 4 + ((b4 + 1 + w) & 3)) * 512 + (lane & 31) * 16 : OOB;
    u32x4 xb, p[NSLOT];
    unsigned bad;
    for (unsigned spins = 0;; spins++) {
      asm volatile("" ::: "memory");
      xb = ldsc(rws, ofb, 0);
#pragma unroll
      for (int s = 0; s < NSLOT; s++) p[s] = ldsc(rws, roff + 8192 + s * 1024, 0);
      unsigned m = spins > SPIN_CAP * 4 ? 0u : 511u;
      asm volatile("" : "+s"(m));
      SB();
      unsigned ax = min((((xb[0] ^ tg.tl) | (xb[2] ^ tg.tl)) >> 24) | (xb[1] ^ tg.th) | (xb[3] ^ tg.th), w < 3 ? 1u : 0u), az = 0;
#pragma unroll
      for (int s = 0; s < NSLOT; s++) { ax |= p[s][0] ^ tg.lo; az |= p[s][2] ^ tg.rb; }
      bad = az & 512u;
      if (__all(((ax | az) & m) == 0u)) break;
    }
    *(volatile LAS f32x2*)rx = f32x2{gval(xb[0]), gval(xb[2])};
    double S0, S1, L;
    if (__builtin_expect(__all(bad == 0u), 1)) {
      S0 = mkd(p[0][0], p[0][1]); S1 = mkd(p[0][2], p[0][3]); L = rec_l(p[0]);
#pragma unroll
      for (int s = 1; s < NSLOT; s++) { S0 += mkd(p[s][0], p[s][1]); S1 += mkd(p[s][2], p[s][3]); L += rec_l(p[s]); }
    } else {
      float R = -INFINITY;
      S0 = S1 = L = 0.0;
#pragma unroll
      for (int s = 0; s < NSLOT; s++) {
        const float Rs = rec_l(p[s]) != 0.0 ? rec_r(p[s]) : -INFINITY, Rn = fmaxf(R, Rs);
        const double s0 = mkd(0u, p2hi(R, Rn)), s1 = mkd(0u, p2hi(Rs, Rn));
        L = __builtin_fma(rec_l(p[s]), s1, L * s0);
        S0 = __builtin_fma(rec_a(p[s]), s1, S0 * s0); S1 = __builtin_fma(rec_b(p[s]), s1, S1 * s0);
        R = Rn;
      }
    }
    {
      const double inv = __builtin_amdgcn_rcp(L);
      const unsigned pr[1] = {cvtpk((float)(S0 * inv), (float)(S1 * inv))};
      const f32x4 y = proj(pr, wf, lane);
      if ((lane & 12) == 0) *(volatile LAS f32x4*)rw = y;
      const unsigned n = arrive(cnt + 2, lane);
      const f32x2 v = rows_sum<11>(rl);
      __builtin_amdgcn_raw_buffer_store_b32(cvtpk(v[0], v[1]), rout, n == 8u ? oo : OOB, 0, AUX_SC1);
      __builtin_amdgcn_raw_buffer_store_b64(__builtin_bit_cast(u32x2, tg.e), rws, n == 8u ? oe : OOB, 0, AUX_SC1);
    }
    return;
  }
  }
  {
    const u32x4 q4 = *(LAS u32x4*)(qst + 256 + l16 * 16);
    const Mrg mg = mkmrg(scr + SCR_SUM + SUM_STR, lane, WS_RS + (head * 16 + 8 + sub) * 1024, mktag(e0));
    Acc A;
    b_units(ra, rk4, rv4, cur, q4, lane, mask, ctl, rws, cntoff, A, vu, ub);
    b_merge(mg, scr + 8192 + 8704, w, lane, A, rws, e0);
    SB();
    asm volatile("" :: "v"(ra[0]), "v"(ra[1]), "v"(ra[2]), "v"(ra[3]), "v"(ra[4]), "v"(ra[5]), "v"(ra[6]), "v"(ra[7]));
  }
}

extern "C" void kernel_launch(void* const* d_in, const int* in_sizes, int n_in, void* d_out, int out_size, void* d_ws, size_t ws_size,
                              hipStream_t stream) {
  k_all<<<256, 512, 0, stream>>>((const char*)d_in[0], (const char*)d_in[1], (const char*)d_in[2], (const char*)d_in[3], (const char*)d_in[4],
                                 (const char*)d_in[5], (const char*)d_in[6], (const char*)d_in[7], (const char*)d_in[8], (const char*)d_in[9],
                                 (const char*)d_in[10], (char*)d_ws, (unsigned*)d_out);
}
```
